```python
import torch

def _softmax(x, axis=-1):
    return torch.softmax(x, dim=axis)
def _gelu(x):
    return torch.nn.functional.gelu(x, approximate="tanh")
def _silu(x):
    return torch.nn.functional.silu(x)
def _sigmoid(x):
    return torch.sigmoid(x)
def _layernorm(x, axis=-1, eps=1e-5):
    m = x.mean(dim=axis, keepdim=True)
    v = x.var(dim=axis, keepdim=True, unbiased=False)
    return (x - m) / torch.sqrt(v + eps)
def _scatter_add(tgt, idx, src, axis=0):
    return tgt.clone().index_add_(axis, idx, src)
def _segment_sum(src, seg, n):
    out = torch.zeros((n,) + src.shape[1:], dtype=src.dtype)
    return out.index_add_(0, seg, src)
def _pad(x, pw):
    flat = tuple(v for pair in reversed(pw) for v in pair)
    return torch.nn.functional.pad(x, flat)

kernel_name = 'recurrent_scan__mha_block_cached__dense_activation_squared_relu__prompt'

def setup_inputs():

    _w = (lambda a: (lambda t: t.to(torch.bfloat16) if t.is_floating_point() else t)(torch.as_tensor(a)))
    def _A_setup():
        B, Sq, D = 1, 2048, 640
        H, Sk, Dh = 32, 2048, 192
        return {
            "x": _w((lambda *s: torch.randn(*s, dtype=torch.bfloat16) * 0.05)(B, Sq, D)),
            "k_cache": _w((lambda *s: torch.randn(*s, dtype=torch.bfloat16) * 0.8)(B, H, Sk, Dh)),
            "v_cache": _w((lambda *s: torch.randn(*s, dtype=torch.bfloat16) * 0.2)(B, H, Sk, Dh)),
            "wq": _w((lambda *s: torch.randn(*s, dtype=torch.bfloat16) * 0.8)(D, H * Dh) * (D ** -0.5)),
            "wo": _w((lambda *s: torch.randn(*s, dtype=torch.bfloat16) * 0.2)(H * Dh, D) * ((H * Dh) ** -0.5)),
        }
    _A_in = _A_setup()

    def _B_setup():
        B, S, D, F = 1, 2048, 640, 1536
        return {
            "x": _w((lambda *s: torch.randn(*s, dtype=torch.bfloat16) * 0.05)(B, S, D)),
            "w1": _w((lambda *s: torch.randn(*s, dtype=torch.bfloat16) * 0.1)(D, F)),
            "b1": _w((lambda *s: torch.randn(*s, dtype=torch.bfloat16) * 0.8)(F)),
            "w2": _w((lambda *s: torch.randn(*s, dtype=torch.bfloat16) * 0.1)(F, D)),
            "b2": _w((lambda *s: torch.randn(*s, dtype=torch.bfloat16) * 12.8)(D)),
        }
    _B_in = _B_setup()

    return {
        "A_x": _A_in["x"],
        "A_k_cache": _A_in["k_cache"],
        "A_v_cache": _A_in["v_cache"],
        "A_wq": _A_in["wq"],
        "A_wo": _A_in["wo"],
        "B_x": _B_in["x"],
        "B_w1": _B_in["w1"],
        "B_b1": _B_in["b1"],
        "B_w2": _B_in["w2"],
        "B_b2": _B_in["b2"],
    }

def reference(A_x, A_k_cache, A_v_cache, A_wq, A_wo, B_x, B_w1, B_b1, B_w2, B_b2):
    def _A_ref(x, k_cache, v_cache, wq, wo):
        B, Sq, D = x.shape
        H, Dh = 32, 192
        q = (lambda x, *d: x.permute(*d))((x @ wq).reshape(B, Sq, H, Dh), 0, 2, 1, 3)
        scores = torch.einsum("bhqd,bhkd->bhqk", q, k_cache) * (Dh ** -0.5)
        p = _softmax(scores, axis=-1)
        o = torch.einsum("bhqk,bhkd->bhqd", p, v_cache)
        return (lambda x, *d: x.permute(*d))(o, 0, 2, 1, 3).reshape(B, Sq, H * Dh) @ wo

    def _B_ref(x, w1, b1, w2, b2):
        h = torch.einsum("bsd,df->bsf", x, w1) + b1
        zero = torch.zeros_like(h)
        r = torch.where(h > zero, h, zero)
        h = r * r
        out = torch.einsum("bsf,fd->bsd", h, w2) + b2
        return out

    x = A_x
    xn = _layernorm(x, axis=-1)
    s = _A_ref(xn, A_k_cache, A_v_cache, A_wq, A_wo)
    g = _silu(_B_ref(xn, B_w1, B_b1, B_w2, B_b2))
    return x + s * g
```

```cpp
#include <hip/hip_runtime.h>
#include <hip/hip_bf16.h>
#include <stdint.h>
typedef unsigned short u16;
__device__ __forceinline__ float bf2f(u16 v){ return __uint_as_float(((unsigned)v)<<16); }
__device__ __forceinline__ u16 f2bf(float f){ unsigned u=__float_as_uint(f); u += 0x7fffu + ((u>>16)&1u); return (u16)(u>>16); }
__device__ __forceinline__ float rbf(float f){ return bf2f(f2bf(f)); }
#define MIMIC 0
constexpr int S=2048, D=640, H=32, DH=192, F=1536, HD=H*DH;

__global__ void ln_k(const u16* x, float* xn){
  int row=blockIdx.x, t=threadIdx.x;
  float v[10]; float s=0;
  for(int i=0;i<10;i++){ v[i]=bf2f(x[row*D+t+64*i]); s+=v[i]; }
  for(int o=32;o>0;o>>=1) s+=__shfl_xor(s,o);
  float m=s/D; float q=0;
  for(int i=0;i<10;i++){ float d=v[i]-m; q+=d*d; }
  for(int o=32;o>0;o>>=1) q+=__shfl_xor(q,o);
  float var=q/D;
  float r=1.0f/sqrtf(var+1e-5f);
  for(int i=0;i<10;i++) xn[row*D+t+64*i]=rbf((v[i]-m)*r);
}
__global__ void gemm_k(const float* A, const u16* B, float* C, int M,int N,int K,const u16* bias,int mode){
  __shared__ float As[16][17], Bs[16][17];
  int tx=threadIdx.x, ty=threadIdx.y; int r=blockIdx.y*16+ty, c=blockIdx.x*16+tx;
  float acc=0;
  for(int k0=0;k0<K;k0+=16){
    As[ty][tx]=A[(long)r*K+k0+tx]; Bs[ty][tx]=bf2f(B[(long)(k0+ty)*N+c]);
    __syncthreads();
    for(int k=0;k<16;k++) acc+=As[ty][k]*Bs[k][tx];
    __syncthreads();
  }
  if(mode==0) C[(long)r*N+c]=rbf(acc);
  else if(mode==1){
    float h=acc+bf2f(bias[c]); h=h>0?h:0; C[(long)r*N+c]=rbf(h*h);
  }
  else if(mode==2){
    C[(long)r*N+c]=acc+bf2f(bias[c]);
  }
  else {
    C[(long)r*N+c]=acc;
  }
}
__global__ void attn_k(const float* Q, const u16* Kc, const u16* Vc, float* O){
  __shared__ float q[DH]; __shared__ float p[S]; __shared__ float red[256];
  int h=blockIdx.y, qi=blockIdx.x, t=threadIdx.x;
  if(t<DH) q[t]=Q[(long)qi*HD+h*DH+t];
  __syncthreads();
  const float scale=0.07216878364870322f;
  float mx=-1e30f;
  for(int k=t;k<S;k+=256){ const u16* kr=Kc+((long)h*S+k)*DH; float a=0; for(int d=0;d<DH;d++) a+=q[d]*bf2f(kr[d]);
    a*=scale;
    p[k]=a; mx=fmaxf(mx,a);}
  red[t]=mx; __syncthreads();
  for(int o=128;o>0;o>>=1){ if(t<o) red[t]=fmaxf(red[t],red[t+o]); __syncthreads(); }
  mx=red[0]; __syncthreads();
  float sm=0; for(int k=t;k<S;k+=256){ float e=__expf(p[k]-mx); p[k]=e; sm+=e; }
  red[t]=sm; __syncthreads();
  for(int o=128;o>0;o>>=1){ if(t<o) red[t]+=red[t+o]; __syncthreads(); }
  sm=red[0]; __syncthreads();
  if(t<DH){ float a=0; for(int k=0;k<S;k++) a+=p[k]*bf2f(Vc[((long)h*S+k)*DH+t]); O[(long)qi*HD+h*DH+t]=rbf(a/sm); }
}
__global__ void fin_k(const u16* x,const float* s,const float* gpre,u16* out){
  int i=blockIdx.x*256+threadIdx.x;
  float g=gpre[i];
  g=g/(1.f+__expf(-g));
  out[i]=f2bf(bf2f(x[i])+s[i]*g);
}
extern "C" void kernel_launch(void* const* d_in, const int* in_sizes, int n_in, void* d_out, int out_size, void* d_ws, size_t ws_size, hipStream_t stream){
  const u16* x=(const u16*)d_in[0]; const u16* kc=(const u16*)d_in[1]; const u16* vc=(const u16*)d_in[2];
  const u16* wq=(const u16*)d_in[3]; const u16* wo=(const u16*)d_in[4];
  const u16* w1=(const u16*)d_in[6]; const u16* b1=(const u16*)d_in[7]; const u16* w2=(const u16*)d_in[8]; const u16* b2=(const u16*)d_in[9];
  float* ws=(float*)d_ws;
  float* xn=ws; float* q=xn+S*D; float* o=q+(long)S*HD; float* hh=o+(long)S*HD; float* s=hh+(long)S*F; float* g=s+S*D;
  ln_k<<<S,64,0,stream>>>(x,xn);
  gemm_k<<<dim3(HD/16,S/16),dim3(16,16),0,stream>>>(xn,wq,q,S,HD,D,nullptr,0);
  attn_k<<<dim3(S,H),256,0,stream>>>(q,kc,vc,o);
  gemm_k<<<dim3(D/16,S/16),dim3(16,16),0,stream>>>(o,wo,s,S,D,HD,nullptr,3);
  gemm_k<<<dim3(F/16,S/16),dim3(16,16),0,stream>>>(xn,w1,hh,S,F,D,b1,1);
  gemm_k<<<dim3(D/16,S/16),dim3(16,16),0,stream>>>(hh,w2,g,S,D,F,b2,2);
  fin_k<<<S*D/256,256,0,stream>>>(x,s,g,(u16*)d_out);
}
```

```cpp
#include <hip/hip_runtime.h>
#include <hip/hip_bf16.h>
#include <stdint.h>
typedef unsigned short u16;
typedef __attribute__((ext_vector_type(8))) short bf16x8;
typedef __attribute__((ext_vector_type(4))) float f32x4;
typedef __attribute__((ext_vector_type(4))) unsigned u32x4;
typedef __attribute__((ext_vector_type(2))) unsigned u32x2;
constexpr int S_ = 2048, D_ = 640, H_ = 32, DH_ = 192, F_ = 1536, HD_ = H_ * DH_;
constexpr int NW = 4;

__device__ __forceinline__ unsigned rfl(unsigned v) { return __builtin_amdgcn_readfirstlane(v); }
__device__ __forceinline__ u32x4 mksrd(const void* base, unsigned byteoff, unsigned total, unsigned bias) {
  const unsigned long long a = (unsigned long long)base + byteoff - bias;
  u32x4 r = {rfl((unsigned)a), rfl((unsigned)(a >> 32) & 0xffffu), rfl(total - byteoff + bias), 0x00020000u}; return r; }
__device__ __forceinline__ u32x2 p2(unsigned long long a) { u32x2 r = {rfl((unsigned)a), rfl((unsigned)(a >> 32))}; return r; }

__global__ __launch_bounds__(256, 1) void k2_all(const u16* __restrict__ wq, const u16* __restrict__ kc, const u16* __restrict__ vc, const u16* __restrict__ w1,
                                                 const u16* __restrict__ b1, const u16* __restrict__ wo, const u16* __restrict__ w2, const u16* __restrict__ b2,
                                                 const u16* __restrict__ x, u16* __restrict__ out, char* __restrict__ wsp) {
  __shared__ __attribute__((aligned(1024))) char smem[163840];
  const unsigned tid = threadIdx.x, lane = tid & 63u, w = rfl(tid >> 6);
  const unsigned b = blockIdx.x, xc = b & 7u, idx = b >> 3, hh = idx >> 3, qb = idx & 7u, h = xc * 4u + hh;
  if ((unsigned)(size_t)smem != 0u) return;
  { unsigned long long pc; unsigned off = (lane * 128u + idx * 4u + w) * 128u;
    const unsigned xoffs = (qb * 256u + h * 8u + w * 2u + (lane >> 5)) * 1280u + (lane & 31u) * 8u, goffs = (3u << 20) + qb * 1024u + 512u;
    asm volatile("s_getpc_b64 %0\n.Lall_pc:\n\tv_min_u32 %1, .Lall_end-.Lall_pc-4, %1\n\tglobal_load_dword a170, %1, %0\n\t"
                 "global_load_dword a182, %3, %5 sc1\n\t"
                 "global_load_dwordx2 a[172:173], %2, %4\n\tglobal_load_dwordx2 a[174:175], %2, %4 offset:256\n\tglobal_load_dwordx2 a[176:177], %2, %4 offset:512\n\t"
                 "global_load_dwordx2 a[178:179], %2, %4 offset:768\n\tglobal_load_dwordx2 a[180:181], %2, %4 offset:1024"
                 : "=&s"(pc), "+v"(off) : "v"(xoffs), "v"(goffs), "s"(x), "s"(wsp)
                 : "memory", "a170", "a172", "a173", "a174", "a175", "a176", "a177", "a178", "a179", "a180", "a181", "a182"); }
  constexpr unsigned BIAS = 2048u, HEADB = 2048u * 384u;
  const unsigned fo = (h >> 1) * 192u + (h & 1u) * 64u;
  const unsigned oS = 320u * hh, oG = oS + 160u * (xc >> 2);
  const u32x4 srdW = mksrd(wq, h * 384u, 640u * 12288u, BIAS), srdF = mksrd(w1, fo, 640u * 3072u, BIAS);
  const u32x4 srdK = mksrd(kc, h * HEADB, 32u * HEADB, BIAS), srdV = mksrd(vc, h * HEADB, 32u * HEADB, BIAS + 12288u);
  const u32x4 srdS = mksrd(wo, xc * 768u * 1280u + (oS & ~127u), 6144u * 1280u, BIAS), srdG = mksrd(w2, oG & ~127u, 1536u * 1280u, BIAS);
  const unsigned rot = (xc & 3u) ^ (2u * (hh & 1u));
  const unsigned xo = (qb * 16u + w * 4u + rot) * 16u * 1280u + oG;
  const u32x2 ws = p2((unsigned long long)wsp), b1base = p2((unsigned long long)b1 + fo), xres = p2((unsigned long long)x + xo),
              outb = p2((unsigned long long)out + xo), b2base = p2((unsigned long long)b2 + oG);
  const unsigned g = lane >> 4, l15 = lane & 15u, r8 = l15 & 7u;
  const unsigned kaddr0 = 3072u * (l15 >> 3) + 128u * r8 + 16u * (g ^ (r8 & 6u));
  const unsigned qq = (lane >> 2) & 3u, pp = lane & 3u, r8v = 4u * (g & 1u) + qq;
  const unsigned vaddr0 = 49152u + 3072u * (g >> 1) + 128u * r8v + 16u * ((pp >> 1) ^ (r8v & 6u)) + 8u * (pp & 1u);
  const unsigned drow = lane >> 3, dch16 = 16u * ((lane & 7u) ^ (drow & 6u)) + BIAS, lane16 = lane * 16u;
  const unsigned xoffw = l15 * 1280u + 32u * (g & 1u) + 16u * (g >> 1), xoffn = l15 * 1280u + 8u * g;
  asm volatile(
R"(.Lall_start:
s_nop 7
; earlier load -> a170
; earlier load -> a182
; earlier load -> a[172:173]
; earlier load -> a[174:175]
; earlier load -> a[176:177]
; earlier load -> a[178:179]
; earlier load -> a[180:181]
s_and_b32 s86, %[bid], 7
s_lshr_b32 s83, %[bid], 3
s_and_b32 s88, s83, 7
s_lshr_b32 s87, s83, 3
s_and_b32 s89, s87, 1
s_lshl_b32 s90, s89, 1
s_and_b32 s83, s86, 3
s_xor_b32 s90, s90, s83
s_lshl_b32 s91, s88, 4
s_lshl_b32 s83, %[wid], 2
s_add_u32 s91, s91, s83
s_mul_i32 s81, %[wid], 3072
s_mul_i32 s77, %[wid], 98304
s_mul_i32 s79, %[wid], 24576
s_lshl_b32 s82, %[wid], 11
s_mul_i32 s84, s91, 20480
s_mov_b64 s[48:49], %[ws]
s_add_u32 s48, s48, s84
s_addc_u32 s49, s49, 0
s_mov_b32 s83, 12288
v_mad_u32_u24 v244, %[drow], s83, %[dch16]
v_subrev_u32 v245, 896, v244
v_subrev_u32 v246, 1792, v244
s_mov_b32 s83, 3072
v_mad_u32_u24 v45, %[drow], s83, %[dch16]
v_add_u32 v46, 97280, v45
s_xor_b32 s83, s90, 0
s_mul_i32 s83, s83, 20480
v_add_u32 v47, s83, %[lane16]
s_xor_b32 s83, s90, 1
s_mul_i32 s83, s83, 20480
v_add_u32 v48, s83, %[lane16]
s_xor_b32 s83, s90, 2
s_mul_i32 s83, s83, 20480
v_add_u32 v49, s83, %[lane16]
s_xor_b32 s83, s90, 3
s_mul_i32 s83, s83, 20480
v_add_u32 v50, s83, %[lane16]
s_add_u32 m0, s81, 0
s_nop 0
buffer_load_dwordx4 v244, %[srdW], s77 offen offset:0 lds
buffer_load_dwordx4 v245, %[srdW], s77 offen offset:1024 lds
buffer_load_dwordx4 v246, %[srdW], s77 offen offset:2048 lds
s_add_u32 m0, s82, 98304
s_nop 0
buffer_load_dwordx4 v45, %[srdF], s79 offen offset:0 lds
buffer_load_dwordx4 v46, %[srdF], s79 offen offset:1024 lds
s_add_u32 m0, s81, 49152
s_add_u32 s78, s77, 393216
buffer_load_dwordx4 v244, %[srdW], s78 offen offset:0 lds
buffer_load_dwordx4 v245, %[srdW], s78 offen offset:1024 lds
buffer_load_dwordx4 v246, %[srdW], s78 offen offset:2048 lds
s_add_u32 s77, s77, 786432
s_add_u32 s79, s79, 196608
s_waitcnt vmcnt(13)
v_accvgpr_read_b32 v103, a182
s_nop 0
v_readfirstlane_b32 s96, v103
s_nop 0
s_mul_i32 s96, s96, 0x9e3779b1
s_add_u32 s96, s96, 0x7f4a7c15
s_waitcnt vmcnt(12)
v_accvgpr_read_b32 v53, a172
v_lshlrev_b32 v52, 16, v53
v_and_b32 v53, 0xffff0000, v53
v_accvgpr_read_b32 v55, a173
v_lshlrev_b32 v54, 16, v55
v_and_b32 v55, 0xffff0000, v55
s_waitcnt vmcnt(11)
v_accvgpr_read_b32 v57, a174
v_lshlrev_b32 v56, 16, v57
v_and_b32 v57, 0xffff0000, v57
v_accvgpr_read_b32 v59, a175
v_lshlrev_b32 v58, 16, v59
v_and_b32 v59, 0xffff0000, v59
s_waitcnt vmcnt(10)
v_accvgpr_read_b32 v61, a176
v_lshlrev_b32 v60, 16, v61
v_and_b32 v61, 0xffff0000, v61
v_accvgpr_read_b32 v63, a177
v_lshlrev_b32 v62, 16, v63
v_and_b32 v63, 0xffff0000, v63
s_waitcnt vmcnt(9)
v_accvgpr_read_b32 v65, a178
v_lshlrev_b32 v64, 16, v65
v_and_b32 v65, 0xffff0000, v65
v_accvgpr_read_b32 v67, a179
v_lshlrev_b32 v66, 16, v67
v_and_b32 v67, 0xffff0000, v67
s_waitcnt vmcnt(8)
v_accvgpr_read_b32 v69, a180
v_lshlrev_b32 v68, 16, v69
v_and_b32 v69, 0xffff0000, v69
v_accvgpr_read_b32 v71, a181
v_lshlrev_b32 v70, 16, v71
v_and_b32 v71, 0xffff0000, v71
v_add_f32 v72, v52, v53
v_add_f32 v73, v54, v55
v_add_f32 v74, v56, v57
v_add_f32 v75, v58, v59
v_add_f32 v76, v60, v61
v_add_f32 v77, v62, v63
v_add_f32 v78, v64, v65
v_add_f32 v79, v66, v67
v_add_f32 v80, v68, v69
v_add_f32 v81, v70, v71
v_add_f32 v72, v72, v73
v_add_f32 v73, v74, v75
v_add_f32 v74, v76, v77
v_add_f32 v75, v78, v79
v_add_f32 v76, v80, v81
v_add_f32 v102, v72, v73
v_add_f32 v74, v74, v75
v_add_f32 v102, v102, v76
v_add_f32 v102, v102, v74
s_nop 1
v_add_f32_dpp v102, v102, v102 quad_perm:[1,0,3,2] row_mask:0xf bank_mask:0xf
s_nop 1
v_add_f32_dpp v102, v102, v102 quad_perm:[2,3,0,1] row_mask:0xf bank_mask:0xf
s_nop 1
v_add_f32_dpp v102, v102, v102 row_half_mirror row_mask:0xf bank_mask:0xf
s_nop 1
v_add_f32_dpp v102, v102, v102 row_mirror row_mask:0xf bank_mask:0xf
v_mov_b32 v103, v102
s_nop 1
v_permlane16_swap_b32 v102, v103
v_add_f32 v102, v102, v103
v_mul_f32 v104, 0x3acccccd, v102
v_sub_f32 v72, v52, v104
v_sub_f32 v73, v53, v104
v_sub_f32 v74, v54, v104
v_sub_f32 v75, v55, v104
v_sub_f32 v76, v56, v104
v_sub_f32 v77, v57, v104
v_sub_f32 v78, v58, v104
v_sub_f32 v79, v59, v104
v_sub_f32 v80, v60, v104
v_sub_f32 v81, v61, v104
v_sub_f32 v82, v62, v104
v_sub_f32 v83, v63, v104
v_sub_f32 v84, v64, v104
v_sub_f32 v85, v65, v104
v_sub_f32 v86, v66, v104
v_sub_f32 v87, v67, v104
v_sub_f32 v88, v68, v104
v_sub_f32 v89, v69, v104
v_sub_f32 v90, v70, v104
v_sub_f32 v91, v71, v104
v_mul_f32 v102, v72, v72
v_mul_f32 v105, v73, v73
v_fmac_f32 v102, v74, v74
v_fmac_f32 v105, v75, v75
v_fmac_f32 v102, v76, v76
v_fmac_f32 v105, v77, v77
v_fmac_f32 v102, v78, v78
v_fmac_f32 v105, v79, v79
v_fmac_f32 v102, v80, v80
v_fmac_f32 v105, v81, v81
v_fmac_f32 v102, v82, v82
v_fmac_f32 v105, v83, v83
v_fmac_f32 v102, v84, v84
v_fmac_f32 v105, v85, v85
v_fmac_f32 v102, v86, v86
v_fmac_f32 v105, v87, v87
v_fmac_f32 v102, v88, v88
v_fmac_f32 v105, v89, v89
v_fmac_f32 v102, v90, v90
v_fmac_f32 v105, v91, v91
v_add_f32 v102, v102, v105
s_nop 1
v_add_f32_dpp v102, v102, v102 quad_perm:[1,0,3,2] row_mask:0xf bank_mask:0xf
s_nop 1
v_add_f32_dpp v102, v102, v102 quad_perm:[2,3,0,1] row_mask:0xf bank_mask:0xf
s_nop 1
v_add_f32_dpp v102, v102, v102 row_half_mirror row_mask:0xf bank_mask:0xf
s_nop 1
v_add_f32_dpp v102, v102, v102 row_mirror row_mask:0xf bank_mask:0xf
v_mov_b32 v103, v102
s_nop 1
v_permlane16_swap_b32 v102, v103
v_add_f32 v102, v102, v103
v_cvt_pk_bf16_f32 v104, v104, v104
v_lshlrev_b32 v104, 16, v104
v_mul_f32 v102, 0x3acccccd, v102
v_cvt_pk_bf16_f32 v102, v102, v102
v_lshlrev_b32 v102, 16, v102
v_add_f32 v102, 0x3727c5ac, v102
v_cvt_pk_bf16_f32 v102, v102, v102
v_lshlrev_b32 v102, 16, v102
v_sqrt_f32 v102, v102
s_nop 0
)"
R"(v_cvt_pk_bf16_f32 v102, v102, v102
v_lshlrev_b32 v102, 16, v102
v_rcp_f32 v105, v102
v_sub_f32 v72, v52, v104
v_sub_f32 v73, v53, v104
v_sub_f32 v74, v54, v104
v_sub_f32 v75, v55, v104
v_sub_f32 v76, v56, v104
v_sub_f32 v77, v57, v104
v_sub_f32 v78, v58, v104
v_sub_f32 v79, v59, v104
v_sub_f32 v80, v60, v104
v_sub_f32 v81, v61, v104
v_sub_f32 v82, v62, v104
v_sub_f32 v83, v63, v104
v_sub_f32 v84, v64, v104
v_sub_f32 v85, v65, v104
v_sub_f32 v86, v66, v104
v_sub_f32 v87, v67, v104
v_sub_f32 v88, v68, v104
v_sub_f32 v89, v69, v104
v_sub_f32 v90, v70, v104
v_sub_f32 v91, v71, v104
v_fma_f32 v103, -v102, v105, 1.0
v_fma_f32 v105, v103, v105, v105
v_cvt_pk_bf16_f32 v53, v72, v73
v_cvt_pk_bf16_f32 v55, v74, v75
v_cvt_pk_bf16_f32 v57, v76, v77
v_cvt_pk_bf16_f32 v59, v78, v79
v_cvt_pk_bf16_f32 v61, v80, v81
v_cvt_pk_bf16_f32 v63, v82, v83
v_cvt_pk_bf16_f32 v65, v84, v85
v_cvt_pk_bf16_f32 v67, v86, v87
v_cvt_pk_bf16_f32 v69, v88, v89
v_cvt_pk_bf16_f32 v71, v90, v91
v_lshlrev_b32 v52, 16, v53
v_and_b32 v53, 0xffff0000, v53
v_lshlrev_b32 v54, 16, v55
v_and_b32 v55, 0xffff0000, v55
v_lshlrev_b32 v56, 16, v57
v_and_b32 v57, 0xffff0000, v57
v_lshlrev_b32 v58, 16, v59
v_and_b32 v59, 0xffff0000, v59
v_lshlrev_b32 v60, 16, v61
v_and_b32 v61, 0xffff0000, v61
v_lshlrev_b32 v62, 16, v63
v_and_b32 v63, 0xffff0000, v63
v_lshlrev_b32 v64, 16, v65
v_and_b32 v65, 0xffff0000, v65
v_lshlrev_b32 v66, 16, v67
v_and_b32 v67, 0xffff0000, v67
v_lshlrev_b32 v68, 16, v69
v_and_b32 v69, 0xffff0000, v69
v_lshlrev_b32 v70, 16, v71
v_and_b32 v71, 0xffff0000, v71
v_mul_f32 v52, v52, v105
v_mul_f32 v53, v53, v105
v_mul_f32 v54, v54, v105
v_mul_f32 v55, v55, v105
v_mul_f32 v56, v56, v105
v_mul_f32 v57, v57, v105
v_mul_f32 v58, v58, v105
v_mul_f32 v59, v59, v105
v_mul_f32 v60, v60, v105
v_mul_f32 v61, v61, v105
v_mul_f32 v62, v62, v105
v_mul_f32 v63, v63, v105
v_mul_f32 v64, v64, v105
v_mul_f32 v65, v65, v105
v_mul_f32 v66, v66, v105
v_mul_f32 v67, v67, v105
v_mul_f32 v68, v68, v105
v_mul_f32 v69, v69, v105
v_mul_f32 v70, v70, v105
v_mul_f32 v71, v71, v105
v_cvt_pk_bf16_f32 v92, v52, v53
v_cvt_pk_bf16_f32 v93, v54, v55
v_cvt_pk_bf16_f32 v94, v56, v57
v_cvt_pk_bf16_f32 v95, v58, v59
v_cvt_pk_bf16_f32 v96, v60, v61
v_cvt_pk_bf16_f32 v97, v62, v63
v_cvt_pk_bf16_f32 v98, v64, v65
v_cvt_pk_bf16_f32 v99, v66, v67
v_cvt_pk_bf16_f32 v100, v68, v69
v_cvt_pk_bf16_f32 v101, v70, v71
v_bfe_u32 v106, %[lane16], 7, 2
v_lshlrev_b32 v106, 9, v106
v_bfe_u32 v103, %[lane16], 4, 2
v_lshl_add_u32 v106, v103, 7, v106
v_bfe_u32 v103, %[lane16], 6, 1
v_lshl_add_u32 v106, v103, 3, v106
v_bfe_u32 v103, %[lane16], 9, 1
v_lshl_add_u32 v106, v103, 4, v106
s_lshl_b32 s83, %[wid], 5
s_add_u32 s83, s83, 147456
v_add_u32 v106, s83, v106
ds_write_b64 v106, v[92:93] offset:0
ds_write_b64 v106, v[94:95] offset:2048
ds_write_b64 v106, v[96:97] offset:4096
ds_write_b64 v106, v[98:99] offset:6144
ds_write_b64 v106, v[100:101] offset:8192
s_lshl_b32 s83, %[wid], 10
v_add_u32 v106, s83, %[lane16]
v_and_b32 v103, 0x70, v106
v_lshrrev_b32 v102, 7, v106
v_lshl_add_u32 v103, v102, 8, v103
v_add_u32 v106, 147456, v106
s_lshl_b32 s83, s86, 1
s_lshr_b32 s84, s87, 1
s_add_u32 s83, s83, s84
s_lshl_b32 s84, s88, 4
s_add_u32 s83, s83, s84
s_mul_i32 s83, s83, 20480
s_lshl_b32 s84, s89, 7
s_add_u32 s83, s83, s84
s_mov_b64 s[72:73], %[ws]
s_add_u32 s72, s72, s83
s_addc_u32 s73, s73, 0
s_mov_b32 s74, 20352
s_mov_b32 s75, 0x20000
s_waitcnt lgkmcnt(0)
s_barrier
ds_read_b128 v[108:111], v106 offset:0
ds_read_b128 v[112:115], v106 offset:4096
ds_read_b128 v[116:119], v106 offset:8192
s_mov_b32 s83, 0
s_waitcnt lgkmcnt(2)
buffer_store_dwordx4 v[108:111], v103, s[72:75], s83 offen sc1
s_mov_b32 s83, 8192
s_waitcnt lgkmcnt(1)
buffer_store_dwordx4 v[112:115], v103, s[72:75], s83 offen sc1
s_mov_b32 s83, 16384
s_waitcnt lgkmcnt(0)
buffer_store_dwordx4 v[116:119], v103, s[72:75], s83 offen sc1
s_lshl_b32 s83, s88, 10
s_mov_b64 s[62:63], %[ws]
s_add_u32 s62, s62, s83
s_addc_u32 s63, s63, 0
s_add_u32 s62, s62, 3145728
s_addc_u32 s63, s63, 0
s_lshl_b32 s83, s86, 2
s_add_u32 s83, s83, s87
s_lshl_b32 s83, s83, 2
s_add_u32 s64, s62, s83
s_addc_u32 s65, s63, 0
v_bfe_u32 v32, %[lane16], 4, 5
v_lshlrev_b32 v32, 2, v32
v_mov_b32 v33, s96
s_waitcnt vmcnt(0)
s_barrier
s_cmp_lg_u32 %[wid], 0
s_cbranch_scc1 .Lxsig
s_mov_b64 exec, 1
global_store_dword v32, v33, s[64:65] offset:0 sc1
s_mov_b64 exec, -1
.Lxsig:
s_mov_b32 s52, 0x3dd53b95
s_mov_b32 s53, 0x3dd53b95
s_mov_b32 s85, 0
s_mov_b32 s76, s81
v_xor_b32 v40, 64, %[kaddr0]
v_xor_b32 v41, 32, %[vaddr0]
v_xor_b32 v42, 64, %[vaddr0]
v_xor_b32 v43, 96, %[vaddr0]
v_subrev_u32 v247, 49152, %[vaddr0]
v_subrev_u32 v248, 49152, v41
v_subrev_u32 v249, 49152, v42
v_subrev_u32 v250, 49152, v43
v_and_b32 v51, 512, %[lane16]
v_lshlrev_b32 v51, 1, v51
v_sub_u32 v51, 49152, v51
s_lshl_b32 s84, s89, 6
v_xor_b32 v251, s84, %[vaddr0]
v_add_u32 v251, v251, v51
v_xor_b32 v252, s84, v41
v_add_u32 v252, v252, v51
v_xor_b32 v253, s84, v42
v_add_u32 v253, v253, v51
v_xor_b32 v254, s84, v43
v_add_u32 v254, v254, v51
s_add_u32 m0, s81, 12288
s_nop 0
buffer_load_dwordx4 v244, %[srdW], s77 offen offset:0 lds
buffer_load_dwordx4 v245, %[srdW], s77 offen offset:1024 lds
buffer_load_dwordx4 v246, %[srdW], s77 offen offset:2048 lds
s_add_u32 m0, s82, 106496
s_nop 0
buffer_load_dwordx4 v45, %[srdF], s79 offen offset:0 lds
buffer_load_dwordx4 v46, %[srdF], s79 offen offset:1024 lds
s_add_u32 m0, s81, 61440
s_add_u32 s78, s77, 393216
buffer_load_dwordx4 v244, %[srdW], s78 offen offset:0 lds
buffer_load_dwordx4 v245, %[srdW], s78 offen offset:1024 lds
buffer_load_dwordx4 v246, %[srdW], s78 offen offset:2048 lds
s_add_u32 s77, s77, 786432
s_add_u32 s79, s79, 196608
v_lshrrev_b32 v36, 8, %[lane16]
v_lshlrev_b32 v36, 3, v36
v_or_b32 v36, s84, v36
v_xor_b32 v37, 64, v36
)"
R"(global_load_dwordx2 a[160:161], v36, %[b1base] offset:0
global_load_dwordx2 a[162:163], v36, %[b1base] offset:32
global_load_dwordx2 a[164:165], v37, %[b1base] offset:0
global_load_dwordx2 a[166:167], v37, %[b1base] offset:32
s_lshl_b32 s84, %[wid], 3
s_lshl_b32 s84, 0xff, s84
s_mov_b32 s95, 0x20000
.Lxpoll:
global_load_dword v34, v32, s[62:63] offset:0 sc1
s_waitcnt vmcnt(0)
v_cmp_ne_u32 vcc, s96, v34
s_sub_u32 s95, s95, 1
s_cbranch_scc1 .Lxpoll_go
s_and_b32 s83, vcc_lo, s84
s_cbranch_scc0 .Lxpoll_go
s_sleep 1
s_branch .Lxpoll
.Lxpoll_go:
global_load_dwordx4 a[0:3], v47, s[48:49] offset:0 sc1
global_load_dwordx4 a[4:7], v48, s[48:49] offset:0 sc1
global_load_dwordx4 a[8:11], v49, s[48:49] offset:0 sc1
global_load_dwordx4 a[12:15], v50, s[48:49] offset:0 sc1
global_load_dwordx4 a[16:19], v47, s[48:49] offset:1024 sc1
global_load_dwordx4 a[20:23], v48, s[48:49] offset:1024 sc1
global_load_dwordx4 a[24:27], v49, s[48:49] offset:1024 sc1
global_load_dwordx4 a[28:31], v50, s[48:49] offset:1024 sc1
s_add_u32 s48, s48, 2048
s_addc_u32 s49, s49, 0
global_load_dwordx4 a[32:35], v47, s[48:49] offset:0 sc1
global_load_dwordx4 a[36:39], v48, s[48:49] offset:0 sc1
global_load_dwordx4 a[40:43], v49, s[48:49] offset:0 sc1
global_load_dwordx4 a[44:47], v50, s[48:49] offset:0 sc1
global_load_dwordx4 a[48:51], v47, s[48:49] offset:1024 sc1
global_load_dwordx4 a[52:55], v48, s[48:49] offset:1024 sc1
global_load_dwordx4 a[56:59], v49, s[48:49] offset:1024 sc1
global_load_dwordx4 a[60:63], v50, s[48:49] offset:1024 sc1
s_add_u32 s48, s48, 2048
s_addc_u32 s49, s49, 0
s_barrier
ds_read_b64_tr_b16 v[8:9], v247 offset:0
ds_read_b64_tr_b16 v[10:11], v247 offset:6144
ds_read_b64_tr_b16 v[12:13], v248 offset:0
ds_read_b64_tr_b16 v[14:15], v248 offset:6144
ds_read_b64_tr_b16 v[16:17], v249 offset:0
ds_read_b64_tr_b16 v[18:19], v249 offset:6144
ds_read_b64_tr_b16 v[20:21], v250 offset:0
ds_read_b64_tr_b16 v[22:23], v250 offset:6144
; ---- projections
s_waitcnt vmcnt(15) lgkmcnt(4)
v_mfma_f32_16x16x32_bf16 v[52:55], v[8:11], a[0:3], 0
ds_read_b64_tr_b16 v[24:25], v247 offset:1024
ds_read_b64_tr_b16 v[26:27], v247 offset:7168
v_mfma_f32_16x16x32_bf16 v[68:71], v[12:15], a[0:3], 0
s_add_u32 m0, s81, 24576
s_waitcnt vmcnt(14)
v_mfma_f32_16x16x32_bf16 v[72:75], v[12:15], a[4:7], 0
buffer_load_dwordx4 v244, %[srdW], s77 offen offset:0 lds
v_mfma_f32_16x16x32_bf16 v[56:59], v[8:11], a[4:7], 0
s_waitcnt vmcnt(14)
v_mfma_f32_16x16x32_bf16 v[60:63], v[8:11], a[8:11], 0
ds_read_b64_tr_b16 v[28:29], v248 offset:1024
ds_read_b64_tr_b16 v[30:31], v248 offset:7168
v_mfma_f32_16x16x32_bf16 v[76:79], v[12:15], a[8:11], 0
s_waitcnt vmcnt(13)
v_mfma_f32_16x16x32_bf16 v[80:83], v[12:15], a[12:15], 0
v_mfma_f32_16x16x32_bf16 v[64:67], v[8:11], a[12:15], 0
s_waitcnt lgkmcnt(4)
v_mfma_f32_16x16x32_bf16 v[96:99], v[16:19], a[12:15], 0
ds_read_b64_tr_b16 v[32:33], v249 offset:1024
ds_read_b64_tr_b16 v[34:35], v249 offset:7168
buffer_load_dwordx4 v245, %[srdW], s77 offen offset:1024 lds
v_mfma_f32_16x16x32_bf16 v[112:115], v[20:23], a[12:15], 0
v_mfma_f32_16x16x32_bf16 v[108:111], v[20:23], a[8:11], 0
v_mfma_f32_16x16x32_bf16 v[92:95], v[16:19], a[8:11], 0
v_mfma_f32_16x16x32_bf16 v[88:91], v[16:19], a[4:7], 0
ds_read_b64_tr_b16 v[36:37], v250 offset:1024
ds_read_b64_tr_b16 v[38:39], v250 offset:7168
v_mfma_f32_16x16x32_bf16 v[104:107], v[20:23], a[4:7], 0
v_mfma_f32_16x16x32_bf16 v[100:103], v[20:23], a[0:3], 0
v_mfma_f32_16x16x32_bf16 v[84:87], v[16:19], a[0:3], 0
buffer_load_dwordx4 v246, %[srdW], s77 offen offset:2048 lds
s_waitcnt lgkmcnt(4)
v_mfma_f32_16x16x32_bf16 v[116:119], v[24:27], a[0:3], 0
ds_read_b64_tr_b16 v[8:9], v247 offset:2048
ds_read_b64_tr_b16 v[10:11], v247 offset:8192
v_mfma_f32_16x16x32_bf16 v[132:135], v[28:31], a[0:3], 0
v_mfma_f32_16x16x32_bf16 v[136:139], v[28:31], a[4:7], 0
v_mfma_f32_16x16x32_bf16 v[120:123], v[24:27], a[4:7], 0
v_mfma_f32_16x16x32_bf16 v[124:127], v[24:27], a[8:11], 0
ds_read_b64_tr_b16 v[12:13], v248 offset:2048
ds_read_b64_tr_b16 v[14:15], v248 offset:8192
v_mfma_f32_16x16x32_bf16 v[140:143], v[28:31], a[8:11], 0
s_add_u32 m0, s82, 114688
v_mfma_f32_16x16x32_bf16 v[144:147], v[28:31], a[12:15], 0
buffer_load_dwordx4 v45, %[srdF], s79 offen offset:0 lds
v_mfma_f32_16x16x32_bf16 v[128:131], v[24:27], a[12:15], 0
s_waitcnt lgkmcnt(4)
v_mfma_f32_16x16x32_bf16 v[160:163], v[32:35], a[12:15], 0
ds_read_b64_tr_b16 v[16:17], v249 offset:2048
ds_read_b64_tr_b16 v[18:19], v249 offset:8192
v_mfma_f32_16x16x32_bf16 v[176:179], v[36:39], a[12:15], 0
v_mfma_f32_16x16x32_bf16 v[172:175], v[36:39], a[8:11], 0
v_mfma_f32_16x16x32_bf16 v[156:159], v[32:35], a[8:11], 0
v_mfma_f32_16x16x32_bf16 v[152:155], v[32:35], a[4:7], 0
ds_read_b64_tr_b16 v[20:21], v250 offset:2048
ds_read_b64_tr_b16 v[22:23], v250 offset:8192
v_mfma_f32_16x16x32_bf16 v[168:171], v[36:39], a[4:7], 0
buffer_load_dwordx4 v46, %[srdF], s79 offen offset:1024 lds
v_mfma_f32_16x16x32_bf16 v[164:167], v[36:39], a[0:3], 0
v_mfma_f32_16x16x32_bf16 v[148:151], v[32:35], a[0:3], 0
s_waitcnt lgkmcnt(4)
v_mfma_f32_16x16x32_bf16 v[180:183], v[8:11], a[0:3], 0
ds_read_b64_tr_b16 v[24:25], v251 offset:0
ds_read_b64_tr_b16 v[26:27], v251 offset:4096
v_mfma_f32_16x16x32_bf16 v[196:199], v[12:15], a[0:3], 0
v_mfma_f32_16x16x32_bf16 v[200:203], v[12:15], a[4:7], 0
v_mfma_f32_16x16x32_bf16 v[184:187], v[8:11], a[4:7], 0
s_add_u32 m0, s81, 73728
s_add_u32 s78, s77, 393216
v_mfma_f32_16x16x32_bf16 v[188:191], v[8:11], a[8:11], 0
ds_read_b64_tr_b16 v[28:29], v252 offset:0
ds_read_b64_tr_b16 v[30:31], v252 offset:4096
buffer_load_dwordx4 v244, %[srdW], s78 offen offset:0 lds
v_mfma_f32_16x16x32_bf16 v[204:207], v[12:15], a[8:11], 0
v_mfma_f32_16x16x32_bf16 v[208:211], v[12:15], a[12:15], 0
v_mfma_f32_16x16x32_bf16 v[192:195], v[8:11], a[12:15], 0
s_waitcnt lgkmcnt(4)
v_mfma_f32_16x16x32_bf16 v[224:227], v[16:19], a[12:15], 0
ds_read_b64_tr_b16 v[32:33], v253 offset:0
ds_read_b64_tr_b16 v[34:35], v253 offset:4096
v_mfma_f32_16x16x32_bf16 v[240:243], v[20:23], a[12:15], 0
v_mfma_f32_16x16x32_bf16 v[236:239], v[20:23], a[8:11], 0
v_mfma_f32_16x16x32_bf16 v[220:223], v[16:19], a[8:11], 0
buffer_load_dwordx4 v245, %[srdW], s78 offen offset:1024 lds
v_mfma_f32_16x16x32_bf16 v[216:219], v[16:19], a[4:7], 0
ds_read_b64_tr_b16 v[36:37], v254 offset:0
ds_read_b64_tr_b16 v[38:39], v254 offset:4096
v_mfma_f32_16x16x32_bf16 v[232:235], v[20:23], a[4:7], 0
v_mfma_f32_16x16x32_bf16 v[228:231], v[20:23], a[0:3], 0
v_mfma_f32_16x16x32_bf16 v[212:215], v[16:19], a[0:3], 0
s_waitcnt lgkmcnt(4)
v_mfma_f32_16x16x32_bf16 a[96:99], v[24:27], a[0:3], 0
ds_read_b64_tr_b16 v[8:9], %[vaddr0] offset:0
ds_read_b64_tr_b16 v[10:11], %[vaddr0] offset:6144
v_mfma_f32_16x16x32_bf16 a[112:115], v[28:31], a[0:3], 0
v_mfma_f32_16x16x32_bf16 a[116:119], v[28:31], a[4:7], 0
buffer_load_dwordx4 v246, %[srdW], s78 offen offset:2048 lds
v_mfma_f32_16x16x32_bf16 a[100:103], v[24:27], a[4:7], 0
v_mfma_f32_16x16x32_bf16 a[104:107], v[24:27], a[8:11], 0
ds_read_b64_tr_b16 v[12:13], v41 offset:0
ds_read_b64_tr_b16 v[14:15], v41 offset:6144
v_mfma_f32_16x16x32_bf16 a[120:123], v[28:31], a[8:11], 0
v_mfma_f32_16x16x32_bf16 a[124:127], v[28:31], a[12:15], 0
v_mfma_f32_16x16x32_bf16 a[108:111], v[24:27], a[12:15], 0
s_waitcnt lgkmcnt(4)
v_mfma_f32_16x16x32_bf16 a[132:135], v[32:35], a[4:7], 0
ds_read_b64_tr_b16 v[16:17], v42 offset:0
ds_read_b64_tr_b16 v[18:19], v42 offset:6144
s_add_u32 s77, s77, 786432
s_add_u32 s79, s79, 196608
v_mfma_f32_16x16x32_bf16 a[148:151], v[36:39], a[4:7], 0
global_load_dwordx4 a[64:67], v47, s[48:49] offset:0 sc1
v_mfma_f32_16x16x32_bf16 a[144:147], v[36:39], a[0:3], 0
ds_read_b64_tr_b16 v[20:21], v43 offset:0
ds_read_b64_tr_b16 v[22:23], v43 offset:6144
v_mfma_f32_16x16x32_bf16 a[128:131], v[32:35], a[0:3], 0
s_waitcnt vmcnt(20) lgkmcnt(4)
v_mfma_f32_16x16x32_bf16 v[52:55], v[8:11], a[16:19], v[52:55]
ds_read_b64_tr_b16 v[24:25], %[vaddr0] offset:1024
ds_read_b64_tr_b16 v[26:27], %[vaddr0] offset:7168
v_mfma_f32_16x16x32_bf16 v[68:71], v[12:15], a[16:19], v[68:71]
s_waitcnt vmcnt(19)
v_mfma_f32_16x16x32_bf16 v[72:75], v[12:15], a[20:23], v[72:75]
v_mfma_f32_16x16x32_bf16 v[56:59], v[8:11], a[20:23], v[56:59]
global_load_dwordx4 a[68:71], v48, s[48:49] offset:0 sc1
s_waitcnt vmcnt(19)
v_mfma_f32_16x16x32_bf16 v[60:63], v[8:11], a[24:27], v[60:63]
ds_read_b64_tr_b16 v[28:29], v41 offset:1024
ds_read_b64_tr_b16 v[30:31], v41 offset:7168
v_mfma_f32_16x16x32_bf16 v[76:79], v[12:15], a[24:27], v[76:79]
s_waitcnt vmcnt(18)
v_mfma_f32_16x16x32_bf16 v[80:83], v[12:15], a[28:31], v[80:83]
v_mfma_f32_16x16x32_bf16 v[64:67], v[8:11], a[28:31], v[64:67]
s_waitcnt lgkmcnt(4)
v_mfma_f32_16x16x32_bf16 v[96:99], v[16:19], a[28:31], v[96:99]
ds_read_b64_tr_b16 v[32:33], v42 offset:1024
ds_read_b64_tr_b16 v[34:35], v42 offset:7168
v_mfma_f32_16x16x32_bf16 v[112:115], v[20:23], a[28:31], v[112:115]
v_mfma_f32_16x16x32_bf16 v[108:111], v[20:23], a[24:27], v[108:111]
global_load_dwordx4 a[72:75], v49, s[48:49] offset:0 sc1
v_mfma_f32_16x16x32_bf16 v[92:95], v[16:19], a[24:27], v[92:95]
v_mfma_f32_16x16x32_bf16 v[88:91], v[16:19], a[20:23], v[88:91]
ds_read_b64_tr_b16 v[36:37], v43 offset:1024
ds_read_b64_tr_b16 v[38:39], v43 offset:7168
v_mfma_f32_16x16x32_bf16 v[104:107], v[20:23], a[20:23], v[104:107]
v_mfma_f32_16x16x32_bf16 v[100:103], v[20:23], a[16:19], v[100:103]
v_mfma_f32_16x16x32_bf16 v[84:87], v[16:19], a[16:19], v[84:87]
s_waitcnt lgkmcnt(4)
v_mfma_f32_16x16x32_bf16 v[116:119], v[24:27], a[16:19], v[116:119]
ds_read_b64_tr_b16 v[8:9], %[vaddr0] offset:2048
)"
R"(ds_read_b64_tr_b16 v[10:11], %[vaddr0] offset:8192
v_mfma_f32_16x16x32_bf16 v[132:135], v[28:31], a[16:19], v[132:135]
global_load_dwordx4 a[76:79], v50, s[48:49] offset:0 sc1
v_mfma_f32_16x16x32_bf16 v[136:139], v[28:31], a[20:23], v[136:139]
v_mfma_f32_16x16x32_bf16 v[120:123], v[24:27], a[20:23], v[120:123]
v_mfma_f32_16x16x32_bf16 v[124:127], v[24:27], a[24:27], v[124:127]
ds_read_b64_tr_b16 v[12:13], v41 offset:2048
ds_read_b64_tr_b16 v[14:15], v41 offset:8192
v_mfma_f32_16x16x32_bf16 v[140:143], v[28:31], a[24:27], v[140:143]
v_mfma_f32_16x16x32_bf16 v[144:147], v[28:31], a[28:31], v[144:147]
v_mfma_f32_16x16x32_bf16 v[128:131], v[24:27], a[28:31], v[128:131]
s_waitcnt lgkmcnt(4)
v_mfma_f32_16x16x32_bf16 v[160:163], v[32:35], a[28:31], v[160:163]
ds_read_b64_tr_b16 v[16:17], v42 offset:2048
ds_read_b64_tr_b16 v[18:19], v42 offset:8192
global_load_dwordx4 a[80:83], v47, s[48:49] offset:1024 sc1
v_mfma_f32_16x16x32_bf16 v[176:179], v[36:39], a[28:31], v[176:179]
v_mfma_f32_16x16x32_bf16 v[172:175], v[36:39], a[24:27], v[172:175]
v_mfma_f32_16x16x32_bf16 v[156:159], v[32:35], a[24:27], v[156:159]
v_mfma_f32_16x16x32_bf16 v[152:155], v[32:35], a[20:23], v[152:155]
ds_read_b64_tr_b16 v[20:21], v43 offset:2048
ds_read_b64_tr_b16 v[22:23], v43 offset:8192
v_mfma_f32_16x16x32_bf16 v[168:171], v[36:39], a[20:23], v[168:171]
v_mfma_f32_16x16x32_bf16 v[164:167], v[36:39], a[16:19], v[164:167]
v_mfma_f32_16x16x32_bf16 v[148:151], v[32:35], a[16:19], v[148:151]
global_load_dwordx4 a[84:87], v48, s[48:49] offset:1024 sc1
s_waitcnt lgkmcnt(4)
v_mfma_f32_16x16x32_bf16 v[180:183], v[8:11], a[16:19], v[180:183]
ds_read_b64_tr_b16 v[24:25], v251 offset:1024
ds_read_b64_tr_b16 v[26:27], v251 offset:5120
v_mfma_f32_16x16x32_bf16 v[196:199], v[12:15], a[16:19], v[196:199]
v_mfma_f32_16x16x32_bf16 v[200:203], v[12:15], a[20:23], v[200:203]
v_mfma_f32_16x16x32_bf16 v[184:187], v[8:11], a[20:23], v[184:187]
v_mfma_f32_16x16x32_bf16 v[188:191], v[8:11], a[24:27], v[188:191]
ds_read_b64_tr_b16 v[28:29], v252 offset:1024
ds_read_b64_tr_b16 v[30:31], v252 offset:5120
v_mfma_f32_16x16x32_bf16 v[204:207], v[12:15], a[24:27], v[204:207]
v_mfma_f32_16x16x32_bf16 v[208:211], v[12:15], a[28:31], v[208:211]
global_load_dwordx4 a[88:91], v49, s[48:49] offset:1024 sc1
v_mfma_f32_16x16x32_bf16 v[192:195], v[8:11], a[28:31], v[192:195]
s_waitcnt lgkmcnt(4)
v_mfma_f32_16x16x32_bf16 v[224:227], v[16:19], a[28:31], v[224:227]
ds_read_b64_tr_b16 v[32:33], v253 offset:1024
ds_read_b64_tr_b16 v[34:35], v253 offset:5120
v_mfma_f32_16x16x32_bf16 v[240:243], v[20:23], a[28:31], v[240:243]
v_mfma_f32_16x16x32_bf16 v[236:239], v[20:23], a[24:27], v[236:239]
v_mfma_f32_16x16x32_bf16 v[220:223], v[16:19], a[24:27], v[220:223]
v_mfma_f32_16x16x32_bf16 v[216:219], v[16:19], a[20:23], v[216:219]
ds_read_b64_tr_b16 v[36:37], v254 offset:1024
ds_read_b64_tr_b16 v[38:39], v254 offset:5120
v_mfma_f32_16x16x32_bf16 v[232:235], v[20:23], a[20:23], v[232:235]
global_load_dwordx4 a[92:95], v50, s[48:49] offset:1024 sc1
v_mfma_f32_16x16x32_bf16 v[228:231], v[20:23], a[16:19], v[228:231]
v_mfma_f32_16x16x32_bf16 v[212:215], v[16:19], a[16:19], v[212:215]
s_waitcnt lgkmcnt(4)
v_mfma_f32_16x16x32_bf16 a[96:99], v[24:27], a[16:19], a[96:99]
s_barrier
ds_read_b64_tr_b16 v[8:9], v247 offset:12288
ds_read_b64_tr_b16 v[10:11], v247 offset:18432
v_mfma_f32_16x16x32_bf16 a[112:115], v[28:31], a[16:19], a[112:115]
v_mfma_f32_16x16x32_bf16 a[116:119], v[28:31], a[20:23], a[116:119]
v_mfma_f32_16x16x32_bf16 a[100:103], v[24:27], a[20:23], a[100:103]
s_add_u32 s48, s48, 2048
s_addc_u32 s49, s49, 0
v_mfma_f32_16x16x32_bf16 a[104:107], v[24:27], a[24:27], a[104:107]
ds_read_b64_tr_b16 v[12:13], v248 offset:12288
ds_read_b64_tr_b16 v[14:15], v248 offset:18432
v_mfma_f32_16x16x32_bf16 a[120:123], v[28:31], a[24:27], a[120:123]
v_mfma_f32_16x16x32_bf16 a[124:127], v[28:31], a[28:31], a[124:127]
v_mfma_f32_16x16x32_bf16 a[108:111], v[24:27], a[28:31], a[108:111]
s_waitcnt lgkmcnt(4)
v_mfma_f32_16x16x32_bf16 a[132:135], v[32:35], a[20:23], a[132:135]
ds_read_b64_tr_b16 v[16:17], v249 offset:12288
ds_read_b64_tr_b16 v[18:19], v249 offset:18432
v_mfma_f32_16x16x32_bf16 a[148:151], v[36:39], a[20:23], a[148:151]
v_mfma_f32_16x16x32_bf16 a[144:147], v[36:39], a[16:19], a[144:147]
ds_read_b64_tr_b16 v[20:21], v250 offset:12288
ds_read_b64_tr_b16 v[22:23], v250 offset:18432
v_mfma_f32_16x16x32_bf16 a[128:131], v[32:35], a[16:19], a[128:131]
s_waitcnt vmcnt(23) lgkmcnt(4)
v_mfma_f32_16x16x32_bf16 v[52:55], v[8:11], a[32:35], v[52:55]
ds_read_b64_tr_b16 v[24:25], v247 offset:13312
ds_read_b64_tr_b16 v[26:27], v247 offset:19456
v_mfma_f32_16x16x32_bf16 v[68:71], v[12:15], a[32:35], v[68:71]
s_add_u32 m0, s81, 36864
s_waitcnt vmcnt(22)
v_mfma_f32_16x16x32_bf16 v[72:75], v[12:15], a[36:39], v[72:75]
buffer_load_dwordx4 v244, %[srdW], s77 offen offset:0 lds
v_mfma_f32_16x16x32_bf16 v[56:59], v[8:11], a[36:39], v[56:59]
s_waitcnt vmcnt(22)
v_mfma_f32_16x16x32_bf16 v[60:63], v[8:11], a[40:43], v[60:63]
ds_read_b64_tr_b16 v[28:29], v248 offset:13312
ds_read_b64_tr_b16 v[30:31], v248 offset:19456
v_mfma_f32_16x16x32_bf16 v[76:79], v[12:15], a[40:43], v[76:79]
s_waitcnt vmcnt(21)
v_mfma_f32_16x16x32_bf16 v[80:83], v[12:15], a[44:47], v[80:83]
v_mfma_f32_16x16x32_bf16 v[64:67], v[8:11], a[44:47], v[64:67]
s_waitcnt lgkmcnt(4)
v_mfma_f32_16x16x32_bf16 v[96:99], v[16:19], a[44:47], v[96:99]
ds_read_b64_tr_b16 v[32:33], v249 offset:13312
ds_read_b64_tr_b16 v[34:35], v249 offset:19456
buffer_load_dwordx4 v245, %[srdW], s77 offen offset:1024 lds
v_mfma_f32_16x16x32_bf16 v[112:115], v[20:23], a[44:47], v[112:115]
v_mfma_f32_16x16x32_bf16 v[108:111], v[20:23], a[40:43], v[108:111]
v_mfma_f32_16x16x32_bf16 v[92:95], v[16:19], a[40:43], v[92:95]
v_mfma_f32_16x16x32_bf16 v[88:91], v[16:19], a[36:39], v[88:91]
ds_read_b64_tr_b16 v[36:37], v250 offset:13312
ds_read_b64_tr_b16 v[38:39], v250 offset:19456
v_mfma_f32_16x16x32_bf16 v[104:107], v[20:23], a[36:39], v[104:107]
v_mfma_f32_16x16x32_bf16 v[100:103], v[20:23], a[32:35], v[100:103]
v_mfma_f32_16x16x32_bf16 v[84:87], v[16:19], a[32:35], v[84:87]
buffer_load_dwordx4 v246, %[srdW], s77 offen offset:2048 lds
s_waitcnt lgkmcnt(4)
v_mfma_f32_16x16x32_bf16 v[116:119], v[24:27], a[32:35], v[116:119]
ds_read_b64_tr_b16 v[8:9], v247 offset:14336
ds_read_b64_tr_b16 v[10:11], v247 offset:20480
v_mfma_f32_16x16x32_bf16 v[132:135], v[28:31], a[32:35], v[132:135]
v_mfma_f32_16x16x32_bf16 v[136:139], v[28:31], a[36:39], v[136:139]
v_mfma_f32_16x16x32_bf16 v[120:123], v[24:27], a[36:39], v[120:123]
v_mfma_f32_16x16x32_bf16 v[124:127], v[24:27], a[40:43], v[124:127]
ds_read_b64_tr_b16 v[12:13], v248 offset:14336
ds_read_b64_tr_b16 v[14:15], v248 offset:20480
v_mfma_f32_16x16x32_bf16 v[140:143], v[28:31], a[40:43], v[140:143]
s_add_u32 m0, s82, 122880
v_mfma_f32_16x16x32_bf16 v[144:147], v[28:31], a[44:47], v[144:147]
buffer_load_dwordx4 v45, %[srdF], s79 offen offset:0 lds
v_mfma_f32_16x16x32_bf16 v[128:131], v[24:27], a[44:47], v[128:131]
s_waitcnt lgkmcnt(4)
v_mfma_f32_16x16x32_bf16 v[160:163], v[32:35], a[44:47], v[160:163]
ds_read_b64_tr_b16 v[16:17], v249 offset:14336
ds_read_b64_tr_b16 v[18:19], v249 offset:20480
v_mfma_f32_16x16x32_bf16 v[176:179], v[36:39], a[44:47], v[176:179]
v_mfma_f32_16x16x32_bf16 v[172:175], v[36:39], a[40:43], v[172:175]
v_mfma_f32_16x16x32_bf16 v[156:159], v[32:35], a[40:43], v[156:159]
v_mfma_f32_16x16x32_bf16 v[152:155], v[32:35], a[36:39], v[152:155]
ds_read_b64_tr_b16 v[20:21], v250 offset:14336
ds_read_b64_tr_b16 v[22:23], v250 offset:20480
v_mfma_f32_16x16x32_bf16 v[168:171], v[36:39], a[36:39], v[168:171]
buffer_load_dwordx4 v46, %[srdF], s79 offen offset:1024 lds
v_mfma_f32_16x16x32_bf16 v[164:167], v[36:39], a[32:35], v[164:167]
v_mfma_f32_16x16x32_bf16 v[148:151], v[32:35], a[32:35], v[148:151]
s_waitcnt lgkmcnt(4)
v_mfma_f32_16x16x32_bf16 v[180:183], v[8:11], a[32:35], v[180:183]
ds_read_b64_tr_b16 v[24:25], v251 offset:8192
ds_read_b64_tr_b16 v[26:27], v251 offset:12288
v_mfma_f32_16x16x32_bf16 v[196:199], v[12:15], a[32:35], v[196:199]
v_mfma_f32_16x16x32_bf16 v[200:203], v[12:15], a[36:39], v[200:203]
v_mfma_f32_16x16x32_bf16 v[184:187], v[8:11], a[36:39], v[184:187]
s_add_u32 m0, s81, 86016
s_add_u32 s78, s77, 393216
v_mfma_f32_16x16x32_bf16 v[188:191], v[8:11], a[40:43], v[188:191]
ds_read_b64_tr_b16 v[28:29], v252 offset:8192
ds_read_b64_tr_b16 v[30:31], v252 offset:12288
buffer_load_dwordx4 v244, %[srdW], s78 offen offset:0 lds
v_mfma_f32_16x16x32_bf16 v[204:207], v[12:15], a[40:43], v[204:207]
v_mfma_f32_16x16x32_bf16 v[208:211], v[12:15], a[44:47], v[208:211]
v_mfma_f32_16x16x32_bf16 v[192:195], v[8:11], a[44:47], v[192:195]
s_waitcnt lgkmcnt(4)
v_mfma_f32_16x16x32_bf16 v[224:227], v[16:19], a[44:47], v[224:227]
ds_read_b64_tr_b16 v[32:33], v253 offset:8192
ds_read_b64_tr_b16 v[34:35], v253 offset:12288
v_mfma_f32_16x16x32_bf16 v[240:243], v[20:23], a[44:47], v[240:243]
v_mfma_f32_16x16x32_bf16 v[236:239], v[20:23], a[40:43], v[236:239]
v_mfma_f32_16x16x32_bf16 v[220:223], v[16:19], a[40:43], v[220:223]
buffer_load_dwordx4 v245, %[srdW], s78 offen offset:1024 lds
v_mfma_f32_16x16x32_bf16 v[216:219], v[16:19], a[36:39], v[216:219]
ds_read_b64_tr_b16 v[36:37], v254 offset:8192
ds_read_b64_tr_b16 v[38:39], v254 offset:12288
v_mfma_f32_16x16x32_bf16 v[232:235], v[20:23], a[36:39], v[232:235]
v_mfma_f32_16x16x32_bf16 v[228:231], v[20:23], a[32:35], v[228:231]
v_mfma_f32_16x16x32_bf16 v[212:215], v[16:19], a[32:35], v[212:215]
s_waitcnt lgkmcnt(4)
v_mfma_f32_16x16x32_bf16 a[96:99], v[24:27], a[32:35], a[96:99]
ds_read_b64_tr_b16 v[8:9], %[vaddr0] offset:12288
ds_read_b64_tr_b16 v[10:11], %[vaddr0] offset:18432
v_mfma_f32_16x16x32_bf16 a[112:115], v[28:31], a[32:35], a[112:115]
v_mfma_f32_16x16x32_bf16 a[116:119], v[28:31], a[36:39], a[116:119]
buffer_load_dwordx4 v246, %[srdW], s78 offen offset:2048 lds
v_mfma_f32_16x16x32_bf16 a[100:103], v[24:27], a[36:39], a[100:103]
v_mfma_f32_16x16x32_bf16 a[104:107], v[24:27], a[40:43], a[104:107]
ds_read_b64_tr_b16 v[12:13], v41 offset:12288
ds_read_b64_tr_b16 v[14:15], v41 offset:18432
v_mfma_f32_16x16x32_bf16 a[120:123], v[28:31], a[40:43], a[120:123]
v_mfma_f32_16x16x32_bf16 a[124:127], v[28:31], a[44:47], a[124:127]
v_mfma_f32_16x16x32_bf16 a[108:111], v[24:27], a[44:47], a[108:111]
s_waitcnt lgkmcnt(4)
v_mfma_f32_16x16x32_bf16 a[132:135], v[32:35], a[36:39], a[132:135]
ds_read_b64_tr_b16 v[16:17], v42 offset:12288
ds_read_b64_tr_b16 v[18:19], v42 offset:18432
s_add_u32 s77, s77, 786432
s_add_u32 s79, s79, 196608
v_mfma_f32_16x16x32_bf16 a[148:151], v[36:39], a[36:39], a[148:151]
global_load_dwordx4 a[0:3], v47, s[48:49] offset:0 sc1
v_mfma_f32_16x16x32_bf16 a[144:147], v[36:39], a[32:35], a[144:147]
ds_read_b64_tr_b16 v[20:21], v43 offset:12288
ds_read_b64_tr_b16 v[22:23], v43 offset:18432
v_mfma_f32_16x16x32_bf16 a[128:131], v[32:35], a[32:35], a[128:131]
s_waitcnt vmcnt(28) lgkmcnt(4)
v_mfma_f32_16x16x32_bf16 v[52:55], v[8:11], a[48:51], v[52:55]
ds_read_b64_tr_b16 v[24:25], %[vaddr0] offset:13312
)"
R"(ds_read_b64_tr_b16 v[26:27], %[vaddr0] offset:19456
v_mfma_f32_16x16x32_bf16 v[68:71], v[12:15], a[48:51], v[68:71]
s_waitcnt vmcnt(27)
v_mfma_f32_16x16x32_bf16 v[72:75], v[12:15], a[52:55], v[72:75]
v_mfma_f32_16x16x32_bf16 v[56:59], v[8:11], a[52:55], v[56:59]
global_load_dwordx4 a[4:7], v48, s[48:49] offset:0 sc1
s_waitcnt vmcnt(27)
v_mfma_f32_16x16x32_bf16 v[60:63], v[8:11], a[56:59], v[60:63]
ds_read_b64_tr_b16 v[28:29], v41 offset:13312
ds_read_b64_tr_b16 v[30:31], v41 offset:19456
v_mfma_f32_16x16x32_bf16 v[76:79], v[12:15], a[56:59], v[76:79]
s_waitcnt vmcnt(26)
v_mfma_f32_16x16x32_bf16 v[80:83], v[12:15], a[60:63], v[80:83]
v_mfma_f32_16x16x32_bf16 v[64:67], v[8:11], a[60:63], v[64:67]
s_waitcnt lgkmcnt(4)
v_mfma_f32_16x16x32_bf16 v[96:99], v[16:19], a[60:63], v[96:99]
ds_read_b64_tr_b16 v[32:33], v42 offset:13312
ds_read_b64_tr_b16 v[34:35], v42 offset:19456
v_mfma_f32_16x16x32_bf16 v[112:115], v[20:23], a[60:63], v[112:115]
v_mfma_f32_16x16x32_bf16 v[108:111], v[20:23], a[56:59], v[108:111]
global_load_dwordx4 a[8:11], v49, s[48:49] offset:0 sc1
v_mfma_f32_16x16x32_bf16 v[92:95], v[16:19], a[56:59], v[92:95]
v_mfma_f32_16x16x32_bf16 v[88:91], v[16:19], a[52:55], v[88:91]
ds_read_b64_tr_b16 v[36:37], v43 offset:13312
ds_read_b64_tr_b16 v[38:39], v43 offset:19456
v_mfma_f32_16x16x32_bf16 v[104:107], v[20:23], a[52:55], v[104:107]
v_mfma_f32_16x16x32_bf16 v[100:103], v[20:23], a[48:51], v[100:103]
v_mfma_f32_16x16x32_bf16 v[84:87], v[16:19], a[48:51], v[84:87]
s_waitcnt lgkmcnt(4)
v_mfma_f32_16x16x32_bf16 v[116:119], v[24:27], a[48:51], v[116:119]
ds_read_b64_tr_b16 v[8:9], %[vaddr0] offset:14336
ds_read_b64_tr_b16 v[10:11], %[vaddr0] offset:20480
v_mfma_f32_16x16x32_bf16 v[132:135], v[28:31], a[48:51], v[132:135]
global_load_dwordx4 a[12:15], v50, s[48:49] offset:0 sc1
v_mfma_f32_16x16x32_bf16 v[136:139], v[28:31], a[52:55], v[136:139]
v_mfma_f32_16x16x32_bf16 v[120:123], v[24:27], a[52:55], v[120:123]
v_mfma_f32_16x16x32_bf16 v[124:127], v[24:27], a[56:59], v[124:127]
ds_read_b64_tr_b16 v[12:13], v41 offset:14336
ds_read_b64_tr_b16 v[14:15], v41 offset:20480
v_mfma_f32_16x16x32_bf16 v[140:143], v[28:31], a[56:59], v[140:143]
v_mfma_f32_16x16x32_bf16 v[144:147], v[28:31], a[60:63], v[144:147]
v_mfma_f32_16x16x32_bf16 v[128:131], v[24:27], a[60:63], v[128:131]
s_waitcnt lgkmcnt(4)
v_mfma_f32_16x16x32_bf16 v[160:163], v[32:35], a[60:63], v[160:163]
ds_read_b64_tr_b16 v[16:17], v42 offset:14336
ds_read_b64_tr_b16 v[18:19], v42 offset:20480
global_load_dwordx4 a[16:19], v47, s[48:49] offset:1024 sc1
v_mfma_f32_16x16x32_bf16 v[176:179], v[36:39], a[60:63], v[176:179]
v_mfma_f32_16x16x32_bf16 v[172:175], v[36:39], a[56:59], v[172:175]
v_mfma_f32_16x16x32_bf16 v[156:159], v[32:35], a[56:59], v[156:159]
v_mfma_f32_16x16x32_bf16 v[152:155], v[32:35], a[52:55], v[152:155]
ds_read_b64_tr_b16 v[20:21], v43 offset:14336
ds_read_b64_tr_b16 v[22:23], v43 offset:20480
v_mfma_f32_16x16x32_bf16 v[168:171], v[36:39], a[52:55], v[168:171]
v_mfma_f32_16x16x32_bf16 v[164:167], v[36:39], a[48:51], v[164:167]
v_mfma_f32_16x16x32_bf16 v[148:151], v[32:35], a[48:51], v[148:151]
global_load_dwordx4 a[20:23], v48, s[48:49] offset:1024 sc1
s_waitcnt lgkmcnt(4)
v_mfma_f32_16x16x32_bf16 v[180:183], v[8:11], a[48:51], v[180:183]
ds_read_b64_tr_b16 v[24:25], v251 offset:9216
ds_read_b64_tr_b16 v[26:27], v251 offset:13312
v_mfma_f32_16x16x32_bf16 v[196:199], v[12:15], a[48:51], v[196:199]
v_mfma_f32_16x16x32_bf16 v[200:203], v[12:15], a[52:55], v[200:203]
v_mfma_f32_16x16x32_bf16 v[184:187], v[8:11], a[52:55], v[184:187]
v_mfma_f32_16x16x32_bf16 v[188:191], v[8:11], a[56:59], v[188:191]
ds_read_b64_tr_b16 v[28:29], v252 offset:9216
ds_read_b64_tr_b16 v[30:31], v252 offset:13312
v_mfma_f32_16x16x32_bf16 v[204:207], v[12:15], a[56:59], v[204:207]
v_mfma_f32_16x16x32_bf16 v[208:211], v[12:15], a[60:63], v[208:211]
global_load_dwordx4 a[24:27], v49, s[48:49] offset:1024 sc1
v_mfma_f32_16x16x32_bf16 v[192:195], v[8:11], a[60:63], v[192:195]
s_waitcnt lgkmcnt(4)
v_mfma_f32_16x16x32_bf16 v[224:227], v[16:19], a[60:63], v[224:227]
ds_read_b64_tr_b16 v[32:33], v253 offset:9216
ds_read_b64_tr_b16 v[34:35], v253 offset:13312
v_mfma_f32_16x16x32_bf16 v[240:243], v[20:23], a[60:63], v[240:243]
v_mfma_f32_16x16x32_bf16 v[236:239], v[20:23], a[56:59], v[236:239]
v_mfma_f32_16x16x32_bf16 v[220:223], v[16:19], a[56:59], v[220:223]
v_mfma_f32_16x16x32_bf16 v[216:219], v[16:19], a[52:55], v[216:219]
ds_read_b64_tr_b16 v[36:37], v254 offset:9216
ds_read_b64_tr_b16 v[38:39], v254 offset:13312
v_mfma_f32_16x16x32_bf16 v[232:235], v[20:23], a[52:55], v[232:235]
global_load_dwordx4 a[28:31], v50, s[48:49] offset:1024 sc1
v_mfma_f32_16x16x32_bf16 v[228:231], v[20:23], a[48:51], v[228:231]
v_mfma_f32_16x16x32_bf16 v[212:215], v[16:19], a[48:51], v[212:215]
s_waitcnt lgkmcnt(4)
v_mfma_f32_16x16x32_bf16 a[96:99], v[24:27], a[48:51], a[96:99]
s_waitcnt vmcnt(24)
s_barrier
ds_read_b64_tr_b16 v[8:9], v247 offset:24576
ds_read_b64_tr_b16 v[10:11], v247 offset:30720
v_mfma_f32_16x16x32_bf16 a[112:115], v[28:31], a[48:51], a[112:115]
v_mfma_f32_16x16x32_bf16 a[116:119], v[28:31], a[52:55], a[116:119]
v_mfma_f32_16x16x32_bf16 a[100:103], v[24:27], a[52:55], a[100:103]
s_add_u32 s48, s48, 2048
s_addc_u32 s49, s49, 0
v_mfma_f32_16x16x32_bf16 a[104:107], v[24:27], a[56:59], a[104:107]
ds_read_b64_tr_b16 v[12:13], v248 offset:24576
ds_read_b64_tr_b16 v[14:15], v248 offset:30720
v_mfma_f32_16x16x32_bf16 a[120:123], v[28:31], a[56:59], a[120:123]
v_mfma_f32_16x16x32_bf16 a[124:127], v[28:31], a[60:63], a[124:127]
v_mfma_f32_16x16x32_bf16 a[108:111], v[24:27], a[60:63], a[108:111]
s_waitcnt lgkmcnt(4)
v_mfma_f32_16x16x32_bf16 a[132:135], v[32:35], a[52:55], a[132:135]
ds_read_b64_tr_b16 v[16:17], v249 offset:24576
ds_read_b64_tr_b16 v[18:19], v249 offset:30720
v_mfma_f32_16x16x32_bf16 a[148:151], v[36:39], a[52:55], a[148:151]
v_mfma_f32_16x16x32_bf16 a[144:147], v[36:39], a[48:51], a[144:147]
ds_read_b64_tr_b16 v[20:21], v250 offset:24576
ds_read_b64_tr_b16 v[22:23], v250 offset:30720
v_mfma_f32_16x16x32_bf16 a[128:131], v[32:35], a[48:51], a[128:131]
s_waitcnt vmcnt(23) lgkmcnt(4)
v_mfma_f32_16x16x32_bf16 v[52:55], v[8:11], a[64:67], v[52:55]
ds_read_b64_tr_b16 v[24:25], v247 offset:25600
ds_read_b64_tr_b16 v[26:27], v247 offset:31744
v_mfma_f32_16x16x32_bf16 v[68:71], v[12:15], a[64:67], v[68:71]
s_add_u32 m0, s81, 0
s_waitcnt vmcnt(22)
v_mfma_f32_16x16x32_bf16 v[72:75], v[12:15], a[68:71], v[72:75]
buffer_load_dwordx4 v244, %[srdW], s77 offen offset:0 lds
v_mfma_f32_16x16x32_bf16 v[56:59], v[8:11], a[68:71], v[56:59]
s_waitcnt vmcnt(22)
v_mfma_f32_16x16x32_bf16 v[60:63], v[8:11], a[72:75], v[60:63]
ds_read_b64_tr_b16 v[28:29], v248 offset:25600
ds_read_b64_tr_b16 v[30:31], v248 offset:31744
v_mfma_f32_16x16x32_bf16 v[76:79], v[12:15], a[72:75], v[76:79]
s_waitcnt vmcnt(21)
v_mfma_f32_16x16x32_bf16 v[80:83], v[12:15], a[76:79], v[80:83]
v_mfma_f32_16x16x32_bf16 v[64:67], v[8:11], a[76:79], v[64:67]
s_waitcnt lgkmcnt(4)
v_mfma_f32_16x16x32_bf16 v[96:99], v[16:19], a[76:79], v[96:99]
ds_read_b64_tr_b16 v[32:33], v249 offset:25600
ds_read_b64_tr_b16 v[34:35], v249 offset:31744
buffer_load_dwordx4 v245, %[srdW], s77 offen offset:1024 lds
v_mfma_f32_16x16x32_bf16 v[112:115], v[20:23], a[76:79], v[112:115]
v_mfma_f32_16x16x32_bf16 v[108:111], v[20:23], a[72:75], v[108:111]
v_mfma_f32_16x16x32_bf16 v[92:95], v[16:19], a[72:75], v[92:95]
v_mfma_f32_16x16x32_bf16 v[88:91], v[16:19], a[68:71], v[88:91]
ds_read_b64_tr_b16 v[36:37], v250 offset:25600
ds_read_b64_tr_b16 v[38:39], v250 offset:31744
v_mfma_f32_16x16x32_bf16 v[104:107], v[20:23], a[68:71], v[104:107]
v_mfma_f32_16x16x32_bf16 v[100:103], v[20:23], a[64:67], v[100:103]
v_mfma_f32_16x16x32_bf16 v[84:87], v[16:19], a[64:67], v[84:87]
buffer_load_dwordx4 v246, %[srdW], s77 offen offset:2048 lds
s_waitcnt lgkmcnt(4)
v_mfma_f32_16x16x32_bf16 v[116:119], v[24:27], a[64:67], v[116:119]
ds_read_b64_tr_b16 v[8:9], v247 offset:26624
ds_read_b64_tr_b16 v[10:11], v247 offset:32768
v_mfma_f32_16x16x32_bf16 v[132:135], v[28:31], a[64:67], v[132:135]
v_mfma_f32_16x16x32_bf16 v[136:139], v[28:31], a[68:71], v[136:139]
v_mfma_f32_16x16x32_bf16 v[120:123], v[24:27], a[68:71], v[120:123]
v_mfma_f32_16x16x32_bf16 v[124:127], v[24:27], a[72:75], v[124:127]
ds_read_b64_tr_b16 v[12:13], v248 offset:26624
ds_read_b64_tr_b16 v[14:15], v248 offset:32768
v_mfma_f32_16x16x32_bf16 v[140:143], v[28:31], a[72:75], v[140:143]
s_add_u32 m0, s82, 98304
v_mfma_f32_16x16x32_bf16 v[144:147], v[28:31], a[76:79], v[144:147]
buffer_load_dwordx4 v45, %[srdF], s79 offen offset:0 lds
v_mfma_f32_16x16x32_bf16 v[128:131], v[24:27], a[76:79], v[128:131]
s_waitcnt lgkmcnt(4)
v_mfma_f32_16x16x32_bf16 v[160:163], v[32:35], a[76:79], v[160:163]
ds_read_b64_tr_b16 v[16:17], v249 offset:26624
ds_read_b64_tr_b16 v[18:19], v249 offset:32768
v_mfma_f32_16x16x32_bf16 v[176:179], v[36:39], a[76:79], v[176:179]
v_mfma_f32_16x16x32_bf16 v[172:175], v[36:39], a[72:75], v[172:175]
v_mfma_f32_16x16x32_bf16 v[156:159], v[32:35], a[72:75], v[156:159]
v_mfma_f32_16x16x32_bf16 v[152:155], v[32:35], a[68:71], v[152:155]
ds_read_b64_tr_b16 v[20:21], v250 offset:26624
ds_read_b64_tr_b16 v[22:23], v250 offset:32768
v_mfma_f32_16x16x32_bf16 v[168:171], v[36:39], a[68:71], v[168:171]
buffer_load_dwordx4 v46, %[srdF], s79 offen offset:1024 lds
v_mfma_f32_16x16x32_bf16 v[164:167], v[36:39], a[64:67], v[164:167]
v_mfma_f32_16x16x32_bf16 v[148:151], v[32:35], a[64:67], v[148:151]
s_waitcnt lgkmcnt(4)
v_mfma_f32_16x16x32_bf16 v[180:183], v[8:11], a[64:67], v[180:183]
ds_read_b64_tr_b16 v[24:25], v251 offset:16384
ds_read_b64_tr_b16 v[26:27], v251 offset:20480
v_mfma_f32_16x16x32_bf16 v[196:199], v[12:15], a[64:67], v[196:199]
v_mfma_f32_16x16x32_bf16 v[200:203], v[12:15], a[68:71], v[200:203]
v_mfma_f32_16x16x32_bf16 v[184:187], v[8:11], a[68:71], v[184:187]
s_add_u32 m0, s81, 49152
s_add_u32 s78, s77, 393216
v_mfma_f32_16x16x32_bf16 v[188:191], v[8:11], a[72:75], v[188:191]
ds_read_b64_tr_b16 v[28:29], v252 offset:16384
ds_read_b64_tr_b16 v[30:31], v252 offset:20480
buffer_load_dwordx4 v244, %[srdW], s78 offen offset:0 lds
v_mfma_f32_16x16x32_bf16 v[204:207], v[12:15], a[72:75], v[204:207]
v_mfma_f32_16x16x32_bf16 v[208:211], v[12:15], a[76:79], v[208:211]
v_mfma_f32_16x16x32_bf16 v[192:195], v[8:11], a[76:79], v[192:195]
s_waitcnt lgkmcnt(4)
v_mfma_f32_16x16x32_bf16 v[224:227], v[16:19], a[76:79], v[224:227]
ds_read_b64_tr_b16 v[32:33], v253 offset:16384
ds_read_b64_tr_b16 v[34:35], v253 offset:20480
v_mfma_f32_16x16x32_bf16 v[240:243], v[20:23], a[76:79], v[240:243]
v_mfma_f32_16x16x32_bf16 v[236:239], v[20:23], a[72:75], v[236:239]
v_mfma_f32_16x16x32_bf16 v[220:223], v[16:19], a[72:75], v[220:223]
buffer_load_dwordx4 v245, %[srdW], s78 offen offset:1024 lds
v_mfma_f32_16x16x32_bf16 v[216:219], v[16:19], a[68:71], v[216:219]
ds_read_b64_tr_b16 v[36:37], v254 offset:16384
ds_read_b64_tr_b16 v[38:39], v254 offset:20480
)"
R"(v_mfma_f32_16x16x32_bf16 v[232:235], v[20:23], a[68:71], v[232:235]
v_mfma_f32_16x16x32_bf16 v[228:231], v[20:23], a[64:67], v[228:231]
v_mfma_f32_16x16x32_bf16 v[212:215], v[16:19], a[64:67], v[212:215]
s_waitcnt lgkmcnt(4)
v_mfma_f32_16x16x32_bf16 a[96:99], v[24:27], a[64:67], a[96:99]
ds_read_b64_tr_b16 v[8:9], %[vaddr0] offset:24576
ds_read_b64_tr_b16 v[10:11], %[vaddr0] offset:30720
v_mfma_f32_16x16x32_bf16 a[112:115], v[28:31], a[64:67], a[112:115]
v_mfma_f32_16x16x32_bf16 a[116:119], v[28:31], a[68:71], a[116:119]
buffer_load_dwordx4 v246, %[srdW], s78 offen offset:2048 lds
v_mfma_f32_16x16x32_bf16 a[100:103], v[24:27], a[68:71], a[100:103]
v_mfma_f32_16x16x32_bf16 a[104:107], v[24:27], a[72:75], a[104:107]
ds_read_b64_tr_b16 v[12:13], v41 offset:24576
ds_read_b64_tr_b16 v[14:15], v41 offset:30720
v_mfma_f32_16x16x32_bf16 a[120:123], v[28:31], a[72:75], a[120:123]
v_mfma_f32_16x16x32_bf16 a[124:127], v[28:31], a[76:79], a[124:127]
v_mfma_f32_16x16x32_bf16 a[108:111], v[24:27], a[76:79], a[108:111]
s_waitcnt lgkmcnt(4)
v_mfma_f32_16x16x32_bf16 a[132:135], v[32:35], a[68:71], a[132:135]
ds_read_b64_tr_b16 v[16:17], v42 offset:24576
ds_read_b64_tr_b16 v[18:19], v42 offset:30720
s_add_u32 s77, s77, 786432
s_add_u32 s79, s79, 196608
v_mfma_f32_16x16x32_bf16 a[148:151], v[36:39], a[68:71], a[148:151]
global_load_dwordx4 a[32:35], v47, s[48:49] offset:0 sc1
v_mfma_f32_16x16x32_bf16 a[144:147], v[36:39], a[64:67], a[144:147]
ds_read_b64_tr_b16 v[20:21], v43 offset:24576
ds_read_b64_tr_b16 v[22:23], v43 offset:30720
v_mfma_f32_16x16x32_bf16 a[128:131], v[32:35], a[64:67], a[128:131]
s_waitcnt vmcnt(28) lgkmcnt(4)
v_mfma_f32_16x16x32_bf16 v[52:55], v[8:11], a[80:83], v[52:55]
ds_read_b64_tr_b16 v[24:25], %[vaddr0] offset:25600
ds_read_b64_tr_b16 v[26:27], %[vaddr0] offset:31744
v_mfma_f32_16x16x32_bf16 v[68:71], v[12:15], a[80:83], v[68:71]
s_waitcnt vmcnt(27)
v_mfma_f32_16x16x32_bf16 v[72:75], v[12:15], a[84:87], v[72:75]
v_mfma_f32_16x16x32_bf16 v[56:59], v[8:11], a[84:87], v[56:59]
global_load_dwordx4 a[36:39], v48, s[48:49] offset:0 sc1
s_waitcnt vmcnt(27)
v_mfma_f32_16x16x32_bf16 v[60:63], v[8:11], a[88:91], v[60:63]
ds_read_b64_tr_b16 v[28:29], v41 offset:25600
ds_read_b64_tr_b16 v[30:31], v41 offset:31744
v_mfma_f32_16x16x32_bf16 v[76:79], v[12:15], a[88:91], v[76:79]
s_waitcnt vmcnt(26)
v_mfma_f32_16x16x32_bf16 v[80:83], v[12:15], a[92:95], v[80:83]
v_mfma_f32_16x16x32_bf16 v[64:67], v[8:11], a[92:95], v[64:67]
s_waitcnt lgkmcnt(4)
v_mfma_f32_16x16x32_bf16 v[96:99], v[16:19], a[92:95], v[96:99]
ds_read_b64_tr_b16 v[32:33], v42 offset:25600
ds_read_b64_tr_b16 v[34:35], v42 offset:31744
v_mfma_f32_16x16x32_bf16 v[112:115], v[20:23], a[92:95], v[112:115]
v_mfma_f32_16x16x32_bf16 v[108:111], v[20:23], a[88:91], v[108:111]
global_load_dwordx4 a[40:43], v49, s[48:49] offset:0 sc1
v_mfma_f32_16x16x32_bf16 v[92:95], v[16:19], a[88:91], v[92:95]
v_mfma_f32_16x16x32_bf16 v[88:91], v[16:19], a[84:87], v[88:91]
ds_read_b64_tr_b16 v[36:37], v43 offset:25600
ds_read_b64_tr_b16 v[38:39], v43 offset:31744
v_mfma_f32_16x16x32_bf16 v[104:107], v[20:23], a[84:87], v[104:107]
v_mfma_f32_16x16x32_bf16 v[100:103], v[20:23], a[80:83], v[100:103]
v_mfma_f32_16x16x32_bf16 v[84:87], v[16:19], a[80:83], v[84:87]
s_waitcnt lgkmcnt(4)
v_mfma_f32_16x16x32_bf16 v[116:119], v[24:27], a[80:83], v[116:119]
ds_read_b64_tr_b16 v[8:9], %[vaddr0] offset:26624
ds_read_b64_tr_b16 v[10:11], %[vaddr0] offset:32768
v_mfma_f32_16x16x32_bf16 v[132:135], v[28:31], a[80:83], v[132:135]
global_load_dwordx4 a[44:47], v50, s[48:49] offset:0 sc1
v_mfma_f32_16x16x32_bf16 v[136:139], v[28:31], a[84:87], v[136:139]
v_mfma_f32_16x16x32_bf16 v[120:123], v[24:27], a[84:87], v[120:123]
v_mfma_f32_16x16x32_bf16 v[124:127], v[24:27], a[88:91], v[124:127]
ds_read_b64_tr_b16 v[12:13], v41 offset:26624
ds_read_b64_tr_b16 v[14:15], v41 offset:32768
v_mfma_f32_16x16x32_bf16 v[140:143], v[28:31], a[88:91], v[140:143]
v_mfma_f32_16x16x32_bf16 v[144:147], v[28:31], a[92:95], v[144:147]
v_mfma_f32_16x16x32_bf16 v[128:131], v[24:27], a[92:95], v[128:131]
s_waitcnt lgkmcnt(4)
v_mfma_f32_16x16x32_bf16 v[160:163], v[32:35], a[92:95], v[160:163]
ds_read_b64_tr_b16 v[16:17], v42 offset:26624
ds_read_b64_tr_b16 v[18:19], v42 offset:32768
global_load_dwordx4 a[48:51], v47, s[48:49] offset:1024 sc1
v_mfma_f32_16x16x32_bf16 v[176:179], v[36:39], a[92:95], v[176:179]
v_mfma_f32_16x16x32_bf16 v[172:175], v[36:39], a[88:91], v[172:175]
v_mfma_f32_16x16x32_bf16 v[156:159], v[32:35], a[88:91], v[156:159]
v_mfma_f32_16x16x32_bf16 v[152:155], v[32:35], a[84:87], v[152:155]
ds_read_b64_tr_b16 v[20:21], v43 offset:26624
ds_read_b64_tr_b16 v[22:23], v43 offset:32768
v_mfma_f32_16x16x32_bf16 v[168:171], v[36:39], a[84:87], v[168:171]
v_mfma_f32_16x16x32_bf16 v[164:167], v[36:39], a[80:83], v[164:167]
v_mfma_f32_16x16x32_bf16 v[148:151], v[32:35], a[80:83], v[148:151]
global_load_dwordx4 a[52:55], v48, s[48:49] offset:1024 sc1
s_waitcnt lgkmcnt(4)
v_mfma_f32_16x16x32_bf16 v[180:183], v[8:11], a[80:83], v[180:183]
ds_read_b64_tr_b16 v[24:25], v251 offset:17408
ds_read_b64_tr_b16 v[26:27], v251 offset:21504
v_mfma_f32_16x16x32_bf16 v[196:199], v[12:15], a[80:83], v[196:199]
v_mfma_f32_16x16x32_bf16 v[200:203], v[12:15], a[84:87], v[200:203]
v_mfma_f32_16x16x32_bf16 v[184:187], v[8:11], a[84:87], v[184:187]
v_mfma_f32_16x16x32_bf16 v[188:191], v[8:11], a[88:91], v[188:191]
ds_read_b64_tr_b16 v[28:29], v252 offset:17408
ds_read_b64_tr_b16 v[30:31], v252 offset:21504
v_mfma_f32_16x16x32_bf16 v[204:207], v[12:15], a[88:91], v[204:207]
v_mfma_f32_16x16x32_bf16 v[208:211], v[12:15], a[92:95], v[208:211]
global_load_dwordx4 a[56:59], v49, s[48:49] offset:1024 sc1
v_mfma_f32_16x16x32_bf16 v[192:195], v[8:11], a[92:95], v[192:195]
s_waitcnt lgkmcnt(4)
v_mfma_f32_16x16x32_bf16 v[224:227], v[16:19], a[92:95], v[224:227]
ds_read_b64_tr_b16 v[32:33], v253 offset:17408
ds_read_b64_tr_b16 v[34:35], v253 offset:21504
v_mfma_f32_16x16x32_bf16 v[240:243], v[20:23], a[92:95], v[240:243]
v_mfma_f32_16x16x32_bf16 v[236:239], v[20:23], a[88:91], v[236:239]
v_mfma_f32_16x16x32_bf16 v[220:223], v[16:19], a[88:91], v[220:223]
v_mfma_f32_16x16x32_bf16 v[216:219], v[16:19], a[84:87], v[216:219]
ds_read_b64_tr_b16 v[36:37], v254 offset:17408
ds_read_b64_tr_b16 v[38:39], v254 offset:21504
v_mfma_f32_16x16x32_bf16 v[232:235], v[20:23], a[84:87], v[232:235]
global_load_dwordx4 a[60:63], v50, s[48:49] offset:1024 sc1
v_mfma_f32_16x16x32_bf16 v[228:231], v[20:23], a[80:83], v[228:231]
v_mfma_f32_16x16x32_bf16 v[212:215], v[16:19], a[80:83], v[212:215]
s_waitcnt lgkmcnt(4)
v_mfma_f32_16x16x32_bf16 a[96:99], v[24:27], a[80:83], a[96:99]
s_waitcnt vmcnt(24)
s_barrier
ds_read_b64_tr_b16 v[8:9], v247 offset:36864
ds_read_b64_tr_b16 v[10:11], v247 offset:43008
v_mfma_f32_16x16x32_bf16 a[112:115], v[28:31], a[80:83], a[112:115]
v_mfma_f32_16x16x32_bf16 a[116:119], v[28:31], a[84:87], a[116:119]
v_mfma_f32_16x16x32_bf16 a[100:103], v[24:27], a[84:87], a[100:103]
s_add_u32 s48, s48, 2048
s_addc_u32 s49, s49, 0
v_mfma_f32_16x16x32_bf16 a[104:107], v[24:27], a[88:91], a[104:107]
ds_read_b64_tr_b16 v[12:13], v248 offset:36864
ds_read_b64_tr_b16 v[14:15], v248 offset:43008
v_mfma_f32_16x16x32_bf16 a[120:123], v[28:31], a[88:91], a[120:123]
v_mfma_f32_16x16x32_bf16 a[124:127], v[28:31], a[92:95], a[124:127]
v_mfma_f32_16x16x32_bf16 a[108:111], v[24:27], a[92:95], a[108:111]
s_waitcnt lgkmcnt(4)
v_mfma_f32_16x16x32_bf16 a[132:135], v[32:35], a[84:87], a[132:135]
ds_read_b64_tr_b16 v[16:17], v249 offset:36864
ds_read_b64_tr_b16 v[18:19], v249 offset:43008
v_mfma_f32_16x16x32_bf16 a[148:151], v[36:39], a[84:87], a[148:151]
v_mfma_f32_16x16x32_bf16 a[144:147], v[36:39], a[80:83], a[144:147]
ds_read_b64_tr_b16 v[20:21], v250 offset:36864
ds_read_b64_tr_b16 v[22:23], v250 offset:43008
v_mfma_f32_16x16x32_bf16 a[128:131], v[32:35], a[80:83], a[128:131]
s_waitcnt vmcnt(23) lgkmcnt(4)
v_mfma_f32_16x16x32_bf16 v[52:55], v[8:11], a[0:3], v[52:55]
ds_read_b64_tr_b16 v[24:25], v247 offset:37888
ds_read_b64_tr_b16 v[26:27], v247 offset:44032
v_mfma_f32_16x16x32_bf16 v[68:71], v[12:15], a[0:3], v[68:71]
s_add_u32 m0, s81, 12288
s_waitcnt vmcnt(22)
v_mfma_f32_16x16x32_bf16 v[72:75], v[12:15], a[4:7], v[72:75]
buffer_load_dwordx4 v244, %[srdW], s77 offen offset:0 lds
v_mfma_f32_16x16x32_bf16 v[56:59], v[8:11], a[4:7], v[56:59]
s_waitcnt vmcnt(22)
v_mfma_f32_16x16x32_bf16 v[60:63], v[8:11], a[8:11], v[60:63]
ds_read_b64_tr_b16 v[28:29], v248 offset:37888
ds_read_b64_tr_b16 v[30:31], v248 offset:44032
v_mfma_f32_16x16x32_bf16 v[76:79], v[12:15], a[8:11], v[76:79]
s_waitcnt vmcnt(21)
v_mfma_f32_16x16x32_bf16 v[80:83], v[12:15], a[12:15], v[80:83]
v_mfma_f32_16x16x32_bf16 v[64:67], v[8:11], a[12:15], v[64:67]
s_waitcnt lgkmcnt(4)
v_mfma_f32_16x16x32_bf16 v[96:99], v[16:19], a[12:15], v[96:99]
ds_read_b64_tr_b16 v[32:33], v249 offset:37888
ds_read_b64_tr_b16 v[34:35], v249 offset:44032
buffer_load_dwordx4 v245, %[srdW], s77 offen offset:1024 lds
v_mfma_f32_16x16x32_bf16 v[112:115], v[20:23], a[12:15], v[112:115]
v_mfma_f32_16x16x32_bf16 v[108:111], v[20:23], a[8:11], v[108:111]
v_mfma_f32_16x16x32_bf16 v[92:95], v[16:19], a[8:11], v[92:95]
v_mfma_f32_16x16x32_bf16 v[88:91], v[16:19], a[4:7], v[88:91]
ds_read_b64_tr_b16 v[36:37], v250 offset:37888
ds_read_b64_tr_b16 v[38:39], v250 offset:44032
v_mfma_f32_16x16x32_bf16 v[104:107], v[20:23], a[4:7], v[104:107]
v_mfma_f32_16x16x32_bf16 v[100:103], v[20:23], a[0:3], v[100:103]
v_mfma_f32_16x16x32_bf16 v[84:87], v[16:19], a[0:3], v[84:87]
buffer_load_dwordx4 v246, %[srdW], s77 offen offset:2048 lds
s_waitcnt lgkmcnt(4)
v_mfma_f32_16x16x32_bf16 v[116:119], v[24:27], a[0:3], v[116:119]
ds_read_b64_tr_b16 v[8:9], v247 offset:38912
ds_read_b64_tr_b16 v[10:11], v247 offset:45056
v_mfma_f32_16x16x32_bf16 v[132:135], v[28:31], a[0:3], v[132:135]
v_mfma_f32_16x16x32_bf16 v[136:139], v[28:31], a[4:7], v[136:139]
v_mfma_f32_16x16x32_bf16 v[120:123], v[24:27], a[4:7], v[120:123]
v_mfma_f32_16x16x32_bf16 v[124:127], v[24:27], a[8:11], v[124:127]
ds_read_b64_tr_b16 v[12:13], v248 offset:38912
ds_read_b64_tr_b16 v[14:15], v248 offset:45056
v_mfma_f32_16x16x32_bf16 v[140:143], v[28:31], a[8:11], v[140:143]
s_add_u32 m0, s82, 106496
v_mfma_f32_16x16x32_bf16 v[144:147], v[28:31], a[12:15], v[144:147]
buffer_load_dwordx4 v45, %[srdF], s79 offen offset:0 lds
v_mfma_f32_16x16x32_bf16 v[128:131], v[24:27], a[12:15], v[128:131]
s_waitcnt lgkmcnt(4)
v_mfma_f32_16x16x32_bf16 v[160:163], v[32:35], a[12:15], v[160:163]
ds_read_b64_tr_b16 v[16:17], v249 offset:38912
ds_read_b64_tr_b16 v[18:19], v249 offset:45056
v_mfma_f32_16x16x32_bf16 v[176:179], v[36:39], a[12:15], v[176:179]
v_mfma_f32_16x16x32_bf16 v[172:175], v[36:39], a[8:11], v[172:175]
v_mfma_f32_16x16x32_bf16 v[156:159], v[32:35], a[8:11], v[156:159]
v_mfma_f32_16x16x32_bf16 v[152:155], v[32:35], a[4:7], v[152:155]
ds_read_b64_tr_b16 v[20:21], v250 offset:38912
)"
R"(ds_read_b64_tr_b16 v[22:23], v250 offset:45056
v_mfma_f32_16x16x32_bf16 v[168:171], v[36:39], a[4:7], v[168:171]
buffer_load_dwordx4 v46, %[srdF], s79 offen offset:1024 lds
v_mfma_f32_16x16x32_bf16 v[164:167], v[36:39], a[0:3], v[164:167]
v_mfma_f32_16x16x32_bf16 v[148:151], v[32:35], a[0:3], v[148:151]
s_waitcnt lgkmcnt(4)
v_mfma_f32_16x16x32_bf16 v[180:183], v[8:11], a[0:3], v[180:183]
ds_read_b64_tr_b16 v[24:25], v251 offset:24576
ds_read_b64_tr_b16 v[26:27], v251 offset:28672
v_mfma_f32_16x16x32_bf16 v[196:199], v[12:15], a[0:3], v[196:199]
v_mfma_f32_16x16x32_bf16 v[200:203], v[12:15], a[4:7], v[200:203]
v_mfma_f32_16x16x32_bf16 v[184:187], v[8:11], a[4:7], v[184:187]
s_add_u32 m0, s81, 61440
s_add_u32 s78, s77, 393216
v_mfma_f32_16x16x32_bf16 v[188:191], v[8:11], a[8:11], v[188:191]
ds_read_b64_tr_b16 v[28:29], v252 offset:24576
ds_read_b64_tr_b16 v[30:31], v252 offset:28672
buffer_load_dwordx4 v244, %[srdW], s78 offen offset:0 lds
v_mfma_f32_16x16x32_bf16 v[204:207], v[12:15], a[8:11], v[204:207]
v_mfma_f32_16x16x32_bf16 v[208:211], v[12:15], a[12:15], v[208:211]
v_mfma_f32_16x16x32_bf16 v[192:195], v[8:11], a[12:15], v[192:195]
s_waitcnt lgkmcnt(4)
v_mfma_f32_16x16x32_bf16 v[224:227], v[16:19], a[12:15], v[224:227]
ds_read_b64_tr_b16 v[32:33], v253 offset:24576
ds_read_b64_tr_b16 v[34:35], v253 offset:28672
v_mfma_f32_16x16x32_bf16 v[240:243], v[20:23], a[12:15], v[240:243]
v_mfma_f32_16x16x32_bf16 v[236:239], v[20:23], a[8:11], v[236:239]
v_mfma_f32_16x16x32_bf16 v[220:223], v[16:19], a[8:11], v[220:223]
buffer_load_dwordx4 v245, %[srdW], s78 offen offset:1024 lds
v_mfma_f32_16x16x32_bf16 v[216:219], v[16:19], a[4:7], v[216:219]
ds_read_b64_tr_b16 v[36:37], v254 offset:24576
ds_read_b64_tr_b16 v[38:39], v254 offset:28672
v_mfma_f32_16x16x32_bf16 v[232:235], v[20:23], a[4:7], v[232:235]
v_mfma_f32_16x16x32_bf16 v[228:231], v[20:23], a[0:3], v[228:231]
v_mfma_f32_16x16x32_bf16 v[212:215], v[16:19], a[0:3], v[212:215]
s_waitcnt lgkmcnt(4)
v_mfma_f32_16x16x32_bf16 a[96:99], v[24:27], a[0:3], a[96:99]
ds_read_b64_tr_b16 v[8:9], %[vaddr0] offset:36864
ds_read_b64_tr_b16 v[10:11], %[vaddr0] offset:43008
v_mfma_f32_16x16x32_bf16 a[112:115], v[28:31], a[0:3], a[112:115]
v_mfma_f32_16x16x32_bf16 a[116:119], v[28:31], a[4:7], a[116:119]
buffer_load_dwordx4 v246, %[srdW], s78 offen offset:2048 lds
v_mfma_f32_16x16x32_bf16 a[100:103], v[24:27], a[4:7], a[100:103]
v_mfma_f32_16x16x32_bf16 a[104:107], v[24:27], a[8:11], a[104:107]
ds_read_b64_tr_b16 v[12:13], v41 offset:36864
ds_read_b64_tr_b16 v[14:15], v41 offset:43008
v_mfma_f32_16x16x32_bf16 a[120:123], v[28:31], a[8:11], a[120:123]
v_mfma_f32_16x16x32_bf16 a[124:127], v[28:31], a[12:15], a[124:127]
v_mfma_f32_16x16x32_bf16 a[108:111], v[24:27], a[12:15], a[108:111]
s_waitcnt lgkmcnt(4)
v_mfma_f32_16x16x32_bf16 a[132:135], v[32:35], a[4:7], a[132:135]
ds_read_b64_tr_b16 v[16:17], v42 offset:36864
ds_read_b64_tr_b16 v[18:19], v42 offset:43008
s_add_u32 s77, s77, 786432
s_add_u32 s79, s79, 196608
v_mfma_f32_16x16x32_bf16 a[148:151], v[36:39], a[4:7], a[148:151]
global_load_dwordx4 a[64:67], v47, s[48:49] offset:0 sc1
v_mfma_f32_16x16x32_bf16 a[144:147], v[36:39], a[0:3], a[144:147]
ds_read_b64_tr_b16 v[20:21], v43 offset:36864
ds_read_b64_tr_b16 v[22:23], v43 offset:43008
v_mfma_f32_16x16x32_bf16 a[128:131], v[32:35], a[0:3], a[128:131]
s_waitcnt vmcnt(28) lgkmcnt(4)
v_mfma_f32_16x16x32_bf16 v[52:55], v[8:11], a[16:19], v[52:55]
ds_read_b64_tr_b16 v[24:25], %[vaddr0] offset:37888
ds_read_b64_tr_b16 v[26:27], %[vaddr0] offset:44032
v_mfma_f32_16x16x32_bf16 v[68:71], v[12:15], a[16:19], v[68:71]
s_waitcnt vmcnt(27)
v_mfma_f32_16x16x32_bf16 v[72:75], v[12:15], a[20:23], v[72:75]
v_mfma_f32_16x16x32_bf16 v[56:59], v[8:11], a[20:23], v[56:59]
global_load_dwordx4 a[68:71], v48, s[48:49] offset:0 sc1
s_waitcnt vmcnt(27)
v_mfma_f32_16x16x32_bf16 v[60:63], v[8:11], a[24:27], v[60:63]
ds_read_b64_tr_b16 v[28:29], v41 offset:37888
ds_read_b64_tr_b16 v[30:31], v41 offset:44032
v_mfma_f32_16x16x32_bf16 v[76:79], v[12:15], a[24:27], v[76:79]
s_waitcnt vmcnt(26)
v_mfma_f32_16x16x32_bf16 v[80:83], v[12:15], a[28:31], v[80:83]
v_mfma_f32_16x16x32_bf16 v[64:67], v[8:11], a[28:31], v[64:67]
s_waitcnt lgkmcnt(4)
v_mfma_f32_16x16x32_bf16 v[96:99], v[16:19], a[28:31], v[96:99]
ds_read_b64_tr_b16 v[32:33], v42 offset:37888
ds_read_b64_tr_b16 v[34:35], v42 offset:44032
v_mfma_f32_16x16x32_bf16 v[112:115], v[20:23], a[28:31], v[112:115]
v_mfma_f32_16x16x32_bf16 v[108:111], v[20:23], a[24:27], v[108:111]
global_load_dwordx4 a[72:75], v49, s[48:49] offset:0 sc1
v_mfma_f32_16x16x32_bf16 v[92:95], v[16:19], a[24:27], v[92:95]
v_mfma_f32_16x16x32_bf16 v[88:91], v[16:19], a[20:23], v[88:91]
ds_read_b64_tr_b16 v[36:37], v43 offset:37888
ds_read_b64_tr_b16 v[38:39], v43 offset:44032
v_mfma_f32_16x16x32_bf16 v[104:107], v[20:23], a[20:23], v[104:107]
v_mfma_f32_16x16x32_bf16 v[100:103], v[20:23], a[16:19], v[100:103]
v_mfma_f32_16x16x32_bf16 v[84:87], v[16:19], a[16:19], v[84:87]
s_waitcnt lgkmcnt(4)
v_mfma_f32_16x16x32_bf16 v[116:119], v[24:27], a[16:19], v[116:119]
ds_read_b64_tr_b16 v[8:9], %[vaddr0] offset:38912
ds_read_b64_tr_b16 v[10:11], %[vaddr0] offset:45056
v_mfma_f32_16x16x32_bf16 v[132:135], v[28:31], a[16:19], v[132:135]
global_load_dwordx4 a[76:79], v50, s[48:49] offset:0 sc1
v_mfma_f32_16x16x32_bf16 v[136:139], v[28:31], a[20:23], v[136:139]
v_mfma_f32_16x16x32_bf16 v[120:123], v[24:27], a[20:23], v[120:123]
v_mfma_f32_16x16x32_bf16 v[124:127], v[24:27], a[24:27], v[124:127]
ds_read_b64_tr_b16 v[12:13], v41 offset:38912
ds_read_b64_tr_b16 v[14:15], v41 offset:45056
v_mfma_f32_16x16x32_bf16 v[140:143], v[28:31], a[24:27], v[140:143]
v_mfma_f32_16x16x32_bf16 v[144:147], v[28:31], a[28:31], v[144:147]
v_mfma_f32_16x16x32_bf16 v[128:131], v[24:27], a[28:31], v[128:131]
s_waitcnt lgkmcnt(4)
v_mfma_f32_16x16x32_bf16 v[160:163], v[32:35], a[28:31], v[160:163]
ds_read_b64_tr_b16 v[16:17], v42 offset:38912
ds_read_b64_tr_b16 v[18:19], v42 offset:45056
global_load_dwordx4 a[80:83], v47, s[48:49] offset:1024 sc1
v_mfma_f32_16x16x32_bf16 v[176:179], v[36:39], a[28:31], v[176:179]
v_mfma_f32_16x16x32_bf16 v[172:175], v[36:39], a[24:27], v[172:175]
v_mfma_f32_16x16x32_bf16 v[156:159], v[32:35], a[24:27], v[156:159]
v_mfma_f32_16x16x32_bf16 v[152:155], v[32:35], a[20:23], v[152:155]
ds_read_b64_tr_b16 v[20:21], v43 offset:38912
ds_read_b64_tr_b16 v[22:23], v43 offset:45056
v_mfma_f32_16x16x32_bf16 v[168:171], v[36:39], a[20:23], v[168:171]
v_mfma_f32_16x16x32_bf16 v[164:167], v[36:39], a[16:19], v[164:167]
v_mfma_f32_16x16x32_bf16 v[148:151], v[32:35], a[16:19], v[148:151]
global_load_dwordx4 a[84:87], v48, s[48:49] offset:1024 sc1
s_waitcnt lgkmcnt(4)
v_mfma_f32_16x16x32_bf16 v[180:183], v[8:11], a[16:19], v[180:183]
ds_read_b64_tr_b16 v[24:25], v251 offset:25600
ds_read_b64_tr_b16 v[26:27], v251 offset:29696
v_mfma_f32_16x16x32_bf16 v[196:199], v[12:15], a[16:19], v[196:199]
v_mfma_f32_16x16x32_bf16 v[200:203], v[12:15], a[20:23], v[200:203]
v_mfma_f32_16x16x32_bf16 v[184:187], v[8:11], a[20:23], v[184:187]
v_mfma_f32_16x16x32_bf16 v[188:191], v[8:11], a[24:27], v[188:191]
ds_read_b64_tr_b16 v[28:29], v252 offset:25600
ds_read_b64_tr_b16 v[30:31], v252 offset:29696
v_mfma_f32_16x16x32_bf16 v[204:207], v[12:15], a[24:27], v[204:207]
v_mfma_f32_16x16x32_bf16 v[208:211], v[12:15], a[28:31], v[208:211]
global_load_dwordx4 a[88:91], v49, s[48:49] offset:1024 sc1
v_mfma_f32_16x16x32_bf16 v[192:195], v[8:11], a[28:31], v[192:195]
s_waitcnt lgkmcnt(4)
v_mfma_f32_16x16x32_bf16 v[224:227], v[16:19], a[28:31], v[224:227]
ds_read_b64_tr_b16 v[32:33], v253 offset:25600
ds_read_b64_tr_b16 v[34:35], v253 offset:29696
v_mfma_f32_16x16x32_bf16 v[240:243], v[20:23], a[28:31], v[240:243]
v_mfma_f32_16x16x32_bf16 v[236:239], v[20:23], a[24:27], v[236:239]
v_mfma_f32_16x16x32_bf16 v[220:223], v[16:19], a[24:27], v[220:223]
v_mfma_f32_16x16x32_bf16 v[216:219], v[16:19], a[20:23], v[216:219]
ds_read_b64_tr_b16 v[36:37], v254 offset:25600
ds_read_b64_tr_b16 v[38:39], v254 offset:29696
v_mfma_f32_16x16x32_bf16 v[232:235], v[20:23], a[20:23], v[232:235]
global_load_dwordx4 a[92:95], v50, s[48:49] offset:1024 sc1
v_mfma_f32_16x16x32_bf16 v[228:231], v[20:23], a[16:19], v[228:231]
v_mfma_f32_16x16x32_bf16 v[212:215], v[16:19], a[16:19], v[212:215]
s_waitcnt lgkmcnt(4)
v_mfma_f32_16x16x32_bf16 a[96:99], v[24:27], a[16:19], a[96:99]
s_waitcnt vmcnt(24)
s_barrier
ds_read_b64_tr_b16 v[8:9], v247 offset:0
ds_read_b64_tr_b16 v[10:11], v247 offset:6144
v_mfma_f32_16x16x32_bf16 a[112:115], v[28:31], a[16:19], a[112:115]
v_mfma_f32_16x16x32_bf16 a[116:119], v[28:31], a[20:23], a[116:119]
v_mfma_f32_16x16x32_bf16 a[100:103], v[24:27], a[20:23], a[100:103]
s_add_u32 s48, s48, 2048
s_addc_u32 s49, s49, 0
v_mfma_f32_16x16x32_bf16 a[104:107], v[24:27], a[24:27], a[104:107]
ds_read_b64_tr_b16 v[12:13], v248 offset:0
ds_read_b64_tr_b16 v[14:15], v248 offset:6144
v_mfma_f32_16x16x32_bf16 a[120:123], v[28:31], a[24:27], a[120:123]
v_mfma_f32_16x16x32_bf16 a[124:127], v[28:31], a[28:31], a[124:127]
v_mfma_f32_16x16x32_bf16 a[108:111], v[24:27], a[28:31], a[108:111]
s_waitcnt lgkmcnt(4)
v_mfma_f32_16x16x32_bf16 a[132:135], v[32:35], a[20:23], a[132:135]
ds_read_b64_tr_b16 v[16:17], v249 offset:0
ds_read_b64_tr_b16 v[18:19], v249 offset:6144
v_mfma_f32_16x16x32_bf16 a[148:151], v[36:39], a[20:23], a[148:151]
v_mfma_f32_16x16x32_bf16 a[144:147], v[36:39], a[16:19], a[144:147]
ds_read_b64_tr_b16 v[20:21], v250 offset:0
ds_read_b64_tr_b16 v[22:23], v250 offset:6144
v_mfma_f32_16x16x32_bf16 a[128:131], v[32:35], a[16:19], a[128:131]
s_waitcnt vmcnt(23) lgkmcnt(4)
v_mfma_f32_16x16x32_bf16 v[52:55], v[8:11], a[32:35], v[52:55]
ds_read_b64_tr_b16 v[24:25], v247 offset:1024
ds_read_b64_tr_b16 v[26:27], v247 offset:7168
v_mfma_f32_16x16x32_bf16 v[68:71], v[12:15], a[32:35], v[68:71]
s_add_u32 m0, s81, 24576
s_waitcnt vmcnt(22)
v_mfma_f32_16x16x32_bf16 v[72:75], v[12:15], a[36:39], v[72:75]
buffer_load_dwordx4 v244, %[srdW], s77 offen offset:0 lds
v_mfma_f32_16x16x32_bf16 v[56:59], v[8:11], a[36:39], v[56:59]
s_waitcnt vmcnt(22)
v_mfma_f32_16x16x32_bf16 v[60:63], v[8:11], a[40:43], v[60:63]
ds_read_b64_tr_b16 v[28:29], v248 offset:1024
ds_read_b64_tr_b16 v[30:31], v248 offset:7168
v_mfma_f32_16x16x32_bf16 v[76:79], v[12:15], a[40:43], v[76:79]
s_waitcnt vmcnt(21)
v_mfma_f32_16x16x32_bf16 v[80:83], v[12:15], a[44:47], v[80:83]
v_mfma_f32_16x16x32_bf16 v[64:67], v[8:11], a[44:47], v[64:67]
s_waitcnt lgkmcnt(4)
v_mfma_f32_16x16x32_bf16 v[96:99], v[16:19], a[44:47], v[96:99]
ds_read_b64_tr_b16 v[32:33], v249 offset:1024
ds_read_b64_tr_b16 v[34:35], v249 offset:7168
buffer_load_dwordx4 v245, %[srdW], s77 offen offset:1024 lds
v_mfma_f32_16x16x32_bf16 v[112:115], v[20:23], a[44:47], v[112:115]
v_mfma_f32_16x16x32_bf16 v[108:111], v[20:23], a[40:43], v[108:111]
)"
R"(v_mfma_f32_16x16x32_bf16 v[92:95], v[16:19], a[40:43], v[92:95]
v_mfma_f32_16x16x32_bf16 v[88:91], v[16:19], a[36:39], v[88:91]
ds_read_b64_tr_b16 v[36:37], v250 offset:1024
ds_read_b64_tr_b16 v[38:39], v250 offset:7168
v_mfma_f32_16x16x32_bf16 v[104:107], v[20:23], a[36:39], v[104:107]
v_mfma_f32_16x16x32_bf16 v[100:103], v[20:23], a[32:35], v[100:103]
v_mfma_f32_16x16x32_bf16 v[84:87], v[16:19], a[32:35], v[84:87]
buffer_load_dwordx4 v246, %[srdW], s77 offen offset:2048 lds
s_waitcnt lgkmcnt(4)
v_mfma_f32_16x16x32_bf16 v[116:119], v[24:27], a[32:35], v[116:119]
ds_read_b64_tr_b16 v[8:9], v247 offset:2048
ds_read_b64_tr_b16 v[10:11], v247 offset:8192
v_mfma_f32_16x16x32_bf16 v[132:135], v[28:31], a[32:35], v[132:135]
v_mfma_f32_16x16x32_bf16 v[136:139], v[28:31], a[36:39], v[136:139]
v_mfma_f32_16x16x32_bf16 v[120:123], v[24:27], a[36:39], v[120:123]
v_mfma_f32_16x16x32_bf16 v[124:127], v[24:27], a[40:43], v[124:127]
ds_read_b64_tr_b16 v[12:13], v248 offset:2048
ds_read_b64_tr_b16 v[14:15], v248 offset:8192
v_mfma_f32_16x16x32_bf16 v[140:143], v[28:31], a[40:43], v[140:143]
s_add_u32 m0, s82, 114688
v_mfma_f32_16x16x32_bf16 v[144:147], v[28:31], a[44:47], v[144:147]
buffer_load_dwordx4 v45, %[srdF], s79 offen offset:0 lds
v_mfma_f32_16x16x32_bf16 v[128:131], v[24:27], a[44:47], v[128:131]
s_waitcnt lgkmcnt(4)
v_mfma_f32_16x16x32_bf16 v[160:163], v[32:35], a[44:47], v[160:163]
ds_read_b64_tr_b16 v[16:17], v249 offset:2048
ds_read_b64_tr_b16 v[18:19], v249 offset:8192
v_mfma_f32_16x16x32_bf16 v[176:179], v[36:39], a[44:47], v[176:179]
v_mfma_f32_16x16x32_bf16 v[172:175], v[36:39], a[40:43], v[172:175]
v_mfma_f32_16x16x32_bf16 v[156:159], v[32:35], a[40:43], v[156:159]
v_mfma_f32_16x16x32_bf16 v[152:155], v[32:35], a[36:39], v[152:155]
ds_read_b64_tr_b16 v[20:21], v250 offset:2048
ds_read_b64_tr_b16 v[22:23], v250 offset:8192
v_mfma_f32_16x16x32_bf16 v[168:171], v[36:39], a[36:39], v[168:171]
buffer_load_dwordx4 v46, %[srdF], s79 offen offset:1024 lds
v_mfma_f32_16x16x32_bf16 v[164:167], v[36:39], a[32:35], v[164:167]
v_mfma_f32_16x16x32_bf16 v[148:151], v[32:35], a[32:35], v[148:151]
s_waitcnt lgkmcnt(4)
v_mfma_f32_16x16x32_bf16 v[180:183], v[8:11], a[32:35], v[180:183]
ds_read_b64_tr_b16 v[24:25], v251 offset:0
ds_read_b64_tr_b16 v[26:27], v251 offset:4096
v_mfma_f32_16x16x32_bf16 v[196:199], v[12:15], a[32:35], v[196:199]
v_mfma_f32_16x16x32_bf16 v[200:203], v[12:15], a[36:39], v[200:203]
v_mfma_f32_16x16x32_bf16 v[184:187], v[8:11], a[36:39], v[184:187]
s_add_u32 m0, s81, 73728
s_add_u32 s78, s77, 393216
v_mfma_f32_16x16x32_bf16 v[188:191], v[8:11], a[40:43], v[188:191]
ds_read_b64_tr_b16 v[28:29], v252 offset:0
ds_read_b64_tr_b16 v[30:31], v252 offset:4096
buffer_load_dwordx4 v244, %[srdW], s78 offen offset:0 lds
v_mfma_f32_16x16x32_bf16 v[204:207], v[12:15], a[40:43], v[204:207]
v_mfma_f32_16x16x32_bf16 v[208:211], v[12:15], a[44:47], v[208:211]
v_mfma_f32_16x16x32_bf16 v[192:195], v[8:11], a[44:47], v[192:195]
s_waitcnt lgkmcnt(4)
v_mfma_f32_16x16x32_bf16 v[224:227], v[16:19], a[44:47], v[224:227]
ds_read_b64_tr_b16 v[32:33], v253 offset:0
ds_read_b64_tr_b16 v[34:35], v253 offset:4096
v_mfma_f32_16x16x32_bf16 v[240:243], v[20:23], a[44:47], v[240:243]
v_mfma_f32_16x16x32_bf16 v[236:239], v[20:23], a[40:43], v[236:239]
v_mfma_f32_16x16x32_bf16 v[220:223], v[16:19], a[40:43], v[220:223]
buffer_load_dwordx4 v245, %[srdW], s78 offen offset:1024 lds
v_mfma_f32_16x16x32_bf16 v[216:219], v[16:19], a[36:39], v[216:219]
ds_read_b64_tr_b16 v[36:37], v254 offset:0
ds_read_b64_tr_b16 v[38:39], v254 offset:4096
v_mfma_f32_16x16x32_bf16 v[232:235], v[20:23], a[36:39], v[232:235]
v_mfma_f32_16x16x32_bf16 v[228:231], v[20:23], a[32:35], v[228:231]
v_mfma_f32_16x16x32_bf16 v[212:215], v[16:19], a[32:35], v[212:215]
s_waitcnt lgkmcnt(4)
v_mfma_f32_16x16x32_bf16 a[96:99], v[24:27], a[32:35], a[96:99]
ds_read_b64_tr_b16 v[8:9], %[vaddr0] offset:0
ds_read_b64_tr_b16 v[10:11], %[vaddr0] offset:6144
v_mfma_f32_16x16x32_bf16 a[112:115], v[28:31], a[32:35], a[112:115]
v_mfma_f32_16x16x32_bf16 a[116:119], v[28:31], a[36:39], a[116:119]
buffer_load_dwordx4 v246, %[srdW], s78 offen offset:2048 lds
v_mfma_f32_16x16x32_bf16 a[100:103], v[24:27], a[36:39], a[100:103]
v_mfma_f32_16x16x32_bf16 a[104:107], v[24:27], a[40:43], a[104:107]
ds_read_b64_tr_b16 v[12:13], v41 offset:0
ds_read_b64_tr_b16 v[14:15], v41 offset:6144
v_mfma_f32_16x16x32_bf16 a[120:123], v[28:31], a[40:43], a[120:123]
v_mfma_f32_16x16x32_bf16 a[124:127], v[28:31], a[44:47], a[124:127]
v_mfma_f32_16x16x32_bf16 a[108:111], v[24:27], a[44:47], a[108:111]
s_waitcnt lgkmcnt(4)
v_mfma_f32_16x16x32_bf16 a[132:135], v[32:35], a[36:39], a[132:135]
ds_read_b64_tr_b16 v[16:17], v42 offset:0
ds_read_b64_tr_b16 v[18:19], v42 offset:6144
s_add_u32 s77, s77, 786432
s_add_u32 s79, s79, 196608
v_mfma_f32_16x16x32_bf16 a[148:151], v[36:39], a[36:39], a[148:151]
global_load_dwordx4 a[0:3], v47, s[48:49] offset:0 sc1
v_mfma_f32_16x16x32_bf16 a[144:147], v[36:39], a[32:35], a[144:147]
ds_read_b64_tr_b16 v[20:21], v43 offset:0
ds_read_b64_tr_b16 v[22:23], v43 offset:6144
v_mfma_f32_16x16x32_bf16 a[128:131], v[32:35], a[32:35], a[128:131]
s_waitcnt vmcnt(28) lgkmcnt(4)
v_mfma_f32_16x16x32_bf16 v[52:55], v[8:11], a[48:51], v[52:55]
ds_read_b64_tr_b16 v[24:25], %[vaddr0] offset:1024
ds_read_b64_tr_b16 v[26:27], %[vaddr0] offset:7168
v_mfma_f32_16x16x32_bf16 v[68:71], v[12:15], a[48:51], v[68:71]
s_waitcnt vmcnt(27)
v_mfma_f32_16x16x32_bf16 v[72:75], v[12:15], a[52:55], v[72:75]
v_mfma_f32_16x16x32_bf16 v[56:59], v[8:11], a[52:55], v[56:59]
global_load_dwordx4 a[4:7], v48, s[48:49] offset:0 sc1
s_waitcnt vmcnt(27)
v_mfma_f32_16x16x32_bf16 v[60:63], v[8:11], a[56:59], v[60:63]
ds_read_b64_tr_b16 v[28:29], v41 offset:1024
ds_read_b64_tr_b16 v[30:31], v41 offset:7168
v_mfma_f32_16x16x32_bf16 v[76:79], v[12:15], a[56:59], v[76:79]
s_waitcnt vmcnt(26)
v_mfma_f32_16x16x32_bf16 v[80:83], v[12:15], a[60:63], v[80:83]
v_mfma_f32_16x16x32_bf16 v[64:67], v[8:11], a[60:63], v[64:67]
s_waitcnt lgkmcnt(4)
v_mfma_f32_16x16x32_bf16 v[96:99], v[16:19], a[60:63], v[96:99]
ds_read_b64_tr_b16 v[32:33], v42 offset:1024
ds_read_b64_tr_b16 v[34:35], v42 offset:7168
v_mfma_f32_16x16x32_bf16 v[112:115], v[20:23], a[60:63], v[112:115]
v_mfma_f32_16x16x32_bf16 v[108:111], v[20:23], a[56:59], v[108:111]
global_load_dwordx4 a[8:11], v49, s[48:49] offset:0 sc1
v_mfma_f32_16x16x32_bf16 v[92:95], v[16:19], a[56:59], v[92:95]
v_mfma_f32_16x16x32_bf16 v[88:91], v[16:19], a[52:55], v[88:91]
ds_read_b64_tr_b16 v[36:37], v43 offset:1024
ds_read_b64_tr_b16 v[38:39], v43 offset:7168
v_mfma_f32_16x16x32_bf16 v[104:107], v[20:23], a[52:55], v[104:107]
v_mfma_f32_16x16x32_bf16 v[100:103], v[20:23], a[48:51], v[100:103]
v_mfma_f32_16x16x32_bf16 v[84:87], v[16:19], a[48:51], v[84:87]
s_waitcnt lgkmcnt(4)
v_mfma_f32_16x16x32_bf16 v[116:119], v[24:27], a[48:51], v[116:119]
ds_read_b64_tr_b16 v[8:9], %[vaddr0] offset:2048
ds_read_b64_tr_b16 v[10:11], %[vaddr0] offset:8192
v_mfma_f32_16x16x32_bf16 v[132:135], v[28:31], a[48:51], v[132:135]
global_load_dwordx4 a[12:15], v50, s[48:49] offset:0 sc1
v_mfma_f32_16x16x32_bf16 v[136:139], v[28:31], a[52:55], v[136:139]
v_mfma_f32_16x16x32_bf16 v[120:123], v[24:27], a[52:55], v[120:123]
v_mfma_f32_16x16x32_bf16 v[124:127], v[24:27], a[56:59], v[124:127]
ds_read_b64_tr_b16 v[12:13], v41 offset:2048
ds_read_b64_tr_b16 v[14:15], v41 offset:8192
v_mfma_f32_16x16x32_bf16 v[140:143], v[28:31], a[56:59], v[140:143]
v_mfma_f32_16x16x32_bf16 v[144:147], v[28:31], a[60:63], v[144:147]
v_mfma_f32_16x16x32_bf16 v[128:131], v[24:27], a[60:63], v[128:131]
s_waitcnt lgkmcnt(4)
v_mfma_f32_16x16x32_bf16 v[160:163], v[32:35], a[60:63], v[160:163]
ds_read_b64_tr_b16 v[16:17], v42 offset:2048
ds_read_b64_tr_b16 v[18:19], v42 offset:8192
global_load_dwordx4 a[16:19], v47, s[48:49] offset:1024 sc1
v_mfma_f32_16x16x32_bf16 v[176:179], v[36:39], a[60:63], v[176:179]
v_mfma_f32_16x16x32_bf16 v[172:175], v[36:39], a[56:59], v[172:175]
v_mfma_f32_16x16x32_bf16 v[156:159], v[32:35], a[56:59], v[156:159]
v_mfma_f32_16x16x32_bf16 v[152:155], v[32:35], a[52:55], v[152:155]
ds_read_b64_tr_b16 v[20:21], v43 offset:2048
ds_read_b64_tr_b16 v[22:23], v43 offset:8192
v_mfma_f32_16x16x32_bf16 v[168:171], v[36:39], a[52:55], v[168:171]
v_mfma_f32_16x16x32_bf16 v[164:167], v[36:39], a[48:51], v[164:167]
v_mfma_f32_16x16x32_bf16 v[148:151], v[32:35], a[48:51], v[148:151]
global_load_dwordx4 a[20:23], v48, s[48:49] offset:1024 sc1
s_waitcnt lgkmcnt(4)
v_mfma_f32_16x16x32_bf16 v[180:183], v[8:11], a[48:51], v[180:183]
ds_read_b64_tr_b16 v[24:25], v251 offset:1024
ds_read_b64_tr_b16 v[26:27], v251 offset:5120
v_mfma_f32_16x16x32_bf16 v[196:199], v[12:15], a[48:51], v[196:199]
v_mfma_f32_16x16x32_bf16 v[200:203], v[12:15], a[52:55], v[200:203]
v_mfma_f32_16x16x32_bf16 v[184:187], v[8:11], a[52:55], v[184:187]
v_mfma_f32_16x16x32_bf16 v[188:191], v[8:11], a[56:59], v[188:191]
ds_read_b64_tr_b16 v[28:29], v252 offset:1024
ds_read_b64_tr_b16 v[30:31], v252 offset:5120
v_mfma_f32_16x16x32_bf16 v[204:207], v[12:15], a[56:59], v[204:207]
v_mfma_f32_16x16x32_bf16 v[208:211], v[12:15], a[60:63], v[208:211]
global_load_dwordx4 a[24:27], v49, s[48:49] offset:1024 sc1
v_mfma_f32_16x16x32_bf16 v[192:195], v[8:11], a[60:63], v[192:195]
s_waitcnt lgkmcnt(4)
v_mfma_f32_16x16x32_bf16 v[224:227], v[16:19], a[60:63], v[224:227]
ds_read_b64_tr_b16 v[32:33], v253 offset:1024
ds_read_b64_tr_b16 v[34:35], v253 offset:5120
v_mfma_f32_16x16x32_bf16 v[240:243], v[20:23], a[60:63], v[240:243]
v_mfma_f32_16x16x32_bf16 v[236:239], v[20:23], a[56:59], v[236:239]
v_mfma_f32_16x16x32_bf16 v[220:223], v[16:19], a[56:59], v[220:223]
v_mfma_f32_16x16x32_bf16 v[216:219], v[16:19], a[52:55], v[216:219]
ds_read_b64_tr_b16 v[36:37], v254 offset:1024
ds_read_b64_tr_b16 v[38:39], v254 offset:5120
v_mfma_f32_16x16x32_bf16 v[232:235], v[20:23], a[52:55], v[232:235]
global_load_dwordx4 a[28:31], v50, s[48:49] offset:1024 sc1
v_mfma_f32_16x16x32_bf16 v[228:231], v[20:23], a[48:51], v[228:231]
v_mfma_f32_16x16x32_bf16 v[212:215], v[16:19], a[48:51], v[212:215]
s_waitcnt lgkmcnt(4)
v_mfma_f32_16x16x32_bf16 a[96:99], v[24:27], a[48:51], a[96:99]
s_waitcnt vmcnt(24)
s_barrier
ds_read_b64_tr_b16 v[8:9], v247 offset:12288
ds_read_b64_tr_b16 v[10:11], v247 offset:18432
v_mfma_f32_16x16x32_bf16 a[112:115], v[28:31], a[48:51], a[112:115]
v_mfma_f32_16x16x32_bf16 a[116:119], v[28:31], a[52:55], a[116:119]
v_mfma_f32_16x16x32_bf16 a[100:103], v[24:27], a[52:55], a[100:103]
s_add_u32 s48, s48, 2048
s_addc_u32 s49, s49, 0
v_mfma_f32_16x16x32_bf16 a[104:107], v[24:27], a[56:59], a[104:107]
ds_read_b64_tr_b16 v[12:13], v248 offset:12288
ds_read_b64_tr_b16 v[14:15], v248 offset:18432
v_mfma_f32_16x16x32_bf16 a[120:123], v[28:31], a[56:59], a[120:123]
v_mfma_f32_16x16x32_bf16 a[124:127], v[28:31], a[60:63], a[124:127]
v_mfma_f32_16x16x32_bf16 a[108:111], v[24:27], a[60:63], a[108:111]
s_waitcnt lgkmcnt(4)
v_mfma_f32_16x16x32_bf16 a[132:135], v[32:35], a[52:55], a[132:135]
)"
R"(ds_read_b64_tr_b16 v[16:17], v249 offset:12288
ds_read_b64_tr_b16 v[18:19], v249 offset:18432
v_mfma_f32_16x16x32_bf16 a[148:151], v[36:39], a[52:55], a[148:151]
v_mfma_f32_16x16x32_bf16 a[144:147], v[36:39], a[48:51], a[144:147]
ds_read_b64_tr_b16 v[20:21], v250 offset:12288
ds_read_b64_tr_b16 v[22:23], v250 offset:18432
v_mfma_f32_16x16x32_bf16 a[128:131], v[32:35], a[48:51], a[128:131]
s_waitcnt vmcnt(23) lgkmcnt(4)
v_mfma_f32_16x16x32_bf16 v[52:55], v[8:11], a[64:67], v[52:55]
ds_read_b64_tr_b16 v[24:25], v247 offset:13312
ds_read_b64_tr_b16 v[26:27], v247 offset:19456
v_mfma_f32_16x16x32_bf16 v[68:71], v[12:15], a[64:67], v[68:71]
s_add_u32 m0, s81, 36864
s_waitcnt vmcnt(22)
v_mfma_f32_16x16x32_bf16 v[72:75], v[12:15], a[68:71], v[72:75]
buffer_load_dwordx4 v244, %[srdW], s77 offen offset:0 lds
v_mfma_f32_16x16x32_bf16 v[56:59], v[8:11], a[68:71], v[56:59]
s_waitcnt vmcnt(22)
v_mfma_f32_16x16x32_bf16 v[60:63], v[8:11], a[72:75], v[60:63]
ds_read_b64_tr_b16 v[28:29], v248 offset:13312
ds_read_b64_tr_b16 v[30:31], v248 offset:19456
v_mfma_f32_16x16x32_bf16 v[76:79], v[12:15], a[72:75], v[76:79]
s_waitcnt vmcnt(21)
v_mfma_f32_16x16x32_bf16 v[80:83], v[12:15], a[76:79], v[80:83]
v_mfma_f32_16x16x32_bf16 v[64:67], v[8:11], a[76:79], v[64:67]
s_waitcnt lgkmcnt(4)
v_mfma_f32_16x16x32_bf16 v[96:99], v[16:19], a[76:79], v[96:99]
ds_read_b64_tr_b16 v[32:33], v249 offset:13312
ds_read_b64_tr_b16 v[34:35], v249 offset:19456
buffer_load_dwordx4 v245, %[srdW], s77 offen offset:1024 lds
v_mfma_f32_16x16x32_bf16 v[112:115], v[20:23], a[76:79], v[112:115]
v_mfma_f32_16x16x32_bf16 v[108:111], v[20:23], a[72:75], v[108:111]
v_mfma_f32_16x16x32_bf16 v[92:95], v[16:19], a[72:75], v[92:95]
v_mfma_f32_16x16x32_bf16 v[88:91], v[16:19], a[68:71], v[88:91]
ds_read_b64_tr_b16 v[36:37], v250 offset:13312
ds_read_b64_tr_b16 v[38:39], v250 offset:19456
v_mfma_f32_16x16x32_bf16 v[104:107], v[20:23], a[68:71], v[104:107]
v_mfma_f32_16x16x32_bf16 v[100:103], v[20:23], a[64:67], v[100:103]
v_mfma_f32_16x16x32_bf16 v[84:87], v[16:19], a[64:67], v[84:87]
buffer_load_dwordx4 v246, %[srdW], s77 offen offset:2048 lds
s_waitcnt lgkmcnt(4)
v_mfma_f32_16x16x32_bf16 v[116:119], v[24:27], a[64:67], v[116:119]
ds_read_b64_tr_b16 v[8:9], v247 offset:14336
ds_read_b64_tr_b16 v[10:11], v247 offset:20480
v_mfma_f32_16x16x32_bf16 v[132:135], v[28:31], a[64:67], v[132:135]
v_mfma_f32_16x16x32_bf16 v[136:139], v[28:31], a[68:71], v[136:139]
v_mfma_f32_16x16x32_bf16 v[120:123], v[24:27], a[68:71], v[120:123]
v_mfma_f32_16x16x32_bf16 v[124:127], v[24:27], a[72:75], v[124:127]
ds_read_b64_tr_b16 v[12:13], v248 offset:14336
ds_read_b64_tr_b16 v[14:15], v248 offset:20480
v_mfma_f32_16x16x32_bf16 v[140:143], v[28:31], a[72:75], v[140:143]
s_add_u32 m0, s82, 122880
v_mfma_f32_16x16x32_bf16 v[144:147], v[28:31], a[76:79], v[144:147]
buffer_load_dwordx4 v45, %[srdF], s79 offen offset:0 lds
v_mfma_f32_16x16x32_bf16 v[128:131], v[24:27], a[76:79], v[128:131]
s_waitcnt lgkmcnt(4)
v_mfma_f32_16x16x32_bf16 v[160:163], v[32:35], a[76:79], v[160:163]
ds_read_b64_tr_b16 v[16:17], v249 offset:14336
ds_read_b64_tr_b16 v[18:19], v249 offset:20480
v_mfma_f32_16x16x32_bf16 v[176:179], v[36:39], a[76:79], v[176:179]
v_mfma_f32_16x16x32_bf16 v[172:175], v[36:39], a[72:75], v[172:175]
v_mfma_f32_16x16x32_bf16 v[156:159], v[32:35], a[72:75], v[156:159]
v_mfma_f32_16x16x32_bf16 v[152:155], v[32:35], a[68:71], v[152:155]
ds_read_b64_tr_b16 v[20:21], v250 offset:14336
ds_read_b64_tr_b16 v[22:23], v250 offset:20480
v_mfma_f32_16x16x32_bf16 v[168:171], v[36:39], a[68:71], v[168:171]
buffer_load_dwordx4 v46, %[srdF], s79 offen offset:1024 lds
v_mfma_f32_16x16x32_bf16 v[164:167], v[36:39], a[64:67], v[164:167]
v_mfma_f32_16x16x32_bf16 v[148:151], v[32:35], a[64:67], v[148:151]
s_waitcnt lgkmcnt(4)
v_mfma_f32_16x16x32_bf16 v[180:183], v[8:11], a[64:67], v[180:183]
ds_read_b64_tr_b16 v[24:25], v251 offset:8192
ds_read_b64_tr_b16 v[26:27], v251 offset:12288
v_mfma_f32_16x16x32_bf16 v[196:199], v[12:15], a[64:67], v[196:199]
v_mfma_f32_16x16x32_bf16 v[200:203], v[12:15], a[68:71], v[200:203]
v_mfma_f32_16x16x32_bf16 v[184:187], v[8:11], a[68:71], v[184:187]
s_add_u32 m0, s81, 86016
s_add_u32 s78, s77, 393216
v_mfma_f32_16x16x32_bf16 v[188:191], v[8:11], a[72:75], v[188:191]
ds_read_b64_tr_b16 v[28:29], v252 offset:8192
ds_read_b64_tr_b16 v[30:31], v252 offset:12288
buffer_load_dwordx4 v244, %[srdW], s78 offen offset:0 lds
v_mfma_f32_16x16x32_bf16 v[204:207], v[12:15], a[72:75], v[204:207]
v_mfma_f32_16x16x32_bf16 v[208:211], v[12:15], a[76:79], v[208:211]
v_mfma_f32_16x16x32_bf16 v[192:195], v[8:11], a[76:79], v[192:195]
s_waitcnt lgkmcnt(4)
v_mfma_f32_16x16x32_bf16 v[224:227], v[16:19], a[76:79], v[224:227]
ds_read_b64_tr_b16 v[32:33], v253 offset:8192
ds_read_b64_tr_b16 v[34:35], v253 offset:12288
v_mfma_f32_16x16x32_bf16 v[240:243], v[20:23], a[76:79], v[240:243]
v_mfma_f32_16x16x32_bf16 v[236:239], v[20:23], a[72:75], v[236:239]
v_mfma_f32_16x16x32_bf16 v[220:223], v[16:19], a[72:75], v[220:223]
buffer_load_dwordx4 v245, %[srdW], s78 offen offset:1024 lds
v_mfma_f32_16x16x32_bf16 v[216:219], v[16:19], a[68:71], v[216:219]
ds_read_b64_tr_b16 v[36:37], v254 offset:8192
ds_read_b64_tr_b16 v[38:39], v254 offset:12288
v_mfma_f32_16x16x32_bf16 v[232:235], v[20:23], a[68:71], v[232:235]
v_mfma_f32_16x16x32_bf16 v[228:231], v[20:23], a[64:67], v[228:231]
v_mfma_f32_16x16x32_bf16 v[212:215], v[16:19], a[64:67], v[212:215]
s_waitcnt lgkmcnt(4)
v_mfma_f32_16x16x32_bf16 a[96:99], v[24:27], a[64:67], a[96:99]
ds_read_b64_tr_b16 v[8:9], %[vaddr0] offset:12288
ds_read_b64_tr_b16 v[10:11], %[vaddr0] offset:18432
v_mfma_f32_16x16x32_bf16 a[112:115], v[28:31], a[64:67], a[112:115]
v_mfma_f32_16x16x32_bf16 a[116:119], v[28:31], a[68:71], a[116:119]
buffer_load_dwordx4 v246, %[srdW], s78 offen offset:2048 lds
v_mfma_f32_16x16x32_bf16 a[100:103], v[24:27], a[68:71], a[100:103]
v_mfma_f32_16x16x32_bf16 a[104:107], v[24:27], a[72:75], a[104:107]
ds_read_b64_tr_b16 v[12:13], v41 offset:12288
ds_read_b64_tr_b16 v[14:15], v41 offset:18432
v_mfma_f32_16x16x32_bf16 a[120:123], v[28:31], a[72:75], a[120:123]
v_mfma_f32_16x16x32_bf16 a[124:127], v[28:31], a[76:79], a[124:127]
v_mfma_f32_16x16x32_bf16 a[108:111], v[24:27], a[76:79], a[108:111]
s_waitcnt lgkmcnt(4)
v_mfma_f32_16x16x32_bf16 a[132:135], v[32:35], a[68:71], a[132:135]
ds_read_b64_tr_b16 v[16:17], v42 offset:12288
ds_read_b64_tr_b16 v[18:19], v42 offset:18432
s_add_u32 s77, s77, 786432
s_add_u32 s79, s79, 196608
v_mfma_f32_16x16x32_bf16 a[148:151], v[36:39], a[68:71], a[148:151]
global_load_dwordx4 a[32:35], v47, s[48:49] offset:0 sc1
v_mfma_f32_16x16x32_bf16 a[144:147], v[36:39], a[64:67], a[144:147]
ds_read_b64_tr_b16 v[20:21], v43 offset:12288
ds_read_b64_tr_b16 v[22:23], v43 offset:18432
v_mfma_f32_16x16x32_bf16 a[128:131], v[32:35], a[64:67], a[128:131]
s_waitcnt vmcnt(28) lgkmcnt(4)
v_mfma_f32_16x16x32_bf16 v[52:55], v[8:11], a[80:83], v[52:55]
ds_read_b64_tr_b16 v[24:25], %[vaddr0] offset:13312
ds_read_b64_tr_b16 v[26:27], %[vaddr0] offset:19456
v_mfma_f32_16x16x32_bf16 v[68:71], v[12:15], a[80:83], v[68:71]
s_waitcnt vmcnt(27)
v_mfma_f32_16x16x32_bf16 v[72:75], v[12:15], a[84:87], v[72:75]
v_mfma_f32_16x16x32_bf16 v[56:59], v[8:11], a[84:87], v[56:59]
global_load_dwordx4 a[36:39], v48, s[48:49] offset:0 sc1
s_waitcnt vmcnt(27)
v_mfma_f32_16x16x32_bf16 v[60:63], v[8:11], a[88:91], v[60:63]
ds_read_b64_tr_b16 v[28:29], v41 offset:13312
ds_read_b64_tr_b16 v[30:31], v41 offset:19456
v_mfma_f32_16x16x32_bf16 v[76:79], v[12:15], a[88:91], v[76:79]
s_waitcnt vmcnt(26)
v_mfma_f32_16x16x32_bf16 v[80:83], v[12:15], a[92:95], v[80:83]
v_mfma_f32_16x16x32_bf16 v[64:67], v[8:11], a[92:95], v[64:67]
s_waitcnt lgkmcnt(4)
v_mfma_f32_16x16x32_bf16 v[96:99], v[16:19], a[92:95], v[96:99]
ds_read_b64_tr_b16 v[32:33], v42 offset:13312
ds_read_b64_tr_b16 v[34:35], v42 offset:19456
v_mfma_f32_16x16x32_bf16 v[112:115], v[20:23], a[92:95], v[112:115]
v_mfma_f32_16x16x32_bf16 v[108:111], v[20:23], a[88:91], v[108:111]
global_load_dwordx4 a[40:43], v49, s[48:49] offset:0 sc1
v_mfma_f32_16x16x32_bf16 v[92:95], v[16:19], a[88:91], v[92:95]
v_mfma_f32_16x16x32_bf16 v[88:91], v[16:19], a[84:87], v[88:91]
ds_read_b64_tr_b16 v[36:37], v43 offset:13312
ds_read_b64_tr_b16 v[38:39], v43 offset:19456
v_mfma_f32_16x16x32_bf16 v[104:107], v[20:23], a[84:87], v[104:107]
v_mfma_f32_16x16x32_bf16 v[100:103], v[20:23], a[80:83], v[100:103]
v_mfma_f32_16x16x32_bf16 v[84:87], v[16:19], a[80:83], v[84:87]
s_waitcnt lgkmcnt(4)
v_mfma_f32_16x16x32_bf16 v[116:119], v[24:27], a[80:83], v[116:119]
ds_read_b64_tr_b16 v[8:9], %[vaddr0] offset:14336
ds_read_b64_tr_b16 v[10:11], %[vaddr0] offset:20480
v_mfma_f32_16x16x32_bf16 v[132:135], v[28:31], a[80:83], v[132:135]
global_load_dwordx4 a[44:47], v50, s[48:49] offset:0 sc1
v_mfma_f32_16x16x32_bf16 v[136:139], v[28:31], a[84:87], v[136:139]
v_mfma_f32_16x16x32_bf16 v[120:123], v[24:27], a[84:87], v[120:123]
v_mfma_f32_16x16x32_bf16 v[124:127], v[24:27], a[88:91], v[124:127]
ds_read_b64_tr_b16 v[12:13], v41 offset:14336
ds_read_b64_tr_b16 v[14:15], v41 offset:20480
v_mfma_f32_16x16x32_bf16 v[140:143], v[28:31], a[88:91], v[140:143]
v_mfma_f32_16x16x32_bf16 v[144:147], v[28:31], a[92:95], v[144:147]
v_mfma_f32_16x16x32_bf16 v[128:131], v[24:27], a[92:95], v[128:131]
s_waitcnt lgkmcnt(4)
v_mfma_f32_16x16x32_bf16 v[160:163], v[32:35], a[92:95], v[160:163]
ds_read_b64_tr_b16 v[16:17], v42 offset:14336
ds_read_b64_tr_b16 v[18:19], v42 offset:20480
global_load_dwordx4 a[48:51], v47, s[48:49] offset:1024 sc1
v_mfma_f32_16x16x32_bf16 v[176:179], v[36:39], a[92:95], v[176:179]
v_mfma_f32_16x16x32_bf16 v[172:175], v[36:39], a[88:91], v[172:175]
v_mfma_f32_16x16x32_bf16 v[156:159], v[32:35], a[88:91], v[156:159]
v_mfma_f32_16x16x32_bf16 v[152:155], v[32:35], a[84:87], v[152:155]
ds_read_b64_tr_b16 v[20:21], v43 offset:14336
ds_read_b64_tr_b16 v[22:23], v43 offset:20480
v_mfma_f32_16x16x32_bf16 v[168:171], v[36:39], a[84:87], v[168:171]
v_mfma_f32_16x16x32_bf16 v[164:167], v[36:39], a[80:83], v[164:167]
v_mfma_f32_16x16x32_bf16 v[148:151], v[32:35], a[80:83], v[148:151]
global_load_dwordx4 a[52:55], v48, s[48:49] offset:1024 sc1
s_waitcnt lgkmcnt(4)
v_mfma_f32_16x16x32_bf16 v[180:183], v[8:11], a[80:83], v[180:183]
ds_read_b64_tr_b16 v[24:25], v251 offset:9216
ds_read_b64_tr_b16 v[26:27], v251 offset:13312
v_mfma_f32_16x16x32_bf16 v[196:199], v[12:15], a[80:83], v[196:199]
v_mfma_f32_16x16x32_bf16 v[200:203], v[12:15], a[84:87], v[200:203]
v_mfma_f32_16x16x32_bf16 v[184:187], v[8:11], a[84:87], v[184:187]
v_mfma_f32_16x16x32_bf16 v[188:191], v[8:11], a[88:91], v[188:191]
ds_read_b64_tr_b16 v[28:29], v252 offset:9216
ds_read_b64_tr_b16 v[30:31], v252 offset:13312
v_mfma_f32_16x16x32_bf16 v[204:207], v[12:15], a[88:91], v[204:207]
v_mfma_f32_16x16x32_bf16 v[208:211], v[12:15], a[92:95], v[208:211]
global_load_dwordx4 a[56:59], v49, s[48:49] offset:1024 sc1
v_mfma_f32_16x16x32_bf16 v[192:195], v[8:11], a[92:95], v[192:195]
s_waitcnt lgkmcnt(4)
)"
R"(v_mfma_f32_16x16x32_bf16 v[224:227], v[16:19], a[92:95], v[224:227]
ds_read_b64_tr_b16 v[32:33], v253 offset:9216
ds_read_b64_tr_b16 v[34:35], v253 offset:13312
v_mfma_f32_16x16x32_bf16 v[240:243], v[20:23], a[92:95], v[240:243]
v_mfma_f32_16x16x32_bf16 v[236:239], v[20:23], a[88:91], v[236:239]
v_mfma_f32_16x16x32_bf16 v[220:223], v[16:19], a[88:91], v[220:223]
v_mfma_f32_16x16x32_bf16 v[216:219], v[16:19], a[84:87], v[216:219]
ds_read_b64_tr_b16 v[36:37], v254 offset:9216
ds_read_b64_tr_b16 v[38:39], v254 offset:13312
v_mfma_f32_16x16x32_bf16 v[232:235], v[20:23], a[84:87], v[232:235]
global_load_dwordx4 a[60:63], v50, s[48:49] offset:1024 sc1
v_mfma_f32_16x16x32_bf16 v[228:231], v[20:23], a[80:83], v[228:231]
v_mfma_f32_16x16x32_bf16 v[212:215], v[16:19], a[80:83], v[212:215]
s_waitcnt lgkmcnt(4)
v_mfma_f32_16x16x32_bf16 a[96:99], v[24:27], a[80:83], a[96:99]
s_waitcnt vmcnt(24)
s_barrier
ds_read_b64_tr_b16 v[8:9], v247 offset:24576
ds_read_b64_tr_b16 v[10:11], v247 offset:30720
v_mfma_f32_16x16x32_bf16 a[112:115], v[28:31], a[80:83], a[112:115]
v_mfma_f32_16x16x32_bf16 a[116:119], v[28:31], a[84:87], a[116:119]
v_mfma_f32_16x16x32_bf16 a[100:103], v[24:27], a[84:87], a[100:103]
s_add_u32 s48, s48, 2048
s_addc_u32 s49, s49, 0
v_mfma_f32_16x16x32_bf16 a[104:107], v[24:27], a[88:91], a[104:107]
ds_read_b64_tr_b16 v[12:13], v248 offset:24576
ds_read_b64_tr_b16 v[14:15], v248 offset:30720
v_mfma_f32_16x16x32_bf16 a[120:123], v[28:31], a[88:91], a[120:123]
v_mfma_f32_16x16x32_bf16 a[124:127], v[28:31], a[92:95], a[124:127]
v_mfma_f32_16x16x32_bf16 a[108:111], v[24:27], a[92:95], a[108:111]
s_waitcnt lgkmcnt(4)
v_mfma_f32_16x16x32_bf16 a[132:135], v[32:35], a[84:87], a[132:135]
ds_read_b64_tr_b16 v[16:17], v249 offset:24576
ds_read_b64_tr_b16 v[18:19], v249 offset:30720
v_mfma_f32_16x16x32_bf16 a[148:151], v[36:39], a[84:87], a[148:151]
v_mfma_f32_16x16x32_bf16 a[144:147], v[36:39], a[80:83], a[144:147]
ds_read_b64_tr_b16 v[20:21], v250 offset:24576
ds_read_b64_tr_b16 v[22:23], v250 offset:30720
v_mfma_f32_16x16x32_bf16 a[128:131], v[32:35], a[80:83], a[128:131]
s_waitcnt vmcnt(23) lgkmcnt(4)
v_mfma_f32_16x16x32_bf16 v[52:55], v[8:11], a[0:3], v[52:55]
ds_read_b64_tr_b16 v[24:25], v247 offset:25600
ds_read_b64_tr_b16 v[26:27], v247 offset:31744
v_mfma_f32_16x16x32_bf16 v[68:71], v[12:15], a[0:3], v[68:71]
s_add_u32 m0, s81, 0
s_waitcnt vmcnt(22)
v_mfma_f32_16x16x32_bf16 v[72:75], v[12:15], a[4:7], v[72:75]
buffer_load_dwordx4 v244, %[srdW], s77 offen offset:0 lds
v_mfma_f32_16x16x32_bf16 v[56:59], v[8:11], a[4:7], v[56:59]
s_waitcnt vmcnt(22)
v_mfma_f32_16x16x32_bf16 v[60:63], v[8:11], a[8:11], v[60:63]
ds_read_b64_tr_b16 v[28:29], v248 offset:25600
ds_read_b64_tr_b16 v[30:31], v248 offset:31744
v_mfma_f32_16x16x32_bf16 v[76:79], v[12:15], a[8:11], v[76:79]
s_waitcnt vmcnt(21)
v_mfma_f32_16x16x32_bf16 v[80:83], v[12:15], a[12:15], v[80:83]
v_mfma_f32_16x16x32_bf16 v[64:67], v[8:11], a[12:15], v[64:67]
s_waitcnt lgkmcnt(4)
v_mfma_f32_16x16x32_bf16 v[96:99], v[16:19], a[12:15], v[96:99]
ds_read_b64_tr_b16 v[32:33], v249 offset:25600
ds_read_b64_tr_b16 v[34:35], v249 offset:31744
buffer_load_dwordx4 v245, %[srdW], s77 offen offset:1024 lds
v_mfma_f32_16x16x32_bf16 v[112:115], v[20:23], a[12:15], v[112:115]
v_mfma_f32_16x16x32_bf16 v[108:111], v[20:23], a[8:11], v[108:111]
v_mfma_f32_16x16x32_bf16 v[92:95], v[16:19], a[8:11], v[92:95]
v_mfma_f32_16x16x32_bf16 v[88:91], v[16:19], a[4:7], v[88:91]
ds_read_b64_tr_b16 v[36:37], v250 offset:25600
ds_read_b64_tr_b16 v[38:39], v250 offset:31744
v_mfma_f32_16x16x32_bf16 v[104:107], v[20:23], a[4:7], v[104:107]
v_mfma_f32_16x16x32_bf16 v[100:103], v[20:23], a[0:3], v[100:103]
v_mfma_f32_16x16x32_bf16 v[84:87], v[16:19], a[0:3], v[84:87]
buffer_load_dwordx4 v246, %[srdW], s77 offen offset:2048 lds
s_waitcnt lgkmcnt(4)
v_mfma_f32_16x16x32_bf16 v[116:119], v[24:27], a[0:3], v[116:119]
ds_read_b64_tr_b16 v[8:9], v247 offset:26624
ds_read_b64_tr_b16 v[10:11], v247 offset:32768
v_mfma_f32_16x16x32_bf16 v[132:135], v[28:31], a[0:3], v[132:135]
v_mfma_f32_16x16x32_bf16 v[136:139], v[28:31], a[4:7], v[136:139]
v_mfma_f32_16x16x32_bf16 v[120:123], v[24:27], a[4:7], v[120:123]
v_mfma_f32_16x16x32_bf16 v[124:127], v[24:27], a[8:11], v[124:127]
ds_read_b64_tr_b16 v[12:13], v248 offset:26624
ds_read_b64_tr_b16 v[14:15], v248 offset:32768
v_mfma_f32_16x16x32_bf16 v[140:143], v[28:31], a[8:11], v[140:143]
s_add_u32 m0, s82, 98304
v_mfma_f32_16x16x32_bf16 v[144:147], v[28:31], a[12:15], v[144:147]
buffer_load_dwordx4 v45, %[srdF], s79 offen offset:0 lds
v_mfma_f32_16x16x32_bf16 v[128:131], v[24:27], a[12:15], v[128:131]
s_waitcnt lgkmcnt(4)
v_mfma_f32_16x16x32_bf16 v[160:163], v[32:35], a[12:15], v[160:163]
ds_read_b64_tr_b16 v[16:17], v249 offset:26624
ds_read_b64_tr_b16 v[18:19], v249 offset:32768
v_mfma_f32_16x16x32_bf16 v[176:179], v[36:39], a[12:15], v[176:179]
v_mfma_f32_16x16x32_bf16 v[172:175], v[36:39], a[8:11], v[172:175]
v_mfma_f32_16x16x32_bf16 v[156:159], v[32:35], a[8:11], v[156:159]
v_mfma_f32_16x16x32_bf16 v[152:155], v[32:35], a[4:7], v[152:155]
ds_read_b64_tr_b16 v[20:21], v250 offset:26624
ds_read_b64_tr_b16 v[22:23], v250 offset:32768
v_mfma_f32_16x16x32_bf16 v[168:171], v[36:39], a[4:7], v[168:171]
buffer_load_dwordx4 v46, %[srdF], s79 offen offset:1024 lds
v_mfma_f32_16x16x32_bf16 v[164:167], v[36:39], a[0:3], v[164:167]
v_mfma_f32_16x16x32_bf16 v[148:151], v[32:35], a[0:3], v[148:151]
s_waitcnt lgkmcnt(4)
v_mfma_f32_16x16x32_bf16 v[180:183], v[8:11], a[0:3], v[180:183]
ds_read_b64_tr_b16 v[24:25], v251 offset:16384
ds_read_b64_tr_b16 v[26:27], v251 offset:20480
v_mfma_f32_16x16x32_bf16 v[196:199], v[12:15], a[0:3], v[196:199]
v_mfma_f32_16x16x32_bf16 v[200:203], v[12:15], a[4:7], v[200:203]
v_mfma_f32_16x16x32_bf16 v[184:187], v[8:11], a[4:7], v[184:187]
s_add_u32 m0, s81, 49152
s_add_u32 s78, s77, 393216
v_mfma_f32_16x16x32_bf16 v[188:191], v[8:11], a[8:11], v[188:191]
ds_read_b64_tr_b16 v[28:29], v252 offset:16384
ds_read_b64_tr_b16 v[30:31], v252 offset:20480
buffer_load_dwordx4 v244, %[srdW], s78 offen offset:0 lds
v_mfma_f32_16x16x32_bf16 v[204:207], v[12:15], a[8:11], v[204:207]
v_mfma_f32_16x16x32_bf16 v[208:211], v[12:15], a[12:15], v[208:211]
v_mfma_f32_16x16x32_bf16 v[192:195], v[8:11], a[12:15], v[192:195]
s_waitcnt lgkmcnt(4)
v_mfma_f32_16x16x32_bf16 v[224:227], v[16:19], a[12:15], v[224:227]
ds_read_b64_tr_b16 v[32:33], v253 offset:16384
ds_read_b64_tr_b16 v[34:35], v253 offset:20480
v_mfma_f32_16x16x32_bf16 v[240:243], v[20:23], a[12:15], v[240:243]
v_mfma_f32_16x16x32_bf16 v[236:239], v[20:23], a[8:11], v[236:239]
v_mfma_f32_16x16x32_bf16 v[220:223], v[16:19], a[8:11], v[220:223]
buffer_load_dwordx4 v245, %[srdW], s78 offen offset:1024 lds
v_mfma_f32_16x16x32_bf16 v[216:219], v[16:19], a[4:7], v[216:219]
ds_read_b64_tr_b16 v[36:37], v254 offset:16384
ds_read_b64_tr_b16 v[38:39], v254 offset:20480
v_mfma_f32_16x16x32_bf16 v[232:235], v[20:23], a[4:7], v[232:235]
v_mfma_f32_16x16x32_bf16 v[228:231], v[20:23], a[0:3], v[228:231]
v_mfma_f32_16x16x32_bf16 v[212:215], v[16:19], a[0:3], v[212:215]
s_waitcnt lgkmcnt(4)
v_mfma_f32_16x16x32_bf16 a[96:99], v[24:27], a[0:3], a[96:99]
ds_read_b64_tr_b16 v[8:9], %[vaddr0] offset:24576
ds_read_b64_tr_b16 v[10:11], %[vaddr0] offset:30720
v_mfma_f32_16x16x32_bf16 a[112:115], v[28:31], a[0:3], a[112:115]
v_mfma_f32_16x16x32_bf16 a[116:119], v[28:31], a[4:7], a[116:119]
buffer_load_dwordx4 v246, %[srdW], s78 offen offset:2048 lds
v_mfma_f32_16x16x32_bf16 a[100:103], v[24:27], a[4:7], a[100:103]
v_mfma_f32_16x16x32_bf16 a[104:107], v[24:27], a[8:11], a[104:107]
ds_read_b64_tr_b16 v[12:13], v41 offset:24576
ds_read_b64_tr_b16 v[14:15], v41 offset:30720
v_mfma_f32_16x16x32_bf16 a[120:123], v[28:31], a[8:11], a[120:123]
v_mfma_f32_16x16x32_bf16 a[124:127], v[28:31], a[12:15], a[124:127]
v_mfma_f32_16x16x32_bf16 a[108:111], v[24:27], a[12:15], a[108:111]
s_waitcnt lgkmcnt(4)
v_mfma_f32_16x16x32_bf16 a[132:135], v[32:35], a[4:7], a[132:135]
ds_read_b64_tr_b16 v[16:17], v42 offset:24576
ds_read_b64_tr_b16 v[18:19], v42 offset:30720
s_add_u32 s77, s77, 786432
s_add_u32 s79, s79, 196608
v_mfma_f32_16x16x32_bf16 a[148:151], v[36:39], a[4:7], a[148:151]
global_load_dwordx4 a[64:67], v47, s[48:49] offset:0 sc1
v_mfma_f32_16x16x32_bf16 a[144:147], v[36:39], a[0:3], a[144:147]
ds_read_b64_tr_b16 v[20:21], v43 offset:24576
ds_read_b64_tr_b16 v[22:23], v43 offset:30720
v_mfma_f32_16x16x32_bf16 a[128:131], v[32:35], a[0:3], a[128:131]
s_waitcnt vmcnt(28) lgkmcnt(4)
v_mfma_f32_16x16x32_bf16 v[52:55], v[8:11], a[16:19], v[52:55]
ds_read_b64_tr_b16 v[24:25], %[vaddr0] offset:25600
ds_read_b64_tr_b16 v[26:27], %[vaddr0] offset:31744
v_mfma_f32_16x16x32_bf16 v[68:71], v[12:15], a[16:19], v[68:71]
s_waitcnt vmcnt(27)
v_mfma_f32_16x16x32_bf16 v[72:75], v[12:15], a[20:23], v[72:75]
v_mfma_f32_16x16x32_bf16 v[56:59], v[8:11], a[20:23], v[56:59]
global_load_dwordx4 a[68:71], v48, s[48:49] offset:0 sc1
s_waitcnt vmcnt(27)
v_mfma_f32_16x16x32_bf16 v[60:63], v[8:11], a[24:27], v[60:63]
ds_read_b64_tr_b16 v[28:29], v41 offset:25600
ds_read_b64_tr_b16 v[30:31], v41 offset:31744
v_mfma_f32_16x16x32_bf16 v[76:79], v[12:15], a[24:27], v[76:79]
s_waitcnt vmcnt(26)
v_mfma_f32_16x16x32_bf16 v[80:83], v[12:15], a[28:31], v[80:83]
v_mfma_f32_16x16x32_bf16 v[64:67], v[8:11], a[28:31], v[64:67]
s_waitcnt lgkmcnt(4)
v_mfma_f32_16x16x32_bf16 v[96:99], v[16:19], a[28:31], v[96:99]
ds_read_b64_tr_b16 v[32:33], v42 offset:25600
ds_read_b64_tr_b16 v[34:35], v42 offset:31744
v_mfma_f32_16x16x32_bf16 v[112:115], v[20:23], a[28:31], v[112:115]
v_mfma_f32_16x16x32_bf16 v[108:111], v[20:23], a[24:27], v[108:111]
global_load_dwordx4 a[72:75], v49, s[48:49] offset:0 sc1
v_mfma_f32_16x16x32_bf16 v[92:95], v[16:19], a[24:27], v[92:95]
v_mfma_f32_16x16x32_bf16 v[88:91], v[16:19], a[20:23], v[88:91]
ds_read_b64_tr_b16 v[36:37], v43 offset:25600
ds_read_b64_tr_b16 v[38:39], v43 offset:31744
v_mfma_f32_16x16x32_bf16 v[104:107], v[20:23], a[20:23], v[104:107]
v_mfma_f32_16x16x32_bf16 v[100:103], v[20:23], a[16:19], v[100:103]
v_mfma_f32_16x16x32_bf16 v[84:87], v[16:19], a[16:19], v[84:87]
s_waitcnt lgkmcnt(4)
v_mfma_f32_16x16x32_bf16 v[116:119], v[24:27], a[16:19], v[116:119]
ds_read_b64_tr_b16 v[8:9], %[vaddr0] offset:26624
ds_read_b64_tr_b16 v[10:11], %[vaddr0] offset:32768
v_mfma_f32_16x16x32_bf16 v[132:135], v[28:31], a[16:19], v[132:135]
global_load_dwordx4 a[76:79], v50, s[48:49] offset:0 sc1
v_mfma_f32_16x16x32_bf16 v[136:139], v[28:31], a[20:23], v[136:139]
v_mfma_f32_16x16x32_bf16 v[120:123], v[24:27], a[20:23], v[120:123]
v_mfma_f32_16x16x32_bf16 v[124:127], v[24:27], a[24:27], v[124:127]
ds_read_b64_tr_b16 v[12:13], v41 offset:26624
ds_read_b64_tr_b16 v[14:15], v41 offset:32768
v_mfma_f32_16x16x32_bf16 v[140:143], v[28:31], a[24:27], v[140:143]
)"
R"(v_mfma_f32_16x16x32_bf16 v[144:147], v[28:31], a[28:31], v[144:147]
v_mfma_f32_16x16x32_bf16 v[128:131], v[24:27], a[28:31], v[128:131]
s_waitcnt lgkmcnt(4)
v_mfma_f32_16x16x32_bf16 v[160:163], v[32:35], a[28:31], v[160:163]
ds_read_b64_tr_b16 v[16:17], v42 offset:26624
ds_read_b64_tr_b16 v[18:19], v42 offset:32768
global_load_dwordx4 a[80:83], v47, s[48:49] offset:1024 sc1
v_mfma_f32_16x16x32_bf16 v[176:179], v[36:39], a[28:31], v[176:179]
v_mfma_f32_16x16x32_bf16 v[172:175], v[36:39], a[24:27], v[172:175]
v_mfma_f32_16x16x32_bf16 v[156:159], v[32:35], a[24:27], v[156:159]
v_mfma_f32_16x16x32_bf16 v[152:155], v[32:35], a[20:23], v[152:155]
ds_read_b64_tr_b16 v[20:21], v43 offset:26624
ds_read_b64_tr_b16 v[22:23], v43 offset:32768
v_mfma_f32_16x16x32_bf16 v[168:171], v[36:39], a[20:23], v[168:171]
v_mfma_f32_16x16x32_bf16 v[164:167], v[36:39], a[16:19], v[164:167]
v_mfma_f32_16x16x32_bf16 v[148:151], v[32:35], a[16:19], v[148:151]
global_load_dwordx4 a[84:87], v48, s[48:49] offset:1024 sc1
s_waitcnt lgkmcnt(4)
v_mfma_f32_16x16x32_bf16 v[180:183], v[8:11], a[16:19], v[180:183]
ds_read_b64_tr_b16 v[24:25], v251 offset:17408
ds_read_b64_tr_b16 v[26:27], v251 offset:21504
v_mfma_f32_16x16x32_bf16 v[196:199], v[12:15], a[16:19], v[196:199]
v_mfma_f32_16x16x32_bf16 v[200:203], v[12:15], a[20:23], v[200:203]
v_mfma_f32_16x16x32_bf16 v[184:187], v[8:11], a[20:23], v[184:187]
v_mfma_f32_16x16x32_bf16 v[188:191], v[8:11], a[24:27], v[188:191]
ds_read_b64_tr_b16 v[28:29], v252 offset:17408
ds_read_b64_tr_b16 v[30:31], v252 offset:21504
v_mfma_f32_16x16x32_bf16 v[204:207], v[12:15], a[24:27], v[204:207]
v_mfma_f32_16x16x32_bf16 v[208:211], v[12:15], a[28:31], v[208:211]
global_load_dwordx4 a[88:91], v49, s[48:49] offset:1024 sc1
v_mfma_f32_16x16x32_bf16 v[192:195], v[8:11], a[28:31], v[192:195]
s_waitcnt lgkmcnt(4)
v_mfma_f32_16x16x32_bf16 v[224:227], v[16:19], a[28:31], v[224:227]
ds_read_b64_tr_b16 v[32:33], v253 offset:17408
ds_read_b64_tr_b16 v[34:35], v253 offset:21504
v_mfma_f32_16x16x32_bf16 v[240:243], v[20:23], a[28:31], v[240:243]
v_mfma_f32_16x16x32_bf16 v[236:239], v[20:23], a[24:27], v[236:239]
v_mfma_f32_16x16x32_bf16 v[220:223], v[16:19], a[24:27], v[220:223]
v_mfma_f32_16x16x32_bf16 v[216:219], v[16:19], a[20:23], v[216:219]
ds_read_b64_tr_b16 v[36:37], v254 offset:17408
ds_read_b64_tr_b16 v[38:39], v254 offset:21504
v_mfma_f32_16x16x32_bf16 v[232:235], v[20:23], a[20:23], v[232:235]
global_load_dwordx4 a[92:95], v50, s[48:49] offset:1024 sc1
v_mfma_f32_16x16x32_bf16 v[228:231], v[20:23], a[16:19], v[228:231]
v_mfma_f32_16x16x32_bf16 v[212:215], v[16:19], a[16:19], v[212:215]
s_waitcnt lgkmcnt(4)
v_mfma_f32_16x16x32_bf16 a[96:99], v[24:27], a[16:19], a[96:99]
s_waitcnt vmcnt(24)
s_barrier
ds_read_b64_tr_b16 v[8:9], v247 offset:36864
ds_read_b64_tr_b16 v[10:11], v247 offset:43008
v_mfma_f32_16x16x32_bf16 a[112:115], v[28:31], a[16:19], a[112:115]
v_mfma_f32_16x16x32_bf16 a[116:119], v[28:31], a[20:23], a[116:119]
v_mfma_f32_16x16x32_bf16 a[100:103], v[24:27], a[20:23], a[100:103]
s_add_u32 s48, s48, 2048
s_addc_u32 s49, s49, 0
v_mfma_f32_16x16x32_bf16 a[104:107], v[24:27], a[24:27], a[104:107]
ds_read_b64_tr_b16 v[12:13], v248 offset:36864
ds_read_b64_tr_b16 v[14:15], v248 offset:43008
v_mfma_f32_16x16x32_bf16 a[120:123], v[28:31], a[24:27], a[120:123]
v_mfma_f32_16x16x32_bf16 a[124:127], v[28:31], a[28:31], a[124:127]
v_mfma_f32_16x16x32_bf16 a[108:111], v[24:27], a[28:31], a[108:111]
s_waitcnt lgkmcnt(4)
v_mfma_f32_16x16x32_bf16 a[132:135], v[32:35], a[20:23], a[132:135]
ds_read_b64_tr_b16 v[16:17], v249 offset:36864
ds_read_b64_tr_b16 v[18:19], v249 offset:43008
v_mfma_f32_16x16x32_bf16 a[148:151], v[36:39], a[20:23], a[148:151]
v_mfma_f32_16x16x32_bf16 a[144:147], v[36:39], a[16:19], a[144:147]
ds_read_b64_tr_b16 v[20:21], v250 offset:36864
ds_read_b64_tr_b16 v[22:23], v250 offset:43008
v_mfma_f32_16x16x32_bf16 a[128:131], v[32:35], a[16:19], a[128:131]
s_waitcnt vmcnt(23) lgkmcnt(4)
v_mfma_f32_16x16x32_bf16 v[52:55], v[8:11], a[32:35], v[52:55]
ds_read_b64_tr_b16 v[24:25], v247 offset:37888
ds_read_b64_tr_b16 v[26:27], v247 offset:44032
v_mfma_f32_16x16x32_bf16 v[68:71], v[12:15], a[32:35], v[68:71]
s_add_u32 m0, s81, 12288
s_waitcnt vmcnt(22)
v_mfma_f32_16x16x32_bf16 v[72:75], v[12:15], a[36:39], v[72:75]
buffer_load_dwordx4 v244, %[srdW], s77 offen offset:0 lds
v_mfma_f32_16x16x32_bf16 v[56:59], v[8:11], a[36:39], v[56:59]
s_waitcnt vmcnt(22)
v_mfma_f32_16x16x32_bf16 v[60:63], v[8:11], a[40:43], v[60:63]
ds_read_b64_tr_b16 v[28:29], v248 offset:37888
ds_read_b64_tr_b16 v[30:31], v248 offset:44032
v_mfma_f32_16x16x32_bf16 v[76:79], v[12:15], a[40:43], v[76:79]
s_waitcnt vmcnt(21)
v_mfma_f32_16x16x32_bf16 v[80:83], v[12:15], a[44:47], v[80:83]
v_mfma_f32_16x16x32_bf16 v[64:67], v[8:11], a[44:47], v[64:67]
s_waitcnt lgkmcnt(4)
v_mfma_f32_16x16x32_bf16 v[96:99], v[16:19], a[44:47], v[96:99]
ds_read_b64_tr_b16 v[32:33], v249 offset:37888
ds_read_b64_tr_b16 v[34:35], v249 offset:44032
buffer_load_dwordx4 v245, %[srdW], s77 offen offset:1024 lds
v_mfma_f32_16x16x32_bf16 v[112:115], v[20:23], a[44:47], v[112:115]
v_mfma_f32_16x16x32_bf16 v[108:111], v[20:23], a[40:43], v[108:111]
v_mfma_f32_16x16x32_bf16 v[92:95], v[16:19], a[40:43], v[92:95]
v_mfma_f32_16x16x32_bf16 v[88:91], v[16:19], a[36:39], v[88:91]
ds_read_b64_tr_b16 v[36:37], v250 offset:37888
ds_read_b64_tr_b16 v[38:39], v250 offset:44032
v_mfma_f32_16x16x32_bf16 v[104:107], v[20:23], a[36:39], v[104:107]
v_mfma_f32_16x16x32_bf16 v[100:103], v[20:23], a[32:35], v[100:103]
v_mfma_f32_16x16x32_bf16 v[84:87], v[16:19], a[32:35], v[84:87]
buffer_load_dwordx4 v246, %[srdW], s77 offen offset:2048 lds
s_waitcnt lgkmcnt(4)
v_mfma_f32_16x16x32_bf16 v[116:119], v[24:27], a[32:35], v[116:119]
ds_read_b64_tr_b16 v[8:9], v247 offset:38912
ds_read_b64_tr_b16 v[10:11], v247 offset:45056
v_mfma_f32_16x16x32_bf16 v[132:135], v[28:31], a[32:35], v[132:135]
v_mfma_f32_16x16x32_bf16 v[136:139], v[28:31], a[36:39], v[136:139]
v_mfma_f32_16x16x32_bf16 v[120:123], v[24:27], a[36:39], v[120:123]
v_mfma_f32_16x16x32_bf16 v[124:127], v[24:27], a[40:43], v[124:127]
ds_read_b64_tr_b16 v[12:13], v248 offset:38912
ds_read_b64_tr_b16 v[14:15], v248 offset:45056
v_mfma_f32_16x16x32_bf16 v[140:143], v[28:31], a[40:43], v[140:143]
s_add_u32 m0, s82, 106496
v_mfma_f32_16x16x32_bf16 v[144:147], v[28:31], a[44:47], v[144:147]
buffer_load_dwordx4 v45, %[srdF], s79 offen offset:0 lds
v_mfma_f32_16x16x32_bf16 v[128:131], v[24:27], a[44:47], v[128:131]
s_waitcnt lgkmcnt(4)
v_mfma_f32_16x16x32_bf16 v[160:163], v[32:35], a[44:47], v[160:163]
ds_read_b64_tr_b16 v[16:17], v249 offset:38912
ds_read_b64_tr_b16 v[18:19], v249 offset:45056
v_mfma_f32_16x16x32_bf16 v[176:179], v[36:39], a[44:47], v[176:179]
v_mfma_f32_16x16x32_bf16 v[172:175], v[36:39], a[40:43], v[172:175]
v_mfma_f32_16x16x32_bf16 v[156:159], v[32:35], a[40:43], v[156:159]
v_mfma_f32_16x16x32_bf16 v[152:155], v[32:35], a[36:39], v[152:155]
ds_read_b64_tr_b16 v[20:21], v250 offset:38912
ds_read_b64_tr_b16 v[22:23], v250 offset:45056
v_mfma_f32_16x16x32_bf16 v[168:171], v[36:39], a[36:39], v[168:171]
buffer_load_dwordx4 v46, %[srdF], s79 offen offset:1024 lds
v_mfma_f32_16x16x32_bf16 v[164:167], v[36:39], a[32:35], v[164:167]
v_mfma_f32_16x16x32_bf16 v[148:151], v[32:35], a[32:35], v[148:151]
s_waitcnt lgkmcnt(4)
v_mfma_f32_16x16x32_bf16 v[180:183], v[8:11], a[32:35], v[180:183]
ds_read_b64_tr_b16 v[24:25], v251 offset:24576
ds_read_b64_tr_b16 v[26:27], v251 offset:28672
v_mfma_f32_16x16x32_bf16 v[196:199], v[12:15], a[32:35], v[196:199]
v_mfma_f32_16x16x32_bf16 v[200:203], v[12:15], a[36:39], v[200:203]
v_mfma_f32_16x16x32_bf16 v[184:187], v[8:11], a[36:39], v[184:187]
s_add_u32 m0, s81, 61440
s_add_u32 s78, s77, 393216
v_mfma_f32_16x16x32_bf16 v[188:191], v[8:11], a[40:43], v[188:191]
ds_read_b64_tr_b16 v[28:29], v252 offset:24576
ds_read_b64_tr_b16 v[30:31], v252 offset:28672
buffer_load_dwordx4 v244, %[srdW], s78 offen offset:0 lds
v_mfma_f32_16x16x32_bf16 v[204:207], v[12:15], a[40:43], v[204:207]
v_mfma_f32_16x16x32_bf16 v[208:211], v[12:15], a[44:47], v[208:211]
v_mfma_f32_16x16x32_bf16 v[192:195], v[8:11], a[44:47], v[192:195]
s_waitcnt lgkmcnt(4)
v_mfma_f32_16x16x32_bf16 v[224:227], v[16:19], a[44:47], v[224:227]
ds_read_b64_tr_b16 v[32:33], v253 offset:24576
ds_read_b64_tr_b16 v[34:35], v253 offset:28672
v_mfma_f32_16x16x32_bf16 v[240:243], v[20:23], a[44:47], v[240:243]
v_mfma_f32_16x16x32_bf16 v[236:239], v[20:23], a[40:43], v[236:239]
v_mfma_f32_16x16x32_bf16 v[220:223], v[16:19], a[40:43], v[220:223]
buffer_load_dwordx4 v245, %[srdW], s78 offen offset:1024 lds
v_mfma_f32_16x16x32_bf16 v[216:219], v[16:19], a[36:39], v[216:219]
ds_read_b64_tr_b16 v[36:37], v254 offset:24576
ds_read_b64_tr_b16 v[38:39], v254 offset:28672
v_mfma_f32_16x16x32_bf16 v[232:235], v[20:23], a[36:39], v[232:235]
v_mfma_f32_16x16x32_bf16 v[228:231], v[20:23], a[32:35], v[228:231]
v_mfma_f32_16x16x32_bf16 v[212:215], v[16:19], a[32:35], v[212:215]
s_waitcnt lgkmcnt(4)
v_mfma_f32_16x16x32_bf16 a[96:99], v[24:27], a[32:35], a[96:99]
ds_read_b64_tr_b16 v[8:9], %[vaddr0] offset:36864
ds_read_b64_tr_b16 v[10:11], %[vaddr0] offset:43008
v_mfma_f32_16x16x32_bf16 a[112:115], v[28:31], a[32:35], a[112:115]
v_mfma_f32_16x16x32_bf16 a[116:119], v[28:31], a[36:39], a[116:119]
buffer_load_dwordx4 v246, %[srdW], s78 offen offset:2048 lds
v_mfma_f32_16x16x32_bf16 a[100:103], v[24:27], a[36:39], a[100:103]
v_mfma_f32_16x16x32_bf16 a[104:107], v[24:27], a[40:43], a[104:107]
ds_read_b64_tr_b16 v[12:13], v41 offset:36864
ds_read_b64_tr_b16 v[14:15], v41 offset:43008
v_mfma_f32_16x16x32_bf16 a[120:123], v[28:31], a[40:43], a[120:123]
v_mfma_f32_16x16x32_bf16 a[124:127], v[28:31], a[44:47], a[124:127]
v_mfma_f32_16x16x32_bf16 a[108:111], v[24:27], a[44:47], a[108:111]
s_waitcnt lgkmcnt(4)
v_mfma_f32_16x16x32_bf16 a[132:135], v[32:35], a[36:39], a[132:135]
ds_read_b64_tr_b16 v[16:17], v42 offset:36864
ds_read_b64_tr_b16 v[18:19], v42 offset:43008
s_add_u32 s77, s77, 786432
s_add_u32 s79, s79, 196608
v_mfma_f32_16x16x32_bf16 a[148:151], v[36:39], a[36:39], a[148:151]
global_load_dwordx4 a[0:3], v47, s[48:49] offset:0 sc1
v_mfma_f32_16x16x32_bf16 a[144:147], v[36:39], a[32:35], a[144:147]
ds_read_b64_tr_b16 v[20:21], v43 offset:36864
ds_read_b64_tr_b16 v[22:23], v43 offset:43008
v_mfma_f32_16x16x32_bf16 a[128:131], v[32:35], a[32:35], a[128:131]
s_waitcnt vmcnt(28) lgkmcnt(4)
v_mfma_f32_16x16x32_bf16 v[52:55], v[8:11], a[48:51], v[52:55]
ds_read_b64_tr_b16 v[24:25], %[vaddr0] offset:37888
ds_read_b64_tr_b16 v[26:27], %[vaddr0] offset:44032
v_mfma_f32_16x16x32_bf16 v[68:71], v[12:15], a[48:51], v[68:71]
s_waitcnt vmcnt(27)
v_mfma_f32_16x16x32_bf16 v[72:75], v[12:15], a[52:55], v[72:75]
v_mfma_f32_16x16x32_bf16 v[56:59], v[8:11], a[52:55], v[56:59]
global_load_dwordx4 a[4:7], v48, s[48:49] offset:0 sc1
s_waitcnt vmcnt(27)
v_mfma_f32_16x16x32_bf16 v[60:63], v[8:11], a[56:59], v[60:63]
)"
R"(ds_read_b64_tr_b16 v[28:29], v41 offset:37888
ds_read_b64_tr_b16 v[30:31], v41 offset:44032
v_mfma_f32_16x16x32_bf16 v[76:79], v[12:15], a[56:59], v[76:79]
s_waitcnt vmcnt(26)
v_mfma_f32_16x16x32_bf16 v[80:83], v[12:15], a[60:63], v[80:83]
v_mfma_f32_16x16x32_bf16 v[64:67], v[8:11], a[60:63], v[64:67]
s_waitcnt lgkmcnt(4)
v_mfma_f32_16x16x32_bf16 v[96:99], v[16:19], a[60:63], v[96:99]
ds_read_b64_tr_b16 v[32:33], v42 offset:37888
ds_read_b64_tr_b16 v[34:35], v42 offset:44032
v_mfma_f32_16x16x32_bf16 v[112:115], v[20:23], a[60:63], v[112:115]
v_mfma_f32_16x16x32_bf16 v[108:111], v[20:23], a[56:59], v[108:111]
global_load_dwordx4 a[8:11], v49, s[48:49] offset:0 sc1
v_mfma_f32_16x16x32_bf16 v[92:95], v[16:19], a[56:59], v[92:95]
v_mfma_f32_16x16x32_bf16 v[88:91], v[16:19], a[52:55], v[88:91]
ds_read_b64_tr_b16 v[36:37], v43 offset:37888
ds_read_b64_tr_b16 v[38:39], v43 offset:44032
v_mfma_f32_16x16x32_bf16 v[104:107], v[20:23], a[52:55], v[104:107]
v_mfma_f32_16x16x32_bf16 v[100:103], v[20:23], a[48:51], v[100:103]
v_mfma_f32_16x16x32_bf16 v[84:87], v[16:19], a[48:51], v[84:87]
s_waitcnt lgkmcnt(4)
v_mfma_f32_16x16x32_bf16 v[116:119], v[24:27], a[48:51], v[116:119]
ds_read_b64_tr_b16 v[8:9], %[vaddr0] offset:38912
ds_read_b64_tr_b16 v[10:11], %[vaddr0] offset:45056
v_mfma_f32_16x16x32_bf16 v[132:135], v[28:31], a[48:51], v[132:135]
global_load_dwordx4 a[12:15], v50, s[48:49] offset:0 sc1
v_mfma_f32_16x16x32_bf16 v[136:139], v[28:31], a[52:55], v[136:139]
v_mfma_f32_16x16x32_bf16 v[120:123], v[24:27], a[52:55], v[120:123]
v_mfma_f32_16x16x32_bf16 v[124:127], v[24:27], a[56:59], v[124:127]
ds_read_b64_tr_b16 v[12:13], v41 offset:38912
ds_read_b64_tr_b16 v[14:15], v41 offset:45056
v_mfma_f32_16x16x32_bf16 v[140:143], v[28:31], a[56:59], v[140:143]
v_mfma_f32_16x16x32_bf16 v[144:147], v[28:31], a[60:63], v[144:147]
v_mfma_f32_16x16x32_bf16 v[128:131], v[24:27], a[60:63], v[128:131]
s_waitcnt lgkmcnt(4)
v_mfma_f32_16x16x32_bf16 v[160:163], v[32:35], a[60:63], v[160:163]
ds_read_b64_tr_b16 v[16:17], v42 offset:38912
ds_read_b64_tr_b16 v[18:19], v42 offset:45056
global_load_dwordx4 a[16:19], v47, s[48:49] offset:1024 sc1
v_mfma_f32_16x16x32_bf16 v[176:179], v[36:39], a[60:63], v[176:179]
v_mfma_f32_16x16x32_bf16 v[172:175], v[36:39], a[56:59], v[172:175]
v_mfma_f32_16x16x32_bf16 v[156:159], v[32:35], a[56:59], v[156:159]
v_mfma_f32_16x16x32_bf16 v[152:155], v[32:35], a[52:55], v[152:155]
ds_read_b64_tr_b16 v[20:21], v43 offset:38912
ds_read_b64_tr_b16 v[22:23], v43 offset:45056
v_mfma_f32_16x16x32_bf16 v[168:171], v[36:39], a[52:55], v[168:171]
v_mfma_f32_16x16x32_bf16 v[164:167], v[36:39], a[48:51], v[164:167]
v_mfma_f32_16x16x32_bf16 v[148:151], v[32:35], a[48:51], v[148:151]
global_load_dwordx4 a[20:23], v48, s[48:49] offset:1024 sc1
s_waitcnt lgkmcnt(4)
v_mfma_f32_16x16x32_bf16 v[180:183], v[8:11], a[48:51], v[180:183]
ds_read_b64_tr_b16 v[24:25], v251 offset:25600
ds_read_b64_tr_b16 v[26:27], v251 offset:29696
v_mfma_f32_16x16x32_bf16 v[196:199], v[12:15], a[48:51], v[196:199]
v_mfma_f32_16x16x32_bf16 v[200:203], v[12:15], a[52:55], v[200:203]
v_mfma_f32_16x16x32_bf16 v[184:187], v[8:11], a[52:55], v[184:187]
v_mfma_f32_16x16x32_bf16 v[188:191], v[8:11], a[56:59], v[188:191]
ds_read_b64_tr_b16 v[28:29], v252 offset:25600
ds_read_b64_tr_b16 v[30:31], v252 offset:29696
v_mfma_f32_16x16x32_bf16 v[204:207], v[12:15], a[56:59], v[204:207]
v_mfma_f32_16x16x32_bf16 v[208:211], v[12:15], a[60:63], v[208:211]
global_load_dwordx4 a[24:27], v49, s[48:49] offset:1024 sc1
v_mfma_f32_16x16x32_bf16 v[192:195], v[8:11], a[60:63], v[192:195]
s_waitcnt lgkmcnt(4)
v_mfma_f32_16x16x32_bf16 v[224:227], v[16:19], a[60:63], v[224:227]
ds_read_b64_tr_b16 v[32:33], v253 offset:25600
ds_read_b64_tr_b16 v[34:35], v253 offset:29696
v_mfma_f32_16x16x32_bf16 v[240:243], v[20:23], a[60:63], v[240:243]
v_mfma_f32_16x16x32_bf16 v[236:239], v[20:23], a[56:59], v[236:239]
v_mfma_f32_16x16x32_bf16 v[220:223], v[16:19], a[56:59], v[220:223]
v_mfma_f32_16x16x32_bf16 v[216:219], v[16:19], a[52:55], v[216:219]
ds_read_b64_tr_b16 v[36:37], v254 offset:25600
ds_read_b64_tr_b16 v[38:39], v254 offset:29696
v_mfma_f32_16x16x32_bf16 v[232:235], v[20:23], a[52:55], v[232:235]
global_load_dwordx4 a[28:31], v50, s[48:49] offset:1024 sc1
v_mfma_f32_16x16x32_bf16 v[228:231], v[20:23], a[48:51], v[228:231]
v_mfma_f32_16x16x32_bf16 v[212:215], v[16:19], a[48:51], v[212:215]
s_waitcnt lgkmcnt(4)
v_mfma_f32_16x16x32_bf16 a[96:99], v[24:27], a[48:51], a[96:99]
s_waitcnt vmcnt(24)
s_barrier
ds_read_b64_tr_b16 v[8:9], v247 offset:0
ds_read_b64_tr_b16 v[10:11], v247 offset:6144
v_mfma_f32_16x16x32_bf16 a[112:115], v[28:31], a[48:51], a[112:115]
v_mfma_f32_16x16x32_bf16 a[116:119], v[28:31], a[52:55], a[116:119]
v_mfma_f32_16x16x32_bf16 a[100:103], v[24:27], a[52:55], a[100:103]
s_add_u32 s48, s48, 2048
s_addc_u32 s49, s49, 0
v_mfma_f32_16x16x32_bf16 a[104:107], v[24:27], a[56:59], a[104:107]
ds_read_b64_tr_b16 v[12:13], v248 offset:0
ds_read_b64_tr_b16 v[14:15], v248 offset:6144
v_mfma_f32_16x16x32_bf16 a[120:123], v[28:31], a[56:59], a[120:123]
v_mfma_f32_16x16x32_bf16 a[124:127], v[28:31], a[60:63], a[124:127]
v_mfma_f32_16x16x32_bf16 a[108:111], v[24:27], a[60:63], a[108:111]
s_waitcnt lgkmcnt(4)
v_mfma_f32_16x16x32_bf16 a[132:135], v[32:35], a[52:55], a[132:135]
ds_read_b64_tr_b16 v[16:17], v249 offset:0
ds_read_b64_tr_b16 v[18:19], v249 offset:6144
v_mfma_f32_16x16x32_bf16 a[148:151], v[36:39], a[52:55], a[148:151]
v_mfma_f32_16x16x32_bf16 a[144:147], v[36:39], a[48:51], a[144:147]
ds_read_b64_tr_b16 v[20:21], v250 offset:0
ds_read_b64_tr_b16 v[22:23], v250 offset:6144
v_mfma_f32_16x16x32_bf16 a[128:131], v[32:35], a[48:51], a[128:131]
s_mov_b32 s83, 384
v_mad_u32_u24 v44, %[drow], s83, %[dch16]
v_subrev_u32 v45, 896, v44
v_subrev_u32 v46, 1792, v44
s_waitcnt vmcnt(23) lgkmcnt(4)
v_mfma_f32_16x16x32_bf16 v[52:55], v[8:11], a[64:67], v[52:55]
ds_read_b64_tr_b16 v[24:25], v247 offset:1024
ds_read_b64_tr_b16 v[26:27], v247 offset:7168
v_mfma_f32_16x16x32_bf16 v[68:71], v[12:15], a[64:67], v[68:71]
s_add_u32 m0, s81, 24576
s_waitcnt vmcnt(22)
v_mfma_f32_16x16x32_bf16 v[72:75], v[12:15], a[68:71], v[72:75]
buffer_load_dwordx4 v44, %[srdK], s76 offen offset:0 lds
v_mfma_f32_16x16x32_bf16 v[56:59], v[8:11], a[68:71], v[56:59]
s_waitcnt vmcnt(22)
v_mfma_f32_16x16x32_bf16 v[60:63], v[8:11], a[72:75], v[60:63]
ds_read_b64_tr_b16 v[28:29], v248 offset:1024
ds_read_b64_tr_b16 v[30:31], v248 offset:7168
v_mfma_f32_16x16x32_bf16 v[76:79], v[12:15], a[72:75], v[76:79]
s_waitcnt vmcnt(21)
v_mfma_f32_16x16x32_bf16 v[80:83], v[12:15], a[76:79], v[80:83]
v_mfma_f32_16x16x32_bf16 v[64:67], v[8:11], a[76:79], v[64:67]
s_waitcnt lgkmcnt(4)
v_mfma_f32_16x16x32_bf16 v[96:99], v[16:19], a[76:79], v[96:99]
ds_read_b64_tr_b16 v[32:33], v249 offset:1024
ds_read_b64_tr_b16 v[34:35], v249 offset:7168
v_mfma_f32_16x16x32_bf16 v[112:115], v[20:23], a[76:79], v[112:115]
v_mfma_f32_16x16x32_bf16 v[108:111], v[20:23], a[72:75], v[108:111]
v_mfma_f32_16x16x32_bf16 v[92:95], v[16:19], a[72:75], v[92:95]
v_mfma_f32_16x16x32_bf16 v[88:91], v[16:19], a[68:71], v[88:91]
ds_read_b64_tr_b16 v[36:37], v250 offset:1024
ds_read_b64_tr_b16 v[38:39], v250 offset:7168
v_mfma_f32_16x16x32_bf16 v[104:107], v[20:23], a[68:71], v[104:107]
v_mfma_f32_16x16x32_bf16 v[100:103], v[20:23], a[64:67], v[100:103]
v_mfma_f32_16x16x32_bf16 v[84:87], v[16:19], a[64:67], v[84:87]
s_waitcnt lgkmcnt(4)
v_mfma_f32_16x16x32_bf16 v[116:119], v[24:27], a[64:67], v[116:119]
ds_read_b64_tr_b16 v[8:9], v247 offset:2048
ds_read_b64_tr_b16 v[10:11], v247 offset:8192
v_mfma_f32_16x16x32_bf16 v[132:135], v[28:31], a[64:67], v[132:135]
v_mfma_f32_16x16x32_bf16 v[136:139], v[28:31], a[68:71], v[136:139]
v_mfma_f32_16x16x32_bf16 v[120:123], v[24:27], a[68:71], v[120:123]
v_mfma_f32_16x16x32_bf16 v[124:127], v[24:27], a[72:75], v[124:127]
ds_read_b64_tr_b16 v[12:13], v248 offset:2048
ds_read_b64_tr_b16 v[14:15], v248 offset:8192
v_mfma_f32_16x16x32_bf16 v[140:143], v[28:31], a[72:75], v[140:143]
v_mfma_f32_16x16x32_bf16 v[144:147], v[28:31], a[76:79], v[144:147]
v_mfma_f32_16x16x32_bf16 v[128:131], v[24:27], a[76:79], v[128:131]
s_waitcnt lgkmcnt(4)
v_mfma_f32_16x16x32_bf16 v[160:163], v[32:35], a[76:79], v[160:163]
ds_read_b64_tr_b16 v[16:17], v249 offset:2048
ds_read_b64_tr_b16 v[18:19], v249 offset:8192
v_mfma_f32_16x16x32_bf16 v[176:179], v[36:39], a[76:79], v[176:179]
v_mfma_f32_16x16x32_bf16 v[172:175], v[36:39], a[72:75], v[172:175]
v_mfma_f32_16x16x32_bf16 v[156:159], v[32:35], a[72:75], v[156:159]
v_mfma_f32_16x16x32_bf16 v[152:155], v[32:35], a[68:71], v[152:155]
ds_read_b64_tr_b16 v[20:21], v250 offset:2048
ds_read_b64_tr_b16 v[22:23], v250 offset:8192
v_mfma_f32_16x16x32_bf16 v[168:171], v[36:39], a[68:71], v[168:171]
v_mfma_f32_16x16x32_bf16 v[164:167], v[36:39], a[64:67], v[164:167]
v_mfma_f32_16x16x32_bf16 v[148:151], v[32:35], a[64:67], v[148:151]
s_waitcnt lgkmcnt(4)
v_mfma_f32_16x16x32_bf16 v[180:183], v[8:11], a[64:67], v[180:183]
ds_read_b64_tr_b16 v[24:25], v251 offset:0
ds_read_b64_tr_b16 v[26:27], v251 offset:4096
v_mfma_f32_16x16x32_bf16 v[196:199], v[12:15], a[64:67], v[196:199]
v_mfma_f32_16x16x32_bf16 v[200:203], v[12:15], a[68:71], v[200:203]
v_mfma_f32_16x16x32_bf16 v[184:187], v[8:11], a[68:71], v[184:187]
v_mfma_f32_16x16x32_bf16 v[188:191], v[8:11], a[72:75], v[188:191]
ds_read_b64_tr_b16 v[28:29], v252 offset:0
ds_read_b64_tr_b16 v[30:31], v252 offset:4096
v_mfma_f32_16x16x32_bf16 v[204:207], v[12:15], a[72:75], v[204:207]
v_mfma_f32_16x16x32_bf16 v[208:211], v[12:15], a[76:79], v[208:211]
buffer_load_dwordx4 v45, %[srdK], s76 offen offset:1024 lds
v_mfma_f32_16x16x32_bf16 v[192:195], v[8:11], a[76:79], v[192:195]
s_waitcnt lgkmcnt(4)
v_mfma_f32_16x16x32_bf16 v[224:227], v[16:19], a[76:79], v[224:227]
ds_read_b64_tr_b16 v[32:33], v253 offset:0
ds_read_b64_tr_b16 v[34:35], v253 offset:4096
v_mfma_f32_16x16x32_bf16 v[240:243], v[20:23], a[76:79], v[240:243]
v_mfma_f32_16x16x32_bf16 v[236:239], v[20:23], a[72:75], v[236:239]
v_mfma_f32_16x16x32_bf16 v[220:223], v[16:19], a[72:75], v[220:223]
v_mfma_f32_16x16x32_bf16 v[216:219], v[16:19], a[68:71], v[216:219]
ds_read_b64_tr_b16 v[36:37], v254 offset:0
ds_read_b64_tr_b16 v[38:39], v254 offset:4096
v_mfma_f32_16x16x32_bf16 v[232:235], v[20:23], a[68:71], v[232:235]
v_mfma_f32_16x16x32_bf16 v[228:231], v[20:23], a[64:67], v[228:231]
v_mfma_f32_16x16x32_bf16 v[212:215], v[16:19], a[64:67], v[212:215]
s_waitcnt lgkmcnt(4)
v_mfma_f32_16x16x32_bf16 a[96:99], v[24:27], a[64:67], a[96:99]
ds_read_b64_tr_b16 v[8:9], %[vaddr0] offset:0
ds_read_b64_tr_b16 v[10:11], %[vaddr0] offset:6144
v_mfma_f32_16x16x32_bf16 a[112:115], v[28:31], a[64:67], a[112:115]
v_mfma_f32_16x16x32_bf16 a[116:119], v[28:31], a[68:71], a[116:119]
v_mfma_f32_16x16x32_bf16 a[100:103], v[24:27], a[68:71], a[100:103]
v_mfma_f32_16x16x32_bf16 a[104:107], v[24:27], a[72:75], a[104:107]
ds_read_b64_tr_b16 v[12:13], v41 offset:0
)"
R"(ds_read_b64_tr_b16 v[14:15], v41 offset:6144
v_mfma_f32_16x16x32_bf16 a[120:123], v[28:31], a[72:75], a[120:123]
v_mfma_f32_16x16x32_bf16 a[124:127], v[28:31], a[76:79], a[124:127]
v_mfma_f32_16x16x32_bf16 a[108:111], v[24:27], a[76:79], a[108:111]
s_waitcnt lgkmcnt(4)
v_mfma_f32_16x16x32_bf16 a[132:135], v[32:35], a[68:71], a[132:135]
ds_read_b64_tr_b16 v[16:17], v42 offset:0
ds_read_b64_tr_b16 v[18:19], v42 offset:6144
v_mfma_f32_16x16x32_bf16 a[148:151], v[36:39], a[68:71], a[148:151]
v_mfma_f32_16x16x32_bf16 a[144:147], v[36:39], a[64:67], a[144:147]
ds_read_b64_tr_b16 v[20:21], v43 offset:0
ds_read_b64_tr_b16 v[22:23], v43 offset:6144
v_mfma_f32_16x16x32_bf16 a[128:131], v[32:35], a[64:67], a[128:131]
s_waitcnt vmcnt(21) lgkmcnt(4)
v_mfma_f32_16x16x32_bf16 v[52:55], v[8:11], a[80:83], v[52:55]
ds_read_b64_tr_b16 v[24:25], %[vaddr0] offset:1024
ds_read_b64_tr_b16 v[26:27], %[vaddr0] offset:7168
v_mfma_f32_16x16x32_bf16 v[68:71], v[12:15], a[80:83], v[68:71]
s_waitcnt vmcnt(20)
v_mfma_f32_16x16x32_bf16 v[72:75], v[12:15], a[84:87], v[72:75]
v_mfma_f32_16x16x32_bf16 v[56:59], v[8:11], a[84:87], v[56:59]
s_waitcnt vmcnt(19)
v_mfma_f32_16x16x32_bf16 v[60:63], v[8:11], a[88:91], v[60:63]
ds_read_b64_tr_b16 v[28:29], v41 offset:1024
ds_read_b64_tr_b16 v[30:31], v41 offset:7168
v_mfma_f32_16x16x32_bf16 v[76:79], v[12:15], a[88:91], v[76:79]
s_waitcnt vmcnt(18)
v_mfma_f32_16x16x32_bf16 v[80:83], v[12:15], a[92:95], v[80:83]
v_mfma_f32_16x16x32_bf16 v[64:67], v[8:11], a[92:95], v[64:67]
s_waitcnt lgkmcnt(4)
v_mfma_f32_16x16x32_bf16 v[96:99], v[16:19], a[92:95], v[96:99]
ds_read_b64_tr_b16 v[32:33], v42 offset:1024
ds_read_b64_tr_b16 v[34:35], v42 offset:7168
v_mfma_f32_16x16x32_bf16 v[112:115], v[20:23], a[92:95], v[112:115]
v_mfma_f32_16x16x32_bf16 v[108:111], v[20:23], a[88:91], v[108:111]
v_mfma_f32_16x16x32_bf16 v[92:95], v[16:19], a[88:91], v[92:95]
v_mfma_f32_16x16x32_bf16 v[88:91], v[16:19], a[84:87], v[88:91]
ds_read_b64_tr_b16 v[36:37], v43 offset:1024
ds_read_b64_tr_b16 v[38:39], v43 offset:7168
v_mfma_f32_16x16x32_bf16 v[104:107], v[20:23], a[84:87], v[104:107]
v_mfma_f32_16x16x32_bf16 v[100:103], v[20:23], a[80:83], v[100:103]
v_mfma_f32_16x16x32_bf16 v[84:87], v[16:19], a[80:83], v[84:87]
buffer_load_dwordx4 v46, %[srdK], s76 offen offset:2048 lds
s_waitcnt lgkmcnt(4)
v_mfma_f32_16x16x32_bf16 v[116:119], v[24:27], a[80:83], v[116:119]
ds_read_b64_tr_b16 v[8:9], %[vaddr0] offset:2048
ds_read_b64_tr_b16 v[10:11], %[vaddr0] offset:8192
v_mfma_f32_16x16x32_bf16 v[132:135], v[28:31], a[80:83], v[132:135]
v_mfma_f32_16x16x32_bf16 v[136:139], v[28:31], a[84:87], v[136:139]
v_mfma_f32_16x16x32_bf16 v[120:123], v[24:27], a[84:87], v[120:123]
v_mfma_f32_16x16x32_bf16 v[124:127], v[24:27], a[88:91], v[124:127]
ds_read_b64_tr_b16 v[12:13], v41 offset:2048
ds_read_b64_tr_b16 v[14:15], v41 offset:8192
v_mfma_f32_16x16x32_bf16 v[140:143], v[28:31], a[88:91], v[140:143]
v_mfma_f32_16x16x32_bf16 v[144:147], v[28:31], a[92:95], v[144:147]
v_mfma_f32_16x16x32_bf16 v[128:131], v[24:27], a[92:95], v[128:131]
s_waitcnt lgkmcnt(4)
v_mfma_f32_16x16x32_bf16 v[160:163], v[32:35], a[92:95], v[160:163]
ds_read_b64_tr_b16 v[16:17], v42 offset:2048
ds_read_b64_tr_b16 v[18:19], v42 offset:8192
v_mfma_f32_16x16x32_bf16 v[176:179], v[36:39], a[92:95], v[176:179]
v_mfma_f32_16x16x32_bf16 v[172:175], v[36:39], a[88:91], v[172:175]
v_mfma_f32_16x16x32_bf16 v[156:159], v[32:35], a[88:91], v[156:159]
v_mfma_f32_16x16x32_bf16 v[152:155], v[32:35], a[84:87], v[152:155]
ds_read_b64_tr_b16 v[20:21], v43 offset:2048
ds_read_b64_tr_b16 v[22:23], v43 offset:8192
v_mfma_f32_16x16x32_bf16 v[168:171], v[36:39], a[84:87], v[168:171]
v_mfma_f32_16x16x32_bf16 v[164:167], v[36:39], a[80:83], v[164:167]
v_mfma_f32_16x16x32_bf16 v[148:151], v[32:35], a[80:83], v[148:151]
s_waitcnt lgkmcnt(4)
v_mfma_f32_16x16x32_bf16 v[180:183], v[8:11], a[80:83], v[180:183]
ds_read_b64_tr_b16 v[24:25], v251 offset:1024
ds_read_b64_tr_b16 v[26:27], v251 offset:5120
v_mfma_f32_16x16x32_bf16 v[196:199], v[12:15], a[80:83], v[196:199]
v_mfma_f32_16x16x32_bf16 v[200:203], v[12:15], a[84:87], v[200:203]
v_mfma_f32_16x16x32_bf16 v[184:187], v[8:11], a[84:87], v[184:187]
v_mfma_f32_16x16x32_bf16 v[188:191], v[8:11], a[88:91], v[188:191]
ds_read_b64_tr_b16 v[28:29], v252 offset:1024
ds_read_b64_tr_b16 v[30:31], v252 offset:5120
v_mfma_f32_16x16x32_bf16 v[204:207], v[12:15], a[88:91], v[204:207]
v_mfma_f32_16x16x32_bf16 v[208:211], v[12:15], a[92:95], v[208:211]
v_mfma_f32_16x16x32_bf16 v[192:195], v[8:11], a[92:95], v[192:195]
s_waitcnt lgkmcnt(4)
v_mfma_f32_16x16x32_bf16 v[224:227], v[16:19], a[92:95], v[224:227]
ds_read_b64_tr_b16 v[32:33], v253 offset:1024
ds_read_b64_tr_b16 v[34:35], v253 offset:5120
v_mfma_f32_16x16x32_bf16 v[240:243], v[20:23], a[92:95], v[240:243]
v_mfma_f32_16x16x32_bf16 v[236:239], v[20:23], a[88:91], v[236:239]
v_mfma_f32_16x16x32_bf16 v[220:223], v[16:19], a[88:91], v[220:223]
v_mfma_f32_16x16x32_bf16 v[216:219], v[16:19], a[84:87], v[216:219]
ds_read_b64_tr_b16 v[36:37], v254 offset:1024
ds_read_b64_tr_b16 v[38:39], v254 offset:5120
v_mfma_f32_16x16x32_bf16 v[232:235], v[20:23], a[84:87], v[232:235]
v_mfma_f32_16x16x32_bf16 v[228:231], v[20:23], a[80:83], v[228:231]
v_mfma_f32_16x16x32_bf16 v[212:215], v[16:19], a[80:83], v[212:215]
s_waitcnt lgkmcnt(4)
v_mfma_f32_16x16x32_bf16 a[96:99], v[24:27], a[80:83], a[96:99]
s_waitcnt vmcnt(11)
s_barrier
ds_read_b64_tr_b16 v[8:9], v247 offset:12288
ds_read_b64_tr_b16 v[10:11], v247 offset:18432
v_mfma_f32_16x16x32_bf16 a[112:115], v[28:31], a[80:83], a[112:115]
v_mfma_f32_16x16x32_bf16 a[116:119], v[28:31], a[84:87], a[116:119]
v_mfma_f32_16x16x32_bf16 a[100:103], v[24:27], a[84:87], a[100:103]
s_add_u32 s76, s76, 12288
v_mfma_f32_16x16x32_bf16 a[104:107], v[24:27], a[88:91], a[104:107]
ds_read_b64_tr_b16 v[12:13], v248 offset:12288
ds_read_b64_tr_b16 v[14:15], v248 offset:18432
v_mfma_f32_16x16x32_bf16 a[120:123], v[28:31], a[88:91], a[120:123]
v_mfma_f32_16x16x32_bf16 a[124:127], v[28:31], a[92:95], a[124:127]
v_mfma_f32_16x16x32_bf16 a[108:111], v[24:27], a[92:95], a[108:111]
s_waitcnt lgkmcnt(4)
v_mfma_f32_16x16x32_bf16 a[132:135], v[32:35], a[84:87], a[132:135]
ds_read_b64_tr_b16 v[16:17], v249 offset:12288
ds_read_b64_tr_b16 v[18:19], v249 offset:18432
v_mfma_f32_16x16x32_bf16 a[148:151], v[36:39], a[84:87], a[148:151]
v_mfma_f32_16x16x32_bf16 a[144:147], v[36:39], a[80:83], a[144:147]
ds_read_b64_tr_b16 v[20:21], v250 offset:12288
ds_read_b64_tr_b16 v[22:23], v250 offset:18432
v_mfma_f32_16x16x32_bf16 a[128:131], v[32:35], a[80:83], a[128:131]
s_waitcnt vmcnt(10) lgkmcnt(4)
v_mfma_f32_16x16x32_bf16 v[52:55], v[8:11], a[0:3], v[52:55]
ds_read_b64_tr_b16 v[24:25], v247 offset:13312
ds_read_b64_tr_b16 v[26:27], v247 offset:19456
v_mfma_f32_16x16x32_bf16 v[68:71], v[12:15], a[0:3], v[68:71]
s_add_u32 m0, s81, 36864
s_waitcnt vmcnt(9)
v_mfma_f32_16x16x32_bf16 v[72:75], v[12:15], a[4:7], v[72:75]
buffer_load_dwordx4 v44, %[srdK], s76 offen offset:0 lds
v_mfma_f32_16x16x32_bf16 v[56:59], v[8:11], a[4:7], v[56:59]
s_waitcnt vmcnt(9)
v_mfma_f32_16x16x32_bf16 v[60:63], v[8:11], a[8:11], v[60:63]
ds_read_b64_tr_b16 v[28:29], v248 offset:13312
ds_read_b64_tr_b16 v[30:31], v248 offset:19456
v_mfma_f32_16x16x32_bf16 v[76:79], v[12:15], a[8:11], v[76:79]
s_waitcnt vmcnt(8)
v_mfma_f32_16x16x32_bf16 v[80:83], v[12:15], a[12:15], v[80:83]
v_mfma_f32_16x16x32_bf16 v[64:67], v[8:11], a[12:15], v[64:67]
s_waitcnt lgkmcnt(4)
v_mfma_f32_16x16x32_bf16 v[96:99], v[16:19], a[12:15], v[96:99]
ds_read_b64_tr_b16 v[32:33], v249 offset:13312
ds_read_b64_tr_b16 v[34:35], v249 offset:19456
v_mfma_f32_16x16x32_bf16 v[112:115], v[20:23], a[12:15], v[112:115]
v_mfma_f32_16x16x32_bf16 v[108:111], v[20:23], a[8:11], v[108:111]
v_mfma_f32_16x16x32_bf16 v[92:95], v[16:19], a[8:11], v[92:95]
v_mfma_f32_16x16x32_bf16 v[88:91], v[16:19], a[4:7], v[88:91]
ds_read_b64_tr_b16 v[36:37], v250 offset:13312
ds_read_b64_tr_b16 v[38:39], v250 offset:19456
v_mfma_f32_16x16x32_bf16 v[104:107], v[20:23], a[4:7], v[104:107]
v_mfma_f32_16x16x32_bf16 v[100:103], v[20:23], a[0:3], v[100:103]
v_mfma_f32_16x16x32_bf16 v[84:87], v[16:19], a[0:3], v[84:87]
s_waitcnt lgkmcnt(4)
v_mfma_f32_16x16x32_bf16 v[116:119], v[24:27], a[0:3], v[116:119]
ds_read_b64_tr_b16 v[8:9], v247 offset:14336
ds_read_b64_tr_b16 v[10:11], v247 offset:20480
v_mfma_f32_16x16x32_bf16 v[132:135], v[28:31], a[0:3], v[132:135]
v_mfma_f32_16x16x32_bf16 v[136:139], v[28:31], a[4:7], v[136:139]
v_mfma_f32_16x16x32_bf16 v[120:123], v[24:27], a[4:7], v[120:123]
v_mfma_f32_16x16x32_bf16 v[124:127], v[24:27], a[8:11], v[124:127]
ds_read_b64_tr_b16 v[12:13], v248 offset:14336
ds_read_b64_tr_b16 v[14:15], v248 offset:20480
buffer_load_dwordx4 v45, %[srdK], s76 offen offset:1024 lds
v_mfma_f32_16x16x32_bf16 v[140:143], v[28:31], a[8:11], v[140:143]
v_mfma_f32_16x16x32_bf16 v[144:147], v[28:31], a[12:15], v[144:147]
v_mfma_f32_16x16x32_bf16 v[128:131], v[24:27], a[12:15], v[128:131]
s_waitcnt lgkmcnt(4)
v_mfma_f32_16x16x32_bf16 v[160:163], v[32:35], a[12:15], v[160:163]
ds_read_b64_tr_b16 v[16:17], v249 offset:14336
ds_read_b64_tr_b16 v[18:19], v249 offset:20480
v_mfma_f32_16x16x32_bf16 v[176:179], v[36:39], a[12:15], v[176:179]
v_mfma_f32_16x16x32_bf16 v[172:175], v[36:39], a[8:11], v[172:175]
v_mfma_f32_16x16x32_bf16 v[156:159], v[32:35], a[8:11], v[156:159]
v_mfma_f32_16x16x32_bf16 v[152:155], v[32:35], a[4:7], v[152:155]
ds_read_b64_tr_b16 v[20:21], v250 offset:14336
ds_read_b64_tr_b16 v[22:23], v250 offset:20480
v_mfma_f32_16x16x32_bf16 v[168:171], v[36:39], a[4:7], v[168:171]
v_mfma_f32_16x16x32_bf16 v[164:167], v[36:39], a[0:3], v[164:167]
v_mfma_f32_16x16x32_bf16 v[148:151], v[32:35], a[0:3], v[148:151]
s_waitcnt lgkmcnt(4)
v_mfma_f32_16x16x32_bf16 v[180:183], v[8:11], a[0:3], v[180:183]
ds_read_b64_tr_b16 v[24:25], v251 offset:8192
ds_read_b64_tr_b16 v[26:27], v251 offset:12288
v_mfma_f32_16x16x32_bf16 v[196:199], v[12:15], a[0:3], v[196:199]
v_mfma_f32_16x16x32_bf16 v[200:203], v[12:15], a[4:7], v[200:203]
v_mfma_f32_16x16x32_bf16 v[184:187], v[8:11], a[4:7], v[184:187]
v_mfma_f32_16x16x32_bf16 v[188:191], v[8:11], a[8:11], v[188:191]
ds_read_b64_tr_b16 v[28:29], v252 offset:8192
ds_read_b64_tr_b16 v[30:31], v252 offset:12288
v_mfma_f32_16x16x32_bf16 v[204:207], v[12:15], a[8:11], v[204:207]
v_mfma_f32_16x16x32_bf16 v[208:211], v[12:15], a[12:15], v[208:211]
buffer_load_dwordx4 v46, %[srdK], s76 offen offset:2048 lds
v_mfma_f32_16x16x32_bf16 v[192:195], v[8:11], a[12:15], v[192:195]
s_waitcnt lgkmcnt(4)
v_mfma_f32_16x16x32_bf16 v[224:227], v[16:19], a[12:15], v[224:227]
ds_read_b64_tr_b16 v[32:33], v253 offset:8192
ds_read_b64_tr_b16 v[34:35], v253 offset:12288
v_mfma_f32_16x16x32_bf16 v[240:243], v[20:23], a[12:15], v[240:243]
v_mfma_f32_16x16x32_bf16 v[236:239], v[20:23], a[8:11], v[236:239]
v_mfma_f32_16x16x32_bf16 v[220:223], v[16:19], a[8:11], v[220:223]
v_mfma_f32_16x16x32_bf16 v[216:219], v[16:19], a[4:7], v[216:219]
)"
R"(ds_read_b64_tr_b16 v[36:37], v254 offset:8192
ds_read_b64_tr_b16 v[38:39], v254 offset:12288
v_mfma_f32_16x16x32_bf16 v[232:235], v[20:23], a[4:7], v[232:235]
v_mfma_f32_16x16x32_bf16 v[228:231], v[20:23], a[0:3], v[228:231]
v_mfma_f32_16x16x32_bf16 v[212:215], v[16:19], a[0:3], v[212:215]
s_waitcnt lgkmcnt(4)
v_mfma_f32_16x16x32_bf16 a[96:99], v[24:27], a[0:3], a[96:99]
ds_read_b64_tr_b16 v[8:9], %[vaddr0] offset:12288
ds_read_b64_tr_b16 v[10:11], %[vaddr0] offset:18432
v_mfma_f32_16x16x32_bf16 a[112:115], v[28:31], a[0:3], a[112:115]
v_mfma_f32_16x16x32_bf16 a[116:119], v[28:31], a[4:7], a[116:119]
v_mfma_f32_16x16x32_bf16 a[100:103], v[24:27], a[4:7], a[100:103]
v_mfma_f32_16x16x32_bf16 a[104:107], v[24:27], a[8:11], a[104:107]
ds_read_b64_tr_b16 v[12:13], v41 offset:12288
ds_read_b64_tr_b16 v[14:15], v41 offset:18432
v_mfma_f32_16x16x32_bf16 a[120:123], v[28:31], a[8:11], a[120:123]
v_mfma_f32_16x16x32_bf16 a[124:127], v[28:31], a[12:15], a[124:127]
v_mfma_f32_16x16x32_bf16 a[108:111], v[24:27], a[12:15], a[108:111]
s_waitcnt lgkmcnt(4)
v_mfma_f32_16x16x32_bf16 a[132:135], v[32:35], a[4:7], a[132:135]
ds_read_b64_tr_b16 v[16:17], v42 offset:12288
ds_read_b64_tr_b16 v[18:19], v42 offset:18432
s_add_u32 m0, s81, 86016
v_mfma_f32_16x16x32_bf16 a[148:151], v[36:39], a[4:7], a[148:151]
buffer_load_dwordx4 v44, %[srdV], s76 offen offset:0 lds
v_mfma_f32_16x16x32_bf16 a[144:147], v[36:39], a[0:3], a[144:147]
ds_read_b64_tr_b16 v[20:21], v43 offset:12288
ds_read_b64_tr_b16 v[22:23], v43 offset:18432
v_mfma_f32_16x16x32_bf16 a[128:131], v[32:35], a[0:3], a[128:131]
s_waitcnt vmcnt(10) lgkmcnt(4)
v_mfma_f32_16x16x32_bf16 v[52:55], v[8:11], a[16:19], v[52:55]
ds_read_b64_tr_b16 v[24:25], %[vaddr0] offset:13312
ds_read_b64_tr_b16 v[26:27], %[vaddr0] offset:19456
v_mfma_f32_16x16x32_bf16 v[68:71], v[12:15], a[16:19], v[68:71]
s_waitcnt vmcnt(9)
v_mfma_f32_16x16x32_bf16 v[72:75], v[12:15], a[20:23], v[72:75]
v_mfma_f32_16x16x32_bf16 v[56:59], v[8:11], a[20:23], v[56:59]
s_waitcnt vmcnt(8)
v_mfma_f32_16x16x32_bf16 v[60:63], v[8:11], a[24:27], v[60:63]
ds_read_b64_tr_b16 v[28:29], v41 offset:13312
ds_read_b64_tr_b16 v[30:31], v41 offset:19456
v_mfma_f32_16x16x32_bf16 v[76:79], v[12:15], a[24:27], v[76:79]
s_waitcnt vmcnt(7)
v_mfma_f32_16x16x32_bf16 v[80:83], v[12:15], a[28:31], v[80:83]
v_mfma_f32_16x16x32_bf16 v[64:67], v[8:11], a[28:31], v[64:67]
s_waitcnt lgkmcnt(4)
v_mfma_f32_16x16x32_bf16 v[96:99], v[16:19], a[28:31], v[96:99]
ds_read_b64_tr_b16 v[32:33], v42 offset:13312
ds_read_b64_tr_b16 v[34:35], v42 offset:19456
v_mfma_f32_16x16x32_bf16 v[112:115], v[20:23], a[28:31], v[112:115]
v_mfma_f32_16x16x32_bf16 v[108:111], v[20:23], a[24:27], v[108:111]
v_mfma_f32_16x16x32_bf16 v[92:95], v[16:19], a[24:27], v[92:95]
v_mfma_f32_16x16x32_bf16 v[88:91], v[16:19], a[20:23], v[88:91]
ds_read_b64_tr_b16 v[36:37], v43 offset:13312
ds_read_b64_tr_b16 v[38:39], v43 offset:19456
v_mfma_f32_16x16x32_bf16 v[104:107], v[20:23], a[20:23], v[104:107]
v_mfma_f32_16x16x32_bf16 v[100:103], v[20:23], a[16:19], v[100:103]
v_mfma_f32_16x16x32_bf16 v[84:87], v[16:19], a[16:19], v[84:87]
buffer_load_dwordx4 v45, %[srdV], s76 offen offset:1024 lds
s_waitcnt lgkmcnt(4)
v_mfma_f32_16x16x32_bf16 v[116:119], v[24:27], a[16:19], v[116:119]
ds_read_b64_tr_b16 v[8:9], %[vaddr0] offset:14336
ds_read_b64_tr_b16 v[10:11], %[vaddr0] offset:20480
v_mfma_f32_16x16x32_bf16 v[132:135], v[28:31], a[16:19], v[132:135]
v_mfma_f32_16x16x32_bf16 v[136:139], v[28:31], a[20:23], v[136:139]
v_mfma_f32_16x16x32_bf16 v[120:123], v[24:27], a[20:23], v[120:123]
v_mfma_f32_16x16x32_bf16 v[124:127], v[24:27], a[24:27], v[124:127]
ds_read_b64_tr_b16 v[12:13], v41 offset:14336
ds_read_b64_tr_b16 v[14:15], v41 offset:20480
v_mfma_f32_16x16x32_bf16 v[140:143], v[28:31], a[24:27], v[140:143]
v_mfma_f32_16x16x32_bf16 v[144:147], v[28:31], a[28:31], v[144:147]
v_mfma_f32_16x16x32_bf16 v[128:131], v[24:27], a[28:31], v[128:131]
s_waitcnt lgkmcnt(4)
v_mfma_f32_16x16x32_bf16 v[160:163], v[32:35], a[28:31], v[160:163]
ds_read_b64_tr_b16 v[16:17], v42 offset:14336
ds_read_b64_tr_b16 v[18:19], v42 offset:20480
v_mfma_f32_16x16x32_bf16 v[176:179], v[36:39], a[28:31], v[176:179]
v_mfma_f32_16x16x32_bf16 v[172:175], v[36:39], a[24:27], v[172:175]
v_mfma_f32_16x16x32_bf16 v[156:159], v[32:35], a[24:27], v[156:159]
v_mfma_f32_16x16x32_bf16 v[152:155], v[32:35], a[20:23], v[152:155]
ds_read_b64_tr_b16 v[20:21], v43 offset:14336
ds_read_b64_tr_b16 v[22:23], v43 offset:20480
v_mfma_f32_16x16x32_bf16 v[168:171], v[36:39], a[20:23], v[168:171]
v_mfma_f32_16x16x32_bf16 v[164:167], v[36:39], a[16:19], v[164:167]
v_mfma_f32_16x16x32_bf16 v[148:151], v[32:35], a[16:19], v[148:151]
s_waitcnt lgkmcnt(4)
v_mfma_f32_16x16x32_bf16 v[180:183], v[8:11], a[16:19], v[180:183]
ds_read_b64_tr_b16 v[24:25], v251 offset:9216
ds_read_b64_tr_b16 v[26:27], v251 offset:13312
v_mfma_f32_16x16x32_bf16 v[196:199], v[12:15], a[16:19], v[196:199]
buffer_load_dwordx4 v46, %[srdV], s76 offen offset:2048 lds
v_mfma_f32_16x16x32_bf16 v[200:203], v[12:15], a[20:23], v[200:203]
v_mfma_f32_16x16x32_bf16 v[184:187], v[8:11], a[20:23], v[184:187]
v_mfma_f32_16x16x32_bf16 v[188:191], v[8:11], a[24:27], v[188:191]
ds_read_b64_tr_b16 v[28:29], v252 offset:9216
ds_read_b64_tr_b16 v[30:31], v252 offset:13312
v_mfma_f32_16x16x32_bf16 v[204:207], v[12:15], a[24:27], v[204:207]
v_mfma_f32_16x16x32_bf16 v[208:211], v[12:15], a[28:31], v[208:211]
v_mfma_f32_16x16x32_bf16 v[192:195], v[8:11], a[28:31], v[192:195]
s_waitcnt lgkmcnt(4)
v_mfma_f32_16x16x32_bf16 v[224:227], v[16:19], a[28:31], v[224:227]
ds_read_b64_tr_b16 v[32:33], v253 offset:9216
ds_read_b64_tr_b16 v[34:35], v253 offset:13312
v_mfma_f32_16x16x32_bf16 v[240:243], v[20:23], a[28:31], v[240:243]
v_mfma_f32_16x16x32_bf16 v[236:239], v[20:23], a[24:27], v[236:239]
v_mfma_f32_16x16x32_bf16 v[220:223], v[16:19], a[24:27], v[220:223]
v_mfma_f32_16x16x32_bf16 v[216:219], v[16:19], a[20:23], v[216:219]
ds_read_b64_tr_b16 v[36:37], v254 offset:9216
ds_read_b64_tr_b16 v[38:39], v254 offset:13312
v_mfma_f32_16x16x32_bf16 v[232:235], v[20:23], a[20:23], v[232:235]
v_mfma_f32_16x16x32_bf16 v[228:231], v[20:23], a[16:19], v[228:231]
v_mfma_f32_16x16x32_bf16 v[212:215], v[16:19], a[16:19], v[212:215]
s_waitcnt lgkmcnt(4)
v_mfma_f32_16x16x32_bf16 a[96:99], v[24:27], a[16:19], a[96:99]
s_waitcnt vmcnt(6)
s_barrier
ds_read_b128 v[8:11], %[kaddr0] offset:24576
v_mfma_f32_16x16x32_bf16 a[112:115], v[28:31], a[16:19], a[112:115]
v_mfma_f32_16x16x32_bf16 a[116:119], v[28:31], a[20:23], a[116:119]
v_mfma_f32_16x16x32_bf16 a[100:103], v[24:27], a[20:23], a[100:103]
s_add_u32 s76, s76, 12288
v_mfma_f32_16x16x32_bf16 a[104:107], v[24:27], a[24:27], a[104:107]
ds_read_b128 v[12:15], v40 offset:24576
v_mfma_f32_16x16x32_bf16 a[120:123], v[28:31], a[24:27], a[120:123]
v_mfma_f32_16x16x32_bf16 a[124:127], v[28:31], a[28:31], a[124:127]
v_mfma_f32_16x16x32_bf16 a[108:111], v[24:27], a[28:31], a[108:111]
s_waitcnt lgkmcnt(2)
v_mfma_f32_16x16x32_bf16 a[132:135], v[32:35], a[20:23], a[132:135]
ds_read_b128 v[16:19], %[kaddr0] offset:25600
v_mfma_f32_16x16x32_bf16 a[148:151], v[36:39], a[20:23], a[148:151]
v_mfma_f32_16x16x32_bf16 a[144:147], v[36:39], a[16:19], a[144:147]
ds_read_b128 v[20:23], v40 offset:25600
v_mfma_f32_16x16x32_bf16 a[128:131], v[32:35], a[16:19], a[128:131]
; ---- FFN1 epilogue + Q convert
s_xor_b32 s83, s90, 0
s_mul_i32 s83, s83, 49152
v_add_u32 v47, s83, %[lane16]
s_xor_b32 s83, s90, 1
s_mul_i32 s83, s83, 49152
v_add_u32 v48, s83, %[lane16]
s_xor_b32 s83, s90, 2
s_mul_i32 s83, s83, 49152
v_add_u32 v49, s83, %[lane16]
s_xor_b32 s83, s90, 3
s_mul_i32 s83, s83, 49152
v_add_u32 v50, s83, %[lane16]
s_mul_i32 s84, s91, 48
s_lshl_b32 s83, s86, 1
s_lshr_b32 s95, s87, 1
s_add_u32 s83, s83, s95
s_mul_i32 s83, s83, 3
s_add_u32 s84, s84, s83
s_lshl_b32 s84, s84, 10
s_mov_b64 s[50:51], %[ws]
s_add_u32 s50, s50, s84
s_addc_u32 s51, s51, 0
s_add_u32 s50, s50, 4194304
s_addc_u32 s51, s51, 0
s_lshl_b32 s83, s89, 11
s_add_u32 s48, s50, s83
s_addc_u32 s49, s51, 0
v_accvgpr_read_b32 v245, a160
v_accvgpr_read_b32 v247, a161
v_lshlrev_b32 v244, 16, v245
v_and_b32 v245, 0xffff0000, v245
v_lshlrev_b32 v246, 16, v247
v_and_b32 v247, 0xffff0000, v247
v_accvgpr_read_b32 v249, a162
v_accvgpr_read_b32 v251, a163
v_lshlrev_b32 v248, 16, v249
v_and_b32 v249, 0xffff0000, v249
v_lshlrev_b32 v250, 16, v251
v_and_b32 v251, 0xffff0000, v251
v_accvgpr_read_b32 v24, a96
v_accvgpr_read_b32 v25, a97
v_accvgpr_read_b32 v26, a98
v_accvgpr_read_b32 v27, a99
v_add_f32 v24, v24, v244
v_add_f32 v25, v25, v245
v_add_f32 v26, v26, v246
v_add_f32 v27, v27, v247
v_max_f32 v24, 0, v24
v_max_f32 v25, 0, v25
v_max_f32 v26, 0, v26
v_max_f32 v27, 0, v27
v_mul_f32 v24, v24, v24
v_mul_f32 v25, v25, v25
v_mul_f32 v26, v26, v26
v_mul_f32 v27, v27, v27
v_cvt_pk_bf16_f32 v28, v24, v25
v_cvt_pk_bf16_f32 v29, v26, v27
v_accvgpr_read_b32 v24, a112
v_accvgpr_read_b32 v25, a113
v_accvgpr_read_b32 v26, a114
v_accvgpr_read_b32 v27, a115
v_add_f32 v24, v24, v248
v_add_f32 v25, v25, v249
v_add_f32 v26, v26, v250
v_add_f32 v27, v27, v251
v_max_f32 v24, 0, v24
)"
R"(v_max_f32 v25, 0, v25
v_max_f32 v26, 0, v26
v_max_f32 v27, 0, v27
v_mul_f32 v24, v24, v24
v_mul_f32 v25, v25, v25
v_mul_f32 v26, v26, v26
v_mul_f32 v27, v27, v27
v_cvt_pk_bf16_f32 v30, v24, v25
v_cvt_pk_bf16_f32 v31, v26, v27
global_store_dwordx4 v47, v[28:31], s[48:49] offset:0 sc1
v_accvgpr_read_b32 v24, a100
v_accvgpr_read_b32 v25, a101
v_accvgpr_read_b32 v26, a102
v_accvgpr_read_b32 v27, a103
v_add_f32 v24, v24, v244
v_add_f32 v25, v25, v245
v_add_f32 v26, v26, v246
v_add_f32 v27, v27, v247
v_max_f32 v24, 0, v24
v_max_f32 v25, 0, v25
v_max_f32 v26, 0, v26
v_max_f32 v27, 0, v27
v_mul_f32 v24, v24, v24
v_mul_f32 v25, v25, v25
v_mul_f32 v26, v26, v26
v_mul_f32 v27, v27, v27
v_cvt_pk_bf16_f32 v32, v24, v25
v_cvt_pk_bf16_f32 v33, v26, v27
v_accvgpr_read_b32 v24, a116
v_accvgpr_read_b32 v25, a117
v_accvgpr_read_b32 v26, a118
v_accvgpr_read_b32 v27, a119
v_add_f32 v24, v24, v248
v_add_f32 v25, v25, v249
v_add_f32 v26, v26, v250
v_add_f32 v27, v27, v251
v_max_f32 v24, 0, v24
v_max_f32 v25, 0, v25
v_max_f32 v26, 0, v26
v_max_f32 v27, 0, v27
v_mul_f32 v24, v24, v24
v_mul_f32 v25, v25, v25
v_mul_f32 v26, v26, v26
v_mul_f32 v27, v27, v27
v_cvt_pk_bf16_f32 v34, v24, v25
v_cvt_pk_bf16_f32 v35, v26, v27
global_store_dwordx4 v48, v[32:35], s[48:49] offset:0 sc1
v_accvgpr_read_b32 v24, a104
v_accvgpr_read_b32 v25, a105
v_accvgpr_read_b32 v26, a106
v_accvgpr_read_b32 v27, a107
v_add_f32 v24, v24, v244
v_add_f32 v25, v25, v245
v_add_f32 v26, v26, v246
v_add_f32 v27, v27, v247
v_max_f32 v24, 0, v24
v_max_f32 v25, 0, v25
v_max_f32 v26, 0, v26
v_max_f32 v27, 0, v27
v_mul_f32 v24, v24, v24
v_mul_f32 v25, v25, v25
v_mul_f32 v26, v26, v26
v_mul_f32 v27, v27, v27
v_cvt_pk_bf16_f32 v28, v24, v25
v_cvt_pk_bf16_f32 v29, v26, v27
v_accvgpr_read_b32 v24, a120
v_accvgpr_read_b32 v25, a121
v_accvgpr_read_b32 v26, a122
v_accvgpr_read_b32 v27, a123
v_add_f32 v24, v24, v248
v_add_f32 v25, v25, v249
v_add_f32 v26, v26, v250
v_add_f32 v27, v27, v251
v_max_f32 v24, 0, v24
v_max_f32 v25, 0, v25
v_max_f32 v26, 0, v26
v_max_f32 v27, 0, v27
v_mul_f32 v24, v24, v24
v_mul_f32 v25, v25, v25
v_mul_f32 v26, v26, v26
v_mul_f32 v27, v27, v27
v_cvt_pk_bf16_f32 v30, v24, v25
v_cvt_pk_bf16_f32 v31, v26, v27
global_store_dwordx4 v49, v[28:31], s[48:49] offset:0 sc1
v_accvgpr_read_b32 v24, a108
v_accvgpr_read_b32 v25, a109
v_accvgpr_read_b32 v26, a110
v_accvgpr_read_b32 v27, a111
v_add_f32 v24, v24, v244
v_add_f32 v25, v25, v245
v_add_f32 v26, v26, v246
v_add_f32 v27, v27, v247
v_max_f32 v24, 0, v24
v_max_f32 v25, 0, v25
v_max_f32 v26, 0, v26
v_max_f32 v27, 0, v27
v_mul_f32 v24, v24, v24
v_mul_f32 v25, v25, v25
v_mul_f32 v26, v26, v26
v_mul_f32 v27, v27, v27
v_cvt_pk_bf16_f32 v32, v24, v25
v_cvt_pk_bf16_f32 v33, v26, v27
v_accvgpr_read_b32 v24, a124
v_accvgpr_read_b32 v25, a125
v_accvgpr_read_b32 v26, a126
v_accvgpr_read_b32 v27, a127
v_add_f32 v24, v24, v248
v_add_f32 v25, v25, v249
v_add_f32 v26, v26, v250
v_add_f32 v27, v27, v251
v_max_f32 v24, 0, v24
v_max_f32 v25, 0, v25
v_max_f32 v26, 0, v26
v_max_f32 v27, 0, v27
v_mul_f32 v24, v24, v24
v_mul_f32 v25, v25, v25
v_mul_f32 v26, v26, v26
v_mul_f32 v27, v27, v27
v_cvt_pk_bf16_f32 v34, v24, v25
v_cvt_pk_bf16_f32 v35, v26, v27
global_store_dwordx4 v50, v[32:35], s[48:49] offset:0 sc1
v_accvgpr_read_b32 v245, a164
v_accvgpr_read_b32 v247, a165
v_lshlrev_b32 v244, 16, v245
v_and_b32 v245, 0xffff0000, v245
v_lshlrev_b32 v246, 16, v247
v_and_b32 v247, 0xffff0000, v247
v_accvgpr_read_b32 v249, a166
v_accvgpr_read_b32 v251, a167
v_lshlrev_b32 v248, 16, v249
v_and_b32 v249, 0xffff0000, v249
v_lshlrev_b32 v250, 16, v251
v_and_b32 v251, 0xffff0000, v251
v_accvgpr_read_b32 v24, a128
v_accvgpr_read_b32 v25, a129
v_accvgpr_read_b32 v26, a130
v_accvgpr_read_b32 v27, a131
v_add_f32 v24, v24, v244
v_add_f32 v25, v25, v245
v_add_f32 v26, v26, v246
v_add_f32 v27, v27, v247
v_max_f32 v24, 0, v24
v_max_f32 v25, 0, v25
v_max_f32 v26, 0, v26
v_max_f32 v27, 0, v27
v_mul_f32 v24, v24, v24
v_mul_f32 v25, v25, v25
v_mul_f32 v26, v26, v26
v_mul_f32 v27, v27, v27
v_cvt_pk_bf16_f32 v28, v24, v25
v_cvt_pk_bf16_f32 v29, v26, v27
v_accvgpr_read_b32 v24, a144
v_accvgpr_read_b32 v25, a145
v_accvgpr_read_b32 v26, a146
v_accvgpr_read_b32 v27, a147
v_add_f32 v24, v24, v248
v_add_f32 v25, v25, v249
v_add_f32 v26, v26, v250
v_add_f32 v27, v27, v251
v_max_f32 v24, 0, v24
v_max_f32 v25, 0, v25
v_max_f32 v26, 0, v26
v_max_f32 v27, 0, v27
v_mul_f32 v24, v24, v24
v_mul_f32 v25, v25, v25
v_mul_f32 v26, v26, v26
v_mul_f32 v27, v27, v27
v_cvt_pk_bf16_f32 v30, v24, v25
v_cvt_pk_bf16_f32 v31, v26, v27
global_store_dwordx4 v47, v[28:31], s[50:51] offset:1024 sc1
v_accvgpr_read_b32 v24, a132
v_accvgpr_read_b32 v25, a133
v_accvgpr_read_b32 v26, a134
v_accvgpr_read_b32 v27, a135
v_add_f32 v24, v24, v244
v_add_f32 v25, v25, v245
v_add_f32 v26, v26, v246
v_add_f32 v27, v27, v247
v_max_f32 v24, 0, v24
v_max_f32 v25, 0, v25
v_max_f32 v26, 0, v26
v_max_f32 v27, 0, v27
v_mul_f32 v24, v24, v24
v_mul_f32 v25, v25, v25
v_mul_f32 v26, v26, v26
v_mul_f32 v27, v27, v27
v_cvt_pk_bf16_f32 v32, v24, v25
v_cvt_pk_bf16_f32 v33, v26, v27
v_accvgpr_read_b32 v24, a148
v_accvgpr_read_b32 v25, a149
v_accvgpr_read_b32 v26, a150
v_accvgpr_read_b32 v27, a151
v_add_f32 v24, v24, v248
v_add_f32 v25, v25, v249
v_add_f32 v26, v26, v250
v_add_f32 v27, v27, v251
v_max_f32 v24, 0, v24
v_max_f32 v25, 0, v25
v_max_f32 v26, 0, v26
v_max_f32 v27, 0, v27
)"
R"(v_mul_f32 v24, v24, v24
v_mul_f32 v25, v25, v25
v_mul_f32 v26, v26, v26
v_mul_f32 v27, v27, v27
v_cvt_pk_bf16_f32 v34, v24, v25
v_cvt_pk_bf16_f32 v35, v26, v27
global_store_dwordx4 v48, v[32:35], s[50:51] offset:1024 sc1
v_pk_mul_f32 v[52:53], v[52:53], s[52:53]
v_pk_mul_f32 v[54:55], v[54:55], s[52:53]
v_pk_mul_f32 v[68:69], v[68:69], s[52:53]
v_pk_mul_f32 v[70:71], v[70:71], s[52:53]
v_cvt_pk_bf16_f32 v52, v52, v53
v_cvt_pk_bf16_f32 v53, v54, v55
v_cvt_pk_bf16_f32 v54, v68, v69
v_cvt_pk_bf16_f32 v55, v70, v71
v_pk_mul_f32 v[56:57], v[56:57], s[52:53]
v_pk_mul_f32 v[58:59], v[58:59], s[52:53]
v_pk_mul_f32 v[72:73], v[72:73], s[52:53]
v_pk_mul_f32 v[74:75], v[74:75], s[52:53]
v_cvt_pk_bf16_f32 v56, v56, v57
v_cvt_pk_bf16_f32 v57, v58, v59
v_cvt_pk_bf16_f32 v58, v72, v73
v_cvt_pk_bf16_f32 v59, v74, v75
v_pk_mul_f32 v[60:61], v[60:61], s[52:53]
v_pk_mul_f32 v[62:63], v[62:63], s[52:53]
v_pk_mul_f32 v[76:77], v[76:77], s[52:53]
v_pk_mul_f32 v[78:79], v[78:79], s[52:53]
v_cvt_pk_bf16_f32 v60, v60, v61
v_cvt_pk_bf16_f32 v61, v62, v63
v_cvt_pk_bf16_f32 v62, v76, v77
v_cvt_pk_bf16_f32 v63, v78, v79
v_pk_mul_f32 v[64:65], v[64:65], s[52:53]
v_pk_mul_f32 v[66:67], v[66:67], s[52:53]
v_pk_mul_f32 v[80:81], v[80:81], s[52:53]
v_pk_mul_f32 v[82:83], v[82:83], s[52:53]
v_cvt_pk_bf16_f32 v64, v64, v65
v_cvt_pk_bf16_f32 v65, v66, v67
v_cvt_pk_bf16_f32 v66, v80, v81
v_cvt_pk_bf16_f32 v67, v82, v83
v_permlane32_swap_b32 v52, v54
v_permlane32_swap_b32 v53, v55
v_permlane32_swap_b32 v56, v58
v_permlane32_swap_b32 v57, v59
v_permlane32_swap_b32 v60, v62
v_permlane32_swap_b32 v61, v63
v_permlane32_swap_b32 v64, v66
v_permlane32_swap_b32 v65, v67
v_permlane16_swap_b32 v52, v54
v_permlane16_swap_b32 v53, v55
v_permlane16_swap_b32 v56, v58
v_permlane16_swap_b32 v57, v59
v_permlane16_swap_b32 v60, v62
v_permlane16_swap_b32 v61, v63
v_permlane16_swap_b32 v64, v66
v_permlane16_swap_b32 v65, v67
v_accvgpr_write_b32 a192, v52
v_accvgpr_write_b32 a193, v53
v_accvgpr_write_b32 a194, v54
v_accvgpr_write_b32 a195, v55
v_accvgpr_write_b32 a196, v56
v_accvgpr_write_b32 a197, v57
v_accvgpr_write_b32 a198, v58
v_accvgpr_write_b32 a199, v59
v_accvgpr_write_b32 a200, v60
v_accvgpr_write_b32 a201, v61
v_accvgpr_write_b32 a202, v62
v_accvgpr_write_b32 a203, v63
v_accvgpr_write_b32 a204, v64
v_accvgpr_write_b32 a205, v65
v_accvgpr_write_b32 a206, v66
v_accvgpr_write_b32 a207, v67
v_pk_mul_f32 v[84:85], v[84:85], s[52:53]
v_pk_mul_f32 v[86:87], v[86:87], s[52:53]
v_pk_mul_f32 v[100:101], v[100:101], s[52:53]
v_pk_mul_f32 v[102:103], v[102:103], s[52:53]
v_cvt_pk_bf16_f32 v84, v84, v85
v_cvt_pk_bf16_f32 v85, v86, v87
v_cvt_pk_bf16_f32 v86, v100, v101
v_cvt_pk_bf16_f32 v87, v102, v103
v_pk_mul_f32 v[88:89], v[88:89], s[52:53]
v_pk_mul_f32 v[90:91], v[90:91], s[52:53]
v_pk_mul_f32 v[104:105], v[104:105], s[52:53]
v_pk_mul_f32 v[106:107], v[106:107], s[52:53]
v_cvt_pk_bf16_f32 v88, v88, v89
v_cvt_pk_bf16_f32 v89, v90, v91
v_cvt_pk_bf16_f32 v90, v104, v105
v_cvt_pk_bf16_f32 v91, v106, v107
v_pk_mul_f32 v[92:93], v[92:93], s[52:53]
v_pk_mul_f32 v[94:95], v[94:95], s[52:53]
v_pk_mul_f32 v[108:109], v[108:109], s[52:53]
v_pk_mul_f32 v[110:111], v[110:111], s[52:53]
v_cvt_pk_bf16_f32 v92, v92, v93
v_cvt_pk_bf16_f32 v93, v94, v95
v_cvt_pk_bf16_f32 v94, v108, v109
v_cvt_pk_bf16_f32 v95, v110, v111
v_pk_mul_f32 v[96:97], v[96:97], s[52:53]
v_pk_mul_f32 v[98:99], v[98:99], s[52:53]
v_pk_mul_f32 v[112:113], v[112:113], s[52:53]
v_pk_mul_f32 v[114:115], v[114:115], s[52:53]
v_cvt_pk_bf16_f32 v96, v96, v97
v_cvt_pk_bf16_f32 v97, v98, v99
v_cvt_pk_bf16_f32 v98, v112, v113
v_cvt_pk_bf16_f32 v99, v114, v115
v_permlane32_swap_b32 v84, v86
v_permlane32_swap_b32 v85, v87
v_permlane32_swap_b32 v88, v90
v_permlane32_swap_b32 v89, v91
v_permlane32_swap_b32 v92, v94
v_permlane32_swap_b32 v93, v95
v_permlane32_swap_b32 v96, v98
v_permlane32_swap_b32 v97, v99
v_permlane16_swap_b32 v84, v86
v_permlane16_swap_b32 v85, v87
v_permlane16_swap_b32 v88, v90
v_permlane16_swap_b32 v89, v91
v_permlane16_swap_b32 v92, v94
v_permlane16_swap_b32 v93, v95
v_permlane16_swap_b32 v96, v98
v_permlane16_swap_b32 v97, v99
v_accvgpr_write_b32 a208, v84
v_accvgpr_write_b32 a209, v85
v_accvgpr_write_b32 a210, v86
v_accvgpr_write_b32 a211, v87
v_accvgpr_write_b32 a212, v88
v_accvgpr_write_b32 a213, v89
v_accvgpr_write_b32 a214, v90
v_accvgpr_write_b32 a215, v91
v_accvgpr_write_b32 a216, v92
v_accvgpr_write_b32 a217, v93
v_accvgpr_write_b32 a218, v94
v_accvgpr_write_b32 a219, v95
v_accvgpr_write_b32 a220, v96
v_accvgpr_write_b32 a221, v97
v_accvgpr_write_b32 a222, v98
v_accvgpr_write_b32 a223, v99
v_pk_mul_f32 v[116:117], v[116:117], s[52:53]
v_pk_mul_f32 v[118:119], v[118:119], s[52:53]
v_pk_mul_f32 v[132:133], v[132:133], s[52:53]
v_pk_mul_f32 v[134:135], v[134:135], s[52:53]
v_cvt_pk_bf16_f32 v116, v116, v117
v_cvt_pk_bf16_f32 v117, v118, v119
v_cvt_pk_bf16_f32 v118, v132, v133
v_cvt_pk_bf16_f32 v119, v134, v135
v_pk_mul_f32 v[120:121], v[120:121], s[52:53]
v_pk_mul_f32 v[122:123], v[122:123], s[52:53]
v_pk_mul_f32 v[136:137], v[136:137], s[52:53]
v_pk_mul_f32 v[138:139], v[138:139], s[52:53]
v_cvt_pk_bf16_f32 v120, v120, v121
v_cvt_pk_bf16_f32 v121, v122, v123
v_cvt_pk_bf16_f32 v122, v136, v137
v_cvt_pk_bf16_f32 v123, v138, v139
v_pk_mul_f32 v[124:125], v[124:125], s[52:53]
v_pk_mul_f32 v[126:127], v[126:127], s[52:53]
v_pk_mul_f32 v[140:141], v[140:141], s[52:53]
v_pk_mul_f32 v[142:143], v[142:143], s[52:53]
v_cvt_pk_bf16_f32 v124, v124, v125
v_cvt_pk_bf16_f32 v125, v126, v127
v_cvt_pk_bf16_f32 v126, v140, v141
v_cvt_pk_bf16_f32 v127, v142, v143
v_pk_mul_f32 v[128:129], v[128:129], s[52:53]
v_pk_mul_f32 v[130:131], v[130:131], s[52:53]
v_pk_mul_f32 v[144:145], v[144:145], s[52:53]
v_pk_mul_f32 v[146:147], v[146:147], s[52:53]
v_cvt_pk_bf16_f32 v128, v128, v129
v_cvt_pk_bf16_f32 v129, v130, v131
v_cvt_pk_bf16_f32 v130, v144, v145
v_cvt_pk_bf16_f32 v131, v146, v147
v_permlane32_swap_b32 v116, v118
v_permlane32_swap_b32 v117, v119
v_permlane32_swap_b32 v120, v122
v_permlane32_swap_b32 v121, v123
v_permlane32_swap_b32 v124, v126
v_permlane32_swap_b32 v125, v127
v_permlane32_swap_b32 v128, v130
v_permlane32_swap_b32 v129, v131
v_permlane16_swap_b32 v116, v118
v_permlane16_swap_b32 v117, v119
v_permlane16_swap_b32 v120, v122
v_permlane16_swap_b32 v121, v123
v_permlane16_swap_b32 v124, v126
v_permlane16_swap_b32 v125, v127
v_permlane16_swap_b32 v128, v130
v_permlane16_swap_b32 v129, v131
v_accvgpr_write_b32 a224, v116
v_accvgpr_write_b32 a225, v117
v_accvgpr_write_b32 a226, v118
v_accvgpr_write_b32 a227, v119
v_accvgpr_write_b32 a228, v120
v_accvgpr_write_b32 a229, v121
v_accvgpr_write_b32 a230, v122
v_accvgpr_write_b32 a231, v123
v_accvgpr_write_b32 a232, v124
v_accvgpr_write_b32 a233, v125
v_accvgpr_write_b32 a234, v126
v_accvgpr_write_b32 a235, v127
v_accvgpr_write_b32 a236, v128
v_accvgpr_write_b32 a237, v129
v_accvgpr_write_b32 a238, v130
v_accvgpr_write_b32 a239, v131
v_pk_mul_f32 v[148:149], v[148:149], s[52:53]
)"
R"(v_pk_mul_f32 v[150:151], v[150:151], s[52:53]
v_pk_mul_f32 v[164:165], v[164:165], s[52:53]
v_pk_mul_f32 v[166:167], v[166:167], s[52:53]
v_cvt_pk_bf16_f32 v148, v148, v149
v_cvt_pk_bf16_f32 v149, v150, v151
v_cvt_pk_bf16_f32 v150, v164, v165
v_cvt_pk_bf16_f32 v151, v166, v167
v_pk_mul_f32 v[152:153], v[152:153], s[52:53]
v_pk_mul_f32 v[154:155], v[154:155], s[52:53]
v_pk_mul_f32 v[168:169], v[168:169], s[52:53]
v_pk_mul_f32 v[170:171], v[170:171], s[52:53]
v_cvt_pk_bf16_f32 v152, v152, v153
v_cvt_pk_bf16_f32 v153, v154, v155
v_cvt_pk_bf16_f32 v154, v168, v169
v_cvt_pk_bf16_f32 v155, v170, v171
v_pk_mul_f32 v[156:157], v[156:157], s[52:53]
v_pk_mul_f32 v[158:159], v[158:159], s[52:53]
v_pk_mul_f32 v[172:173], v[172:173], s[52:53]
v_pk_mul_f32 v[174:175], v[174:175], s[52:53]
v_cvt_pk_bf16_f32 v156, v156, v157
v_cvt_pk_bf16_f32 v157, v158, v159
v_cvt_pk_bf16_f32 v158, v172, v173
v_cvt_pk_bf16_f32 v159, v174, v175
v_pk_mul_f32 v[160:161], v[160:161], s[52:53]
v_pk_mul_f32 v[162:163], v[162:163], s[52:53]
v_pk_mul_f32 v[176:177], v[176:177], s[52:53]
v_pk_mul_f32 v[178:179], v[178:179], s[52:53]
v_cvt_pk_bf16_f32 v160, v160, v161
v_cvt_pk_bf16_f32 v161, v162, v163
v_cvt_pk_bf16_f32 v162, v176, v177
v_cvt_pk_bf16_f32 v163, v178, v179
v_permlane32_swap_b32 v148, v150
v_permlane32_swap_b32 v149, v151
v_permlane32_swap_b32 v152, v154
v_permlane32_swap_b32 v153, v155
v_permlane32_swap_b32 v156, v158
v_permlane32_swap_b32 v157, v159
v_permlane32_swap_b32 v160, v162
v_permlane32_swap_b32 v161, v163
v_permlane16_swap_b32 v148, v150
v_permlane16_swap_b32 v149, v151
v_permlane16_swap_b32 v152, v154
v_permlane16_swap_b32 v153, v155
v_permlane16_swap_b32 v156, v158
v_permlane16_swap_b32 v157, v159
v_permlane16_swap_b32 v160, v162
v_permlane16_swap_b32 v161, v163
v_accvgpr_write_b32 a240, v148
v_accvgpr_write_b32 a241, v149
v_accvgpr_write_b32 a242, v150
v_accvgpr_write_b32 a243, v151
v_accvgpr_write_b32 a244, v152
v_accvgpr_write_b32 a245, v153
v_accvgpr_write_b32 a246, v154
v_accvgpr_write_b32 a247, v155
v_accvgpr_write_b32 a248, v156
v_accvgpr_write_b32 a249, v157
v_accvgpr_write_b32 a250, v158
v_accvgpr_write_b32 a251, v159
v_accvgpr_write_b32 a252, v160
v_accvgpr_write_b32 a253, v161
v_accvgpr_write_b32 a254, v162
v_accvgpr_write_b32 a255, v163
v_pk_mul_f32 v[180:181], v[180:181], s[52:53]
v_pk_mul_f32 v[182:183], v[182:183], s[52:53]
v_pk_mul_f32 v[196:197], v[196:197], s[52:53]
v_pk_mul_f32 v[198:199], v[198:199], s[52:53]
v_cvt_pk_bf16_f32 v52, v180, v181
v_cvt_pk_bf16_f32 v53, v182, v183
v_cvt_pk_bf16_f32 v54, v196, v197
v_cvt_pk_bf16_f32 v55, v198, v199
v_pk_mul_f32 v[184:185], v[184:185], s[52:53]
v_pk_mul_f32 v[186:187], v[186:187], s[52:53]
v_pk_mul_f32 v[200:201], v[200:201], s[52:53]
v_pk_mul_f32 v[202:203], v[202:203], s[52:53]
v_cvt_pk_bf16_f32 v56, v184, v185
v_cvt_pk_bf16_f32 v57, v186, v187
v_cvt_pk_bf16_f32 v58, v200, v201
v_cvt_pk_bf16_f32 v59, v202, v203
v_pk_mul_f32 v[188:189], v[188:189], s[52:53]
v_pk_mul_f32 v[190:191], v[190:191], s[52:53]
v_pk_mul_f32 v[204:205], v[204:205], s[52:53]
v_pk_mul_f32 v[206:207], v[206:207], s[52:53]
v_cvt_pk_bf16_f32 v60, v188, v189
v_cvt_pk_bf16_f32 v61, v190, v191
v_cvt_pk_bf16_f32 v62, v204, v205
v_cvt_pk_bf16_f32 v63, v206, v207
v_pk_mul_f32 v[192:193], v[192:193], s[52:53]
v_pk_mul_f32 v[194:195], v[194:195], s[52:53]
v_pk_mul_f32 v[208:209], v[208:209], s[52:53]
v_pk_mul_f32 v[210:211], v[210:211], s[52:53]
v_cvt_pk_bf16_f32 v64, v192, v193
v_cvt_pk_bf16_f32 v65, v194, v195
v_cvt_pk_bf16_f32 v66, v208, v209
v_cvt_pk_bf16_f32 v67, v210, v211
v_permlane32_swap_b32 v52, v54
v_permlane32_swap_b32 v53, v55
v_permlane32_swap_b32 v56, v58
v_permlane32_swap_b32 v57, v59
v_permlane32_swap_b32 v60, v62
v_permlane32_swap_b32 v61, v63
v_permlane32_swap_b32 v64, v66
v_permlane32_swap_b32 v65, v67
v_permlane16_swap_b32 v52, v54
v_permlane16_swap_b32 v53, v55
v_permlane16_swap_b32 v56, v58
v_permlane16_swap_b32 v57, v59
v_permlane16_swap_b32 v60, v62
v_permlane16_swap_b32 v61, v63
v_permlane16_swap_b32 v64, v66
v_permlane16_swap_b32 v65, v67
v_pk_mul_f32 v[212:213], v[212:213], s[52:53]
v_pk_mul_f32 v[214:215], v[214:215], s[52:53]
v_pk_mul_f32 v[228:229], v[228:229], s[52:53]
v_pk_mul_f32 v[230:231], v[230:231], s[52:53]
v_cvt_pk_bf16_f32 v68, v212, v213
v_cvt_pk_bf16_f32 v69, v214, v215
v_cvt_pk_bf16_f32 v70, v228, v229
v_cvt_pk_bf16_f32 v71, v230, v231
v_pk_mul_f32 v[216:217], v[216:217], s[52:53]
v_pk_mul_f32 v[218:219], v[218:219], s[52:53]
v_pk_mul_f32 v[232:233], v[232:233], s[52:53]
v_pk_mul_f32 v[234:235], v[234:235], s[52:53]
v_cvt_pk_bf16_f32 v72, v216, v217
v_cvt_pk_bf16_f32 v73, v218, v219
v_cvt_pk_bf16_f32 v74, v232, v233
v_cvt_pk_bf16_f32 v75, v234, v235
v_pk_mul_f32 v[220:221], v[220:221], s[52:53]
v_pk_mul_f32 v[222:223], v[222:223], s[52:53]
v_pk_mul_f32 v[236:237], v[236:237], s[52:53]
v_pk_mul_f32 v[238:239], v[238:239], s[52:53]
v_cvt_pk_bf16_f32 v76, v220, v221
v_cvt_pk_bf16_f32 v77, v222, v223
v_cvt_pk_bf16_f32 v78, v236, v237
v_cvt_pk_bf16_f32 v79, v238, v239
v_pk_mul_f32 v[224:225], v[224:225], s[52:53]
v_pk_mul_f32 v[226:227], v[226:227], s[52:53]
v_pk_mul_f32 v[240:241], v[240:241], s[52:53]
v_pk_mul_f32 v[242:243], v[242:243], s[52:53]
v_cvt_pk_bf16_f32 v80, v224, v225
v_cvt_pk_bf16_f32 v81, v226, v227
v_cvt_pk_bf16_f32 v82, v240, v241
v_cvt_pk_bf16_f32 v83, v242, v243
v_permlane32_swap_b32 v68, v70
v_permlane32_swap_b32 v69, v71
v_permlane32_swap_b32 v72, v74
v_permlane32_swap_b32 v73, v75
v_permlane32_swap_b32 v76, v78
v_permlane32_swap_b32 v77, v79
v_permlane32_swap_b32 v80, v82
v_permlane32_swap_b32 v81, v83
v_permlane16_swap_b32 v68, v70
v_permlane16_swap_b32 v69, v71
v_permlane16_swap_b32 v72, v74
v_permlane16_swap_b32 v73, v75
v_permlane16_swap_b32 v76, v78
v_permlane16_swap_b32 v77, v79
v_permlane16_swap_b32 v80, v82
v_permlane16_swap_b32 v81, v83
v_bfe_u32 v251, %[lane16], 4, 5
v_lshlrev_b32 v251, 2, v251
v_mov_b32 v252, s96
.Lqk0:
v_mov_b32 v47, 0
v_mov_b32 v48, 0
v_mov_b32 v49, 0
v_mov_b32 v50, 0
s_waitcnt lgkmcnt(1)
v_mfma_f32_16x16x32_bf16 v[84:87], v[8:11], a[192:195], 0
ds_read_b128 v[24:27], %[kaddr0] offset:26624
v_mfma_f32_16x16x32_bf16 v[84:87], v[12:15], a[208:211], v[84:87]
s_add_u32 m0, s81, 0
v_mfma_f32_16x16x32_bf16 v[84:87], v[16:19], a[224:227], v[84:87]
buffer_load_dwordx4 v44, %[srdK], s76 offen offset:0 lds
v_mfma_f32_16x16x32_bf16 v[88:91], v[16:19], a[228:231], 0
v_mfma_f32_16x16x32_bf16 v[88:91], v[12:15], a[212:215], v[88:91]
ds_read_b128 v[28:31], v40 offset:26624
v_mfma_f32_16x16x32_bf16 v[88:91], v[8:11], a[196:199], v[88:91]
v_mfma_f32_16x16x32_bf16 v[92:95], v[8:11], a[200:203], 0
v_mfma_f32_16x16x32_bf16 v[92:95], v[12:15], a[216:219], v[92:95]
v_mfma_f32_16x16x32_bf16 v[92:95], v[16:19], a[232:235], v[92:95]
ds_read_b128 v[32:35], %[kaddr0] offset:30720
buffer_load_dwordx4 v45, %[srdK], s76 offen offset:1024 lds
v_mfma_f32_16x16x32_bf16 v[96:99], v[16:19], a[236:239], 0
v_mfma_f32_16x16x32_bf16 v[96:99], v[12:15], a[220:223], v[96:99]
v_mfma_f32_16x16x32_bf16 v[96:99], v[8:11], a[204:207], v[96:99]
s_waitcnt lgkmcnt(1)
v_mfma_f32_16x16x32_bf16 v[96:99], v[20:23], a[252:255], v[96:99]
ds_read_b128 v[36:39], v40 offset:30720
v_mfma_f32_16x16x32_bf16 v[96:99], v[24:27], v[64:67], v[96:99]
v_mfma_f32_16x16x32_bf16 v[96:99], v[28:31], v[80:83], v[96:99]
buffer_load_dwordx4 v46, %[srdK], s76 offen offset:2048 lds
v_mfma_f32_16x16x32_bf16 v[92:95], v[28:31], v[76:79], v[92:95]
v_mfma_f32_16x16x32_bf16 v[92:95], v[24:27], v[60:63], v[92:95]
ds_read_b128 v[8:11], %[kaddr0] offset:31744
v_mfma_f32_16x16x32_bf16 v[92:95], v[20:23], a[248:251], v[92:95]
v_mfma_f32_16x16x32_bf16 v[88:91], v[20:23], a[244:247], v[88:91]
v_mfma_f32_16x16x32_bf16 v[88:91], v[24:27], v[56:59], v[88:91]
v_mfma_f32_16x16x32_bf16 v[88:91], v[28:31], v[72:75], v[88:91]
ds_read_b128 v[12:15], v40 offset:31744
)"
R"(s_add_u32 m0, s81, 49152
v_mfma_f32_16x16x32_bf16 v[84:87], v[28:31], v[68:71], v[84:87]
buffer_load_dwordx4 v44, %[srdV], s76 offen offset:0 lds
v_mfma_f32_16x16x32_bf16 v[84:87], v[24:27], v[52:55], v[84:87]
v_mfma_f32_16x16x32_bf16 v[84:87], v[20:23], a[240:243], v[84:87]
s_waitcnt lgkmcnt(1)
v_mfma_f32_16x16x32_bf16 v[100:103], v[32:35], a[192:195], 0
ds_read_b128 v[16:19], %[kaddr0] offset:32768
v_mfma_f32_16x16x32_bf16 v[100:103], v[36:39], a[208:211], v[100:103]
v_mfma_f32_16x16x32_bf16 v[100:103], v[8:11], a[224:227], v[100:103]
v_mfma_f32_16x16x32_bf16 v[104:107], v[8:11], a[228:231], 0
buffer_load_dwordx4 v45, %[srdV], s76 offen offset:1024 lds
v_mfma_f32_16x16x32_bf16 v[104:107], v[36:39], a[212:215], v[104:107]
ds_read_b128 v[20:23], v40 offset:32768
v_mfma_f32_16x16x32_bf16 v[104:107], v[32:35], a[196:199], v[104:107]
v_mfma_f32_16x16x32_bf16 v[108:111], v[32:35], a[200:203], 0
v_mfma_f32_16x16x32_bf16 v[108:111], v[36:39], a[216:219], v[108:111]
v_mfma_f32_16x16x32_bf16 v[108:111], v[8:11], a[232:235], v[108:111]
s_waitcnt vmcnt(5) lgkmcnt(0)
s_barrier
ds_read_b128 v[24:27], %[kaddr0] offset:36864
v_mfma_f32_16x16x32_bf16 v[112:115], v[8:11], a[236:239], 0
buffer_load_dwordx4 v46, %[srdV], s76 offen offset:2048 lds
v_mfma_f32_16x16x32_bf16 v[112:115], v[36:39], a[220:223], v[112:115]
v_mfma_f32_16x16x32_bf16 v[112:115], v[32:35], a[204:207], v[112:115]
v_mfma_f32_16x16x32_bf16 v[112:115], v[12:15], a[252:255], v[112:115]
ds_read_b128 v[28:31], v40 offset:36864
v_mfma_f32_16x16x32_bf16 v[112:115], v[16:19], v[64:67], v[112:115]
v_mfma_f32_16x16x32_bf16 v[112:115], v[20:23], v[80:83], v[112:115]
v_mfma_f32_16x16x32_bf16 v[108:111], v[20:23], v[76:79], v[108:111]
s_add_u32 s76, s76, 12288
v_mfma_f32_16x16x32_bf16 v[108:111], v[16:19], v[60:63], v[108:111]
ds_read_b128 v[32:35], %[kaddr0] offset:37888
v_mfma_f32_16x16x32_bf16 v[108:111], v[12:15], a[248:251], v[108:111]
v_mfma_f32_16x16x32_bf16 v[104:107], v[12:15], a[244:247], v[104:107]
v_mfma_f32_16x16x32_bf16 v[104:107], v[16:19], v[56:59], v[104:107]
v_mfma_f32_16x16x32_bf16 v[104:107], v[20:23], v[72:75], v[104:107]
ds_read_b128 v[36:39], v40 offset:37888
v_mfma_f32_16x16x32_bf16 v[100:103], v[20:23], v[68:71], v[100:103]
v_mfma_f32_16x16x32_bf16 v[100:103], v[16:19], v[52:55], v[100:103]
v_mfma_f32_16x16x32_bf16 v[100:103], v[12:15], a[240:243], v[100:103]
s_cmp_lg_u32 s85, 0
s_cbranch_scc1 .Lhave_ref
v_max3_f32 v198, v96, v97, v98
v_max3_f32 v198, v198, v99, v112
v_max3_f32 v198, v198, v113, v114
v_max_f32 v198, v198, v115
v_mov_b32 v199, v198
s_nop 1
v_permlane32_swap_b32 v198, v199
v_max_f32 v198, v198, v199
v_mov_b32 v199, v198
s_nop 1
v_permlane16_swap_b32 v198, v199
v_max_f32 v198, v198, v199
v_sub_f32 v192, 0, v198
v_sub_f32 v193, 0, v198
v_sub_f32 v194, 0, v198
v_sub_f32 v195, 0, v198
v_max3_f32 v196, v92, v93, v94
v_max3_f32 v196, v196, v95, v108
v_max3_f32 v196, v196, v109, v110
v_max_f32 v196, v196, v111
v_mov_b32 v197, v196
s_nop 1
v_permlane32_swap_b32 v196, v197
v_max_f32 v196, v196, v197
v_mov_b32 v197, v196
s_nop 1
v_permlane16_swap_b32 v196, v197
v_max_f32 v196, v196, v197
v_sub_f32 v188, 0, v196
v_sub_f32 v189, 0, v196
v_sub_f32 v190, 0, v196
v_sub_f32 v191, 0, v196
v_max3_f32 v198, v88, v89, v90
v_max3_f32 v198, v198, v91, v104
v_max3_f32 v198, v198, v105, v106
v_max_f32 v198, v198, v107
v_mov_b32 v199, v198
s_nop 1
v_permlane32_swap_b32 v198, v199
v_max_f32 v198, v198, v199
v_mov_b32 v199, v198
s_nop 1
v_permlane16_swap_b32 v198, v199
v_max_f32 v198, v198, v199
v_sub_f32 v184, 0, v198
v_sub_f32 v185, 0, v198
v_sub_f32 v186, 0, v198
v_sub_f32 v187, 0, v198
v_max3_f32 v196, v84, v85, v86
v_max3_f32 v196, v196, v87, v100
v_max3_f32 v196, v196, v101, v102
v_max_f32 v196, v196, v103
v_mov_b32 v197, v196
s_nop 1
v_permlane32_swap_b32 v196, v197
v_max_f32 v196, v196, v197
v_mov_b32 v197, v196
s_nop 1
v_permlane16_swap_b32 v196, v197
v_max_f32 v196, v196, v197
v_sub_f32 v180, 0, v196
v_sub_f32 v181, 0, v196
v_sub_f32 v182, 0, v196
v_sub_f32 v183, 0, v196
.Lhave_ref:
v_add_f32 v96, v96, v192
v_add_f32 v97, v97, v193
v_add_f32 v98, v98, v194
v_add_f32 v99, v99, v195
v_add_f32 v112, v112, v192
v_add_f32 v113, v113, v193
v_add_f32 v114, v114, v194
v_add_f32 v115, v115, v195
v_add_f32 v92, v92, v188
v_add_f32 v93, v93, v189
v_add_f32 v94, v94, v190
v_add_f32 v95, v95, v191
v_add_f32 v108, v108, v188
v_add_f32 v109, v109, v189
v_add_f32 v110, v110, v190
v_add_f32 v111, v111, v191
v_add_f32 v88, v88, v184
v_add_f32 v89, v89, v185
v_add_f32 v90, v90, v186
v_add_f32 v91, v91, v187
v_add_f32 v104, v104, v184
v_add_f32 v105, v105, v185
v_add_f32 v106, v106, v186
v_add_f32 v107, v107, v187
v_add_f32 v84, v84, v180
v_add_f32 v85, v85, v181
v_add_f32 v86, v86, v182
v_add_f32 v87, v87, v183
v_add_f32 v100, v100, v180
v_add_f32 v101, v101, v181
v_add_f32 v102, v102, v182
v_add_f32 v103, v103, v183
v_exp_f32 v96, v96
v_exp_f32 v97, v97
v_exp_f32 v98, v98
v_exp_f32 v99, v99
v_add_f32 v50, v50, v96
v_add_f32 v50, v50, v97
v_add_f32 v50, v50, v98
v_add_f32 v50, v50, v99
v_cvt_pk_bf16_f32 v160, v96, v97
v_cvt_pk_bf16_f32 v161, v98, v99
v_exp_f32 v112, v112
v_exp_f32 v113, v113
v_exp_f32 v114, v114
v_exp_f32 v115, v115
v_add_f32 v50, v50, v112
v_add_f32 v50, v50, v113
v_add_f32 v50, v50, v114
v_add_f32 v50, v50, v115
v_cvt_pk_bf16_f32 v162, v112, v113
v_cvt_pk_bf16_f32 v163, v114, v115
v_exp_f32 v92, v92
v_exp_f32 v93, v93
v_exp_f32 v94, v94
v_exp_f32 v95, v95
v_add_f32 v49, v49, v92
v_add_f32 v49, v49, v93
v_add_f32 v49, v49, v94
v_add_f32 v49, v49, v95
v_cvt_pk_bf16_f32 v156, v92, v93
v_cvt_pk_bf16_f32 v157, v94, v95
v_exp_f32 v108, v108
v_exp_f32 v109, v109
v_exp_f32 v110, v110
v_exp_f32 v111, v111
v_add_f32 v49, v49, v108
v_add_f32 v49, v49, v109
v_add_f32 v49, v49, v110
v_add_f32 v49, v49, v111
v_cvt_pk_bf16_f32 v158, v108, v109
v_cvt_pk_bf16_f32 v159, v110, v111
v_exp_f32 v88, v88
v_exp_f32 v89, v89
v_add_f32 v48, v48, v88
s_waitcnt lgkmcnt(1)
v_mfma_f32_16x16x32_bf16 v[116:119], v[24:27], a[192:195], v[180:183]
ds_read_b128 v[8:11], %[kaddr0] offset:38912
s_add_u32 m0, s81, 12288
v_mfma_f32_16x16x32_bf16 v[116:119], v[28:31], a[208:211], v[116:119]
v_exp_f32 v90, v90
v_mfma_f32_16x16x32_bf16 v[116:119], v[32:35], a[224:227], v[116:119]
buffer_load_dwordx4 v44, %[srdK], s76 offen offset:0 lds
v_mfma_f32_16x16x32_bf16 v[120:123], v[32:35], a[228:231], v[184:187]
v_exp_f32 v91, v91
v_mfma_f32_16x16x32_bf16 v[120:123], v[28:31], a[212:215], v[120:123]
ds_read_b128 v[12:15], v40 offset:38912
v_add_f32 v48, v48, v89
v_mfma_f32_16x16x32_bf16 v[120:123], v[24:27], a[196:199], v[120:123]
v_add_f32 v48, v48, v90
v_add_f32 v48, v48, v91
v_mfma_f32_16x16x32_bf16 v[124:127], v[24:27], a[200:203], v[188:191]
)"
R"(v_exp_f32 v104, v104
v_mfma_f32_16x16x32_bf16 v[124:127], v[28:31], a[216:219], v[124:127]
v_exp_f32 v105, v105
v_mfma_f32_16x16x32_bf16 v[124:127], v[32:35], a[232:235], v[124:127]
ds_read_b128 v[16:19], %[kaddr0] offset:43008
v_cvt_pk_bf16_f32 v152, v88, v89
v_mfma_f32_16x16x32_bf16 v[128:131], v[32:35], a[236:239], v[192:195]
v_cvt_pk_bf16_f32 v153, v90, v91
v_add_f32 v48, v48, v104
v_mfma_f32_16x16x32_bf16 v[128:131], v[28:31], a[220:223], v[128:131]
buffer_load_dwordx4 v45, %[srdK], s76 offen offset:1024 lds
v_mfma_f32_16x16x32_bf16 v[128:131], v[24:27], a[204:207], v[128:131]
v_add_f32 v48, v48, v105
s_waitcnt lgkmcnt(1)
v_mfma_f32_16x16x32_bf16 v[128:131], v[36:39], a[252:255], v[128:131]
ds_read_b128 v[20:23], v40 offset:43008
v_cvt_pk_bf16_f32 v154, v104, v105
v_mfma_f32_16x16x32_bf16 v[128:131], v[8:11], v[64:67], v[128:131]
v_exp_f32 v106, v106
v_mfma_f32_16x16x32_bf16 v[128:131], v[12:15], v[80:83], v[128:131]
v_exp_f32 v107, v107
v_mfma_f32_16x16x32_bf16 v[124:127], v[12:15], v[76:79], v[124:127]
v_exp_f32 v84, v84
v_mfma_f32_16x16x32_bf16 v[124:127], v[8:11], v[60:63], v[124:127]
ds_read_b128 v[24:27], %[kaddr0] offset:44032
v_add_f32 v48, v48, v106
v_mfma_f32_16x16x32_bf16 v[124:127], v[36:39], a[248:251], v[124:127]
v_exp_f32 v85, v85
v_mfma_f32_16x16x32_bf16 v[120:123], v[36:39], a[244:247], v[120:123]
buffer_load_dwordx4 v46, %[srdK], s76 offen offset:2048 lds
v_mfma_f32_16x16x32_bf16 v[120:123], v[8:11], v[56:59], v[120:123]
v_add_f32 v48, v48, v107
v_cvt_pk_bf16_f32 v155, v106, v107
v_mfma_f32_16x16x32_bf16 v[120:123], v[12:15], v[72:75], v[120:123]
ds_read_b128 v[28:31], v40 offset:44032
s_add_u32 m0, s81, 61440
v_mfma_f32_16x16x32_bf16 v[116:119], v[12:15], v[68:71], v[116:119]
v_exp_f32 v86, v86
v_mfma_f32_16x16x32_bf16 v[116:119], v[8:11], v[52:55], v[116:119]
v_add_f32 v47, v47, v84
v_add_f32 v47, v47, v85
v_mfma_f32_16x16x32_bf16 v[116:119], v[36:39], a[240:243], v[116:119]
v_add_f32 v47, v47, v86
s_waitcnt lgkmcnt(1)
v_mfma_f32_16x16x32_bf16 v[132:135], v[16:19], a[192:195], v[180:183]
ds_read_b128 v[32:35], %[kaddr0] offset:45056
v_cvt_pk_bf16_f32 v148, v84, v85
v_mfma_f32_16x16x32_bf16 v[132:135], v[20:23], a[208:211], v[132:135]
v_exp_f32 v87, v87
v_mfma_f32_16x16x32_bf16 v[132:135], v[24:27], a[224:227], v[132:135]
buffer_load_dwordx4 v44, %[srdV], s76 offen offset:0 lds
v_mfma_f32_16x16x32_bf16 v[136:139], v[24:27], a[228:231], v[184:187]
v_exp_f32 v100, v100
v_mfma_f32_16x16x32_bf16 v[136:139], v[20:23], a[212:215], v[136:139]
ds_read_b128 v[36:39], v40 offset:45056
v_add_f32 v47, v47, v87
v_mfma_f32_16x16x32_bf16 v[136:139], v[16:19], a[196:199], v[136:139]
v_exp_f32 v101, v101
v_mfma_f32_16x16x32_bf16 v[140:143], v[16:19], a[200:203], v[188:191]
v_cvt_pk_bf16_f32 v149, v86, v87
v_add_f32 v47, v47, v100
v_mfma_f32_16x16x32_bf16 v[140:143], v[20:23], a[216:219], v[140:143]
v_exp_f32 v102, v102
v_mfma_f32_16x16x32_bf16 v[140:143], v[24:27], a[232:235], v[140:143]
ds_read_b64_tr_b16 v[8:9], %[vaddr0] offset:36864
ds_read_b64_tr_b16 v[10:11], %[vaddr0] offset:43008
v_mfma_f32_16x16x32_bf16 v[144:147], v[24:27], a[236:239], v[192:195]
v_exp_f32 v103, v103
v_mfma_f32_16x16x32_bf16 v[144:147], v[20:23], a[220:223], v[144:147]
buffer_load_dwordx4 v45, %[srdV], s76 offen offset:1024 lds
v_mfma_f32_16x16x32_bf16 v[144:147], v[16:19], a[204:207], v[144:147]
v_add_f32 v47, v47, v101
s_waitcnt lgkmcnt(2)
v_mfma_f32_16x16x32_bf16 v[144:147], v[28:31], a[252:255], v[144:147]
ds_read_b64_tr_b16 v[12:13], v41 offset:36864
ds_read_b64_tr_b16 v[14:15], v41 offset:43008
v_mfma_f32_16x16x32_bf16 v[144:147], v[32:35], v[64:67], v[144:147]
v_add_f32 v47, v47, v102
v_add_f32 v47, v47, v103
v_mfma_f32_16x16x32_bf16 v[144:147], v[36:39], v[80:83], v[144:147]
v_cvt_pk_bf16_f32 v150, v100, v101
v_cvt_pk_bf16_f32 v151, v102, v103
v_mfma_f32_16x16x32_bf16 v[140:143], v[36:39], v[76:79], v[140:143]
v_mfma_f32_16x16x32_bf16 v[140:143], v[32:35], v[60:63], v[140:143]
ds_read_b64_tr_b16 v[16:17], v42 offset:36864
ds_read_b64_tr_b16 v[18:19], v42 offset:43008
v_mfma_f32_16x16x32_bf16 v[140:143], v[28:31], a[248:251], v[140:143]
v_mfma_f32_16x16x32_bf16 v[136:139], v[28:31], a[244:247], v[136:139]
buffer_load_dwordx4 v46, %[srdV], s76 offen offset:2048 lds
v_mfma_f32_16x16x32_bf16 v[136:139], v[32:35], v[56:59], v[136:139]
v_mfma_f32_16x16x32_bf16 v[136:139], v[36:39], v[72:75], v[136:139]
ds_read_b64_tr_b16 v[20:21], v43 offset:36864
ds_read_b64_tr_b16 v[22:23], v43 offset:43008
v_mfma_f32_16x16x32_bf16 v[132:135], v[36:39], v[68:71], v[132:135]
v_mfma_f32_16x16x32_bf16 v[132:135], v[32:35], v[52:55], v[132:135]
s_add_u32 s76, s76, 12288
v_mfma_f32_16x16x32_bf16 v[132:135], v[28:31], a[240:243], v[132:135]
s_waitcnt lgkmcnt(4)
v_mfma_f32_16x16x32_bf16 a[0:3], v[8:11], v[148:151], 0
ds_read_b64_tr_b16 v[24:25], %[vaddr0] offset:37888
ds_read_b64_tr_b16 v[26:27], %[vaddr0] offset:44032
v_mfma_f32_16x16x32_bf16 a[16:19], v[12:15], v[148:151], 0
s_add_u32 m0, s81, 24576
v_mfma_f32_16x16x32_bf16 a[20:23], v[12:15], v[152:155], 0
buffer_load_dwordx4 v44, %[srdK], s76 offen offset:0 lds
v_mfma_f32_16x16x32_bf16 a[4:7], v[8:11], v[152:155], 0
v_exp_f32 v128, v128
v_mfma_f32_16x16x32_bf16 a[8:11], v[8:11], v[156:159], 0
ds_read_b64_tr_b16 v[28:29], v41 offset:37888
ds_read_b64_tr_b16 v[30:31], v41 offset:44032
v_mfma_f32_16x16x32_bf16 a[24:27], v[12:15], v[156:159], 0
v_exp_f32 v129, v129
v_mfma_f32_16x16x32_bf16 a[28:31], v[12:15], v[160:163], 0
v_exp_f32 v130, v130
v_mfma_f32_16x16x32_bf16 a[12:15], v[8:11], v[160:163], 0
v_add_f32 v50, v50, v128
s_waitcnt lgkmcnt(4)
v_mfma_f32_16x16x32_bf16 a[44:47], v[16:19], v[160:163], 0
ds_read_b64_tr_b16 v[32:33], v42 offset:37888
ds_read_b64_tr_b16 v[34:35], v42 offset:44032
v_mfma_f32_16x16x32_bf16 a[60:63], v[20:23], v[160:163], 0
buffer_load_dwordx4 v45, %[srdK], s76 offen offset:1024 lds
v_exp_f32 v131, v131
v_mfma_f32_16x16x32_bf16 a[56:59], v[20:23], v[156:159], 0
v_mfma_f32_16x16x32_bf16 a[40:43], v[16:19], v[156:159], 0
v_add_f32 v50, v50, v129
v_add_f32 v50, v50, v130
v_mfma_f32_16x16x32_bf16 a[36:39], v[16:19], v[152:155], 0
ds_read_b64_tr_b16 v[36:37], v43 offset:37888
ds_read_b64_tr_b16 v[38:39], v43 offset:44032
v_mfma_f32_16x16x32_bf16 a[52:55], v[20:23], v[152:155], 0
v_exp_f32 v144, v144
v_mfma_f32_16x16x32_bf16 a[48:51], v[20:23], v[148:151], 0
v_add_f32 v50, v50, v131
v_cvt_pk_bf16_f32 v176, v128, v129
v_mfma_f32_16x16x32_bf16 a[32:35], v[16:19], v[148:151], 0
v_cvt_pk_bf16_f32 v177, v130, v131
s_waitcnt lgkmcnt(4)
v_mfma_f32_16x16x32_bf16 a[64:67], v[24:27], v[148:151], 0
ds_read_b64_tr_b16 v[8:9], %[vaddr0] offset:38912
ds_read_b64_tr_b16 v[10:11], %[vaddr0] offset:45056
buffer_load_dwordx4 v46, %[srdK], s76 offen offset:2048 lds
v_mfma_f32_16x16x32_bf16 a[80:83], v[28:31], v[148:151], 0
v_exp_f32 v145, v145
v_mfma_f32_16x16x32_bf16 a[84:87], v[28:31], v[152:155], 0
v_mfma_f32_16x16x32_bf16 a[68:71], v[24:27], v[152:155], 0
v_exp_f32 v146, v146
v_mfma_f32_16x16x32_bf16 a[72:75], v[24:27], v[156:159], 0
ds_read_b64_tr_b16 v[12:13], v41 offset:38912
ds_read_b64_tr_b16 v[14:15], v41 offset:45056
v_mfma_f32_16x16x32_bf16 a[88:91], v[28:31], v[156:159], 0
v_add_f32 v50, v50, v144
v_add_f32 v50, v50, v145
v_mfma_f32_16x16x32_bf16 a[92:95], v[28:31], v[160:163], 0
s_add_u32 m0, s81, 73728
v_exp_f32 v147, v147
v_mfma_f32_16x16x32_bf16 a[76:79], v[24:27], v[160:163], 0
buffer_load_dwordx4 v44, %[srdV], s76 offen offset:0 lds
v_add_f32 v50, v50, v146
s_waitcnt lgkmcnt(4)
v_mfma_f32_16x16x32_bf16 a[108:111], v[32:35], v[160:163], 0
ds_read_b64_tr_b16 v[16:17], v42 offset:38912
ds_read_b64_tr_b16 v[18:19], v42 offset:45056
v_mfma_f32_16x16x32_bf16 a[124:127], v[36:39], v[160:163], 0
v_exp_f32 v124, v124
v_mfma_f32_16x16x32_bf16 a[120:123], v[36:39], v[156:159], 0
v_add_f32 v50, v50, v147
v_cvt_pk_bf16_f32 v178, v144, v145
v_mfma_f32_16x16x32_bf16 a[104:107], v[32:35], v[156:159], 0
v_exp_f32 v125, v125
v_mfma_f32_16x16x32_bf16 a[100:103], v[32:35], v[152:155], 0
ds_read_b64_tr_b16 v[20:21], v43 offset:38912
ds_read_b64_tr_b16 v[22:23], v43 offset:45056
v_mfma_f32_16x16x32_bf16 a[116:119], v[36:39], v[152:155], 0
v_cvt_pk_bf16_f32 v179, v146, v147
v_add_f32 v49, v49, v124
v_mfma_f32_16x16x32_bf16 a[112:115], v[36:39], v[148:151], 0
buffer_load_dwordx4 v45, %[srdV], s76 offen offset:1024 lds
v_exp_f32 v126, v126
v_mfma_f32_16x16x32_bf16 a[96:99], v[32:35], v[148:151], 0
v_add_f32 v49, v49, v125
s_waitcnt lgkmcnt(4)
v_mfma_f32_16x16x32_bf16 a[128:131], v[8:11], v[148:151], 0
s_waitcnt vmcnt(11) lgkmcnt(0)
s_barrier
ds_read_b128 v[24:27], %[kaddr0] offset:0
v_mfma_f32_16x16x32_bf16 a[144:147], v[12:15], v[148:151], 0
v_exp_f32 v127, v127
v_mfma_f32_16x16x32_bf16 a[148:151], v[12:15], v[152:155], 0
v_add_f32 v49, v49, v126
v_add_f32 v49, v49, v127
v_mfma_f32_16x16x32_bf16 a[132:135], v[8:11], v[152:155], 0
v_exp_f32 v140, v140
v_mfma_f32_16x16x32_bf16 a[136:139], v[8:11], v[156:159], 0
ds_read_b128 v[28:31], v40 offset:0
v_cvt_pk_bf16_f32 v172, v124, v125
v_mfma_f32_16x16x32_bf16 a[152:155], v[12:15], v[156:159], 0
buffer_load_dwordx4 v46, %[srdV], s76 offen offset:2048 lds
v_exp_f32 v141, v141
v_mfma_f32_16x16x32_bf16 a[156:159], v[12:15], v[160:163], 0
)"
R"(v_cvt_pk_bf16_f32 v173, v126, v127
v_add_f32 v49, v49, v140
v_mfma_f32_16x16x32_bf16 a[140:143], v[8:11], v[160:163], 0
v_add_f32 v49, v49, v141
v_mfma_f32_16x16x32_bf16 a[172:175], v[16:19], v[160:163], 0
ds_read_b128 v[32:35], %[kaddr0] offset:1024
v_cvt_pk_bf16_f32 v174, v140, v141
v_mfma_f32_16x16x32_bf16 a[188:191], v[20:23], v[160:163], 0
v_exp_f32 v142, v142
v_mfma_f32_16x16x32_bf16 a[184:187], v[20:23], v[156:159], 0
v_exp_f32 v143, v143
v_mfma_f32_16x16x32_bf16 a[168:171], v[16:19], v[156:159], 0
s_add_u32 s76, s76, 12288
v_exp_f32 v120, v120
v_mfma_f32_16x16x32_bf16 a[164:167], v[16:19], v[152:155], 0
ds_read_b128 v[36:39], v40 offset:1024
v_add_f32 v49, v49, v142
v_mfma_f32_16x16x32_bf16 a[180:183], v[20:23], v[152:155], 0
v_exp_f32 v121, v121
v_mfma_f32_16x16x32_bf16 a[176:179], v[20:23], v[148:151], 0
v_add_f32 v49, v49, v143
v_cvt_pk_bf16_f32 v175, v142, v143
v_mfma_f32_16x16x32_bf16 a[160:163], v[16:19], v[148:151], 0
v_add_f32 v48, v48, v120
s_cmp_lg_u32 %[wid], 0
s_cbranch_scc1 .Lhsig
s_mov_b64 exec, 1
global_store_dword v251, v252, s[64:65] offset:128 sc1
s_mov_b64 exec, -1
.Lhsig:
v_mov_b32 v204, 0
v_mov_b32 v205, 0
v_mov_b32 v206, 0
v_mov_b32 v207, 0
v_mov_b32 v208, 0
v_mov_b32 v209, 0
v_mov_b32 v210, 0
v_mov_b32 v211, 0
v_mov_b32 v212, 0
v_mov_b32 v213, 0
v_mov_b32 v214, 0
v_mov_b32 v215, 0
v_mov_b32 v216, 0
v_mov_b32 v217, 0
v_mov_b32 v218, 0
v_mov_b32 v219, 0
v_mov_b32 v220, 0
v_mov_b32 v221, 0
v_mov_b32 v222, 0
v_mov_b32 v223, 0
s_mov_b32 s83, 1280
v_mad_u32_u24 v248, %[drow], s83, %[dch16]
v_subrev_u32 v249, 896, v248
v_subrev_u32 v250, 1792, v248
v_bfe_u32 v51, %[lane16], 9, 1
v_lshlrev_b32 v51, 10, v51
v_sub_u32 v51, %[vaddr0], v51
s_lshl_b32 s84, s87, 1
s_lshr_b32 s83, s86, 2
s_add_u32 s84, s84, s83
s_mul_i32 s84, s84, 5
s_and_b32 s84, s84, 3
s_add_u32 s83, s84, 0
s_and_b32 s95, s83, 3
s_lshl_b32 s95, s95, 5
s_lshr_b32 s83, s83, 2
s_lshl_b32 s83, s83, 10
s_add_u32 s83, s83, 49152
v_xor_b32 v244, s95, v51
v_add_u32 v244, s83, v244
s_add_u32 s83, s84, 1
s_and_b32 s95, s83, 3
s_lshl_b32 s95, s95, 5
s_lshr_b32 s83, s83, 2
s_lshl_b32 s83, s83, 10
s_add_u32 s83, s83, 49152
v_xor_b32 v245, s95, v51
v_add_u32 v245, s83, v245
s_add_u32 s83, s84, 2
s_and_b32 s95, s83, 3
s_lshl_b32 s95, s95, 5
s_lshr_b32 s83, s83, 2
s_lshl_b32 s83, s83, 10
s_add_u32 s83, s83, 49152
v_xor_b32 v246, s95, v51
v_add_u32 v246, s83, v246
s_add_u32 s83, s84, 3
s_and_b32 s95, s83, 3
s_lshl_b32 s95, s95, 5
s_lshr_b32 s83, s83, 2
s_lshl_b32 s83, s83, 10
s_add_u32 s83, s83, 49152
v_xor_b32 v247, s95, v51
v_add_u32 v247, s83, v247
s_mul_i32 s93, %[wid], 10240
s_mov_b32 s94, 0
s_add_u32 s84, s91, s90
s_mul_i32 s84, s84, 49152
s_mov_b64 s[72:73], %[ws]
s_add_u32 s72, s72, s84
s_addc_u32 s73, s73, 0
s_add_u32 s72, s72, 4194304
s_addc_u32 s73, s73, 0
s_mov_b32 s74, 49152
s_mov_b32 s75, 0x20000
v_lshrrev_b32 v238, 4, %[lane16]
v_min_u32 v238, 8, v238
v_mul_u32_u24 v240, 0x5556, v238
v_lshrrev_b32 v240, 16, v240
v_mul_u32_u24 v239, 3, v240
v_sub_u32 v239, v238, v239
v_mul_u32_u24 v240, 1280, v240
v_lshl_add_u32 v240, v239, 7, v240
s_lshl_b32 s84, s88, 2
s_add_u32 s84, s84, %[wid]
s_mul_i32 s92, s84, 30720
s_add_u32 s92, s92, 2048
v_min_u32 v238, 3, v238
v_lshrrev_b32 v241, 1, v238
v_and_b32 v239, 1, v238
v_mul_u32_u24 v241, 1280, v241
v_lshl_add_u32 v241, v239, 7, v241
s_and_b32 s83, s86, 3
s_lshl_b32 s83, s83, 5
s_add_u32 s83, s83, s84
s_mul_i32 s79, s83, 15360
s_add_u32 s79, s79, 2048
s_mov_b32 s80, 2
.Lk2_loopa:
s_waitcnt lgkmcnt(1)
v_mfma_f32_16x16x32_bf16 v[84:87], v[24:27], a[192:195], v[180:183]
ds_read_b128 v[8:11], %[kaddr0] offset:2048
s_add_u32 m0, s81, 36864
v_mfma_f32_16x16x32_bf16 v[84:87], v[28:31], a[208:211], v[84:87]
v_exp_f32 v122, v122
v_mfma_f32_16x16x32_bf16 v[84:87], v[32:35], a[224:227], v[84:87]
buffer_load_dwordx4 v44, %[srdK], s76 offen offset:0 lds
v_mfma_f32_16x16x32_bf16 v[88:91], v[32:35], a[228:231], v[184:187]
v_exp_f32 v123, v123
v_mfma_f32_16x16x32_bf16 v[88:91], v[28:31], a[212:215], v[88:91]
ds_read_b128 v[12:15], v40 offset:2048
v_add_f32 v48, v48, v121
v_mfma_f32_16x16x32_bf16 v[88:91], v[24:27], a[196:199], v[88:91]
v_add_f32 v48, v48, v122
v_add_f32 v48, v48, v123
v_mfma_f32_16x16x32_bf16 v[92:95], v[24:27], a[200:203], v[188:191]
v_exp_f32 v136, v136
v_mfma_f32_16x16x32_bf16 v[92:95], v[28:31], a[216:219], v[92:95]
v_exp_f32 v137, v137
v_mfma_f32_16x16x32_bf16 v[92:95], v[32:35], a[232:235], v[92:95]
ds_read_b128 v[16:19], %[kaddr0] offset:6144
v_cvt_pk_bf16_f32 v168, v120, v121
v_mfma_f32_16x16x32_bf16 v[96:99], v[32:35], a[236:239], v[192:195]
v_cvt_pk_bf16_f32 v169, v122, v123
v_add_f32 v48, v48, v136
v_mfma_f32_16x16x32_bf16 v[96:99], v[28:31], a[220:223], v[96:99]
buffer_load_dwordx4 v45, %[srdK], s76 offen offset:1024 lds
v_mfma_f32_16x16x32_bf16 v[96:99], v[24:27], a[204:207], v[96:99]
v_add_f32 v48, v48, v137
s_waitcnt lgkmcnt(1)
v_mfma_f32_16x16x32_bf16 v[96:99], v[36:39], a[252:255], v[96:99]
ds_read_b128 v[20:23], v40 offset:6144
v_cvt_pk_bf16_f32 v170, v136, v137
v_mfma_f32_16x16x32_bf16 v[96:99], v[8:11], v[64:67], v[96:99]
v_exp_f32 v138, v138
v_mfma_f32_16x16x32_bf16 v[96:99], v[12:15], v[80:83], v[96:99]
v_exp_f32 v139, v139
v_mfma_f32_16x16x32_bf16 v[92:95], v[12:15], v[76:79], v[92:95]
v_exp_f32 v116, v116
v_mfma_f32_16x16x32_bf16 v[92:95], v[8:11], v[60:63], v[92:95]
ds_read_b128 v[24:27], %[kaddr0] offset:7168
v_add_f32 v48, v48, v138
v_mfma_f32_16x16x32_bf16 v[92:95], v[36:39], a[248:251], v[92:95]
v_exp_f32 v117, v117
v_mfma_f32_16x16x32_bf16 v[88:91], v[36:39], a[244:247], v[88:91]
buffer_load_dwordx4 v46, %[srdK], s76 offen offset:2048 lds
v_mfma_f32_16x16x32_bf16 v[88:91], v[8:11], v[56:59], v[88:91]
v_add_f32 v48, v48, v139
v_cvt_pk_bf16_f32 v171, v138, v139
v_mfma_f32_16x16x32_bf16 v[88:91], v[12:15], v[72:75], v[88:91]
ds_read_b128 v[28:31], v40 offset:7168
s_add_u32 m0, s81, 86016
v_mfma_f32_16x16x32_bf16 v[84:87], v[12:15], v[68:71], v[84:87]
v_exp_f32 v118, v118
v_mfma_f32_16x16x32_bf16 v[84:87], v[8:11], v[52:55], v[84:87]
v_add_f32 v47, v47, v116
v_add_f32 v47, v47, v117
v_mfma_f32_16x16x32_bf16 v[84:87], v[36:39], a[240:243], v[84:87]
v_add_f32 v47, v47, v118
s_waitcnt lgkmcnt(1)
v_mfma_f32_16x16x32_bf16 v[100:103], v[16:19], a[192:195], v[180:183]
ds_read_b128 v[32:35], %[kaddr0] offset:8192
v_cvt_pk_bf16_f32 v164, v116, v117
v_mfma_f32_16x16x32_bf16 v[100:103], v[20:23], a[208:211], v[100:103]
v_exp_f32 v119, v119
v_mfma_f32_16x16x32_bf16 v[100:103], v[24:27], a[224:227], v[100:103]
buffer_load_dwordx4 v44, %[srdV], s76 offen offset:0 lds
v_mfma_f32_16x16x32_bf16 v[104:107], v[24:27], a[228:231], v[184:187]
v_exp_f32 v132, v132
v_mfma_f32_16x16x32_bf16 v[104:107], v[20:23], a[212:215], v[104:107]
)"
R"(ds_read_b128 v[36:39], v40 offset:8192
v_add_f32 v47, v47, v119
v_mfma_f32_16x16x32_bf16 v[104:107], v[16:19], a[196:199], v[104:107]
v_exp_f32 v133, v133
v_mfma_f32_16x16x32_bf16 v[108:111], v[16:19], a[200:203], v[188:191]
v_cvt_pk_bf16_f32 v165, v118, v119
v_add_f32 v47, v47, v132
v_mfma_f32_16x16x32_bf16 v[108:111], v[20:23], a[216:219], v[108:111]
v_exp_f32 v134, v134
v_mfma_f32_16x16x32_bf16 v[108:111], v[24:27], a[232:235], v[108:111]
ds_read_b64_tr_b16 v[8:9], %[vaddr0] offset:0
ds_read_b64_tr_b16 v[10:11], %[vaddr0] offset:6144
v_mfma_f32_16x16x32_bf16 v[112:115], v[24:27], a[236:239], v[192:195]
v_exp_f32 v135, v135
v_mfma_f32_16x16x32_bf16 v[112:115], v[20:23], a[220:223], v[112:115]
buffer_load_dwordx4 v45, %[srdV], s76 offen offset:1024 lds
v_mfma_f32_16x16x32_bf16 v[112:115], v[16:19], a[204:207], v[112:115]
v_add_f32 v47, v47, v133
s_waitcnt lgkmcnt(2)
v_mfma_f32_16x16x32_bf16 v[112:115], v[28:31], a[252:255], v[112:115]
ds_read_b64_tr_b16 v[12:13], v41 offset:0
ds_read_b64_tr_b16 v[14:15], v41 offset:6144
v_mfma_f32_16x16x32_bf16 v[112:115], v[32:35], v[64:67], v[112:115]
v_add_f32 v47, v47, v134
v_add_f32 v47, v47, v135
v_mfma_f32_16x16x32_bf16 v[112:115], v[36:39], v[80:83], v[112:115]
v_cvt_pk_bf16_f32 v166, v132, v133
v_cvt_pk_bf16_f32 v167, v134, v135
v_mfma_f32_16x16x32_bf16 v[108:111], v[36:39], v[76:79], v[108:111]
v_mfma_f32_16x16x32_bf16 v[108:111], v[32:35], v[60:63], v[108:111]
ds_read_b64_tr_b16 v[16:17], v42 offset:0
ds_read_b64_tr_b16 v[18:19], v42 offset:6144
v_mfma_f32_16x16x32_bf16 v[108:111], v[28:31], a[248:251], v[108:111]
v_mfma_f32_16x16x32_bf16 v[104:107], v[28:31], a[244:247], v[104:107]
buffer_load_dwordx4 v46, %[srdV], s76 offen offset:2048 lds
v_mfma_f32_16x16x32_bf16 v[104:107], v[32:35], v[56:59], v[104:107]
v_mfma_f32_16x16x32_bf16 v[104:107], v[36:39], v[72:75], v[104:107]
ds_read_b64_tr_b16 v[20:21], v43 offset:0
ds_read_b64_tr_b16 v[22:23], v43 offset:6144
v_mfma_f32_16x16x32_bf16 v[100:103], v[36:39], v[68:71], v[100:103]
v_mfma_f32_16x16x32_bf16 v[100:103], v[32:35], v[52:55], v[100:103]
s_add_u32 s76, s76, 12288
v_mfma_f32_16x16x32_bf16 v[100:103], v[28:31], a[240:243], v[100:103]
s_waitcnt lgkmcnt(4)
v_mfma_f32_16x16x32_bf16 a[0:3], v[8:11], v[164:167], a[0:3]
ds_read_b64_tr_b16 v[24:25], %[vaddr0] offset:1024
ds_read_b64_tr_b16 v[26:27], %[vaddr0] offset:7168
v_mfma_f32_16x16x32_bf16 a[16:19], v[12:15], v[164:167], a[16:19]
v_mfma_f32_16x16x32_bf16 a[20:23], v[12:15], v[168:171], a[20:23]
buffer_load_dword v236, v240, %[srdS], s92 offen
v_mfma_f32_16x16x32_bf16 a[4:7], v[8:11], v[168:171], a[4:7]
v_exp_f32 v96, v96
v_mfma_f32_16x16x32_bf16 a[8:11], v[8:11], v[172:175], a[8:11]
ds_read_b64_tr_b16 v[28:29], v41 offset:1024
ds_read_b64_tr_b16 v[30:31], v41 offset:7168
v_mfma_f32_16x16x32_bf16 a[24:27], v[12:15], v[172:175], a[24:27]
v_exp_f32 v97, v97
v_mfma_f32_16x16x32_bf16 a[28:31], v[12:15], v[176:179], a[28:31]
v_exp_f32 v98, v98
v_mfma_f32_16x16x32_bf16 a[12:15], v[8:11], v[176:179], a[12:15]
v_add_f32 v50, v50, v96
s_waitcnt lgkmcnt(4)
v_mfma_f32_16x16x32_bf16 a[44:47], v[16:19], v[176:179], a[44:47]
ds_read_b64_tr_b16 v[32:33], v42 offset:1024
ds_read_b64_tr_b16 v[34:35], v42 offset:7168
v_mfma_f32_16x16x32_bf16 a[60:63], v[20:23], v[176:179], a[60:63]
v_exp_f32 v99, v99
v_mfma_f32_16x16x32_bf16 a[56:59], v[20:23], v[172:175], a[56:59]
buffer_load_dword v237, v241, %[srdG], s79 offen
v_mfma_f32_16x16x32_bf16 a[40:43], v[16:19], v[172:175], a[40:43]
v_add_f32 v50, v50, v97
v_add_f32 v50, v50, v98
v_mfma_f32_16x16x32_bf16 a[36:39], v[16:19], v[168:171], a[36:39]
ds_read_b64_tr_b16 v[36:37], v43 offset:1024
ds_read_b64_tr_b16 v[38:39], v43 offset:7168
v_mfma_f32_16x16x32_bf16 a[52:55], v[20:23], v[168:171], a[52:55]
v_exp_f32 v112, v112
v_mfma_f32_16x16x32_bf16 a[48:51], v[20:23], v[164:167], a[48:51]
v_add_f32 v50, v50, v99
v_cvt_pk_bf16_f32 v160, v96, v97
v_mfma_f32_16x16x32_bf16 a[32:35], v[16:19], v[164:167], a[32:35]
v_cvt_pk_bf16_f32 v161, v98, v99
s_waitcnt lgkmcnt(4)
v_mfma_f32_16x16x32_bf16 a[64:67], v[24:27], v[164:167], a[64:67]
ds_read_b64_tr_b16 v[8:9], %[vaddr0] offset:2048
ds_read_b64_tr_b16 v[10:11], %[vaddr0] offset:8192
v_mfma_f32_16x16x32_bf16 a[80:83], v[28:31], v[164:167], a[80:83]
v_exp_f32 v113, v113
v_mfma_f32_16x16x32_bf16 a[84:87], v[28:31], v[168:171], a[84:87]
v_mfma_f32_16x16x32_bf16 a[68:71], v[24:27], v[168:171], a[68:71]
v_exp_f32 v114, v114
v_mfma_f32_16x16x32_bf16 a[72:75], v[24:27], v[172:175], a[72:75]
ds_read_b64_tr_b16 v[12:13], v41 offset:2048
ds_read_b64_tr_b16 v[14:15], v41 offset:8192
s_add_u32 s92, s92, 3840
s_add_u32 s79, s79, 2560
v_mfma_f32_16x16x32_bf16 a[88:91], v[28:31], v[172:175], a[88:91]
v_add_f32 v50, v50, v112
v_add_f32 v50, v50, v113
v_mfma_f32_16x16x32_bf16 a[92:95], v[28:31], v[176:179], a[92:95]
v_exp_f32 v115, v115
v_mfma_f32_16x16x32_bf16 a[76:79], v[24:27], v[176:179], a[76:79]
v_add_f32 v50, v50, v114
s_waitcnt lgkmcnt(4)
v_mfma_f32_16x16x32_bf16 a[108:111], v[32:35], v[176:179], a[108:111]
ds_read_b64_tr_b16 v[16:17], v42 offset:2048
ds_read_b64_tr_b16 v[18:19], v42 offset:8192
v_mfma_f32_16x16x32_bf16 a[124:127], v[36:39], v[176:179], a[124:127]
v_exp_f32 v92, v92
v_mfma_f32_16x16x32_bf16 a[120:123], v[36:39], v[172:175], a[120:123]
v_add_f32 v50, v50, v115
v_cvt_pk_bf16_f32 v162, v112, v113
v_mfma_f32_16x16x32_bf16 a[104:107], v[32:35], v[172:175], a[104:107]
v_exp_f32 v93, v93
v_mfma_f32_16x16x32_bf16 a[100:103], v[32:35], v[168:171], a[100:103]
ds_read_b64_tr_b16 v[20:21], v43 offset:2048
ds_read_b64_tr_b16 v[22:23], v43 offset:8192
v_mfma_f32_16x16x32_bf16 a[116:119], v[36:39], v[168:171], a[116:119]
v_cvt_pk_bf16_f32 v163, v114, v115
v_add_f32 v49, v49, v92
v_mfma_f32_16x16x32_bf16 a[112:115], v[36:39], v[164:167], a[112:115]
v_exp_f32 v94, v94
v_mfma_f32_16x16x32_bf16 a[96:99], v[32:35], v[164:167], a[96:99]
v_add_f32 v49, v49, v93
s_waitcnt lgkmcnt(4)
v_mfma_f32_16x16x32_bf16 a[128:131], v[8:11], v[164:167], a[128:131]
s_waitcnt vmcnt(14) lgkmcnt(0)
s_barrier
ds_read_b128 v[24:27], %[kaddr0] offset:12288
v_mfma_f32_16x16x32_bf16 a[144:147], v[12:15], v[164:167], a[144:147]
v_exp_f32 v95, v95
v_mfma_f32_16x16x32_bf16 a[148:151], v[12:15], v[168:171], a[148:151]
v_add_f32 v49, v49, v94
v_add_f32 v49, v49, v95
v_mfma_f32_16x16x32_bf16 a[132:135], v[8:11], v[168:171], a[132:135]
v_exp_f32 v108, v108
v_mfma_f32_16x16x32_bf16 a[136:139], v[8:11], v[172:175], a[136:139]
ds_read_b128 v[28:31], v40 offset:12288
v_cvt_pk_bf16_f32 v156, v92, v93
v_mfma_f32_16x16x32_bf16 a[152:155], v[12:15], v[172:175], a[152:155]
v_exp_f32 v109, v109
v_mfma_f32_16x16x32_bf16 a[156:159], v[12:15], v[176:179], a[156:159]
v_cvt_pk_bf16_f32 v157, v94, v95
v_add_f32 v49, v49, v108
v_mfma_f32_16x16x32_bf16 a[140:143], v[8:11], v[176:179], a[140:143]
v_add_f32 v49, v49, v109
v_mfma_f32_16x16x32_bf16 a[172:175], v[16:19], v[176:179], a[172:175]
ds_read_b128 v[32:35], %[kaddr0] offset:13312
v_cvt_pk_bf16_f32 v158, v108, v109
v_mfma_f32_16x16x32_bf16 a[188:191], v[20:23], v[176:179], a[188:191]
v_exp_f32 v110, v110
v_mfma_f32_16x16x32_bf16 a[184:187], v[20:23], v[172:175], a[184:187]
v_exp_f32 v111, v111
v_mfma_f32_16x16x32_bf16 a[168:171], v[16:19], v[172:175], a[168:171]
v_exp_f32 v88, v88
v_mfma_f32_16x16x32_bf16 a[164:167], v[16:19], v[168:171], a[164:167]
ds_read_b128 v[36:39], v40 offset:13312
v_add_f32 v49, v49, v110
v_mfma_f32_16x16x32_bf16 a[180:183], v[20:23], v[168:171], a[180:183]
v_exp_f32 v89, v89
v_mfma_f32_16x16x32_bf16 a[176:179], v[20:23], v[164:167], a[176:179]
v_add_f32 v49, v49, v111
v_cvt_pk_bf16_f32 v159, v110, v111
v_mfma_f32_16x16x32_bf16 a[160:163], v[16:19], v[164:167], a[160:163]
v_add_f32 v48, v48, v88
s_waitcnt lgkmcnt(1)
v_mfma_f32_16x16x32_bf16 v[116:119], v[24:27], a[192:195], v[180:183]
ds_read_b128 v[8:11], %[kaddr0] offset:14336
s_add_u32 m0, s81, 0
v_mfma_f32_16x16x32_bf16 v[116:119], v[28:31], a[208:211], v[116:119]
v_exp_f32 v90, v90
v_mfma_f32_16x16x32_bf16 v[116:119], v[32:35], a[224:227], v[116:119]
buffer_load_dwordx4 v44, %[srdK], s76 offen offset:0 lds
v_mfma_f32_16x16x32_bf16 v[120:123], v[32:35], a[228:231], v[184:187]
v_exp_f32 v91, v91
v_mfma_f32_16x16x32_bf16 v[120:123], v[28:31], a[212:215], v[120:123]
ds_read_b128 v[12:15], v40 offset:14336
v_add_f32 v48, v48, v89
v_mfma_f32_16x16x32_bf16 v[120:123], v[24:27], a[196:199], v[120:123]
v_add_f32 v48, v48, v90
v_add_f32 v48, v48, v91
v_mfma_f32_16x16x32_bf16 v[124:127], v[24:27], a[200:203], v[188:191]
v_exp_f32 v104, v104
v_mfma_f32_16x16x32_bf16 v[124:127], v[28:31], a[216:219], v[124:127]
v_exp_f32 v105, v105
v_mfma_f32_16x16x32_bf16 v[124:127], v[32:35], a[232:235], v[124:127]
ds_read_b128 v[16:19], %[kaddr0] offset:18432
v_cvt_pk_bf16_f32 v152, v88, v89
v_mfma_f32_16x16x32_bf16 v[128:131], v[32:35], a[236:239], v[192:195]
v_cvt_pk_bf16_f32 v153, v90, v91
v_add_f32 v48, v48, v104
v_mfma_f32_16x16x32_bf16 v[128:131], v[28:31], a[220:223], v[128:131]
buffer_load_dwordx4 v45, %[srdK], s76 offen offset:1024 lds
v_mfma_f32_16x16x32_bf16 v[128:131], v[24:27], a[204:207], v[128:131]
v_add_f32 v48, v48, v105
s_waitcnt lgkmcnt(1)
v_mfma_f32_16x16x32_bf16 v[128:131], v[36:39], a[252:255], v[128:131]
ds_read_b128 v[20:23], v40 offset:18432
v_cvt_pk_bf16_f32 v154, v104, v105
v_mfma_f32_16x16x32_bf16 v[128:131], v[8:11], v[64:67], v[128:131]
)"
R"(v_exp_f32 v106, v106
v_mfma_f32_16x16x32_bf16 v[128:131], v[12:15], v[80:83], v[128:131]
v_exp_f32 v107, v107
v_mfma_f32_16x16x32_bf16 v[124:127], v[12:15], v[76:79], v[124:127]
v_exp_f32 v84, v84
v_mfma_f32_16x16x32_bf16 v[124:127], v[8:11], v[60:63], v[124:127]
ds_read_b128 v[24:27], %[kaddr0] offset:19456
v_add_f32 v48, v48, v106
v_mfma_f32_16x16x32_bf16 v[124:127], v[36:39], a[248:251], v[124:127]
v_exp_f32 v85, v85
v_mfma_f32_16x16x32_bf16 v[120:123], v[36:39], a[244:247], v[120:123]
buffer_load_dwordx4 v46, %[srdK], s76 offen offset:2048 lds
v_mfma_f32_16x16x32_bf16 v[120:123], v[8:11], v[56:59], v[120:123]
v_add_f32 v48, v48, v107
v_cvt_pk_bf16_f32 v155, v106, v107
v_mfma_f32_16x16x32_bf16 v[120:123], v[12:15], v[72:75], v[120:123]
ds_read_b128 v[28:31], v40 offset:19456
s_add_u32 m0, s81, 49152
v_mfma_f32_16x16x32_bf16 v[116:119], v[12:15], v[68:71], v[116:119]
v_exp_f32 v86, v86
v_mfma_f32_16x16x32_bf16 v[116:119], v[8:11], v[52:55], v[116:119]
v_add_f32 v47, v47, v84
v_add_f32 v47, v47, v85
v_mfma_f32_16x16x32_bf16 v[116:119], v[36:39], a[240:243], v[116:119]
v_add_f32 v47, v47, v86
s_waitcnt lgkmcnt(1)
v_mfma_f32_16x16x32_bf16 v[132:135], v[16:19], a[192:195], v[180:183]
ds_read_b128 v[32:35], %[kaddr0] offset:20480
v_cvt_pk_bf16_f32 v148, v84, v85
v_mfma_f32_16x16x32_bf16 v[132:135], v[20:23], a[208:211], v[132:135]
v_exp_f32 v87, v87
v_mfma_f32_16x16x32_bf16 v[132:135], v[24:27], a[224:227], v[132:135]
buffer_load_dwordx4 v44, %[srdV], s76 offen offset:0 lds
v_mfma_f32_16x16x32_bf16 v[136:139], v[24:27], a[228:231], v[184:187]
v_exp_f32 v100, v100
v_mfma_f32_16x16x32_bf16 v[136:139], v[20:23], a[212:215], v[136:139]
ds_read_b128 v[36:39], v40 offset:20480
v_add_f32 v47, v47, v87
v_mfma_f32_16x16x32_bf16 v[136:139], v[16:19], a[196:199], v[136:139]
v_exp_f32 v101, v101
v_mfma_f32_16x16x32_bf16 v[140:143], v[16:19], a[200:203], v[188:191]
v_cvt_pk_bf16_f32 v149, v86, v87
v_add_f32 v47, v47, v100
v_mfma_f32_16x16x32_bf16 v[140:143], v[20:23], a[216:219], v[140:143]
v_exp_f32 v102, v102
v_mfma_f32_16x16x32_bf16 v[140:143], v[24:27], a[232:235], v[140:143]
ds_read_b64_tr_b16 v[8:9], %[vaddr0] offset:12288
ds_read_b64_tr_b16 v[10:11], %[vaddr0] offset:18432
v_mfma_f32_16x16x32_bf16 v[144:147], v[24:27], a[236:239], v[192:195]
v_exp_f32 v103, v103
v_mfma_f32_16x16x32_bf16 v[144:147], v[20:23], a[220:223], v[144:147]
buffer_load_dwordx4 v45, %[srdV], s76 offen offset:1024 lds
v_mfma_f32_16x16x32_bf16 v[144:147], v[16:19], a[204:207], v[144:147]
v_add_f32 v47, v47, v101
s_waitcnt lgkmcnt(2)
v_mfma_f32_16x16x32_bf16 v[144:147], v[28:31], a[252:255], v[144:147]
ds_read_b64_tr_b16 v[12:13], v41 offset:12288
ds_read_b64_tr_b16 v[14:15], v41 offset:18432
v_mfma_f32_16x16x32_bf16 v[144:147], v[32:35], v[64:67], v[144:147]
v_add_f32 v47, v47, v102
v_add_f32 v47, v47, v103
v_mfma_f32_16x16x32_bf16 v[144:147], v[36:39], v[80:83], v[144:147]
v_cvt_pk_bf16_f32 v150, v100, v101
v_cvt_pk_bf16_f32 v151, v102, v103
v_mfma_f32_16x16x32_bf16 v[140:143], v[36:39], v[76:79], v[140:143]
v_mfma_f32_16x16x32_bf16 v[140:143], v[32:35], v[60:63], v[140:143]
ds_read_b64_tr_b16 v[16:17], v42 offset:12288
ds_read_b64_tr_b16 v[18:19], v42 offset:18432
v_mfma_f32_16x16x32_bf16 v[140:143], v[28:31], a[248:251], v[140:143]
v_mfma_f32_16x16x32_bf16 v[136:139], v[28:31], a[244:247], v[136:139]
buffer_load_dwordx4 v46, %[srdV], s76 offen offset:2048 lds
v_mfma_f32_16x16x32_bf16 v[136:139], v[32:35], v[56:59], v[136:139]
v_mfma_f32_16x16x32_bf16 v[136:139], v[36:39], v[72:75], v[136:139]
ds_read_b64_tr_b16 v[20:21], v43 offset:12288
ds_read_b64_tr_b16 v[22:23], v43 offset:18432
v_mfma_f32_16x16x32_bf16 v[132:135], v[36:39], v[68:71], v[132:135]
v_mfma_f32_16x16x32_bf16 v[132:135], v[32:35], v[52:55], v[132:135]
s_add_u32 s76, s76, 12288
v_mfma_f32_16x16x32_bf16 v[132:135], v[28:31], a[240:243], v[132:135]
s_waitcnt lgkmcnt(4)
v_mfma_f32_16x16x32_bf16 a[0:3], v[8:11], v[148:151], a[0:3]
ds_read_b64_tr_b16 v[24:25], %[vaddr0] offset:13312
ds_read_b64_tr_b16 v[26:27], %[vaddr0] offset:19456
v_mfma_f32_16x16x32_bf16 a[16:19], v[12:15], v[148:151], a[16:19]
v_mfma_f32_16x16x32_bf16 a[20:23], v[12:15], v[152:155], a[20:23]
buffer_load_dword v236, v240, %[srdS], s92 offen
v_mfma_f32_16x16x32_bf16 a[4:7], v[8:11], v[152:155], a[4:7]
v_exp_f32 v128, v128
v_mfma_f32_16x16x32_bf16 a[8:11], v[8:11], v[156:159], a[8:11]
ds_read_b64_tr_b16 v[28:29], v41 offset:13312
ds_read_b64_tr_b16 v[30:31], v41 offset:19456
v_mfma_f32_16x16x32_bf16 a[24:27], v[12:15], v[156:159], a[24:27]
v_exp_f32 v129, v129
v_mfma_f32_16x16x32_bf16 a[28:31], v[12:15], v[160:163], a[28:31]
v_exp_f32 v130, v130
v_mfma_f32_16x16x32_bf16 a[12:15], v[8:11], v[160:163], a[12:15]
v_add_f32 v50, v50, v128
s_waitcnt lgkmcnt(4)
v_mfma_f32_16x16x32_bf16 a[44:47], v[16:19], v[160:163], a[44:47]
ds_read_b64_tr_b16 v[32:33], v42 offset:13312
ds_read_b64_tr_b16 v[34:35], v42 offset:19456
v_mfma_f32_16x16x32_bf16 a[60:63], v[20:23], v[160:163], a[60:63]
v_exp_f32 v131, v131
v_mfma_f32_16x16x32_bf16 a[56:59], v[20:23], v[156:159], a[56:59]
buffer_load_dword v237, v241, %[srdG], s79 offen
v_mfma_f32_16x16x32_bf16 a[40:43], v[16:19], v[156:159], a[40:43]
v_add_f32 v50, v50, v129
v_add_f32 v50, v50, v130
v_mfma_f32_16x16x32_bf16 a[36:39], v[16:19], v[152:155], a[36:39]
ds_read_b64_tr_b16 v[36:37], v43 offset:13312
ds_read_b64_tr_b16 v[38:39], v43 offset:19456
v_mfma_f32_16x16x32_bf16 a[52:55], v[20:23], v[152:155], a[52:55]
v_exp_f32 v144, v144
v_mfma_f32_16x16x32_bf16 a[48:51], v[20:23], v[148:151], a[48:51]
v_add_f32 v50, v50, v131
v_cvt_pk_bf16_f32 v176, v128, v129
v_mfma_f32_16x16x32_bf16 a[32:35], v[16:19], v[148:151], a[32:35]
v_cvt_pk_bf16_f32 v177, v130, v131
s_waitcnt lgkmcnt(4)
v_mfma_f32_16x16x32_bf16 a[64:67], v[24:27], v[148:151], a[64:67]
ds_read_b64_tr_b16 v[8:9], %[vaddr0] offset:14336
ds_read_b64_tr_b16 v[10:11], %[vaddr0] offset:20480
v_mfma_f32_16x16x32_bf16 a[80:83], v[28:31], v[148:151], a[80:83]
v_exp_f32 v145, v145
v_mfma_f32_16x16x32_bf16 a[84:87], v[28:31], v[152:155], a[84:87]
v_mfma_f32_16x16x32_bf16 a[68:71], v[24:27], v[152:155], a[68:71]
v_exp_f32 v146, v146
v_mfma_f32_16x16x32_bf16 a[72:75], v[24:27], v[156:159], a[72:75]
ds_read_b64_tr_b16 v[12:13], v41 offset:14336
ds_read_b64_tr_b16 v[14:15], v41 offset:20480
s_add_u32 s92, s92, 3840
s_add_u32 s79, s79, 2560
v_mfma_f32_16x16x32_bf16 a[88:91], v[28:31], v[156:159], a[88:91]
v_add_f32 v50, v50, v144
v_add_f32 v50, v50, v145
v_mfma_f32_16x16x32_bf16 a[92:95], v[28:31], v[160:163], a[92:95]
v_exp_f32 v147, v147
v_mfma_f32_16x16x32_bf16 a[76:79], v[24:27], v[160:163], a[76:79]
v_add_f32 v50, v50, v146
s_waitcnt lgkmcnt(4)
v_mfma_f32_16x16x32_bf16 a[108:111], v[32:35], v[160:163], a[108:111]
ds_read_b64_tr_b16 v[16:17], v42 offset:14336
ds_read_b64_tr_b16 v[18:19], v42 offset:20480
v_mfma_f32_16x16x32_bf16 a[124:127], v[36:39], v[160:163], a[124:127]
v_exp_f32 v124, v124
v_mfma_f32_16x16x32_bf16 a[120:123], v[36:39], v[156:159], a[120:123]
v_add_f32 v50, v50, v147
v_cvt_pk_bf16_f32 v178, v144, v145
v_mfma_f32_16x16x32_bf16 a[104:107], v[32:35], v[156:159], a[104:107]
v_exp_f32 v125, v125
v_mfma_f32_16x16x32_bf16 a[100:103], v[32:35], v[152:155], a[100:103]
ds_read_b64_tr_b16 v[20:21], v43 offset:14336
ds_read_b64_tr_b16 v[22:23], v43 offset:20480
v_mfma_f32_16x16x32_bf16 a[116:119], v[36:39], v[152:155], a[116:119]
v_cvt_pk_bf16_f32 v179, v146, v147
v_add_f32 v49, v49, v124
v_mfma_f32_16x16x32_bf16 a[112:115], v[36:39], v[148:151], a[112:115]
v_exp_f32 v126, v126
v_mfma_f32_16x16x32_bf16 a[96:99], v[32:35], v[148:151], a[96:99]
v_add_f32 v49, v49, v125
s_waitcnt lgkmcnt(4)
v_mfma_f32_16x16x32_bf16 a[128:131], v[8:11], v[148:151], a[128:131]
s_waitcnt vmcnt(16) lgkmcnt(0)
s_barrier
ds_read_b128 v[24:27], %[kaddr0] offset:24576
v_mfma_f32_16x16x32_bf16 a[144:147], v[12:15], v[148:151], a[144:147]
v_exp_f32 v127, v127
v_mfma_f32_16x16x32_bf16 a[148:151], v[12:15], v[152:155], a[148:151]
v_add_f32 v49, v49, v126
v_add_f32 v49, v49, v127
v_mfma_f32_16x16x32_bf16 a[132:135], v[8:11], v[152:155], a[132:135]
v_exp_f32 v140, v140
v_mfma_f32_16x16x32_bf16 a[136:139], v[8:11], v[156:159], a[136:139]
ds_read_b128 v[28:31], v40 offset:24576
v_cvt_pk_bf16_f32 v172, v124, v125
v_mfma_f32_16x16x32_bf16 a[152:155], v[12:15], v[156:159], a[152:155]
v_exp_f32 v141, v141
v_mfma_f32_16x16x32_bf16 a[156:159], v[12:15], v[160:163], a[156:159]
v_cvt_pk_bf16_f32 v173, v126, v127
v_add_f32 v49, v49, v140
v_mfma_f32_16x16x32_bf16 a[140:143], v[8:11], v[160:163], a[140:143]
v_add_f32 v49, v49, v141
v_mfma_f32_16x16x32_bf16 a[172:175], v[16:19], v[160:163], a[172:175]
ds_read_b128 v[32:35], %[kaddr0] offset:25600
v_cvt_pk_bf16_f32 v174, v140, v141
v_mfma_f32_16x16x32_bf16 a[188:191], v[20:23], v[160:163], a[188:191]
v_exp_f32 v142, v142
v_mfma_f32_16x16x32_bf16 a[184:187], v[20:23], v[156:159], a[184:187]
v_exp_f32 v143, v143
v_mfma_f32_16x16x32_bf16 a[168:171], v[16:19], v[156:159], a[168:171]
v_exp_f32 v120, v120
v_mfma_f32_16x16x32_bf16 a[164:167], v[16:19], v[152:155], a[164:167]
ds_read_b128 v[36:39], v40 offset:25600
v_add_f32 v49, v49, v142
v_mfma_f32_16x16x32_bf16 a[180:183], v[20:23], v[152:155], a[180:183]
v_exp_f32 v121, v121
v_mfma_f32_16x16x32_bf16 a[176:179], v[20:23], v[148:151], a[176:179]
v_add_f32 v49, v49, v143
v_cvt_pk_bf16_f32 v175, v142, v143
v_mfma_f32_16x16x32_bf16 a[160:163], v[16:19], v[148:151], a[160:163]
)"
R"(v_add_f32 v48, v48, v120
s_waitcnt lgkmcnt(1)
v_mfma_f32_16x16x32_bf16 v[84:87], v[24:27], a[192:195], v[180:183]
ds_read_b128 v[8:11], %[kaddr0] offset:26624
s_add_u32 m0, s81, 12288
v_mfma_f32_16x16x32_bf16 v[84:87], v[28:31], a[208:211], v[84:87]
v_exp_f32 v122, v122
v_mfma_f32_16x16x32_bf16 v[84:87], v[32:35], a[224:227], v[84:87]
buffer_load_dwordx4 v44, %[srdK], s76 offen offset:0 lds
v_mfma_f32_16x16x32_bf16 v[88:91], v[32:35], a[228:231], v[184:187]
v_exp_f32 v123, v123
v_mfma_f32_16x16x32_bf16 v[88:91], v[28:31], a[212:215], v[88:91]
ds_read_b128 v[12:15], v40 offset:26624
v_add_f32 v48, v48, v121
v_mfma_f32_16x16x32_bf16 v[88:91], v[24:27], a[196:199], v[88:91]
v_add_f32 v48, v48, v122
v_add_f32 v48, v48, v123
v_mfma_f32_16x16x32_bf16 v[92:95], v[24:27], a[200:203], v[188:191]
v_exp_f32 v136, v136
v_mfma_f32_16x16x32_bf16 v[92:95], v[28:31], a[216:219], v[92:95]
v_exp_f32 v137, v137
v_mfma_f32_16x16x32_bf16 v[92:95], v[32:35], a[232:235], v[92:95]
ds_read_b128 v[16:19], %[kaddr0] offset:30720
v_cvt_pk_bf16_f32 v168, v120, v121
v_mfma_f32_16x16x32_bf16 v[96:99], v[32:35], a[236:239], v[192:195]
v_cvt_pk_bf16_f32 v169, v122, v123
v_add_f32 v48, v48, v136
v_mfma_f32_16x16x32_bf16 v[96:99], v[28:31], a[220:223], v[96:99]
buffer_load_dwordx4 v45, %[srdK], s76 offen offset:1024 lds
v_mfma_f32_16x16x32_bf16 v[96:99], v[24:27], a[204:207], v[96:99]
v_add_f32 v48, v48, v137
s_waitcnt lgkmcnt(1)
v_mfma_f32_16x16x32_bf16 v[96:99], v[36:39], a[252:255], v[96:99]
ds_read_b128 v[20:23], v40 offset:30720
v_cvt_pk_bf16_f32 v170, v136, v137
v_mfma_f32_16x16x32_bf16 v[96:99], v[8:11], v[64:67], v[96:99]
v_exp_f32 v138, v138
v_mfma_f32_16x16x32_bf16 v[96:99], v[12:15], v[80:83], v[96:99]
v_exp_f32 v139, v139
v_mfma_f32_16x16x32_bf16 v[92:95], v[12:15], v[76:79], v[92:95]
v_exp_f32 v116, v116
v_mfma_f32_16x16x32_bf16 v[92:95], v[8:11], v[60:63], v[92:95]
ds_read_b128 v[24:27], %[kaddr0] offset:31744
v_add_f32 v48, v48, v138
v_mfma_f32_16x16x32_bf16 v[92:95], v[36:39], a[248:251], v[92:95]
v_exp_f32 v117, v117
v_mfma_f32_16x16x32_bf16 v[88:91], v[36:39], a[244:247], v[88:91]
buffer_load_dwordx4 v46, %[srdK], s76 offen offset:2048 lds
v_mfma_f32_16x16x32_bf16 v[88:91], v[8:11], v[56:59], v[88:91]
v_add_f32 v48, v48, v139
v_cvt_pk_bf16_f32 v171, v138, v139
v_mfma_f32_16x16x32_bf16 v[88:91], v[12:15], v[72:75], v[88:91]
ds_read_b128 v[28:31], v40 offset:31744
s_add_u32 m0, s81, 61440
v_mfma_f32_16x16x32_bf16 v[84:87], v[12:15], v[68:71], v[84:87]
v_exp_f32 v118, v118
v_mfma_f32_16x16x32_bf16 v[84:87], v[8:11], v[52:55], v[84:87]
v_add_f32 v47, v47, v116
v_add_f32 v47, v47, v117
v_mfma_f32_16x16x32_bf16 v[84:87], v[36:39], a[240:243], v[84:87]
v_add_f32 v47, v47, v118
s_waitcnt lgkmcnt(1)
v_mfma_f32_16x16x32_bf16 v[100:103], v[16:19], a[192:195], v[180:183]
ds_read_b128 v[32:35], %[kaddr0] offset:32768
v_cvt_pk_bf16_f32 v164, v116, v117
v_mfma_f32_16x16x32_bf16 v[100:103], v[20:23], a[208:211], v[100:103]
v_exp_f32 v119, v119
v_mfma_f32_16x16x32_bf16 v[100:103], v[24:27], a[224:227], v[100:103]
buffer_load_dwordx4 v44, %[srdV], s76 offen offset:0 lds
v_mfma_f32_16x16x32_bf16 v[104:107], v[24:27], a[228:231], v[184:187]
v_exp_f32 v132, v132
v_mfma_f32_16x16x32_bf16 v[104:107], v[20:23], a[212:215], v[104:107]
ds_read_b128 v[36:39], v40 offset:32768
v_add_f32 v47, v47, v119
v_mfma_f32_16x16x32_bf16 v[104:107], v[16:19], a[196:199], v[104:107]
v_exp_f32 v133, v133
v_mfma_f32_16x16x32_bf16 v[108:111], v[16:19], a[200:203], v[188:191]
v_cvt_pk_bf16_f32 v165, v118, v119
v_add_f32 v47, v47, v132
v_mfma_f32_16x16x32_bf16 v[108:111], v[20:23], a[216:219], v[108:111]
v_exp_f32 v134, v134
v_mfma_f32_16x16x32_bf16 v[108:111], v[24:27], a[232:235], v[108:111]
ds_read_b64_tr_b16 v[8:9], %[vaddr0] offset:24576
ds_read_b64_tr_b16 v[10:11], %[vaddr0] offset:30720
v_mfma_f32_16x16x32_bf16 v[112:115], v[24:27], a[236:239], v[192:195]
v_exp_f32 v135, v135
v_mfma_f32_16x16x32_bf16 v[112:115], v[20:23], a[220:223], v[112:115]
buffer_load_dwordx4 v45, %[srdV], s76 offen offset:1024 lds
v_mfma_f32_16x16x32_bf16 v[112:115], v[16:19], a[204:207], v[112:115]
v_add_f32 v47, v47, v133
s_waitcnt lgkmcnt(2)
v_mfma_f32_16x16x32_bf16 v[112:115], v[28:31], a[252:255], v[112:115]
ds_read_b64_tr_b16 v[12:13], v41 offset:24576
ds_read_b64_tr_b16 v[14:15], v41 offset:30720
v_mfma_f32_16x16x32_bf16 v[112:115], v[32:35], v[64:67], v[112:115]
v_add_f32 v47, v47, v134
v_add_f32 v47, v47, v135
v_mfma_f32_16x16x32_bf16 v[112:115], v[36:39], v[80:83], v[112:115]
v_cvt_pk_bf16_f32 v166, v132, v133
v_cvt_pk_bf16_f32 v167, v134, v135
v_mfma_f32_16x16x32_bf16 v[108:111], v[36:39], v[76:79], v[108:111]
v_mfma_f32_16x16x32_bf16 v[108:111], v[32:35], v[60:63], v[108:111]
ds_read_b64_tr_b16 v[16:17], v42 offset:24576
ds_read_b64_tr_b16 v[18:19], v42 offset:30720
v_mfma_f32_16x16x32_bf16 v[108:111], v[28:31], a[248:251], v[108:111]
v_mfma_f32_16x16x32_bf16 v[104:107], v[28:31], a[244:247], v[104:107]
buffer_load_dwordx4 v46, %[srdV], s76 offen offset:2048 lds
v_mfma_f32_16x16x32_bf16 v[104:107], v[32:35], v[56:59], v[104:107]
v_mfma_f32_16x16x32_bf16 v[104:107], v[36:39], v[72:75], v[104:107]
ds_read_b64_tr_b16 v[20:21], v43 offset:24576
ds_read_b64_tr_b16 v[22:23], v43 offset:30720
v_mfma_f32_16x16x32_bf16 v[100:103], v[36:39], v[68:71], v[100:103]
v_mfma_f32_16x16x32_bf16 v[100:103], v[32:35], v[52:55], v[100:103]
s_add_u32 s76, s76, 12288
v_mfma_f32_16x16x32_bf16 v[100:103], v[28:31], a[240:243], v[100:103]
s_waitcnt lgkmcnt(4)
v_mfma_f32_16x16x32_bf16 a[0:3], v[8:11], v[164:167], a[0:3]
ds_read_b64_tr_b16 v[24:25], %[vaddr0] offset:25600
ds_read_b64_tr_b16 v[26:27], %[vaddr0] offset:31744
v_mfma_f32_16x16x32_bf16 a[16:19], v[12:15], v[164:167], a[16:19]
v_mfma_f32_16x16x32_bf16 a[20:23], v[12:15], v[168:171], a[20:23]
buffer_load_dword v236, v240, %[srdS], s92 offen
v_mfma_f32_16x16x32_bf16 a[4:7], v[8:11], v[168:171], a[4:7]
v_exp_f32 v96, v96
v_mfma_f32_16x16x32_bf16 a[8:11], v[8:11], v[172:175], a[8:11]
ds_read_b64_tr_b16 v[28:29], v41 offset:25600
ds_read_b64_tr_b16 v[30:31], v41 offset:31744
v_mfma_f32_16x16x32_bf16 a[24:27], v[12:15], v[172:175], a[24:27]
v_exp_f32 v97, v97
v_mfma_f32_16x16x32_bf16 a[28:31], v[12:15], v[176:179], a[28:31]
v_exp_f32 v98, v98
v_mfma_f32_16x16x32_bf16 a[12:15], v[8:11], v[176:179], a[12:15]
v_add_f32 v50, v50, v96
s_waitcnt lgkmcnt(4)
v_mfma_f32_16x16x32_bf16 a[44:47], v[16:19], v[176:179], a[44:47]
ds_read_b64_tr_b16 v[32:33], v42 offset:25600
ds_read_b64_tr_b16 v[34:35], v42 offset:31744
v_mfma_f32_16x16x32_bf16 a[60:63], v[20:23], v[176:179], a[60:63]
v_exp_f32 v99, v99
v_mfma_f32_16x16x32_bf16 a[56:59], v[20:23], v[172:175], a[56:59]
buffer_load_dword v237, v241, %[srdG], s79 offen
v_mfma_f32_16x16x32_bf16 a[40:43], v[16:19], v[172:175], a[40:43]
v_add_f32 v50, v50, v97
v_add_f32 v50, v50, v98
v_mfma_f32_16x16x32_bf16 a[36:39], v[16:19], v[168:171], a[36:39]
ds_read_b64_tr_b16 v[36:37], v43 offset:25600
ds_read_b64_tr_b16 v[38:39], v43 offset:31744
v_mfma_f32_16x16x32_bf16 a[52:55], v[20:23], v[168:171], a[52:55]
v_exp_f32 v112, v112
v_mfma_f32_16x16x32_bf16 a[48:51], v[20:23], v[164:167], a[48:51]
v_add_f32 v50, v50, v99
v_cvt_pk_bf16_f32 v160, v96, v97
v_mfma_f32_16x16x32_bf16 a[32:35], v[16:19], v[164:167], a[32:35]
v_cvt_pk_bf16_f32 v161, v98, v99
s_waitcnt lgkmcnt(4)
v_mfma_f32_16x16x32_bf16 a[64:67], v[24:27], v[164:167], a[64:67]
ds_read_b64_tr_b16 v[8:9], %[vaddr0] offset:26624
ds_read_b64_tr_b16 v[10:11], %[vaddr0] offset:32768
v_mfma_f32_16x16x32_bf16 a[80:83], v[28:31], v[164:167], a[80:83]
v_exp_f32 v113, v113
v_mfma_f32_16x16x32_bf16 a[84:87], v[28:31], v[168:171], a[84:87]
v_mfma_f32_16x16x32_bf16 a[68:71], v[24:27], v[168:171], a[68:71]
v_exp_f32 v114, v114
v_mfma_f32_16x16x32_bf16 a[72:75], v[24:27], v[172:175], a[72:75]
ds_read_b64_tr_b16 v[12:13], v41 offset:26624
ds_read_b64_tr_b16 v[14:15], v41 offset:32768
s_add_u32 s92, s92, 3840
s_add_u32 s79, s79, 2560
v_mfma_f32_16x16x32_bf16 a[88:91], v[28:31], v[172:175], a[88:91]
v_add_f32 v50, v50, v112
v_add_f32 v50, v50, v113
v_mfma_f32_16x16x32_bf16 a[92:95], v[28:31], v[176:179], a[92:95]
v_exp_f32 v115, v115
v_mfma_f32_16x16x32_bf16 a[76:79], v[24:27], v[176:179], a[76:79]
v_add_f32 v50, v50, v114
s_waitcnt lgkmcnt(4)
v_mfma_f32_16x16x32_bf16 a[108:111], v[32:35], v[176:179], a[108:111]
ds_read_b64_tr_b16 v[16:17], v42 offset:26624
ds_read_b64_tr_b16 v[18:19], v42 offset:32768
v_mfma_f32_16x16x32_bf16 a[124:127], v[36:39], v[176:179], a[124:127]
v_exp_f32 v92, v92
v_mfma_f32_16x16x32_bf16 a[120:123], v[36:39], v[172:175], a[120:123]
v_add_f32 v50, v50, v115
v_cvt_pk_bf16_f32 v162, v112, v113
v_mfma_f32_16x16x32_bf16 a[104:107], v[32:35], v[172:175], a[104:107]
v_exp_f32 v93, v93
v_mfma_f32_16x16x32_bf16 a[100:103], v[32:35], v[168:171], a[100:103]
ds_read_b64_tr_b16 v[20:21], v43 offset:26624
ds_read_b64_tr_b16 v[22:23], v43 offset:32768
v_mfma_f32_16x16x32_bf16 a[116:119], v[36:39], v[168:171], a[116:119]
v_cvt_pk_bf16_f32 v163, v114, v115
v_add_f32 v49, v49, v92
v_mfma_f32_16x16x32_bf16 a[112:115], v[36:39], v[164:167], a[112:115]
v_exp_f32 v94, v94
v_mfma_f32_16x16x32_bf16 a[96:99], v[32:35], v[164:167], a[96:99]
v_add_f32 v49, v49, v93
s_waitcnt lgkmcnt(4)
v_mfma_f32_16x16x32_bf16 a[128:131], v[8:11], v[164:167], a[128:131]
s_waitcnt vmcnt(18) lgkmcnt(0)
s_barrier
)"
R"(ds_read_b128 v[24:27], %[kaddr0] offset:36864
v_mfma_f32_16x16x32_bf16 a[144:147], v[12:15], v[164:167], a[144:147]
v_exp_f32 v95, v95
v_mfma_f32_16x16x32_bf16 a[148:151], v[12:15], v[168:171], a[148:151]
v_add_f32 v49, v49, v94
v_add_f32 v49, v49, v95
v_mfma_f32_16x16x32_bf16 a[132:135], v[8:11], v[168:171], a[132:135]
v_exp_f32 v108, v108
v_mfma_f32_16x16x32_bf16 a[136:139], v[8:11], v[172:175], a[136:139]
ds_read_b128 v[28:31], v40 offset:36864
v_cvt_pk_bf16_f32 v156, v92, v93
v_mfma_f32_16x16x32_bf16 a[152:155], v[12:15], v[172:175], a[152:155]
v_exp_f32 v109, v109
v_mfma_f32_16x16x32_bf16 a[156:159], v[12:15], v[176:179], a[156:159]
v_cvt_pk_bf16_f32 v157, v94, v95
v_add_f32 v49, v49, v108
v_mfma_f32_16x16x32_bf16 a[140:143], v[8:11], v[176:179], a[140:143]
v_add_f32 v49, v49, v109
v_mfma_f32_16x16x32_bf16 a[172:175], v[16:19], v[176:179], a[172:175]
ds_read_b128 v[32:35], %[kaddr0] offset:37888
v_cvt_pk_bf16_f32 v158, v108, v109
v_mfma_f32_16x16x32_bf16 a[188:191], v[20:23], v[176:179], a[188:191]
v_exp_f32 v110, v110
v_mfma_f32_16x16x32_bf16 a[184:187], v[20:23], v[172:175], a[184:187]
v_exp_f32 v111, v111
v_mfma_f32_16x16x32_bf16 a[168:171], v[16:19], v[172:175], a[168:171]
v_exp_f32 v88, v88
v_mfma_f32_16x16x32_bf16 a[164:167], v[16:19], v[168:171], a[164:167]
ds_read_b128 v[36:39], v40 offset:37888
v_add_f32 v49, v49, v110
v_mfma_f32_16x16x32_bf16 a[180:183], v[20:23], v[168:171], a[180:183]
v_exp_f32 v89, v89
v_mfma_f32_16x16x32_bf16 a[176:179], v[20:23], v[164:167], a[176:179]
v_add_f32 v49, v49, v111
v_cvt_pk_bf16_f32 v159, v110, v111
v_mfma_f32_16x16x32_bf16 a[160:163], v[16:19], v[164:167], a[160:163]
v_add_f32 v48, v48, v88
s_waitcnt lgkmcnt(1)
v_mfma_f32_16x16x32_bf16 v[116:119], v[24:27], a[192:195], v[180:183]
ds_read_b128 v[8:11], %[kaddr0] offset:38912
s_add_u32 m0, s81, 24576
v_mfma_f32_16x16x32_bf16 v[116:119], v[28:31], a[208:211], v[116:119]
v_exp_f32 v90, v90
v_mfma_f32_16x16x32_bf16 v[116:119], v[32:35], a[224:227], v[116:119]
buffer_load_dwordx4 v44, %[srdK], s76 offen offset:0 lds
v_mfma_f32_16x16x32_bf16 v[120:123], v[32:35], a[228:231], v[184:187]
v_exp_f32 v91, v91
v_mfma_f32_16x16x32_bf16 v[120:123], v[28:31], a[212:215], v[120:123]
ds_read_b128 v[12:15], v40 offset:38912
v_add_f32 v48, v48, v89
v_mfma_f32_16x16x32_bf16 v[120:123], v[24:27], a[196:199], v[120:123]
v_add_f32 v48, v48, v90
v_add_f32 v48, v48, v91
v_mfma_f32_16x16x32_bf16 v[124:127], v[24:27], a[200:203], v[188:191]
v_exp_f32 v104, v104
v_mfma_f32_16x16x32_bf16 v[124:127], v[28:31], a[216:219], v[124:127]
v_exp_f32 v105, v105
v_mfma_f32_16x16x32_bf16 v[124:127], v[32:35], a[232:235], v[124:127]
ds_read_b128 v[16:19], %[kaddr0] offset:43008
v_cvt_pk_bf16_f32 v152, v88, v89
v_mfma_f32_16x16x32_bf16 v[128:131], v[32:35], a[236:239], v[192:195]
v_cvt_pk_bf16_f32 v153, v90, v91
v_add_f32 v48, v48, v104
v_mfma_f32_16x16x32_bf16 v[128:131], v[28:31], a[220:223], v[128:131]
buffer_load_dwordx4 v45, %[srdK], s76 offen offset:1024 lds
v_mfma_f32_16x16x32_bf16 v[128:131], v[24:27], a[204:207], v[128:131]
v_add_f32 v48, v48, v105
s_waitcnt lgkmcnt(1)
v_mfma_f32_16x16x32_bf16 v[128:131], v[36:39], a[252:255], v[128:131]
ds_read_b128 v[20:23], v40 offset:43008
v_cvt_pk_bf16_f32 v154, v104, v105
v_mfma_f32_16x16x32_bf16 v[128:131], v[8:11], v[64:67], v[128:131]
v_exp_f32 v106, v106
v_mfma_f32_16x16x32_bf16 v[128:131], v[12:15], v[80:83], v[128:131]
v_exp_f32 v107, v107
v_mfma_f32_16x16x32_bf16 v[124:127], v[12:15], v[76:79], v[124:127]
v_exp_f32 v84, v84
v_mfma_f32_16x16x32_bf16 v[124:127], v[8:11], v[60:63], v[124:127]
ds_read_b128 v[24:27], %[kaddr0] offset:44032
v_add_f32 v48, v48, v106
v_mfma_f32_16x16x32_bf16 v[124:127], v[36:39], a[248:251], v[124:127]
v_exp_f32 v85, v85
v_mfma_f32_16x16x32_bf16 v[120:123], v[36:39], a[244:247], v[120:123]
buffer_load_dwordx4 v46, %[srdK], s76 offen offset:2048 lds
v_mfma_f32_16x16x32_bf16 v[120:123], v[8:11], v[56:59], v[120:123]
v_add_f32 v48, v48, v107
v_cvt_pk_bf16_f32 v155, v106, v107
v_mfma_f32_16x16x32_bf16 v[120:123], v[12:15], v[72:75], v[120:123]
ds_read_b128 v[28:31], v40 offset:44032
s_add_u32 m0, s81, 73728
v_mfma_f32_16x16x32_bf16 v[116:119], v[12:15], v[68:71], v[116:119]
v_exp_f32 v86, v86
v_mfma_f32_16x16x32_bf16 v[116:119], v[8:11], v[52:55], v[116:119]
v_add_f32 v47, v47, v84
v_add_f32 v47, v47, v85
v_mfma_f32_16x16x32_bf16 v[116:119], v[36:39], a[240:243], v[116:119]
v_add_f32 v47, v47, v86
s_waitcnt lgkmcnt(1)
v_mfma_f32_16x16x32_bf16 v[132:135], v[16:19], a[192:195], v[180:183]
ds_read_b128 v[32:35], %[kaddr0] offset:45056
v_cvt_pk_bf16_f32 v148, v84, v85
v_mfma_f32_16x16x32_bf16 v[132:135], v[20:23], a[208:211], v[132:135]
v_exp_f32 v87, v87
v_mfma_f32_16x16x32_bf16 v[132:135], v[24:27], a[224:227], v[132:135]
buffer_load_dwordx4 v44, %[srdV], s76 offen offset:0 lds
v_mfma_f32_16x16x32_bf16 v[136:139], v[24:27], a[228:231], v[184:187]
v_exp_f32 v100, v100
v_mfma_f32_16x16x32_bf16 v[136:139], v[20:23], a[212:215], v[136:139]
ds_read_b128 v[36:39], v40 offset:45056
v_add_f32 v47, v47, v87
v_mfma_f32_16x16x32_bf16 v[136:139], v[16:19], a[196:199], v[136:139]
v_exp_f32 v101, v101
v_mfma_f32_16x16x32_bf16 v[140:143], v[16:19], a[200:203], v[188:191]
v_cvt_pk_bf16_f32 v149, v86, v87
v_add_f32 v47, v47, v100
v_mfma_f32_16x16x32_bf16 v[140:143], v[20:23], a[216:219], v[140:143]
v_exp_f32 v102, v102
v_mfma_f32_16x16x32_bf16 v[140:143], v[24:27], a[232:235], v[140:143]
ds_read_b64_tr_b16 v[8:9], %[vaddr0] offset:36864
ds_read_b64_tr_b16 v[10:11], %[vaddr0] offset:43008
v_mfma_f32_16x16x32_bf16 v[144:147], v[24:27], a[236:239], v[192:195]
v_exp_f32 v103, v103
v_mfma_f32_16x16x32_bf16 v[144:147], v[20:23], a[220:223], v[144:147]
buffer_load_dwordx4 v45, %[srdV], s76 offen offset:1024 lds
v_mfma_f32_16x16x32_bf16 v[144:147], v[16:19], a[204:207], v[144:147]
v_add_f32 v47, v47, v101
s_waitcnt lgkmcnt(2)
v_mfma_f32_16x16x32_bf16 v[144:147], v[28:31], a[252:255], v[144:147]
ds_read_b64_tr_b16 v[12:13], v41 offset:36864
ds_read_b64_tr_b16 v[14:15], v41 offset:43008
v_mfma_f32_16x16x32_bf16 v[144:147], v[32:35], v[64:67], v[144:147]
v_add_f32 v47, v47, v102
v_add_f32 v47, v47, v103
v_mfma_f32_16x16x32_bf16 v[144:147], v[36:39], v[80:83], v[144:147]
v_cvt_pk_bf16_f32 v150, v100, v101
v_cvt_pk_bf16_f32 v151, v102, v103
v_mfma_f32_16x16x32_bf16 v[140:143], v[36:39], v[76:79], v[140:143]
v_mfma_f32_16x16x32_bf16 v[140:143], v[32:35], v[60:63], v[140:143]
ds_read_b64_tr_b16 v[16:17], v42 offset:36864
ds_read_b64_tr_b16 v[18:19], v42 offset:43008
v_mfma_f32_16x16x32_bf16 v[140:143], v[28:31], a[248:251], v[140:143]
v_mfma_f32_16x16x32_bf16 v[136:139], v[28:31], a[244:247], v[136:139]
buffer_load_dwordx4 v46, %[srdV], s76 offen offset:2048 lds
v_mfma_f32_16x16x32_bf16 v[136:139], v[32:35], v[56:59], v[136:139]
v_mfma_f32_16x16x32_bf16 v[136:139], v[36:39], v[72:75], v[136:139]
ds_read_b64_tr_b16 v[20:21], v43 offset:36864
ds_read_b64_tr_b16 v[22:23], v43 offset:43008
v_mfma_f32_16x16x32_bf16 v[132:135], v[36:39], v[68:71], v[132:135]
v_mfma_f32_16x16x32_bf16 v[132:135], v[32:35], v[52:55], v[132:135]
s_add_u32 s76, s76, 12288
v_mfma_f32_16x16x32_bf16 v[132:135], v[28:31], a[240:243], v[132:135]
s_waitcnt lgkmcnt(4)
v_mfma_f32_16x16x32_bf16 a[0:3], v[8:11], v[148:151], a[0:3]
ds_read_b64_tr_b16 v[24:25], %[vaddr0] offset:37888
ds_read_b64_tr_b16 v[26:27], %[vaddr0] offset:44032
v_mfma_f32_16x16x32_bf16 a[16:19], v[12:15], v[148:151], a[16:19]
v_mfma_f32_16x16x32_bf16 a[20:23], v[12:15], v[152:155], a[20:23]
buffer_load_dword v236, v240, %[srdS], s92 offen
v_mfma_f32_16x16x32_bf16 a[4:7], v[8:11], v[152:155], a[4:7]
v_exp_f32 v128, v128
v_mfma_f32_16x16x32_bf16 a[8:11], v[8:11], v[156:159], a[8:11]
ds_read_b64_tr_b16 v[28:29], v41 offset:37888
ds_read_b64_tr_b16 v[30:31], v41 offset:44032
v_mfma_f32_16x16x32_bf16 a[24:27], v[12:15], v[156:159], a[24:27]
v_exp_f32 v129, v129
v_mfma_f32_16x16x32_bf16 a[28:31], v[12:15], v[160:163], a[28:31]
v_exp_f32 v130, v130
v_mfma_f32_16x16x32_bf16 a[12:15], v[8:11], v[160:163], a[12:15]
v_add_f32 v50, v50, v128
s_waitcnt lgkmcnt(4)
v_mfma_f32_16x16x32_bf16 a[44:47], v[16:19], v[160:163], a[44:47]
ds_read_b64_tr_b16 v[32:33], v42 offset:37888
ds_read_b64_tr_b16 v[34:35], v42 offset:44032
v_mfma_f32_16x16x32_bf16 a[60:63], v[20:23], v[160:163], a[60:63]
v_exp_f32 v131, v131
v_mfma_f32_16x16x32_bf16 a[56:59], v[20:23], v[156:159], a[56:59]
buffer_load_dword v237, v241, %[srdG], s79 offen
v_mfma_f32_16x16x32_bf16 a[40:43], v[16:19], v[156:159], a[40:43]
v_add_f32 v50, v50, v129
v_add_f32 v50, v50, v130
v_mfma_f32_16x16x32_bf16 a[36:39], v[16:19], v[152:155], a[36:39]
ds_read_b64_tr_b16 v[36:37], v43 offset:37888
ds_read_b64_tr_b16 v[38:39], v43 offset:44032
v_mfma_f32_16x16x32_bf16 a[52:55], v[20:23], v[152:155], a[52:55]
v_exp_f32 v144, v144
v_mfma_f32_16x16x32_bf16 a[48:51], v[20:23], v[148:151], a[48:51]
v_add_f32 v50, v50, v131
v_cvt_pk_bf16_f32 v176, v128, v129
v_mfma_f32_16x16x32_bf16 a[32:35], v[16:19], v[148:151], a[32:35]
v_cvt_pk_bf16_f32 v177, v130, v131
s_waitcnt lgkmcnt(4)
v_mfma_f32_16x16x32_bf16 a[64:67], v[24:27], v[148:151], a[64:67]
ds_read_b64_tr_b16 v[8:9], %[vaddr0] offset:38912
ds_read_b64_tr_b16 v[10:11], %[vaddr0] offset:45056
v_mfma_f32_16x16x32_bf16 a[80:83], v[28:31], v[148:151], a[80:83]
v_exp_f32 v145, v145
v_mfma_f32_16x16x32_bf16 a[84:87], v[28:31], v[152:155], a[84:87]
v_mfma_f32_16x16x32_bf16 a[68:71], v[24:27], v[152:155], a[68:71]
v_exp_f32 v146, v146
v_mfma_f32_16x16x32_bf16 a[72:75], v[24:27], v[156:159], a[72:75]
)"
R"(ds_read_b64_tr_b16 v[12:13], v41 offset:38912
ds_read_b64_tr_b16 v[14:15], v41 offset:45056
s_add_u32 s92, s92, 3840
s_add_u32 s79, s79, 2560
v_mfma_f32_16x16x32_bf16 a[88:91], v[28:31], v[156:159], a[88:91]
v_add_f32 v50, v50, v144
v_add_f32 v50, v50, v145
v_mfma_f32_16x16x32_bf16 a[92:95], v[28:31], v[160:163], a[92:95]
v_exp_f32 v147, v147
v_mfma_f32_16x16x32_bf16 a[76:79], v[24:27], v[160:163], a[76:79]
v_add_f32 v50, v50, v146
s_waitcnt lgkmcnt(4)
v_mfma_f32_16x16x32_bf16 a[108:111], v[32:35], v[160:163], a[108:111]
ds_read_b64_tr_b16 v[16:17], v42 offset:38912
ds_read_b64_tr_b16 v[18:19], v42 offset:45056
v_mfma_f32_16x16x32_bf16 a[124:127], v[36:39], v[160:163], a[124:127]
v_exp_f32 v124, v124
v_mfma_f32_16x16x32_bf16 a[120:123], v[36:39], v[156:159], a[120:123]
v_add_f32 v50, v50, v147
v_cvt_pk_bf16_f32 v178, v144, v145
v_mfma_f32_16x16x32_bf16 a[104:107], v[32:35], v[156:159], a[104:107]
v_exp_f32 v125, v125
v_mfma_f32_16x16x32_bf16 a[100:103], v[32:35], v[152:155], a[100:103]
ds_read_b64_tr_b16 v[20:21], v43 offset:38912
ds_read_b64_tr_b16 v[22:23], v43 offset:45056
v_mfma_f32_16x16x32_bf16 a[116:119], v[36:39], v[152:155], a[116:119]
v_cvt_pk_bf16_f32 v179, v146, v147
v_add_f32 v49, v49, v124
v_mfma_f32_16x16x32_bf16 a[112:115], v[36:39], v[148:151], a[112:115]
v_exp_f32 v126, v126
v_mfma_f32_16x16x32_bf16 a[96:99], v[32:35], v[148:151], a[96:99]
v_add_f32 v49, v49, v125
s_waitcnt lgkmcnt(4)
v_mfma_f32_16x16x32_bf16 a[128:131], v[8:11], v[148:151], a[128:131]
s_waitcnt vmcnt(18) lgkmcnt(0)
s_barrier
ds_read_b128 v[24:27], %[kaddr0] offset:0
v_mfma_f32_16x16x32_bf16 a[144:147], v[12:15], v[148:151], a[144:147]
v_exp_f32 v127, v127
v_mfma_f32_16x16x32_bf16 a[148:151], v[12:15], v[152:155], a[148:151]
v_add_f32 v49, v49, v126
v_add_f32 v49, v49, v127
v_mfma_f32_16x16x32_bf16 a[132:135], v[8:11], v[152:155], a[132:135]
v_exp_f32 v140, v140
v_mfma_f32_16x16x32_bf16 a[136:139], v[8:11], v[156:159], a[136:139]
ds_read_b128 v[28:31], v40 offset:0
v_cvt_pk_bf16_f32 v172, v124, v125
v_mfma_f32_16x16x32_bf16 a[152:155], v[12:15], v[156:159], a[152:155]
v_exp_f32 v141, v141
v_mfma_f32_16x16x32_bf16 a[156:159], v[12:15], v[160:163], a[156:159]
v_cvt_pk_bf16_f32 v173, v126, v127
v_add_f32 v49, v49, v140
v_mfma_f32_16x16x32_bf16 a[140:143], v[8:11], v[160:163], a[140:143]
v_add_f32 v49, v49, v141
v_mfma_f32_16x16x32_bf16 a[172:175], v[16:19], v[160:163], a[172:175]
ds_read_b128 v[32:35], %[kaddr0] offset:1024
v_cvt_pk_bf16_f32 v174, v140, v141
v_mfma_f32_16x16x32_bf16 a[188:191], v[20:23], v[160:163], a[188:191]
v_exp_f32 v142, v142
v_mfma_f32_16x16x32_bf16 a[184:187], v[20:23], v[156:159], a[184:187]
v_exp_f32 v143, v143
v_mfma_f32_16x16x32_bf16 a[168:171], v[16:19], v[156:159], a[168:171]
v_exp_f32 v120, v120
v_mfma_f32_16x16x32_bf16 a[164:167], v[16:19], v[152:155], a[164:167]
ds_read_b128 v[36:39], v40 offset:1024
v_add_f32 v49, v49, v142
v_mfma_f32_16x16x32_bf16 a[180:183], v[20:23], v[152:155], a[180:183]
v_exp_f32 v121, v121
v_mfma_f32_16x16x32_bf16 a[176:179], v[20:23], v[148:151], a[176:179]
v_add_f32 v49, v49, v143
v_cvt_pk_bf16_f32 v175, v142, v143
v_mfma_f32_16x16x32_bf16 a[160:163], v[16:19], v[148:151], a[160:163]
v_add_f32 v48, v48, v120
s_sub_u32 s80, s80, 1
s_cmp_lg_u32 s80, 0
s_cbranch_scc1 .Lk2_loopa
.Lk2_loopa_end:
s_waitcnt lgkmcnt(1)
v_mfma_f32_16x16x32_bf16 v[84:87], v[24:27], a[192:195], v[180:183]
ds_read_b128 v[8:11], %[kaddr0] offset:2048
s_add_u32 m0, s81, 36864
v_mfma_f32_16x16x32_bf16 v[84:87], v[28:31], a[208:211], v[84:87]
v_exp_f32 v122, v122
v_mfma_f32_16x16x32_bf16 v[84:87], v[32:35], a[224:227], v[84:87]
buffer_load_dwordx4 v44, %[srdK], s76 offen offset:0 lds
v_mfma_f32_16x16x32_bf16 v[88:91], v[32:35], a[228:231], v[184:187]
v_exp_f32 v123, v123
v_mfma_f32_16x16x32_bf16 v[88:91], v[28:31], a[212:215], v[88:91]
ds_read_b128 v[12:15], v40 offset:2048
v_add_f32 v48, v48, v121
v_mfma_f32_16x16x32_bf16 v[88:91], v[24:27], a[196:199], v[88:91]
v_add_f32 v48, v48, v122
v_add_f32 v48, v48, v123
v_mfma_f32_16x16x32_bf16 v[92:95], v[24:27], a[200:203], v[188:191]
v_exp_f32 v136, v136
v_mfma_f32_16x16x32_bf16 v[92:95], v[28:31], a[216:219], v[92:95]
v_exp_f32 v137, v137
v_mfma_f32_16x16x32_bf16 v[92:95], v[32:35], a[232:235], v[92:95]
ds_read_b128 v[16:19], %[kaddr0] offset:6144
v_cvt_pk_bf16_f32 v168, v120, v121
v_mfma_f32_16x16x32_bf16 v[96:99], v[32:35], a[236:239], v[192:195]
v_cvt_pk_bf16_f32 v169, v122, v123
v_add_f32 v48, v48, v136
v_mfma_f32_16x16x32_bf16 v[96:99], v[28:31], a[220:223], v[96:99]
buffer_load_dwordx4 v45, %[srdK], s76 offen offset:1024 lds
v_mfma_f32_16x16x32_bf16 v[96:99], v[24:27], a[204:207], v[96:99]
v_add_f32 v48, v48, v137
s_waitcnt lgkmcnt(1)
v_mfma_f32_16x16x32_bf16 v[96:99], v[36:39], a[252:255], v[96:99]
ds_read_b128 v[20:23], v40 offset:6144
v_cvt_pk_bf16_f32 v170, v136, v137
v_mfma_f32_16x16x32_bf16 v[96:99], v[8:11], v[64:67], v[96:99]
v_exp_f32 v138, v138
v_mfma_f32_16x16x32_bf16 v[96:99], v[12:15], v[80:83], v[96:99]
v_exp_f32 v139, v139
v_mfma_f32_16x16x32_bf16 v[92:95], v[12:15], v[76:79], v[92:95]
v_exp_f32 v116, v116
v_mfma_f32_16x16x32_bf16 v[92:95], v[8:11], v[60:63], v[92:95]
ds_read_b128 v[24:27], %[kaddr0] offset:7168
v_add_f32 v48, v48, v138
v_mfma_f32_16x16x32_bf16 v[92:95], v[36:39], a[248:251], v[92:95]
v_exp_f32 v117, v117
v_mfma_f32_16x16x32_bf16 v[88:91], v[36:39], a[244:247], v[88:91]
buffer_load_dwordx4 v46, %[srdK], s76 offen offset:2048 lds
v_mfma_f32_16x16x32_bf16 v[88:91], v[8:11], v[56:59], v[88:91]
v_add_f32 v48, v48, v139
v_cvt_pk_bf16_f32 v171, v138, v139
v_mfma_f32_16x16x32_bf16 v[88:91], v[12:15], v[72:75], v[88:91]
ds_read_b128 v[28:31], v40 offset:7168
s_add_u32 m0, s81, 86016
v_mfma_f32_16x16x32_bf16 v[84:87], v[12:15], v[68:71], v[84:87]
v_exp_f32 v118, v118
v_mfma_f32_16x16x32_bf16 v[84:87], v[8:11], v[52:55], v[84:87]
v_add_f32 v47, v47, v116
v_add_f32 v47, v47, v117
v_mfma_f32_16x16x32_bf16 v[84:87], v[36:39], a[240:243], v[84:87]
v_add_f32 v47, v47, v118
s_waitcnt lgkmcnt(1)
v_mfma_f32_16x16x32_bf16 v[100:103], v[16:19], a[192:195], v[180:183]
ds_read_b128 v[32:35], %[kaddr0] offset:8192
v_cvt_pk_bf16_f32 v164, v116, v117
v_mfma_f32_16x16x32_bf16 v[100:103], v[20:23], a[208:211], v[100:103]
v_exp_f32 v119, v119
v_mfma_f32_16x16x32_bf16 v[100:103], v[24:27], a[224:227], v[100:103]
buffer_load_dwordx4 v44, %[srdV], s76 offen offset:0 lds
v_mfma_f32_16x16x32_bf16 v[104:107], v[24:27], a[228:231], v[184:187]
v_exp_f32 v132, v132
v_mfma_f32_16x16x32_bf16 v[104:107], v[20:23], a[212:215], v[104:107]
ds_read_b128 v[36:39], v40 offset:8192
v_add_f32 v47, v47, v119
v_mfma_f32_16x16x32_bf16 v[104:107], v[16:19], a[196:199], v[104:107]
v_exp_f32 v133, v133
v_mfma_f32_16x16x32_bf16 v[108:111], v[16:19], a[200:203], v[188:191]
v_cvt_pk_bf16_f32 v165, v118, v119
v_add_f32 v47, v47, v132
v_mfma_f32_16x16x32_bf16 v[108:111], v[20:23], a[216:219], v[108:111]
v_exp_f32 v134, v134
v_mfma_f32_16x16x32_bf16 v[108:111], v[24:27], a[232:235], v[108:111]
ds_read_b64_tr_b16 v[8:9], %[vaddr0] offset:0
ds_read_b64_tr_b16 v[10:11], %[vaddr0] offset:6144
v_mfma_f32_16x16x32_bf16 v[112:115], v[24:27], a[236:239], v[192:195]
v_exp_f32 v135, v135
v_mfma_f32_16x16x32_bf16 v[112:115], v[20:23], a[220:223], v[112:115]
buffer_load_dwordx4 v45, %[srdV], s76 offen offset:1024 lds
v_mfma_f32_16x16x32_bf16 v[112:115], v[16:19], a[204:207], v[112:115]
v_add_f32 v47, v47, v133
s_waitcnt lgkmcnt(2)
v_mfma_f32_16x16x32_bf16 v[112:115], v[28:31], a[252:255], v[112:115]
ds_read_b64_tr_b16 v[12:13], v41 offset:0
ds_read_b64_tr_b16 v[14:15], v41 offset:6144
v_mfma_f32_16x16x32_bf16 v[112:115], v[32:35], v[64:67], v[112:115]
v_add_f32 v47, v47, v134
v_add_f32 v47, v47, v135
v_mfma_f32_16x16x32_bf16 v[112:115], v[36:39], v[80:83], v[112:115]
v_cvt_pk_bf16_f32 v166, v132, v133
v_cvt_pk_bf16_f32 v167, v134, v135
v_mfma_f32_16x16x32_bf16 v[108:111], v[36:39], v[76:79], v[108:111]
v_mfma_f32_16x16x32_bf16 v[108:111], v[32:35], v[60:63], v[108:111]
ds_read_b64_tr_b16 v[16:17], v42 offset:0
ds_read_b64_tr_b16 v[18:19], v42 offset:6144
v_mfma_f32_16x16x32_bf16 v[108:111], v[28:31], a[248:251], v[108:111]
v_mfma_f32_16x16x32_bf16 v[104:107], v[28:31], a[244:247], v[104:107]
buffer_load_dwordx4 v46, %[srdV], s76 offen offset:2048 lds
v_mfma_f32_16x16x32_bf16 v[104:107], v[32:35], v[56:59], v[104:107]
v_mfma_f32_16x16x32_bf16 v[104:107], v[36:39], v[72:75], v[104:107]
ds_read_b64_tr_b16 v[20:21], v43 offset:0
ds_read_b64_tr_b16 v[22:23], v43 offset:6144
v_mfma_f32_16x16x32_bf16 v[100:103], v[36:39], v[68:71], v[100:103]
v_mfma_f32_16x16x32_bf16 v[100:103], v[32:35], v[52:55], v[100:103]
s_add_u32 s76, s76, 12288
v_mfma_f32_16x16x32_bf16 v[100:103], v[28:31], a[240:243], v[100:103]
s_waitcnt lgkmcnt(4)
v_mfma_f32_16x16x32_bf16 a[0:3], v[8:11], v[164:167], a[0:3]
ds_read_b64_tr_b16 v[24:25], %[vaddr0] offset:1024
ds_read_b64_tr_b16 v[26:27], %[vaddr0] offset:7168
v_mfma_f32_16x16x32_bf16 a[16:19], v[12:15], v[164:167], a[16:19]
v_mfma_f32_16x16x32_bf16 a[20:23], v[12:15], v[168:171], a[20:23]
global_load_dword v253, v251, s[62:63] offset:128 sc1
v_mfma_f32_16x16x32_bf16 a[4:7], v[8:11], v[168:171], a[4:7]
v_exp_f32 v96, v96
)"
R"(v_mfma_f32_16x16x32_bf16 a[8:11], v[8:11], v[172:175], a[8:11]
ds_read_b64_tr_b16 v[28:29], v41 offset:1024
ds_read_b64_tr_b16 v[30:31], v41 offset:7168
v_mfma_f32_16x16x32_bf16 a[24:27], v[12:15], v[172:175], a[24:27]
v_exp_f32 v97, v97
v_mfma_f32_16x16x32_bf16 a[28:31], v[12:15], v[176:179], a[28:31]
v_exp_f32 v98, v98
v_mfma_f32_16x16x32_bf16 a[12:15], v[8:11], v[176:179], a[12:15]
v_add_f32 v50, v50, v96
s_waitcnt lgkmcnt(4)
v_mfma_f32_16x16x32_bf16 a[44:47], v[16:19], v[176:179], a[44:47]
ds_read_b64_tr_b16 v[32:33], v42 offset:1024
ds_read_b64_tr_b16 v[34:35], v42 offset:7168
v_mfma_f32_16x16x32_bf16 a[60:63], v[20:23], v[176:179], a[60:63]
v_exp_f32 v99, v99
v_mfma_f32_16x16x32_bf16 a[56:59], v[20:23], v[172:175], a[56:59]
v_mfma_f32_16x16x32_bf16 a[40:43], v[16:19], v[172:175], a[40:43]
v_add_f32 v50, v50, v97
v_add_f32 v50, v50, v98
v_mfma_f32_16x16x32_bf16 a[36:39], v[16:19], v[168:171], a[36:39]
ds_read_b64_tr_b16 v[36:37], v43 offset:1024
ds_read_b64_tr_b16 v[38:39], v43 offset:7168
v_mfma_f32_16x16x32_bf16 a[52:55], v[20:23], v[168:171], a[52:55]
v_exp_f32 v112, v112
v_mfma_f32_16x16x32_bf16 a[48:51], v[20:23], v[164:167], a[48:51]
v_add_f32 v50, v50, v99
v_cvt_pk_bf16_f32 v160, v96, v97
v_mfma_f32_16x16x32_bf16 a[32:35], v[16:19], v[164:167], a[32:35]
v_cvt_pk_bf16_f32 v161, v98, v99
s_waitcnt lgkmcnt(4)
v_mfma_f32_16x16x32_bf16 a[64:67], v[24:27], v[164:167], a[64:67]
ds_read_b64_tr_b16 v[8:9], %[vaddr0] offset:2048
ds_read_b64_tr_b16 v[10:11], %[vaddr0] offset:8192
v_mfma_f32_16x16x32_bf16 a[80:83], v[28:31], v[164:167], a[80:83]
v_exp_f32 v113, v113
v_mfma_f32_16x16x32_bf16 a[84:87], v[28:31], v[168:171], a[84:87]
v_mfma_f32_16x16x32_bf16 a[68:71], v[24:27], v[168:171], a[68:71]
v_exp_f32 v114, v114
v_mfma_f32_16x16x32_bf16 a[72:75], v[24:27], v[172:175], a[72:75]
ds_read_b64_tr_b16 v[12:13], v41 offset:2048
ds_read_b64_tr_b16 v[14:15], v41 offset:8192
v_mfma_f32_16x16x32_bf16 a[88:91], v[28:31], v[172:175], a[88:91]
v_add_f32 v50, v50, v112
v_add_f32 v50, v50, v113
v_mfma_f32_16x16x32_bf16 a[92:95], v[28:31], v[176:179], a[92:95]
v_exp_f32 v115, v115
v_mfma_f32_16x16x32_bf16 a[76:79], v[24:27], v[176:179], a[76:79]
v_add_f32 v50, v50, v114
s_waitcnt lgkmcnt(4)
v_mfma_f32_16x16x32_bf16 a[108:111], v[32:35], v[176:179], a[108:111]
ds_read_b64_tr_b16 v[16:17], v42 offset:2048
ds_read_b64_tr_b16 v[18:19], v42 offset:8192
v_mfma_f32_16x16x32_bf16 a[124:127], v[36:39], v[176:179], a[124:127]
v_exp_f32 v92, v92
v_mfma_f32_16x16x32_bf16 a[120:123], v[36:39], v[172:175], a[120:123]
v_add_f32 v50, v50, v115
v_cvt_pk_bf16_f32 v162, v112, v113
v_mfma_f32_16x16x32_bf16 a[104:107], v[32:35], v[172:175], a[104:107]
v_exp_f32 v93, v93
v_mfma_f32_16x16x32_bf16 a[100:103], v[32:35], v[168:171], a[100:103]
ds_read_b64_tr_b16 v[20:21], v43 offset:2048
ds_read_b64_tr_b16 v[22:23], v43 offset:8192
v_mfma_f32_16x16x32_bf16 a[116:119], v[36:39], v[168:171], a[116:119]
v_cvt_pk_bf16_f32 v163, v114, v115
v_add_f32 v49, v49, v92
v_mfma_f32_16x16x32_bf16 a[112:115], v[36:39], v[164:167], a[112:115]
v_exp_f32 v94, v94
v_mfma_f32_16x16x32_bf16 a[96:99], v[32:35], v[164:167], a[96:99]
v_add_f32 v49, v49, v93
s_waitcnt lgkmcnt(4)
v_mfma_f32_16x16x32_bf16 a[128:131], v[8:11], v[164:167], a[128:131]
s_waitcnt vmcnt(17) lgkmcnt(0)
s_barrier
ds_read_b128 v[24:27], %[kaddr0] offset:12288
v_mfma_f32_16x16x32_bf16 a[144:147], v[12:15], v[164:167], a[144:147]
v_exp_f32 v95, v95
v_mfma_f32_16x16x32_bf16 a[148:151], v[12:15], v[168:171], a[148:151]
v_add_f32 v49, v49, v94
v_add_f32 v49, v49, v95
v_mfma_f32_16x16x32_bf16 a[132:135], v[8:11], v[168:171], a[132:135]
v_exp_f32 v108, v108
v_mfma_f32_16x16x32_bf16 a[136:139], v[8:11], v[172:175], a[136:139]
ds_read_b128 v[28:31], v40 offset:12288
v_cvt_pk_bf16_f32 v156, v92, v93
v_mfma_f32_16x16x32_bf16 a[152:155], v[12:15], v[172:175], a[152:155]
v_exp_f32 v109, v109
v_mfma_f32_16x16x32_bf16 a[156:159], v[12:15], v[176:179], a[156:159]
v_cvt_pk_bf16_f32 v157, v94, v95
v_add_f32 v49, v49, v108
v_mfma_f32_16x16x32_bf16 a[140:143], v[8:11], v[176:179], a[140:143]
v_add_f32 v49, v49, v109
v_mfma_f32_16x16x32_bf16 a[172:175], v[16:19], v[176:179], a[172:175]
ds_read_b128 v[32:35], %[kaddr0] offset:13312
v_cvt_pk_bf16_f32 v158, v108, v109
v_mfma_f32_16x16x32_bf16 a[188:191], v[20:23], v[176:179], a[188:191]
v_exp_f32 v110, v110
v_mfma_f32_16x16x32_bf16 a[184:187], v[20:23], v[172:175], a[184:187]
v_exp_f32 v111, v111
v_mfma_f32_16x16x32_bf16 a[168:171], v[16:19], v[172:175], a[168:171]
v_exp_f32 v88, v88
v_mfma_f32_16x16x32_bf16 a[164:167], v[16:19], v[168:171], a[164:167]
ds_read_b128 v[36:39], v40 offset:13312
v_add_f32 v49, v49, v110
v_mfma_f32_16x16x32_bf16 a[180:183], v[20:23], v[168:171], a[180:183]
v_exp_f32 v89, v89
v_mfma_f32_16x16x32_bf16 a[176:179], v[20:23], v[164:167], a[176:179]
v_add_f32 v49, v49, v111
v_cvt_pk_bf16_f32 v159, v110, v111
v_mfma_f32_16x16x32_bf16 a[160:163], v[16:19], v[164:167], a[160:163]
v_add_f32 v48, v48, v88
s_waitcnt lgkmcnt(1)
v_mfma_f32_16x16x32_bf16 v[116:119], v[24:27], a[192:195], v[180:183]
ds_read_b128 v[8:11], %[kaddr0] offset:14336
s_add_u32 m0, s81, 0
v_mfma_f32_16x16x32_bf16 v[116:119], v[28:31], a[208:211], v[116:119]
v_exp_f32 v90, v90
v_mfma_f32_16x16x32_bf16 v[116:119], v[32:35], a[224:227], v[116:119]
buffer_load_dwordx4 v44, %[srdK], s76 offen offset:0 lds
v_mfma_f32_16x16x32_bf16 v[120:123], v[32:35], a[228:231], v[184:187]
v_exp_f32 v91, v91
v_mfma_f32_16x16x32_bf16 v[120:123], v[28:31], a[212:215], v[120:123]
ds_read_b128 v[12:15], v40 offset:14336
v_add_f32 v48, v48, v89
v_mfma_f32_16x16x32_bf16 v[120:123], v[24:27], a[196:199], v[120:123]
v_add_f32 v48, v48, v90
v_add_f32 v48, v48, v91
v_mfma_f32_16x16x32_bf16 v[124:127], v[24:27], a[200:203], v[188:191]
v_exp_f32 v104, v104
v_mfma_f32_16x16x32_bf16 v[124:127], v[28:31], a[216:219], v[124:127]
v_exp_f32 v105, v105
v_mfma_f32_16x16x32_bf16 v[124:127], v[32:35], a[232:235], v[124:127]
ds_read_b128 v[16:19], %[kaddr0] offset:18432
v_cvt_pk_bf16_f32 v152, v88, v89
v_mfma_f32_16x16x32_bf16 v[128:131], v[32:35], a[236:239], v[192:195]
v_cvt_pk_bf16_f32 v153, v90, v91
v_add_f32 v48, v48, v104
v_mfma_f32_16x16x32_bf16 v[128:131], v[28:31], a[220:223], v[128:131]
buffer_load_dwordx4 v45, %[srdK], s76 offen offset:1024 lds
v_mfma_f32_16x16x32_bf16 v[128:131], v[24:27], a[204:207], v[128:131]
v_add_f32 v48, v48, v105
s_waitcnt lgkmcnt(1)
v_mfma_f32_16x16x32_bf16 v[128:131], v[36:39], a[252:255], v[128:131]
ds_read_b128 v[20:23], v40 offset:18432
v_cvt_pk_bf16_f32 v154, v104, v105
v_mfma_f32_16x16x32_bf16 v[128:131], v[8:11], v[64:67], v[128:131]
v_exp_f32 v106, v106
v_mfma_f32_16x16x32_bf16 v[128:131], v[12:15], v[80:83], v[128:131]
v_exp_f32 v107, v107
v_mfma_f32_16x16x32_bf16 v[124:127], v[12:15], v[76:79], v[124:127]
v_exp_f32 v84, v84
v_mfma_f32_16x16x32_bf16 v[124:127], v[8:11], v[60:63], v[124:127]
ds_read_b128 v[24:27], %[kaddr0] offset:19456
v_add_f32 v48, v48, v106
v_mfma_f32_16x16x32_bf16 v[124:127], v[36:39], a[248:251], v[124:127]
v_exp_f32 v85, v85
v_mfma_f32_16x16x32_bf16 v[120:123], v[36:39], a[244:247], v[120:123]
buffer_load_dwordx4 v46, %[srdK], s76 offen offset:2048 lds
v_mfma_f32_16x16x32_bf16 v[120:123], v[8:11], v[56:59], v[120:123]
v_add_f32 v48, v48, v107
v_cvt_pk_bf16_f32 v155, v106, v107
v_mfma_f32_16x16x32_bf16 v[120:123], v[12:15], v[72:75], v[120:123]
ds_read_b128 v[28:31], v40 offset:19456
s_add_u32 m0, s81, 49152
v_mfma_f32_16x16x32_bf16 v[116:119], v[12:15], v[68:71], v[116:119]
v_exp_f32 v86, v86
v_mfma_f32_16x16x32_bf16 v[116:119], v[8:11], v[52:55], v[116:119]
v_add_f32 v47, v47, v84
v_add_f32 v47, v47, v85
v_mfma_f32_16x16x32_bf16 v[116:119], v[36:39], a[240:243], v[116:119]
v_add_f32 v47, v47, v86
s_waitcnt lgkmcnt(1)
v_mfma_f32_16x16x32_bf16 v[132:135], v[16:19], a[192:195], v[180:183]
ds_read_b128 v[32:35], %[kaddr0] offset:20480
v_cvt_pk_bf16_f32 v148, v84, v85
v_mfma_f32_16x16x32_bf16 v[132:135], v[20:23], a[208:211], v[132:135]
v_exp_f32 v87, v87
v_mfma_f32_16x16x32_bf16 v[132:135], v[24:27], a[224:227], v[132:135]
buffer_load_dwordx4 v44, %[srdV], s76 offen offset:0 lds
v_mfma_f32_16x16x32_bf16 v[136:139], v[24:27], a[228:231], v[184:187]
v_exp_f32 v100, v100
v_mfma_f32_16x16x32_bf16 v[136:139], v[20:23], a[212:215], v[136:139]
ds_read_b128 v[36:39], v40 offset:20480
v_add_f32 v47, v47, v87
v_mfma_f32_16x16x32_bf16 v[136:139], v[16:19], a[196:199], v[136:139]
v_exp_f32 v101, v101
v_mfma_f32_16x16x32_bf16 v[140:143], v[16:19], a[200:203], v[188:191]
v_cvt_pk_bf16_f32 v149, v86, v87
v_add_f32 v47, v47, v100
v_mfma_f32_16x16x32_bf16 v[140:143], v[20:23], a[216:219], v[140:143]
v_exp_f32 v102, v102
v_mfma_f32_16x16x32_bf16 v[140:143], v[24:27], a[232:235], v[140:143]
ds_read_b64_tr_b16 v[8:9], %[vaddr0] offset:12288
ds_read_b64_tr_b16 v[10:11], %[vaddr0] offset:18432
v_mfma_f32_16x16x32_bf16 v[144:147], v[24:27], a[236:239], v[192:195]
v_exp_f32 v103, v103
v_mfma_f32_16x16x32_bf16 v[144:147], v[20:23], a[220:223], v[144:147]
buffer_load_dwordx4 v45, %[srdV], s76 offen offset:1024 lds
v_mfma_f32_16x16x32_bf16 v[144:147], v[16:19], a[204:207], v[144:147]
v_add_f32 v47, v47, v101
s_waitcnt lgkmcnt(2)
)"
R"(v_mfma_f32_16x16x32_bf16 v[144:147], v[28:31], a[252:255], v[144:147]
ds_read_b64_tr_b16 v[12:13], v41 offset:12288
ds_read_b64_tr_b16 v[14:15], v41 offset:18432
v_mfma_f32_16x16x32_bf16 v[144:147], v[32:35], v[64:67], v[144:147]
v_add_f32 v47, v47, v102
v_add_f32 v47, v47, v103
v_mfma_f32_16x16x32_bf16 v[144:147], v[36:39], v[80:83], v[144:147]
v_cvt_pk_bf16_f32 v150, v100, v101
v_cvt_pk_bf16_f32 v151, v102, v103
v_mfma_f32_16x16x32_bf16 v[140:143], v[36:39], v[76:79], v[140:143]
v_mfma_f32_16x16x32_bf16 v[140:143], v[32:35], v[60:63], v[140:143]
ds_read_b64_tr_b16 v[16:17], v42 offset:12288
ds_read_b64_tr_b16 v[18:19], v42 offset:18432
v_mfma_f32_16x16x32_bf16 v[140:143], v[28:31], a[248:251], v[140:143]
v_mfma_f32_16x16x32_bf16 v[136:139], v[28:31], a[244:247], v[136:139]
buffer_load_dwordx4 v46, %[srdV], s76 offen offset:2048 lds
v_mfma_f32_16x16x32_bf16 v[136:139], v[32:35], v[56:59], v[136:139]
v_mfma_f32_16x16x32_bf16 v[136:139], v[36:39], v[72:75], v[136:139]
ds_read_b64_tr_b16 v[20:21], v43 offset:12288
ds_read_b64_tr_b16 v[22:23], v43 offset:18432
v_mfma_f32_16x16x32_bf16 v[132:135], v[36:39], v[68:71], v[132:135]
v_mfma_f32_16x16x32_bf16 v[132:135], v[32:35], v[52:55], v[132:135]
s_add_u32 s76, s76, 12288
v_mfma_f32_16x16x32_bf16 v[132:135], v[28:31], a[240:243], v[132:135]
s_waitcnt vmcnt(6)
v_cmp_ne_u32 vcc, s96, v253
s_nop 0
s_and_b32 s83, vcc_lo, -1
s_cbranch_scc0 .Lhpoll_go
s_mov_b32 s95, 0x20000
.Lhpoll:
global_load_dword v253, v251, s[62:63] offset:128 sc1
s_waitcnt vmcnt(0)
v_cmp_ne_u32 vcc, s96, v253
s_sub_u32 s95, s95, 1
s_cbranch_scc1 .Lhpoll_go
s_and_b32 s83, vcc_lo, -1
s_cbranch_scc0 .Lhpoll_go
s_sleep 2
s_branch .Lhpoll
.Lhpoll_go:
s_waitcnt lgkmcnt(4)
v_mfma_f32_16x16x32_bf16 a[0:3], v[8:11], v[148:151], a[0:3]
ds_read_b64_tr_b16 v[24:25], %[vaddr0] offset:13312
ds_read_b64_tr_b16 v[26:27], %[vaddr0] offset:19456
v_mfma_f32_16x16x32_bf16 a[16:19], v[12:15], v[148:151], a[16:19]
s_add_u32 m0, s82, 98304
v_mfma_f32_16x16x32_bf16 a[20:23], v[12:15], v[152:155], a[20:23]
buffer_load_dwordx4 v248, %[srdG], s93 offen offset:0 lds
v_mfma_f32_16x16x32_bf16 a[4:7], v[8:11], v[152:155], a[4:7]
v_exp_f32 v128, v128
v_mfma_f32_16x16x32_bf16 a[8:11], v[8:11], v[156:159], a[8:11]
ds_read_b64_tr_b16 v[28:29], v41 offset:13312
ds_read_b64_tr_b16 v[30:31], v41 offset:19456
v_mfma_f32_16x16x32_bf16 a[24:27], v[12:15], v[156:159], a[24:27]
v_exp_f32 v129, v129
v_mfma_f32_16x16x32_bf16 a[28:31], v[12:15], v[160:163], a[28:31]
v_exp_f32 v130, v130
v_mfma_f32_16x16x32_bf16 a[12:15], v[8:11], v[160:163], a[12:15]
v_add_f32 v50, v50, v128
s_waitcnt lgkmcnt(4)
v_mfma_f32_16x16x32_bf16 a[44:47], v[16:19], v[160:163], a[44:47]
ds_read_b64_tr_b16 v[32:33], v42 offset:13312
ds_read_b64_tr_b16 v[34:35], v42 offset:19456
v_mfma_f32_16x16x32_bf16 a[60:63], v[20:23], v[160:163], a[60:63]
v_exp_f32 v131, v131
v_mfma_f32_16x16x32_bf16 a[56:59], v[20:23], v[156:159], a[56:59]
buffer_load_dwordx4 v249, %[srdG], s93 offen offset:1024 lds
v_mfma_f32_16x16x32_bf16 a[40:43], v[16:19], v[156:159], a[40:43]
v_add_f32 v50, v50, v129
v_add_f32 v50, v50, v130
v_mfma_f32_16x16x32_bf16 a[36:39], v[16:19], v[152:155], a[36:39]
ds_read_b64_tr_b16 v[36:37], v43 offset:13312
ds_read_b64_tr_b16 v[38:39], v43 offset:19456
v_mfma_f32_16x16x32_bf16 a[52:55], v[20:23], v[152:155], a[52:55]
v_exp_f32 v144, v144
v_mfma_f32_16x16x32_bf16 a[48:51], v[20:23], v[148:151], a[48:51]
v_add_f32 v50, v50, v131
v_cvt_pk_bf16_f32 v176, v128, v129
v_mfma_f32_16x16x32_bf16 a[32:35], v[16:19], v[148:151], a[32:35]
v_cvt_pk_bf16_f32 v177, v130, v131
s_waitcnt lgkmcnt(4)
v_mfma_f32_16x16x32_bf16 a[64:67], v[24:27], v[148:151], a[64:67]
ds_read_b64_tr_b16 v[8:9], %[vaddr0] offset:14336
ds_read_b64_tr_b16 v[10:11], %[vaddr0] offset:20480
v_mfma_f32_16x16x32_bf16 a[80:83], v[28:31], v[148:151], a[80:83]
v_exp_f32 v145, v145
v_mfma_f32_16x16x32_bf16 a[84:87], v[28:31], v[152:155], a[84:87]
buffer_load_dwordx4 v[228:231], %[lane16], s[72:75], s94 offen sc1
v_mfma_f32_16x16x32_bf16 a[68:71], v[24:27], v[152:155], a[68:71]
v_exp_f32 v146, v146
v_mfma_f32_16x16x32_bf16 a[72:75], v[24:27], v[156:159], a[72:75]
ds_read_b64_tr_b16 v[12:13], v41 offset:14336
ds_read_b64_tr_b16 v[14:15], v41 offset:20480
s_add_u32 s93, s93, 40960
s_add_u32 s94, s94, 1024
v_mfma_f32_16x16x32_bf16 a[88:91], v[28:31], v[156:159], a[88:91]
v_add_f32 v50, v50, v144
v_add_f32 v50, v50, v145
v_mfma_f32_16x16x32_bf16 a[92:95], v[28:31], v[160:163], a[92:95]
v_exp_f32 v147, v147
v_mfma_f32_16x16x32_bf16 a[76:79], v[24:27], v[160:163], a[76:79]
v_add_f32 v50, v50, v146
s_waitcnt lgkmcnt(4)
v_mfma_f32_16x16x32_bf16 a[108:111], v[32:35], v[160:163], a[108:111]
ds_read_b64_tr_b16 v[16:17], v42 offset:14336
ds_read_b64_tr_b16 v[18:19], v42 offset:20480
v_mfma_f32_16x16x32_bf16 a[124:127], v[36:39], v[160:163], a[124:127]
v_exp_f32 v124, v124
v_mfma_f32_16x16x32_bf16 a[120:123], v[36:39], v[156:159], a[120:123]
v_add_f32 v50, v50, v147
v_cvt_pk_bf16_f32 v178, v144, v145
v_mfma_f32_16x16x32_bf16 a[104:107], v[32:35], v[156:159], a[104:107]
v_exp_f32 v125, v125
v_mfma_f32_16x16x32_bf16 a[100:103], v[32:35], v[152:155], a[100:103]
ds_read_b64_tr_b16 v[20:21], v43 offset:14336
ds_read_b64_tr_b16 v[22:23], v43 offset:20480
v_mfma_f32_16x16x32_bf16 a[116:119], v[36:39], v[152:155], a[116:119]
v_cvt_pk_bf16_f32 v179, v146, v147
v_add_f32 v49, v49, v124
v_mfma_f32_16x16x32_bf16 a[112:115], v[36:39], v[148:151], a[112:115]
v_exp_f32 v126, v126
v_mfma_f32_16x16x32_bf16 a[96:99], v[32:35], v[148:151], a[96:99]
v_add_f32 v49, v49, v125
s_waitcnt lgkmcnt(4)
v_mfma_f32_16x16x32_bf16 a[128:131], v[8:11], v[148:151], a[128:131]
s_waitcnt lgkmcnt(0)
s_barrier
ds_read_b128 v[24:27], %[kaddr0] offset:24576
v_mfma_f32_16x16x32_bf16 a[144:147], v[12:15], v[148:151], a[144:147]
v_exp_f32 v127, v127
v_mfma_f32_16x16x32_bf16 a[148:151], v[12:15], v[152:155], a[148:151]
v_add_f32 v49, v49, v126
v_add_f32 v49, v49, v127
v_mfma_f32_16x16x32_bf16 a[132:135], v[8:11], v[152:155], a[132:135]
v_exp_f32 v140, v140
v_mfma_f32_16x16x32_bf16 a[136:139], v[8:11], v[156:159], a[136:139]
ds_read_b128 v[28:31], v40 offset:24576
v_cvt_pk_bf16_f32 v172, v124, v125
v_mfma_f32_16x16x32_bf16 a[152:155], v[12:15], v[156:159], a[152:155]
v_exp_f32 v141, v141
v_mfma_f32_16x16x32_bf16 a[156:159], v[12:15], v[160:163], a[156:159]
v_cvt_pk_bf16_f32 v173, v126, v127
v_add_f32 v49, v49, v140
v_mfma_f32_16x16x32_bf16 a[140:143], v[8:11], v[160:163], a[140:143]
v_add_f32 v49, v49, v141
v_mfma_f32_16x16x32_bf16 a[172:175], v[16:19], v[160:163], a[172:175]
ds_read_b128 v[32:35], %[kaddr0] offset:25600
v_cvt_pk_bf16_f32 v174, v140, v141
v_mfma_f32_16x16x32_bf16 a[188:191], v[20:23], v[160:163], a[188:191]
v_exp_f32 v142, v142
v_mfma_f32_16x16x32_bf16 a[184:187], v[20:23], v[156:159], a[184:187]
v_exp_f32 v143, v143
v_mfma_f32_16x16x32_bf16 a[168:171], v[16:19], v[156:159], a[168:171]
v_exp_f32 v120, v120
v_mfma_f32_16x16x32_bf16 a[164:167], v[16:19], v[152:155], a[164:167]
ds_read_b128 v[36:39], v40 offset:25600
v_add_f32 v49, v49, v142
v_mfma_f32_16x16x32_bf16 a[180:183], v[20:23], v[152:155], a[180:183]
v_exp_f32 v121, v121
v_mfma_f32_16x16x32_bf16 a[176:179], v[20:23], v[148:151], a[176:179]
v_add_f32 v49, v49, v143
v_cvt_pk_bf16_f32 v175, v142, v143
v_mfma_f32_16x16x32_bf16 a[160:163], v[16:19], v[148:151], a[160:163]
v_add_f32 v48, v48, v120
s_waitcnt lgkmcnt(1)
v_mfma_f32_16x16x32_bf16 v[84:87], v[24:27], a[192:195], v[180:183]
ds_read_b128 v[8:11], %[kaddr0] offset:26624
s_add_u32 m0, s81, 12288
v_mfma_f32_16x16x32_bf16 v[84:87], v[28:31], a[208:211], v[84:87]
v_exp_f32 v122, v122
v_mfma_f32_16x16x32_bf16 v[84:87], v[32:35], a[224:227], v[84:87]
buffer_load_dwordx4 v44, %[srdK], s76 offen offset:0 lds
v_mfma_f32_16x16x32_bf16 v[88:91], v[32:35], a[228:231], v[184:187]
v_exp_f32 v123, v123
v_mfma_f32_16x16x32_bf16 v[88:91], v[28:31], a[212:215], v[88:91]
ds_read_b128 v[12:15], v40 offset:26624
v_add_f32 v48, v48, v121
v_mfma_f32_16x16x32_bf16 v[88:91], v[24:27], a[196:199], v[88:91]
v_add_f32 v48, v48, v122
v_add_f32 v48, v48, v123
v_mfma_f32_16x16x32_bf16 v[92:95], v[24:27], a[200:203], v[188:191]
v_exp_f32 v136, v136
v_mfma_f32_16x16x32_bf16 v[92:95], v[28:31], a[216:219], v[92:95]
v_exp_f32 v137, v137
v_mfma_f32_16x16x32_bf16 v[92:95], v[32:35], a[232:235], v[92:95]
ds_read_b128 v[16:19], %[kaddr0] offset:30720
v_cvt_pk_bf16_f32 v168, v120, v121
v_mfma_f32_16x16x32_bf16 v[96:99], v[32:35], a[236:239], v[192:195]
v_cvt_pk_bf16_f32 v169, v122, v123
v_add_f32 v48, v48, v136
v_mfma_f32_16x16x32_bf16 v[96:99], v[28:31], a[220:223], v[96:99]
buffer_load_dwordx4 v45, %[srdK], s76 offen offset:1024 lds
v_mfma_f32_16x16x32_bf16 v[96:99], v[24:27], a[204:207], v[96:99]
v_add_f32 v48, v48, v137
s_waitcnt lgkmcnt(1)
v_mfma_f32_16x16x32_bf16 v[96:99], v[36:39], a[252:255], v[96:99]
ds_read_b128 v[20:23], v40 offset:30720
v_cvt_pk_bf16_f32 v170, v136, v137
v_mfma_f32_16x16x32_bf16 v[96:99], v[8:11], v[64:67], v[96:99]
)"
R"(v_exp_f32 v138, v138
v_mfma_f32_16x16x32_bf16 v[96:99], v[12:15], v[80:83], v[96:99]
v_exp_f32 v139, v139
v_mfma_f32_16x16x32_bf16 v[92:95], v[12:15], v[76:79], v[92:95]
v_exp_f32 v116, v116
v_mfma_f32_16x16x32_bf16 v[92:95], v[8:11], v[60:63], v[92:95]
ds_read_b128 v[24:27], %[kaddr0] offset:31744
v_add_f32 v48, v48, v138
v_mfma_f32_16x16x32_bf16 v[92:95], v[36:39], a[248:251], v[92:95]
v_exp_f32 v117, v117
v_mfma_f32_16x16x32_bf16 v[88:91], v[36:39], a[244:247], v[88:91]
buffer_load_dwordx4 v46, %[srdK], s76 offen offset:2048 lds
v_mfma_f32_16x16x32_bf16 v[88:91], v[8:11], v[56:59], v[88:91]
v_add_f32 v48, v48, v139
v_cvt_pk_bf16_f32 v171, v138, v139
v_mfma_f32_16x16x32_bf16 v[88:91], v[12:15], v[72:75], v[88:91]
ds_read_b128 v[28:31], v40 offset:31744
s_add_u32 m0, s81, 61440
v_mfma_f32_16x16x32_bf16 v[84:87], v[12:15], v[68:71], v[84:87]
v_exp_f32 v118, v118
v_mfma_f32_16x16x32_bf16 v[84:87], v[8:11], v[52:55], v[84:87]
v_add_f32 v47, v47, v116
v_add_f32 v47, v47, v117
v_mfma_f32_16x16x32_bf16 v[84:87], v[36:39], a[240:243], v[84:87]
v_add_f32 v47, v47, v118
s_waitcnt lgkmcnt(1)
v_mfma_f32_16x16x32_bf16 v[100:103], v[16:19], a[192:195], v[180:183]
ds_read_b128 v[32:35], %[kaddr0] offset:32768
v_cvt_pk_bf16_f32 v164, v116, v117
v_mfma_f32_16x16x32_bf16 v[100:103], v[20:23], a[208:211], v[100:103]
v_exp_f32 v119, v119
v_mfma_f32_16x16x32_bf16 v[100:103], v[24:27], a[224:227], v[100:103]
buffer_load_dwordx4 v44, %[srdV], s76 offen offset:0 lds
v_mfma_f32_16x16x32_bf16 v[104:107], v[24:27], a[228:231], v[184:187]
v_exp_f32 v132, v132
v_mfma_f32_16x16x32_bf16 v[104:107], v[20:23], a[212:215], v[104:107]
ds_read_b128 v[36:39], v40 offset:32768
v_add_f32 v47, v47, v119
v_mfma_f32_16x16x32_bf16 v[104:107], v[16:19], a[196:199], v[104:107]
v_exp_f32 v133, v133
v_mfma_f32_16x16x32_bf16 v[108:111], v[16:19], a[200:203], v[188:191]
v_cvt_pk_bf16_f32 v165, v118, v119
v_add_f32 v47, v47, v132
v_mfma_f32_16x16x32_bf16 v[108:111], v[20:23], a[216:219], v[108:111]
v_exp_f32 v134, v134
v_mfma_f32_16x16x32_bf16 v[108:111], v[24:27], a[232:235], v[108:111]
ds_read_b64_tr_b16 v[8:9], %[vaddr0] offset:24576
ds_read_b64_tr_b16 v[10:11], %[vaddr0] offset:30720
v_mfma_f32_16x16x32_bf16 v[112:115], v[24:27], a[236:239], v[192:195]
v_exp_f32 v135, v135
v_mfma_f32_16x16x32_bf16 v[112:115], v[20:23], a[220:223], v[112:115]
buffer_load_dwordx4 v45, %[srdV], s76 offen offset:1024 lds
v_mfma_f32_16x16x32_bf16 v[112:115], v[16:19], a[204:207], v[112:115]
v_add_f32 v47, v47, v133
s_waitcnt lgkmcnt(2)
v_mfma_f32_16x16x32_bf16 v[112:115], v[28:31], a[252:255], v[112:115]
ds_read_b64_tr_b16 v[12:13], v41 offset:24576
ds_read_b64_tr_b16 v[14:15], v41 offset:30720
v_mfma_f32_16x16x32_bf16 v[112:115], v[32:35], v[64:67], v[112:115]
v_add_f32 v47, v47, v134
v_add_f32 v47, v47, v135
v_mfma_f32_16x16x32_bf16 v[112:115], v[36:39], v[80:83], v[112:115]
v_cvt_pk_bf16_f32 v166, v132, v133
v_cvt_pk_bf16_f32 v167, v134, v135
v_mfma_f32_16x16x32_bf16 v[108:111], v[36:39], v[76:79], v[108:111]
v_mfma_f32_16x16x32_bf16 v[108:111], v[32:35], v[60:63], v[108:111]
ds_read_b64_tr_b16 v[16:17], v42 offset:24576
ds_read_b64_tr_b16 v[18:19], v42 offset:30720
v_mfma_f32_16x16x32_bf16 v[108:111], v[28:31], a[248:251], v[108:111]
v_mfma_f32_16x16x32_bf16 v[104:107], v[28:31], a[244:247], v[104:107]
buffer_load_dwordx4 v46, %[srdV], s76 offen offset:2048 lds
v_mfma_f32_16x16x32_bf16 v[104:107], v[32:35], v[56:59], v[104:107]
v_mfma_f32_16x16x32_bf16 v[104:107], v[36:39], v[72:75], v[104:107]
ds_read_b64_tr_b16 v[20:21], v43 offset:24576
ds_read_b64_tr_b16 v[22:23], v43 offset:30720
v_mfma_f32_16x16x32_bf16 v[100:103], v[36:39], v[68:71], v[100:103]
v_mfma_f32_16x16x32_bf16 v[100:103], v[32:35], v[52:55], v[100:103]
s_add_u32 s76, s76, 12288
v_mfma_f32_16x16x32_bf16 v[100:103], v[28:31], a[240:243], v[100:103]
s_waitcnt lgkmcnt(4)
v_mfma_f32_16x16x32_bf16 a[0:3], v[8:11], v[164:167], a[0:3]
ds_read_b64_tr_b16 v[24:25], %[vaddr0] offset:25600
ds_read_b64_tr_b16 v[26:27], %[vaddr0] offset:31744
v_mfma_f32_16x16x32_bf16 a[16:19], v[12:15], v[164:167], a[16:19]
s_add_u32 m0, s82, 106496
v_mfma_f32_16x16x32_bf16 a[20:23], v[12:15], v[168:171], a[20:23]
buffer_load_dwordx4 v248, %[srdG], s93 offen offset:0 lds
v_mfma_f32_16x16x32_bf16 a[4:7], v[8:11], v[168:171], a[4:7]
v_exp_f32 v96, v96
v_mfma_f32_16x16x32_bf16 a[8:11], v[8:11], v[172:175], a[8:11]
ds_read_b64_tr_b16 v[28:29], v41 offset:25600
ds_read_b64_tr_b16 v[30:31], v41 offset:31744
v_mfma_f32_16x16x32_bf16 a[24:27], v[12:15], v[172:175], a[24:27]
v_exp_f32 v97, v97
v_mfma_f32_16x16x32_bf16 a[28:31], v[12:15], v[176:179], a[28:31]
v_exp_f32 v98, v98
v_mfma_f32_16x16x32_bf16 a[12:15], v[8:11], v[176:179], a[12:15]
v_add_f32 v50, v50, v96
s_waitcnt lgkmcnt(4)
v_mfma_f32_16x16x32_bf16 a[44:47], v[16:19], v[176:179], a[44:47]
ds_read_b64_tr_b16 v[32:33], v42 offset:25600
ds_read_b64_tr_b16 v[34:35], v42 offset:31744
v_mfma_f32_16x16x32_bf16 a[60:63], v[20:23], v[176:179], a[60:63]
v_exp_f32 v99, v99
v_mfma_f32_16x16x32_bf16 a[56:59], v[20:23], v[172:175], a[56:59]
buffer_load_dwordx4 v249, %[srdG], s93 offen offset:1024 lds
v_mfma_f32_16x16x32_bf16 a[40:43], v[16:19], v[172:175], a[40:43]
v_add_f32 v50, v50, v97
v_add_f32 v50, v50, v98
v_mfma_f32_16x16x32_bf16 a[36:39], v[16:19], v[168:171], a[36:39]
ds_read_b64_tr_b16 v[36:37], v43 offset:25600
ds_read_b64_tr_b16 v[38:39], v43 offset:31744
v_mfma_f32_16x16x32_bf16 a[52:55], v[20:23], v[168:171], a[52:55]
v_exp_f32 v112, v112
v_mfma_f32_16x16x32_bf16 a[48:51], v[20:23], v[164:167], a[48:51]
v_add_f32 v50, v50, v99
v_cvt_pk_bf16_f32 v160, v96, v97
v_mfma_f32_16x16x32_bf16 a[32:35], v[16:19], v[164:167], a[32:35]
v_cvt_pk_bf16_f32 v161, v98, v99
s_waitcnt lgkmcnt(4)
v_mfma_f32_16x16x32_bf16 a[64:67], v[24:27], v[164:167], a[64:67]
ds_read_b64_tr_b16 v[8:9], %[vaddr0] offset:26624
ds_read_b64_tr_b16 v[10:11], %[vaddr0] offset:32768
v_mfma_f32_16x16x32_bf16 a[80:83], v[28:31], v[164:167], a[80:83]
v_exp_f32 v113, v113
v_mfma_f32_16x16x32_bf16 a[84:87], v[28:31], v[168:171], a[84:87]
buffer_load_dwordx4 v[232:235], %[lane16], s[72:75], s94 offen sc1
v_mfma_f32_16x16x32_bf16 a[68:71], v[24:27], v[168:171], a[68:71]
v_exp_f32 v114, v114
v_mfma_f32_16x16x32_bf16 a[72:75], v[24:27], v[172:175], a[72:75]
ds_read_b64_tr_b16 v[12:13], v41 offset:26624
ds_read_b64_tr_b16 v[14:15], v41 offset:32768
s_add_u32 s93, s93, 40960
s_add_u32 s94, s94, 1024
v_mfma_f32_16x16x32_bf16 a[88:91], v[28:31], v[172:175], a[88:91]
v_add_f32 v50, v50, v112
v_add_f32 v50, v50, v113
v_mfma_f32_16x16x32_bf16 a[92:95], v[28:31], v[176:179], a[92:95]
v_exp_f32 v115, v115
v_mfma_f32_16x16x32_bf16 a[76:79], v[24:27], v[176:179], a[76:79]
v_add_f32 v50, v50, v114
s_waitcnt lgkmcnt(4)
v_mfma_f32_16x16x32_bf16 a[108:111], v[32:35], v[176:179], a[108:111]
ds_read_b64_tr_b16 v[16:17], v42 offset:26624
ds_read_b64_tr_b16 v[18:19], v42 offset:32768
v_mfma_f32_16x16x32_bf16 a[124:127], v[36:39], v[176:179], a[124:127]
v_exp_f32 v92, v92
v_mfma_f32_16x16x32_bf16 a[120:123], v[36:39], v[172:175], a[120:123]
v_add_f32 v50, v50, v115
v_cvt_pk_bf16_f32 v162, v112, v113
v_mfma_f32_16x16x32_bf16 a[104:107], v[32:35], v[172:175], a[104:107]
v_exp_f32 v93, v93
v_mfma_f32_16x16x32_bf16 a[100:103], v[32:35], v[168:171], a[100:103]
ds_read_b64_tr_b16 v[20:21], v43 offset:26624
ds_read_b64_tr_b16 v[22:23], v43 offset:32768
v_mfma_f32_16x16x32_bf16 a[116:119], v[36:39], v[168:171], a[116:119]
v_cvt_pk_bf16_f32 v163, v114, v115
v_add_f32 v49, v49, v92
v_mfma_f32_16x16x32_bf16 a[112:115], v[36:39], v[164:167], a[112:115]
v_exp_f32 v94, v94
v_mfma_f32_16x16x32_bf16 a[96:99], v[32:35], v[164:167], a[96:99]
v_add_f32 v49, v49, v93
s_waitcnt lgkmcnt(4)
v_mfma_f32_16x16x32_bf16 a[128:131], v[8:11], v[164:167], a[128:131]
s_waitcnt lgkmcnt(0)
s_barrier
ds_read_b128 v[24:27], %[kaddr0] offset:36864
v_mfma_f32_16x16x32_bf16 a[144:147], v[12:15], v[164:167], a[144:147]
v_exp_f32 v95, v95
v_mfma_f32_16x16x32_bf16 a[148:151], v[12:15], v[168:171], a[148:151]
v_add_f32 v49, v49, v94
v_add_f32 v49, v49, v95
v_mfma_f32_16x16x32_bf16 a[132:135], v[8:11], v[168:171], a[132:135]
v_exp_f32 v108, v108
v_mfma_f32_16x16x32_bf16 a[136:139], v[8:11], v[172:175], a[136:139]
ds_read_b128 v[28:31], v40 offset:36864
v_cvt_pk_bf16_f32 v156, v92, v93
v_mfma_f32_16x16x32_bf16 a[152:155], v[12:15], v[172:175], a[152:155]
v_exp_f32 v109, v109
v_mfma_f32_16x16x32_bf16 a[156:159], v[12:15], v[176:179], a[156:159]
v_cvt_pk_bf16_f32 v157, v94, v95
v_add_f32 v49, v49, v108
v_mfma_f32_16x16x32_bf16 a[140:143], v[8:11], v[176:179], a[140:143]
v_add_f32 v49, v49, v109
v_mfma_f32_16x16x32_bf16 a[172:175], v[16:19], v[176:179], a[172:175]
ds_read_b128 v[32:35], %[kaddr0] offset:37888
v_cvt_pk_bf16_f32 v158, v108, v109
v_mfma_f32_16x16x32_bf16 a[188:191], v[20:23], v[176:179], a[188:191]
v_exp_f32 v110, v110
v_mfma_f32_16x16x32_bf16 a[184:187], v[20:23], v[172:175], a[184:187]
v_exp_f32 v111, v111
v_mfma_f32_16x16x32_bf16 a[168:171], v[16:19], v[172:175], a[168:171]
v_exp_f32 v88, v88
v_mfma_f32_16x16x32_bf16 a[164:167], v[16:19], v[168:171], a[164:167]
ds_read_b128 v[36:39], v40 offset:37888
v_add_f32 v49, v49, v110
v_mfma_f32_16x16x32_bf16 a[180:183], v[20:23], v[168:171], a[180:183]
v_exp_f32 v89, v89
v_mfma_f32_16x16x32_bf16 a[176:179], v[20:23], v[164:167], a[176:179]
v_add_f32 v49, v49, v111
)"
R"(v_cvt_pk_bf16_f32 v159, v110, v111
v_mfma_f32_16x16x32_bf16 a[160:163], v[16:19], v[164:167], a[160:163]
v_add_f32 v48, v48, v88
s_waitcnt lgkmcnt(1)
v_mfma_f32_16x16x32_bf16 v[116:119], v[24:27], a[192:195], v[180:183]
ds_read_b128 v[8:11], %[kaddr0] offset:38912
s_add_u32 m0, s81, 24576
v_mfma_f32_16x16x32_bf16 v[116:119], v[28:31], a[208:211], v[116:119]
v_exp_f32 v90, v90
v_mfma_f32_16x16x32_bf16 v[116:119], v[32:35], a[224:227], v[116:119]
buffer_load_dwordx4 v44, %[srdK], s76 offen offset:0 lds
v_mfma_f32_16x16x32_bf16 v[120:123], v[32:35], a[228:231], v[184:187]
v_exp_f32 v91, v91
v_mfma_f32_16x16x32_bf16 v[120:123], v[28:31], a[212:215], v[120:123]
ds_read_b128 v[12:15], v40 offset:38912
v_add_f32 v48, v48, v89
v_mfma_f32_16x16x32_bf16 v[120:123], v[24:27], a[196:199], v[120:123]
v_add_f32 v48, v48, v90
v_add_f32 v48, v48, v91
v_mfma_f32_16x16x32_bf16 v[124:127], v[24:27], a[200:203], v[188:191]
v_exp_f32 v104, v104
v_mfma_f32_16x16x32_bf16 v[124:127], v[28:31], a[216:219], v[124:127]
v_exp_f32 v105, v105
v_mfma_f32_16x16x32_bf16 v[124:127], v[32:35], a[232:235], v[124:127]
ds_read_b128 v[16:19], %[kaddr0] offset:43008
v_cvt_pk_bf16_f32 v152, v88, v89
v_mfma_f32_16x16x32_bf16 v[128:131], v[32:35], a[236:239], v[192:195]
v_cvt_pk_bf16_f32 v153, v90, v91
v_add_f32 v48, v48, v104
v_mfma_f32_16x16x32_bf16 v[128:131], v[28:31], a[220:223], v[128:131]
buffer_load_dwordx4 v45, %[srdK], s76 offen offset:1024 lds
v_mfma_f32_16x16x32_bf16 v[128:131], v[24:27], a[204:207], v[128:131]
v_add_f32 v48, v48, v105
s_waitcnt lgkmcnt(1)
v_mfma_f32_16x16x32_bf16 v[128:131], v[36:39], a[252:255], v[128:131]
ds_read_b128 v[20:23], v40 offset:43008
v_cvt_pk_bf16_f32 v154, v104, v105
v_mfma_f32_16x16x32_bf16 v[128:131], v[8:11], v[64:67], v[128:131]
v_exp_f32 v106, v106
v_mfma_f32_16x16x32_bf16 v[128:131], v[12:15], v[80:83], v[128:131]
v_exp_f32 v107, v107
v_mfma_f32_16x16x32_bf16 v[124:127], v[12:15], v[76:79], v[124:127]
v_exp_f32 v84, v84
v_mfma_f32_16x16x32_bf16 v[124:127], v[8:11], v[60:63], v[124:127]
ds_read_b128 v[24:27], %[kaddr0] offset:44032
v_add_f32 v48, v48, v106
v_mfma_f32_16x16x32_bf16 v[124:127], v[36:39], a[248:251], v[124:127]
v_exp_f32 v85, v85
v_mfma_f32_16x16x32_bf16 v[120:123], v[36:39], a[244:247], v[120:123]
buffer_load_dwordx4 v46, %[srdK], s76 offen offset:2048 lds
v_mfma_f32_16x16x32_bf16 v[120:123], v[8:11], v[56:59], v[120:123]
v_add_f32 v48, v48, v107
v_cvt_pk_bf16_f32 v155, v106, v107
v_mfma_f32_16x16x32_bf16 v[120:123], v[12:15], v[72:75], v[120:123]
ds_read_b128 v[28:31], v40 offset:44032
s_add_u32 m0, s81, 73728
v_mfma_f32_16x16x32_bf16 v[116:119], v[12:15], v[68:71], v[116:119]
v_exp_f32 v86, v86
v_mfma_f32_16x16x32_bf16 v[116:119], v[8:11], v[52:55], v[116:119]
v_add_f32 v47, v47, v84
v_add_f32 v47, v47, v85
v_mfma_f32_16x16x32_bf16 v[116:119], v[36:39], a[240:243], v[116:119]
v_add_f32 v47, v47, v86
s_waitcnt lgkmcnt(1)
v_mfma_f32_16x16x32_bf16 v[132:135], v[16:19], a[192:195], v[180:183]
ds_read_b128 v[32:35], %[kaddr0] offset:45056
v_cvt_pk_bf16_f32 v148, v84, v85
v_mfma_f32_16x16x32_bf16 v[132:135], v[20:23], a[208:211], v[132:135]
v_exp_f32 v87, v87
v_mfma_f32_16x16x32_bf16 v[132:135], v[24:27], a[224:227], v[132:135]
buffer_load_dwordx4 v44, %[srdV], s76 offen offset:0 lds
v_mfma_f32_16x16x32_bf16 v[136:139], v[24:27], a[228:231], v[184:187]
v_exp_f32 v100, v100
v_mfma_f32_16x16x32_bf16 v[136:139], v[20:23], a[212:215], v[136:139]
ds_read_b128 v[36:39], v40 offset:45056
v_add_f32 v47, v47, v87
v_mfma_f32_16x16x32_bf16 v[136:139], v[16:19], a[196:199], v[136:139]
v_exp_f32 v101, v101
v_mfma_f32_16x16x32_bf16 v[140:143], v[16:19], a[200:203], v[188:191]
v_cvt_pk_bf16_f32 v149, v86, v87
v_add_f32 v47, v47, v100
v_mfma_f32_16x16x32_bf16 v[140:143], v[20:23], a[216:219], v[140:143]
v_exp_f32 v102, v102
v_mfma_f32_16x16x32_bf16 v[140:143], v[24:27], a[232:235], v[140:143]
ds_read_b64_tr_b16 v[8:9], %[vaddr0] offset:36864
ds_read_b64_tr_b16 v[10:11], %[vaddr0] offset:43008
v_mfma_f32_16x16x32_bf16 v[144:147], v[24:27], a[236:239], v[192:195]
v_exp_f32 v103, v103
v_mfma_f32_16x16x32_bf16 v[144:147], v[20:23], a[220:223], v[144:147]
buffer_load_dwordx4 v45, %[srdV], s76 offen offset:1024 lds
v_mfma_f32_16x16x32_bf16 v[144:147], v[16:19], a[204:207], v[144:147]
v_add_f32 v47, v47, v101
s_waitcnt lgkmcnt(2)
v_mfma_f32_16x16x32_bf16 v[144:147], v[28:31], a[252:255], v[144:147]
ds_read_b64_tr_b16 v[12:13], v41 offset:36864
ds_read_b64_tr_b16 v[14:15], v41 offset:43008
v_mfma_f32_16x16x32_bf16 v[144:147], v[32:35], v[64:67], v[144:147]
v_add_f32 v47, v47, v102
v_add_f32 v47, v47, v103
v_mfma_f32_16x16x32_bf16 v[144:147], v[36:39], v[80:83], v[144:147]
v_cvt_pk_bf16_f32 v150, v100, v101
v_cvt_pk_bf16_f32 v151, v102, v103
v_mfma_f32_16x16x32_bf16 v[140:143], v[36:39], v[76:79], v[140:143]
v_mfma_f32_16x16x32_bf16 v[140:143], v[32:35], v[60:63], v[140:143]
ds_read_b64_tr_b16 v[16:17], v42 offset:36864
ds_read_b64_tr_b16 v[18:19], v42 offset:43008
v_mfma_f32_16x16x32_bf16 v[140:143], v[28:31], a[248:251], v[140:143]
v_mfma_f32_16x16x32_bf16 v[136:139], v[28:31], a[244:247], v[136:139]
buffer_load_dwordx4 v46, %[srdV], s76 offen offset:2048 lds
v_mfma_f32_16x16x32_bf16 v[136:139], v[32:35], v[56:59], v[136:139]
v_mfma_f32_16x16x32_bf16 v[136:139], v[36:39], v[72:75], v[136:139]
ds_read_b64_tr_b16 v[20:21], v43 offset:36864
ds_read_b64_tr_b16 v[22:23], v43 offset:43008
v_mfma_f32_16x16x32_bf16 v[132:135], v[36:39], v[68:71], v[132:135]
v_mfma_f32_16x16x32_bf16 v[132:135], v[32:35], v[52:55], v[132:135]
s_add_u32 s76, s76, 12288
v_mfma_f32_16x16x32_bf16 v[132:135], v[28:31], a[240:243], v[132:135]
s_waitcnt lgkmcnt(4)
v_mfma_f32_16x16x32_bf16 a[0:3], v[8:11], v[148:151], a[0:3]
ds_read_b64_tr_b16 v[24:25], %[vaddr0] offset:37888
ds_read_b64_tr_b16 v[26:27], %[vaddr0] offset:44032
v_mfma_f32_16x16x32_bf16 a[16:19], v[12:15], v[148:151], a[16:19]
s_add_u32 m0, s82, 114688
v_mfma_f32_16x16x32_bf16 a[20:23], v[12:15], v[152:155], a[20:23]
buffer_load_dwordx4 v248, %[srdG], s93 offen offset:0 lds
v_mfma_f32_16x16x32_bf16 a[4:7], v[8:11], v[152:155], a[4:7]
v_exp_f32 v128, v128
v_mfma_f32_16x16x32_bf16 a[8:11], v[8:11], v[156:159], a[8:11]
ds_read_b64_tr_b16 v[28:29], v41 offset:37888
ds_read_b64_tr_b16 v[30:31], v41 offset:44032
v_mfma_f32_16x16x32_bf16 a[24:27], v[12:15], v[156:159], a[24:27]
v_exp_f32 v129, v129
v_mfma_f32_16x16x32_bf16 a[28:31], v[12:15], v[160:163], a[28:31]
v_exp_f32 v130, v130
v_mfma_f32_16x16x32_bf16 a[12:15], v[8:11], v[160:163], a[12:15]
v_add_f32 v50, v50, v128
s_waitcnt lgkmcnt(4)
v_mfma_f32_16x16x32_bf16 a[44:47], v[16:19], v[160:163], a[44:47]
ds_read_b64_tr_b16 v[32:33], v42 offset:37888
ds_read_b64_tr_b16 v[34:35], v42 offset:44032
v_mfma_f32_16x16x32_bf16 a[60:63], v[20:23], v[160:163], a[60:63]
v_exp_f32 v131, v131
v_mfma_f32_16x16x32_bf16 a[56:59], v[20:23], v[156:159], a[56:59]
buffer_load_dwordx4 v249, %[srdG], s93 offen offset:1024 lds
v_mfma_f32_16x16x32_bf16 a[40:43], v[16:19], v[156:159], a[40:43]
v_add_f32 v50, v50, v129
v_add_f32 v50, v50, v130
v_mfma_f32_16x16x32_bf16 a[36:39], v[16:19], v[152:155], a[36:39]
ds_read_b64_tr_b16 v[36:37], v43 offset:37888
ds_read_b64_tr_b16 v[38:39], v43 offset:44032
v_mfma_f32_16x16x32_bf16 a[52:55], v[20:23], v[152:155], a[52:55]
v_exp_f32 v144, v144
v_mfma_f32_16x16x32_bf16 a[48:51], v[20:23], v[148:151], a[48:51]
v_add_f32 v50, v50, v131
v_cvt_pk_bf16_f32 v176, v128, v129
v_mfma_f32_16x16x32_bf16 a[32:35], v[16:19], v[148:151], a[32:35]
v_cvt_pk_bf16_f32 v177, v130, v131
s_waitcnt lgkmcnt(4)
v_mfma_f32_16x16x32_bf16 a[64:67], v[24:27], v[148:151], a[64:67]
ds_read_b64_tr_b16 v[8:9], %[vaddr0] offset:38912
ds_read_b64_tr_b16 v[10:11], %[vaddr0] offset:45056
v_mfma_f32_16x16x32_bf16 a[80:83], v[28:31], v[148:151], a[80:83]
v_exp_f32 v145, v145
v_mfma_f32_16x16x32_bf16 a[84:87], v[28:31], v[152:155], a[84:87]
buffer_load_dwordx4 v[236:239], %[lane16], s[72:75], s94 offen sc1
v_mfma_f32_16x16x32_bf16 a[68:71], v[24:27], v[152:155], a[68:71]
v_exp_f32 v146, v146
v_mfma_f32_16x16x32_bf16 a[72:75], v[24:27], v[156:159], a[72:75]
ds_read_b64_tr_b16 v[12:13], v41 offset:38912
ds_read_b64_tr_b16 v[14:15], v41 offset:45056
s_add_u32 s93, s93, 40960
s_add_u32 s94, s94, 1024
v_mfma_f32_16x16x32_bf16 a[88:91], v[28:31], v[156:159], a[88:91]
v_add_f32 v50, v50, v144
v_add_f32 v50, v50, v145
v_mfma_f32_16x16x32_bf16 a[92:95], v[28:31], v[160:163], a[92:95]
v_exp_f32 v147, v147
v_mfma_f32_16x16x32_bf16 a[76:79], v[24:27], v[160:163], a[76:79]
v_add_f32 v50, v50, v146
s_waitcnt lgkmcnt(4)
v_mfma_f32_16x16x32_bf16 a[108:111], v[32:35], v[160:163], a[108:111]
ds_read_b64_tr_b16 v[16:17], v42 offset:38912
ds_read_b64_tr_b16 v[18:19], v42 offset:45056
v_mfma_f32_16x16x32_bf16 a[124:127], v[36:39], v[160:163], a[124:127]
v_exp_f32 v124, v124
v_mfma_f32_16x16x32_bf16 a[120:123], v[36:39], v[156:159], a[120:123]
v_add_f32 v50, v50, v147
v_cvt_pk_bf16_f32 v178, v144, v145
v_mfma_f32_16x16x32_bf16 a[104:107], v[32:35], v[156:159], a[104:107]
v_exp_f32 v125, v125
v_mfma_f32_16x16x32_bf16 a[100:103], v[32:35], v[152:155], a[100:103]
ds_read_b64_tr_b16 v[20:21], v43 offset:38912
ds_read_b64_tr_b16 v[22:23], v43 offset:45056
v_mfma_f32_16x16x32_bf16 a[116:119], v[36:39], v[152:155], a[116:119]
v_cvt_pk_bf16_f32 v179, v146, v147
v_add_f32 v49, v49, v124
v_mfma_f32_16x16x32_bf16 a[112:115], v[36:39], v[148:151], a[112:115]
v_exp_f32 v126, v126
v_mfma_f32_16x16x32_bf16 a[96:99], v[32:35], v[148:151], a[96:99]
v_add_f32 v49, v49, v125
)"
R"(s_waitcnt lgkmcnt(4)
v_mfma_f32_16x16x32_bf16 a[128:131], v[8:11], v[148:151], a[128:131]
s_waitcnt vmcnt(18) lgkmcnt(0)
s_barrier
ds_read_b128 v[24:27], %[kaddr0] offset:0
v_mfma_f32_16x16x32_bf16 a[144:147], v[12:15], v[148:151], a[144:147]
v_exp_f32 v127, v127
v_mfma_f32_16x16x32_bf16 a[148:151], v[12:15], v[152:155], a[148:151]
v_add_f32 v49, v49, v126
v_add_f32 v49, v49, v127
v_mfma_f32_16x16x32_bf16 a[132:135], v[8:11], v[152:155], a[132:135]
v_exp_f32 v140, v140
v_mfma_f32_16x16x32_bf16 a[136:139], v[8:11], v[156:159], a[136:139]
ds_read_b128 v[28:31], v40 offset:0
v_cvt_pk_bf16_f32 v172, v124, v125
v_mfma_f32_16x16x32_bf16 a[152:155], v[12:15], v[156:159], a[152:155]
v_exp_f32 v141, v141
ds_read_b64_tr_b16 v[224:225], v244 offset:0
ds_read_b64_tr_b16 v[226:227], v244 offset:4096
v_mfma_f32_16x16x32_bf16 a[156:159], v[12:15], v[160:163], a[156:159]
v_cvt_pk_bf16_f32 v173, v126, v127
v_add_f32 v49, v49, v140
v_mfma_f32_16x16x32_bf16 a[140:143], v[8:11], v[160:163], a[140:143]
v_add_f32 v49, v49, v141
v_mfma_f32_16x16x32_bf16 a[172:175], v[16:19], v[160:163], a[172:175]
ds_read_b128 v[32:35], %[kaddr0] offset:1024
v_cvt_pk_bf16_f32 v174, v140, v141
v_mfma_f32_16x16x32_bf16 a[188:191], v[20:23], v[160:163], a[188:191]
v_exp_f32 v142, v142
v_mfma_f32_16x16x32_bf16 a[184:187], v[20:23], v[156:159], a[184:187]
v_exp_f32 v143, v143
v_mfma_f32_16x16x32_bf16 a[168:171], v[16:19], v[156:159], a[168:171]
v_exp_f32 v120, v120
v_mfma_f32_16x16x32_bf16 a[164:167], v[16:19], v[152:155], a[164:167]
ds_read_b128 v[36:39], v40 offset:1024
v_add_f32 v49, v49, v142
v_mfma_f32_16x16x32_bf16 a[180:183], v[20:23], v[152:155], a[180:183]
v_exp_f32 v121, v121
v_mfma_f32_16x16x32_bf16 a[176:179], v[20:23], v[148:151], a[176:179]
v_add_f32 v49, v49, v143
v_cvt_pk_bf16_f32 v175, v142, v143
v_mfma_f32_16x16x32_bf16 a[160:163], v[16:19], v[148:151], a[160:163]
v_add_f32 v48, v48, v120
s_mov_b32 s80, 11
.Lk2_loop:
s_waitcnt lgkmcnt(1)
v_mfma_f32_16x16x32_bf16 v[84:87], v[24:27], a[192:195], v[180:183]
ds_read_b128 v[8:11], %[kaddr0] offset:2048
s_add_u32 m0, s81, 36864
v_mfma_f32_16x16x32_bf16 v[84:87], v[28:31], a[208:211], v[84:87]
v_exp_f32 v122, v122
v_mfma_f32_16x16x32_bf16 v[84:87], v[32:35], a[224:227], v[84:87]
buffer_load_dwordx4 v44, %[srdK], s76 offen offset:0 lds
v_mfma_f32_16x16x32_bf16 v[88:91], v[32:35], a[228:231], v[184:187]
v_exp_f32 v123, v123
v_mfma_f32_16x16x32_bf16 v[88:91], v[28:31], a[212:215], v[88:91]
ds_read_b128 v[12:15], v40 offset:2048
v_add_f32 v48, v48, v121
v_mfma_f32_16x16x32_bf16 v[88:91], v[24:27], a[196:199], v[88:91]
v_add_f32 v48, v48, v122
v_add_f32 v48, v48, v123
v_mfma_f32_16x16x32_bf16 v[92:95], v[24:27], a[200:203], v[188:191]
v_exp_f32 v136, v136
v_mfma_f32_16x16x32_bf16 v[92:95], v[28:31], a[216:219], v[92:95]
v_exp_f32 v137, v137
v_mfma_f32_16x16x32_bf16 v[92:95], v[32:35], a[232:235], v[92:95]
ds_read_b128 v[16:19], %[kaddr0] offset:6144
v_cvt_pk_bf16_f32 v168, v120, v121
v_mfma_f32_16x16x32_bf16 v[204:207], v[224:227], v[228:231], v[204:207]
ds_read_b64_tr_b16 v[224:225], v245 offset:0
ds_read_b64_tr_b16 v[226:227], v245 offset:4096
v_mfma_f32_16x16x32_bf16 v[96:99], v[32:35], a[236:239], v[192:195]
v_cvt_pk_bf16_f32 v169, v122, v123
v_add_f32 v48, v48, v136
v_mfma_f32_16x16x32_bf16 v[96:99], v[28:31], a[220:223], v[96:99]
buffer_load_dwordx4 v45, %[srdK], s76 offen offset:1024 lds
v_mfma_f32_16x16x32_bf16 v[96:99], v[24:27], a[204:207], v[96:99]
v_add_f32 v48, v48, v137
s_waitcnt lgkmcnt(3)
v_mfma_f32_16x16x32_bf16 v[96:99], v[36:39], a[252:255], v[96:99]
ds_read_b128 v[20:23], v40 offset:6144
v_cvt_pk_bf16_f32 v170, v136, v137
v_mfma_f32_16x16x32_bf16 v[96:99], v[8:11], v[64:67], v[96:99]
v_exp_f32 v138, v138
v_mfma_f32_16x16x32_bf16 v[96:99], v[12:15], v[80:83], v[96:99]
v_exp_f32 v139, v139
v_mfma_f32_16x16x32_bf16 v[92:95], v[12:15], v[76:79], v[92:95]
v_exp_f32 v116, v116
v_mfma_f32_16x16x32_bf16 v[92:95], v[8:11], v[60:63], v[92:95]
ds_read_b128 v[24:27], %[kaddr0] offset:7168
v_add_f32 v48, v48, v138
v_mfma_f32_16x16x32_bf16 v[92:95], v[36:39], a[248:251], v[92:95]
v_exp_f32 v117, v117
v_mfma_f32_16x16x32_bf16 v[88:91], v[36:39], a[244:247], v[88:91]
buffer_load_dwordx4 v46, %[srdK], s76 offen offset:2048 lds
v_mfma_f32_16x16x32_bf16 v[88:91], v[8:11], v[56:59], v[88:91]
v_add_f32 v48, v48, v139
v_cvt_pk_bf16_f32 v171, v138, v139
v_mfma_f32_16x16x32_bf16 v[88:91], v[12:15], v[72:75], v[88:91]
ds_read_b128 v[28:31], v40 offset:7168
s_add_u32 m0, s81, 86016
v_mfma_f32_16x16x32_bf16 v[84:87], v[12:15], v[68:71], v[84:87]
v_exp_f32 v118, v118
v_mfma_f32_16x16x32_bf16 v[84:87], v[8:11], v[52:55], v[84:87]
v_add_f32 v47, v47, v116
v_add_f32 v47, v47, v117
v_mfma_f32_16x16x32_bf16 v[84:87], v[36:39], a[240:243], v[84:87]
v_add_f32 v47, v47, v118
s_waitcnt lgkmcnt(1)
v_mfma_f32_16x16x32_bf16 v[100:103], v[16:19], a[192:195], v[180:183]
ds_read_b128 v[32:35], %[kaddr0] offset:8192
v_cvt_pk_bf16_f32 v164, v116, v117
v_mfma_f32_16x16x32_bf16 v[100:103], v[20:23], a[208:211], v[100:103]
v_exp_f32 v119, v119
v_mfma_f32_16x16x32_bf16 v[100:103], v[24:27], a[224:227], v[100:103]
buffer_load_dwordx4 v44, %[srdV], s76 offen offset:0 lds
v_mfma_f32_16x16x32_bf16 v[208:211], v[224:227], v[228:231], v[208:211]
ds_read_b64_tr_b16 v[224:225], v246 offset:0
ds_read_b64_tr_b16 v[226:227], v246 offset:4096
v_mfma_f32_16x16x32_bf16 v[104:107], v[24:27], a[228:231], v[184:187]
v_exp_f32 v132, v132
v_mfma_f32_16x16x32_bf16 v[104:107], v[20:23], a[212:215], v[104:107]
ds_read_b128 v[36:39], v40 offset:8192
v_add_f32 v47, v47, v119
v_mfma_f32_16x16x32_bf16 v[104:107], v[16:19], a[196:199], v[104:107]
v_exp_f32 v133, v133
v_mfma_f32_16x16x32_bf16 v[108:111], v[16:19], a[200:203], v[188:191]
v_cvt_pk_bf16_f32 v165, v118, v119
v_add_f32 v47, v47, v132
v_mfma_f32_16x16x32_bf16 v[108:111], v[20:23], a[216:219], v[108:111]
v_exp_f32 v134, v134
v_mfma_f32_16x16x32_bf16 v[108:111], v[24:27], a[232:235], v[108:111]
ds_read_b64_tr_b16 v[8:9], %[vaddr0] offset:0
ds_read_b64_tr_b16 v[10:11], %[vaddr0] offset:6144
v_mfma_f32_16x16x32_bf16 v[112:115], v[24:27], a[236:239], v[192:195]
v_exp_f32 v135, v135
v_mfma_f32_16x16x32_bf16 v[112:115], v[20:23], a[220:223], v[112:115]
buffer_load_dwordx4 v45, %[srdV], s76 offen offset:1024 lds
v_mfma_f32_16x16x32_bf16 v[112:115], v[16:19], a[204:207], v[112:115]
v_add_f32 v47, v47, v133
s_waitcnt lgkmcnt(2)
v_mfma_f32_16x16x32_bf16 v[112:115], v[28:31], a[252:255], v[112:115]
ds_read_b64_tr_b16 v[12:13], v41 offset:0
ds_read_b64_tr_b16 v[14:15], v41 offset:6144
v_mfma_f32_16x16x32_bf16 v[112:115], v[32:35], v[64:67], v[112:115]
v_add_f32 v47, v47, v134
v_add_f32 v47, v47, v135
v_mfma_f32_16x16x32_bf16 v[112:115], v[36:39], v[80:83], v[112:115]
v_cvt_pk_bf16_f32 v166, v132, v133
v_cvt_pk_bf16_f32 v167, v134, v135
v_mfma_f32_16x16x32_bf16 v[108:111], v[36:39], v[76:79], v[108:111]
v_mfma_f32_16x16x32_bf16 v[108:111], v[32:35], v[60:63], v[108:111]
ds_read_b64_tr_b16 v[16:17], v42 offset:0
ds_read_b64_tr_b16 v[18:19], v42 offset:6144
v_mfma_f32_16x16x32_bf16 v[108:111], v[28:31], a[248:251], v[108:111]
v_mfma_f32_16x16x32_bf16 v[104:107], v[28:31], a[244:247], v[104:107]
buffer_load_dwordx4 v46, %[srdV], s76 offen offset:2048 lds
v_mfma_f32_16x16x32_bf16 v[104:107], v[32:35], v[56:59], v[104:107]
v_mfma_f32_16x16x32_bf16 v[104:107], v[36:39], v[72:75], v[104:107]
ds_read_b64_tr_b16 v[20:21], v43 offset:0
ds_read_b64_tr_b16 v[22:23], v43 offset:6144
v_mfma_f32_16x16x32_bf16 v[212:215], v[224:227], v[228:231], v[212:215]
ds_read_b64_tr_b16 v[224:225], v247 offset:0
ds_read_b64_tr_b16 v[226:227], v247 offset:4096
v_mfma_f32_16x16x32_bf16 v[100:103], v[36:39], v[68:71], v[100:103]
v_mfma_f32_16x16x32_bf16 v[100:103], v[32:35], v[52:55], v[100:103]
s_add_u32 s76, s76, 12288
v_mfma_f32_16x16x32_bf16 v[100:103], v[28:31], a[240:243], v[100:103]
s_waitcnt lgkmcnt(6)
v_mfma_f32_16x16x32_bf16 a[0:3], v[8:11], v[164:167], a[0:3]
ds_read_b64_tr_b16 v[24:25], %[vaddr0] offset:1024
ds_read_b64_tr_b16 v[26:27], %[vaddr0] offset:7168
v_mfma_f32_16x16x32_bf16 a[16:19], v[12:15], v[164:167], a[16:19]
s_add_u32 m0, s82, 122880
v_mfma_f32_16x16x32_bf16 a[20:23], v[12:15], v[168:171], a[20:23]
buffer_load_dwordx4 v248, %[srdG], s93 offen offset:0 lds
v_mfma_f32_16x16x32_bf16 a[4:7], v[8:11], v[168:171], a[4:7]
v_exp_f32 v96, v96
v_mfma_f32_16x16x32_bf16 a[8:11], v[8:11], v[172:175], a[8:11]
ds_read_b64_tr_b16 v[28:29], v41 offset:1024
ds_read_b64_tr_b16 v[30:31], v41 offset:7168
v_mfma_f32_16x16x32_bf16 a[24:27], v[12:15], v[172:175], a[24:27]
v_exp_f32 v97, v97
v_mfma_f32_16x16x32_bf16 a[28:31], v[12:15], v[176:179], a[28:31]
v_exp_f32 v98, v98
v_mfma_f32_16x16x32_bf16 a[12:15], v[8:11], v[176:179], a[12:15]
v_add_f32 v50, v50, v96
s_waitcnt lgkmcnt(6)
v_mfma_f32_16x16x32_bf16 a[44:47], v[16:19], v[176:179], a[44:47]
ds_read_b64_tr_b16 v[32:33], v42 offset:1024
ds_read_b64_tr_b16 v[34:35], v42 offset:7168
v_mfma_f32_16x16x32_bf16 a[60:63], v[20:23], v[176:179], a[60:63]
v_exp_f32 v99, v99
v_mfma_f32_16x16x32_bf16 a[56:59], v[20:23], v[172:175], a[56:59]
buffer_load_dwordx4 v249, %[srdG], s93 offen offset:1024 lds
v_mfma_f32_16x16x32_bf16 a[40:43], v[16:19], v[172:175], a[40:43]
v_add_f32 v50, v50, v97
v_add_f32 v50, v50, v98
v_mfma_f32_16x16x32_bf16 a[36:39], v[16:19], v[168:171], a[36:39]
ds_read_b64_tr_b16 v[36:37], v43 offset:1024
)"
R"(ds_read_b64_tr_b16 v[38:39], v43 offset:7168
v_mfma_f32_16x16x32_bf16 a[52:55], v[20:23], v[168:171], a[52:55]
v_exp_f32 v112, v112
s_waitcnt lgkmcnt(8)
v_mfma_f32_16x16x32_bf16 v[216:219], v[224:227], v[228:231], v[216:219]
ds_read_b64_tr_b16 v[224:225], v244 offset:1024
ds_read_b64_tr_b16 v[226:227], v244 offset:5120
v_mfma_f32_16x16x32_bf16 a[48:51], v[20:23], v[164:167], a[48:51]
v_add_f32 v50, v50, v99
v_cvt_pk_bf16_f32 v160, v96, v97
v_mfma_f32_16x16x32_bf16 a[32:35], v[16:19], v[164:167], a[32:35]
v_cvt_pk_bf16_f32 v161, v98, v99
s_waitcnt lgkmcnt(6)
v_mfma_f32_16x16x32_bf16 a[64:67], v[24:27], v[164:167], a[64:67]
ds_read_b64_tr_b16 v[8:9], %[vaddr0] offset:2048
ds_read_b64_tr_b16 v[10:11], %[vaddr0] offset:8192
v_mfma_f32_16x16x32_bf16 a[80:83], v[28:31], v[164:167], a[80:83]
v_exp_f32 v113, v113
v_mfma_f32_16x16x32_bf16 a[84:87], v[28:31], v[168:171], a[84:87]
buffer_load_dwordx4 v[240:243], %[lane16], s[72:75], s94 offen sc1
v_mfma_f32_16x16x32_bf16 a[68:71], v[24:27], v[168:171], a[68:71]
v_exp_f32 v114, v114
v_mfma_f32_16x16x32_bf16 a[72:75], v[24:27], v[172:175], a[72:75]
ds_read_b64_tr_b16 v[12:13], v41 offset:2048
ds_read_b64_tr_b16 v[14:15], v41 offset:8192
s_add_u32 s93, s93, 40960
s_add_u32 s94, s94, 1024
v_mfma_f32_16x16x32_bf16 a[88:91], v[28:31], v[172:175], a[88:91]
v_add_f32 v50, v50, v112
v_add_f32 v50, v50, v113
v_mfma_f32_16x16x32_bf16 a[92:95], v[28:31], v[176:179], a[92:95]
v_exp_f32 v115, v115
v_mfma_f32_16x16x32_bf16 a[76:79], v[24:27], v[176:179], a[76:79]
v_add_f32 v50, v50, v114
s_waitcnt lgkmcnt(6)
v_mfma_f32_16x16x32_bf16 a[108:111], v[32:35], v[176:179], a[108:111]
ds_read_b64_tr_b16 v[16:17], v42 offset:2048
ds_read_b64_tr_b16 v[18:19], v42 offset:8192
v_mfma_f32_16x16x32_bf16 a[124:127], v[36:39], v[176:179], a[124:127]
v_exp_f32 v92, v92
v_mfma_f32_16x16x32_bf16 a[120:123], v[36:39], v[172:175], a[120:123]
v_add_f32 v50, v50, v115
v_cvt_pk_bf16_f32 v162, v112, v113
v_mfma_f32_16x16x32_bf16 a[104:107], v[32:35], v[172:175], a[104:107]
v_exp_f32 v93, v93
v_mfma_f32_16x16x32_bf16 a[100:103], v[32:35], v[168:171], a[100:103]
ds_read_b64_tr_b16 v[20:21], v43 offset:2048
ds_read_b64_tr_b16 v[22:23], v43 offset:8192
v_mfma_f32_16x16x32_bf16 a[116:119], v[36:39], v[168:171], a[116:119]
v_cvt_pk_bf16_f32 v163, v114, v115
v_add_f32 v49, v49, v92
v_mfma_f32_16x16x32_bf16 a[112:115], v[36:39], v[164:167], a[112:115]
v_exp_f32 v94, v94
v_mfma_f32_16x16x32_bf16 a[96:99], v[32:35], v[164:167], a[96:99]
v_add_f32 v49, v49, v93
s_waitcnt lgkmcnt(4)
v_mfma_f32_16x16x32_bf16 a[128:131], v[8:11], v[164:167], a[128:131]
s_waitcnt vmcnt(18) lgkmcnt(0)
s_barrier
ds_read_b128 v[24:27], %[kaddr0] offset:12288
v_mfma_f32_16x16x32_bf16 a[144:147], v[12:15], v[164:167], a[144:147]
v_exp_f32 v95, v95
v_mfma_f32_16x16x32_bf16 a[148:151], v[12:15], v[168:171], a[148:151]
v_add_f32 v49, v49, v94
v_add_f32 v49, v49, v95
v_mfma_f32_16x16x32_bf16 a[132:135], v[8:11], v[168:171], a[132:135]
v_exp_f32 v108, v108
v_mfma_f32_16x16x32_bf16 a[136:139], v[8:11], v[172:175], a[136:139]
ds_read_b128 v[28:31], v40 offset:12288
v_cvt_pk_bf16_f32 v156, v92, v93
v_mfma_f32_16x16x32_bf16 a[152:155], v[12:15], v[172:175], a[152:155]
v_exp_f32 v109, v109
v_mfma_f32_16x16x32_bf16 v[220:223], v[224:227], v[228:231], v[220:223]
ds_read_b64_tr_b16 v[224:225], v244 offset:8192
ds_read_b64_tr_b16 v[226:227], v244 offset:12288
v_mfma_f32_16x16x32_bf16 a[156:159], v[12:15], v[176:179], a[156:159]
v_cvt_pk_bf16_f32 v157, v94, v95
v_add_f32 v49, v49, v108
v_mfma_f32_16x16x32_bf16 a[140:143], v[8:11], v[176:179], a[140:143]
v_add_f32 v49, v49, v109
v_mfma_f32_16x16x32_bf16 a[172:175], v[16:19], v[176:179], a[172:175]
ds_read_b128 v[32:35], %[kaddr0] offset:13312
v_cvt_pk_bf16_f32 v158, v108, v109
v_mfma_f32_16x16x32_bf16 a[188:191], v[20:23], v[176:179], a[188:191]
v_exp_f32 v110, v110
v_mfma_f32_16x16x32_bf16 a[184:187], v[20:23], v[172:175], a[184:187]
v_exp_f32 v111, v111
v_mfma_f32_16x16x32_bf16 a[168:171], v[16:19], v[172:175], a[168:171]
v_exp_f32 v88, v88
v_mfma_f32_16x16x32_bf16 a[164:167], v[16:19], v[168:171], a[164:167]
ds_read_b128 v[36:39], v40 offset:13312
v_add_f32 v49, v49, v110
v_mfma_f32_16x16x32_bf16 a[180:183], v[20:23], v[168:171], a[180:183]
v_exp_f32 v89, v89
v_mfma_f32_16x16x32_bf16 a[176:179], v[20:23], v[164:167], a[176:179]
v_add_f32 v49, v49, v111
v_cvt_pk_bf16_f32 v159, v110, v111
v_mfma_f32_16x16x32_bf16 a[160:163], v[16:19], v[164:167], a[160:163]
v_add_f32 v48, v48, v88
s_waitcnt lgkmcnt(1)
v_mfma_f32_16x16x32_bf16 v[116:119], v[24:27], a[192:195], v[180:183]
ds_read_b128 v[8:11], %[kaddr0] offset:14336
s_add_u32 m0, s81, 0
v_mfma_f32_16x16x32_bf16 v[116:119], v[28:31], a[208:211], v[116:119]
v_exp_f32 v90, v90
v_mfma_f32_16x16x32_bf16 v[116:119], v[32:35], a[224:227], v[116:119]
buffer_load_dwordx4 v44, %[srdK], s76 offen offset:0 lds
v_mfma_f32_16x16x32_bf16 v[120:123], v[32:35], a[228:231], v[184:187]
v_exp_f32 v91, v91
v_mfma_f32_16x16x32_bf16 v[120:123], v[28:31], a[212:215], v[120:123]
ds_read_b128 v[12:15], v40 offset:14336
v_add_f32 v48, v48, v89
v_mfma_f32_16x16x32_bf16 v[120:123], v[24:27], a[196:199], v[120:123]
v_add_f32 v48, v48, v90
v_add_f32 v48, v48, v91
v_mfma_f32_16x16x32_bf16 v[124:127], v[24:27], a[200:203], v[188:191]
v_exp_f32 v104, v104
v_mfma_f32_16x16x32_bf16 v[124:127], v[28:31], a[216:219], v[124:127]
v_exp_f32 v105, v105
v_mfma_f32_16x16x32_bf16 v[124:127], v[32:35], a[232:235], v[124:127]
ds_read_b128 v[16:19], %[kaddr0] offset:18432
v_cvt_pk_bf16_f32 v152, v88, v89
v_mfma_f32_16x16x32_bf16 v[204:207], v[224:227], v[232:235], v[204:207]
ds_read_b64_tr_b16 v[224:225], v245 offset:8192
ds_read_b64_tr_b16 v[226:227], v245 offset:12288
v_mfma_f32_16x16x32_bf16 v[128:131], v[32:35], a[236:239], v[192:195]
v_cvt_pk_bf16_f32 v153, v90, v91
v_add_f32 v48, v48, v104
v_mfma_f32_16x16x32_bf16 v[128:131], v[28:31], a[220:223], v[128:131]
buffer_load_dwordx4 v45, %[srdK], s76 offen offset:1024 lds
v_mfma_f32_16x16x32_bf16 v[128:131], v[24:27], a[204:207], v[128:131]
v_add_f32 v48, v48, v105
s_waitcnt lgkmcnt(3)
v_mfma_f32_16x16x32_bf16 v[128:131], v[36:39], a[252:255], v[128:131]
ds_read_b128 v[20:23], v40 offset:18432
v_cvt_pk_bf16_f32 v154, v104, v105
v_mfma_f32_16x16x32_bf16 v[128:131], v[8:11], v[64:67], v[128:131]
v_exp_f32 v106, v106
v_mfma_f32_16x16x32_bf16 v[128:131], v[12:15], v[80:83], v[128:131]
v_exp_f32 v107, v107
v_mfma_f32_16x16x32_bf16 v[124:127], v[12:15], v[76:79], v[124:127]
v_exp_f32 v84, v84
v_mfma_f32_16x16x32_bf16 v[124:127], v[8:11], v[60:63], v[124:127]
ds_read_b128 v[24:27], %[kaddr0] offset:19456
v_add_f32 v48, v48, v106
v_mfma_f32_16x16x32_bf16 v[124:127], v[36:39], a[248:251], v[124:127]
v_exp_f32 v85, v85
v_mfma_f32_16x16x32_bf16 v[120:123], v[36:39], a[244:247], v[120:123]
buffer_load_dwordx4 v46, %[srdK], s76 offen offset:2048 lds
v_mfma_f32_16x16x32_bf16 v[120:123], v[8:11], v[56:59], v[120:123]
v_add_f32 v48, v48, v107
v_cvt_pk_bf16_f32 v155, v106, v107
v_mfma_f32_16x16x32_bf16 v[120:123], v[12:15], v[72:75], v[120:123]
ds_read_b128 v[28:31], v40 offset:19456
s_add_u32 m0, s81, 49152
v_mfma_f32_16x16x32_bf16 v[116:119], v[12:15], v[68:71], v[116:119]
v_exp_f32 v86, v86
v_mfma_f32_16x16x32_bf16 v[116:119], v[8:11], v[52:55], v[116:119]
v_add_f32 v47, v47, v84
v_add_f32 v47, v47, v85
v_mfma_f32_16x16x32_bf16 v[116:119], v[36:39], a[240:243], v[116:119]
v_add_f32 v47, v47, v86
s_waitcnt lgkmcnt(1)
v_mfma_f32_16x16x32_bf16 v[132:135], v[16:19], a[192:195], v[180:183]
ds_read_b128 v[32:35], %[kaddr0] offset:20480
v_cvt_pk_bf16_f32 v148, v84, v85
v_mfma_f32_16x16x32_bf16 v[132:135], v[20:23], a[208:211], v[132:135]
v_exp_f32 v87, v87
v_mfma_f32_16x16x32_bf16 v[132:135], v[24:27], a[224:227], v[132:135]
buffer_load_dwordx4 v44, %[srdV], s76 offen offset:0 lds
v_mfma_f32_16x16x32_bf16 v[208:211], v[224:227], v[232:235], v[208:211]
ds_read_b64_tr_b16 v[224:225], v246 offset:8192
ds_read_b64_tr_b16 v[226:227], v246 offset:12288
v_mfma_f32_16x16x32_bf16 v[136:139], v[24:27], a[228:231], v[184:187]
v_exp_f32 v100, v100
v_mfma_f32_16x16x32_bf16 v[136:139], v[20:23], a[212:215], v[136:139]
ds_read_b128 v[36:39], v40 offset:20480
v_add_f32 v47, v47, v87
v_mfma_f32_16x16x32_bf16 v[136:139], v[16:19], a[196:199], v[136:139]
v_exp_f32 v101, v101
v_mfma_f32_16x16x32_bf16 v[140:143], v[16:19], a[200:203], v[188:191]
v_cvt_pk_bf16_f32 v149, v86, v87
v_add_f32 v47, v47, v100
v_mfma_f32_16x16x32_bf16 v[140:143], v[20:23], a[216:219], v[140:143]
v_exp_f32 v102, v102
v_mfma_f32_16x16x32_bf16 v[140:143], v[24:27], a[232:235], v[140:143]
ds_read_b64_tr_b16 v[8:9], %[vaddr0] offset:12288
ds_read_b64_tr_b16 v[10:11], %[vaddr0] offset:18432
v_mfma_f32_16x16x32_bf16 v[144:147], v[24:27], a[236:239], v[192:195]
v_exp_f32 v103, v103
v_mfma_f32_16x16x32_bf16 v[144:147], v[20:23], a[220:223], v[144:147]
buffer_load_dwordx4 v45, %[srdV], s76 offen offset:1024 lds
v_mfma_f32_16x16x32_bf16 v[144:147], v[16:19], a[204:207], v[144:147]
v_add_f32 v47, v47, v101
s_waitcnt lgkmcnt(2)
v_mfma_f32_16x16x32_bf16 v[144:147], v[28:31], a[252:255], v[144:147]
ds_read_b64_tr_b16 v[12:13], v41 offset:12288
ds_read_b64_tr_b16 v[14:15], v41 offset:18432
v_mfma_f32_16x16x32_bf16 v[144:147], v[32:35], v[64:67], v[144:147]
v_add_f32 v47, v47, v102
)"
R"(v_add_f32 v47, v47, v103
v_mfma_f32_16x16x32_bf16 v[144:147], v[36:39], v[80:83], v[144:147]
v_cvt_pk_bf16_f32 v150, v100, v101
v_cvt_pk_bf16_f32 v151, v102, v103
v_mfma_f32_16x16x32_bf16 v[140:143], v[36:39], v[76:79], v[140:143]
v_mfma_f32_16x16x32_bf16 v[140:143], v[32:35], v[60:63], v[140:143]
ds_read_b64_tr_b16 v[16:17], v42 offset:12288
ds_read_b64_tr_b16 v[18:19], v42 offset:18432
v_mfma_f32_16x16x32_bf16 v[140:143], v[28:31], a[248:251], v[140:143]
v_mfma_f32_16x16x32_bf16 v[136:139], v[28:31], a[244:247], v[136:139]
buffer_load_dwordx4 v46, %[srdV], s76 offen offset:2048 lds
v_mfma_f32_16x16x32_bf16 v[136:139], v[32:35], v[56:59], v[136:139]
v_mfma_f32_16x16x32_bf16 v[136:139], v[36:39], v[72:75], v[136:139]
ds_read_b64_tr_b16 v[20:21], v43 offset:12288
ds_read_b64_tr_b16 v[22:23], v43 offset:18432
v_mfma_f32_16x16x32_bf16 v[212:215], v[224:227], v[232:235], v[212:215]
ds_read_b64_tr_b16 v[224:225], v247 offset:8192
ds_read_b64_tr_b16 v[226:227], v247 offset:12288
v_mfma_f32_16x16x32_bf16 v[132:135], v[36:39], v[68:71], v[132:135]
v_mfma_f32_16x16x32_bf16 v[132:135], v[32:35], v[52:55], v[132:135]
s_add_u32 s76, s76, 12288
v_mfma_f32_16x16x32_bf16 v[132:135], v[28:31], a[240:243], v[132:135]
s_waitcnt lgkmcnt(6)
v_mfma_f32_16x16x32_bf16 a[0:3], v[8:11], v[148:151], a[0:3]
ds_read_b64_tr_b16 v[24:25], %[vaddr0] offset:13312
ds_read_b64_tr_b16 v[26:27], %[vaddr0] offset:19456
v_mfma_f32_16x16x32_bf16 a[16:19], v[12:15], v[148:151], a[16:19]
s_add_u32 m0, s82, 98304
v_mfma_f32_16x16x32_bf16 a[20:23], v[12:15], v[152:155], a[20:23]
buffer_load_dwordx4 v248, %[srdG], s93 offen offset:0 lds
v_mfma_f32_16x16x32_bf16 a[4:7], v[8:11], v[152:155], a[4:7]
v_exp_f32 v128, v128
v_mfma_f32_16x16x32_bf16 a[8:11], v[8:11], v[156:159], a[8:11]
ds_read_b64_tr_b16 v[28:29], v41 offset:13312
ds_read_b64_tr_b16 v[30:31], v41 offset:19456
v_mfma_f32_16x16x32_bf16 a[24:27], v[12:15], v[156:159], a[24:27]
v_exp_f32 v129, v129
v_mfma_f32_16x16x32_bf16 a[28:31], v[12:15], v[160:163], a[28:31]
v_exp_f32 v130, v130
v_mfma_f32_16x16x32_bf16 a[12:15], v[8:11], v[160:163], a[12:15]
v_add_f32 v50, v50, v128
s_waitcnt lgkmcnt(6)
v_mfma_f32_16x16x32_bf16 a[44:47], v[16:19], v[160:163], a[44:47]
ds_read_b64_tr_b16 v[32:33], v42 offset:13312
ds_read_b64_tr_b16 v[34:35], v42 offset:19456
v_mfma_f32_16x16x32_bf16 a[60:63], v[20:23], v[160:163], a[60:63]
v_exp_f32 v131, v131
v_mfma_f32_16x16x32_bf16 a[56:59], v[20:23], v[156:159], a[56:59]
buffer_load_dwordx4 v249, %[srdG], s93 offen offset:1024 lds
v_mfma_f32_16x16x32_bf16 a[40:43], v[16:19], v[156:159], a[40:43]
v_add_f32 v50, v50, v129
v_add_f32 v50, v50, v130
v_mfma_f32_16x16x32_bf16 a[36:39], v[16:19], v[152:155], a[36:39]
ds_read_b64_tr_b16 v[36:37], v43 offset:13312
ds_read_b64_tr_b16 v[38:39], v43 offset:19456
v_mfma_f32_16x16x32_bf16 a[52:55], v[20:23], v[152:155], a[52:55]
v_exp_f32 v144, v144
s_waitcnt lgkmcnt(8)
v_mfma_f32_16x16x32_bf16 v[216:219], v[224:227], v[232:235], v[216:219]
ds_read_b64_tr_b16 v[224:225], v244 offset:9216
ds_read_b64_tr_b16 v[226:227], v244 offset:13312
v_mfma_f32_16x16x32_bf16 a[48:51], v[20:23], v[148:151], a[48:51]
v_add_f32 v50, v50, v131
v_cvt_pk_bf16_f32 v176, v128, v129
v_mfma_f32_16x16x32_bf16 a[32:35], v[16:19], v[148:151], a[32:35]
v_cvt_pk_bf16_f32 v177, v130, v131
s_waitcnt lgkmcnt(6)
v_mfma_f32_16x16x32_bf16 a[64:67], v[24:27], v[148:151], a[64:67]
ds_read_b64_tr_b16 v[8:9], %[vaddr0] offset:14336
ds_read_b64_tr_b16 v[10:11], %[vaddr0] offset:20480
v_mfma_f32_16x16x32_bf16 a[80:83], v[28:31], v[148:151], a[80:83]
v_exp_f32 v145, v145
v_mfma_f32_16x16x32_bf16 a[84:87], v[28:31], v[152:155], a[84:87]
buffer_load_dwordx4 v[228:231], %[lane16], s[72:75], s94 offen sc1
v_mfma_f32_16x16x32_bf16 a[68:71], v[24:27], v[152:155], a[68:71]
v_exp_f32 v146, v146
v_mfma_f32_16x16x32_bf16 a[72:75], v[24:27], v[156:159], a[72:75]
ds_read_b64_tr_b16 v[12:13], v41 offset:14336
ds_read_b64_tr_b16 v[14:15], v41 offset:20480
s_add_u32 s93, s93, 40960
s_add_u32 s94, s94, 1024
v_mfma_f32_16x16x32_bf16 a[88:91], v[28:31], v[156:159], a[88:91]
v_add_f32 v50, v50, v144
v_add_f32 v50, v50, v145
v_mfma_f32_16x16x32_bf16 a[92:95], v[28:31], v[160:163], a[92:95]
v_exp_f32 v147, v147
v_mfma_f32_16x16x32_bf16 a[76:79], v[24:27], v[160:163], a[76:79]
v_add_f32 v50, v50, v146
s_waitcnt lgkmcnt(6)
v_mfma_f32_16x16x32_bf16 a[108:111], v[32:35], v[160:163], a[108:111]
ds_read_b64_tr_b16 v[16:17], v42 offset:14336
ds_read_b64_tr_b16 v[18:19], v42 offset:20480
v_mfma_f32_16x16x32_bf16 a[124:127], v[36:39], v[160:163], a[124:127]
v_exp_f32 v124, v124
v_mfma_f32_16x16x32_bf16 a[120:123], v[36:39], v[156:159], a[120:123]
v_add_f32 v50, v50, v147
v_cvt_pk_bf16_f32 v178, v144, v145
v_mfma_f32_16x16x32_bf16 a[104:107], v[32:35], v[156:159], a[104:107]
v_exp_f32 v125, v125
v_mfma_f32_16x16x32_bf16 a[100:103], v[32:35], v[152:155], a[100:103]
ds_read_b64_tr_b16 v[20:21], v43 offset:14336
ds_read_b64_tr_b16 v[22:23], v43 offset:20480
v_mfma_f32_16x16x32_bf16 a[116:119], v[36:39], v[152:155], a[116:119]
v_cvt_pk_bf16_f32 v179, v146, v147
v_add_f32 v49, v49, v124
v_mfma_f32_16x16x32_bf16 a[112:115], v[36:39], v[148:151], a[112:115]
v_exp_f32 v126, v126
v_mfma_f32_16x16x32_bf16 a[96:99], v[32:35], v[148:151], a[96:99]
v_add_f32 v49, v49, v125
s_waitcnt lgkmcnt(4)
v_mfma_f32_16x16x32_bf16 a[128:131], v[8:11], v[148:151], a[128:131]
s_waitcnt vmcnt(18) lgkmcnt(0)
s_barrier
ds_read_b128 v[24:27], %[kaddr0] offset:24576
v_mfma_f32_16x16x32_bf16 a[144:147], v[12:15], v[148:151], a[144:147]
v_exp_f32 v127, v127
v_mfma_f32_16x16x32_bf16 a[148:151], v[12:15], v[152:155], a[148:151]
v_add_f32 v49, v49, v126
v_add_f32 v49, v49, v127
v_mfma_f32_16x16x32_bf16 a[132:135], v[8:11], v[152:155], a[132:135]
v_exp_f32 v140, v140
v_mfma_f32_16x16x32_bf16 a[136:139], v[8:11], v[156:159], a[136:139]
ds_read_b128 v[28:31], v40 offset:24576
v_cvt_pk_bf16_f32 v172, v124, v125
v_mfma_f32_16x16x32_bf16 a[152:155], v[12:15], v[156:159], a[152:155]
v_exp_f32 v141, v141
v_mfma_f32_16x16x32_bf16 v[220:223], v[224:227], v[232:235], v[220:223]
ds_read_b64_tr_b16 v[224:225], v244 offset:16384
ds_read_b64_tr_b16 v[226:227], v244 offset:20480
v_mfma_f32_16x16x32_bf16 a[156:159], v[12:15], v[160:163], a[156:159]
v_cvt_pk_bf16_f32 v173, v126, v127
v_add_f32 v49, v49, v140
v_mfma_f32_16x16x32_bf16 a[140:143], v[8:11], v[160:163], a[140:143]
v_add_f32 v49, v49, v141
v_mfma_f32_16x16x32_bf16 a[172:175], v[16:19], v[160:163], a[172:175]
ds_read_b128 v[32:35], %[kaddr0] offset:25600
v_cvt_pk_bf16_f32 v174, v140, v141
v_mfma_f32_16x16x32_bf16 a[188:191], v[20:23], v[160:163], a[188:191]
v_exp_f32 v142, v142
v_mfma_f32_16x16x32_bf16 a[184:187], v[20:23], v[156:159], a[184:187]
v_exp_f32 v143, v143
v_mfma_f32_16x16x32_bf16 a[168:171], v[16:19], v[156:159], a[168:171]
v_exp_f32 v120, v120
v_mfma_f32_16x16x32_bf16 a[164:167], v[16:19], v[152:155], a[164:167]
ds_read_b128 v[36:39], v40 offset:25600
v_add_f32 v49, v49, v142
v_mfma_f32_16x16x32_bf16 a[180:183], v[20:23], v[152:155], a[180:183]
v_exp_f32 v121, v121
v_mfma_f32_16x16x32_bf16 a[176:179], v[20:23], v[148:151], a[176:179]
v_add_f32 v49, v49, v143
v_cvt_pk_bf16_f32 v175, v142, v143
v_mfma_f32_16x16x32_bf16 a[160:163], v[16:19], v[148:151], a[160:163]
v_add_f32 v48, v48, v120
s_waitcnt lgkmcnt(1)
v_mfma_f32_16x16x32_bf16 v[84:87], v[24:27], a[192:195], v[180:183]
ds_read_b128 v[8:11], %[kaddr0] offset:26624
s_add_u32 m0, s81, 12288
v_mfma_f32_16x16x32_bf16 v[84:87], v[28:31], a[208:211], v[84:87]
v_exp_f32 v122, v122
v_mfma_f32_16x16x32_bf16 v[84:87], v[32:35], a[224:227], v[84:87]
buffer_load_dwordx4 v44, %[srdK], s76 offen offset:0 lds
v_mfma_f32_16x16x32_bf16 v[88:91], v[32:35], a[228:231], v[184:187]
v_exp_f32 v123, v123
v_mfma_f32_16x16x32_bf16 v[88:91], v[28:31], a[212:215], v[88:91]
ds_read_b128 v[12:15], v40 offset:26624
v_add_f32 v48, v48, v121
v_mfma_f32_16x16x32_bf16 v[88:91], v[24:27], a[196:199], v[88:91]
v_add_f32 v48, v48, v122
v_add_f32 v48, v48, v123
v_mfma_f32_16x16x32_bf16 v[92:95], v[24:27], a[200:203], v[188:191]
v_exp_f32 v136, v136
v_mfma_f32_16x16x32_bf16 v[92:95], v[28:31], a[216:219], v[92:95]
v_exp_f32 v137, v137
v_mfma_f32_16x16x32_bf16 v[92:95], v[32:35], a[232:235], v[92:95]
ds_read_b128 v[16:19], %[kaddr0] offset:30720
v_cvt_pk_bf16_f32 v168, v120, v121
v_mfma_f32_16x16x32_bf16 v[204:207], v[224:227], v[236:239], v[204:207]
ds_read_b64_tr_b16 v[224:225], v245 offset:16384
ds_read_b64_tr_b16 v[226:227], v245 offset:20480
v_mfma_f32_16x16x32_bf16 v[96:99], v[32:35], a[236:239], v[192:195]
v_cvt_pk_bf16_f32 v169, v122, v123
v_add_f32 v48, v48, v136
v_mfma_f32_16x16x32_bf16 v[96:99], v[28:31], a[220:223], v[96:99]
buffer_load_dwordx4 v45, %[srdK], s76 offen offset:1024 lds
v_mfma_f32_16x16x32_bf16 v[96:99], v[24:27], a[204:207], v[96:99]
v_add_f32 v48, v48, v137
s_waitcnt lgkmcnt(3)
v_mfma_f32_16x16x32_bf16 v[96:99], v[36:39], a[252:255], v[96:99]
ds_read_b128 v[20:23], v40 offset:30720
v_cvt_pk_bf16_f32 v170, v136, v137
v_mfma_f32_16x16x32_bf16 v[96:99], v[8:11], v[64:67], v[96:99]
v_exp_f32 v138, v138
v_mfma_f32_16x16x32_bf16 v[96:99], v[12:15], v[80:83], v[96:99]
v_exp_f32 v139, v139
v_mfma_f32_16x16x32_bf16 v[92:95], v[12:15], v[76:79], v[92:95]
v_exp_f32 v116, v116
v_mfma_f32_16x16x32_bf16 v[92:95], v[8:11], v[60:63], v[92:95]
ds_read_b128 v[24:27], %[kaddr0] offset:31744
v_add_f32 v48, v48, v138
v_mfma_f32_16x16x32_bf16 v[92:95], v[36:39], a[248:251], v[92:95]
)"
R"(v_exp_f32 v117, v117
v_mfma_f32_16x16x32_bf16 v[88:91], v[36:39], a[244:247], v[88:91]
buffer_load_dwordx4 v46, %[srdK], s76 offen offset:2048 lds
v_mfma_f32_16x16x32_bf16 v[88:91], v[8:11], v[56:59], v[88:91]
v_add_f32 v48, v48, v139
v_cvt_pk_bf16_f32 v171, v138, v139
v_mfma_f32_16x16x32_bf16 v[88:91], v[12:15], v[72:75], v[88:91]
ds_read_b128 v[28:31], v40 offset:31744
s_add_u32 m0, s81, 61440
v_mfma_f32_16x16x32_bf16 v[84:87], v[12:15], v[68:71], v[84:87]
v_exp_f32 v118, v118
v_mfma_f32_16x16x32_bf16 v[84:87], v[8:11], v[52:55], v[84:87]
v_add_f32 v47, v47, v116
v_add_f32 v47, v47, v117
v_mfma_f32_16x16x32_bf16 v[84:87], v[36:39], a[240:243], v[84:87]
v_add_f32 v47, v47, v118
s_waitcnt lgkmcnt(1)
v_mfma_f32_16x16x32_bf16 v[100:103], v[16:19], a[192:195], v[180:183]
ds_read_b128 v[32:35], %[kaddr0] offset:32768
v_cvt_pk_bf16_f32 v164, v116, v117
v_mfma_f32_16x16x32_bf16 v[100:103], v[20:23], a[208:211], v[100:103]
v_exp_f32 v119, v119
v_mfma_f32_16x16x32_bf16 v[100:103], v[24:27], a[224:227], v[100:103]
buffer_load_dwordx4 v44, %[srdV], s76 offen offset:0 lds
v_mfma_f32_16x16x32_bf16 v[208:211], v[224:227], v[236:239], v[208:211]
ds_read_b64_tr_b16 v[224:225], v246 offset:16384
ds_read_b64_tr_b16 v[226:227], v246 offset:20480
v_mfma_f32_16x16x32_bf16 v[104:107], v[24:27], a[228:231], v[184:187]
v_exp_f32 v132, v132
v_mfma_f32_16x16x32_bf16 v[104:107], v[20:23], a[212:215], v[104:107]
ds_read_b128 v[36:39], v40 offset:32768
v_add_f32 v47, v47, v119
v_mfma_f32_16x16x32_bf16 v[104:107], v[16:19], a[196:199], v[104:107]
v_exp_f32 v133, v133
v_mfma_f32_16x16x32_bf16 v[108:111], v[16:19], a[200:203], v[188:191]
v_cvt_pk_bf16_f32 v165, v118, v119
v_add_f32 v47, v47, v132
v_mfma_f32_16x16x32_bf16 v[108:111], v[20:23], a[216:219], v[108:111]
v_exp_f32 v134, v134
v_mfma_f32_16x16x32_bf16 v[108:111], v[24:27], a[232:235], v[108:111]
ds_read_b64_tr_b16 v[8:9], %[vaddr0] offset:24576
ds_read_b64_tr_b16 v[10:11], %[vaddr0] offset:30720
v_mfma_f32_16x16x32_bf16 v[112:115], v[24:27], a[236:239], v[192:195]
v_exp_f32 v135, v135
v_mfma_f32_16x16x32_bf16 v[112:115], v[20:23], a[220:223], v[112:115]
buffer_load_dwordx4 v45, %[srdV], s76 offen offset:1024 lds
v_mfma_f32_16x16x32_bf16 v[112:115], v[16:19], a[204:207], v[112:115]
v_add_f32 v47, v47, v133
s_waitcnt lgkmcnt(2)
v_mfma_f32_16x16x32_bf16 v[112:115], v[28:31], a[252:255], v[112:115]
ds_read_b64_tr_b16 v[12:13], v41 offset:24576
ds_read_b64_tr_b16 v[14:15], v41 offset:30720
v_mfma_f32_16x16x32_bf16 v[112:115], v[32:35], v[64:67], v[112:115]
v_add_f32 v47, v47, v134
v_add_f32 v47, v47, v135
v_mfma_f32_16x16x32_bf16 v[112:115], v[36:39], v[80:83], v[112:115]
v_cvt_pk_bf16_f32 v166, v132, v133
v_cvt_pk_bf16_f32 v167, v134, v135
v_mfma_f32_16x16x32_bf16 v[108:111], v[36:39], v[76:79], v[108:111]
v_mfma_f32_16x16x32_bf16 v[108:111], v[32:35], v[60:63], v[108:111]
ds_read_b64_tr_b16 v[16:17], v42 offset:24576
ds_read_b64_tr_b16 v[18:19], v42 offset:30720
v_mfma_f32_16x16x32_bf16 v[108:111], v[28:31], a[248:251], v[108:111]
v_mfma_f32_16x16x32_bf16 v[104:107], v[28:31], a[244:247], v[104:107]
buffer_load_dwordx4 v46, %[srdV], s76 offen offset:2048 lds
v_mfma_f32_16x16x32_bf16 v[104:107], v[32:35], v[56:59], v[104:107]
v_mfma_f32_16x16x32_bf16 v[104:107], v[36:39], v[72:75], v[104:107]
ds_read_b64_tr_b16 v[20:21], v43 offset:24576
ds_read_b64_tr_b16 v[22:23], v43 offset:30720
v_mfma_f32_16x16x32_bf16 v[212:215], v[224:227], v[236:239], v[212:215]
ds_read_b64_tr_b16 v[224:225], v247 offset:16384
ds_read_b64_tr_b16 v[226:227], v247 offset:20480
v_mfma_f32_16x16x32_bf16 v[100:103], v[36:39], v[68:71], v[100:103]
v_mfma_f32_16x16x32_bf16 v[100:103], v[32:35], v[52:55], v[100:103]
s_add_u32 s76, s76, 12288
v_mfma_f32_16x16x32_bf16 v[100:103], v[28:31], a[240:243], v[100:103]
s_waitcnt lgkmcnt(6)
v_mfma_f32_16x16x32_bf16 a[0:3], v[8:11], v[164:167], a[0:3]
ds_read_b64_tr_b16 v[24:25], %[vaddr0] offset:25600
ds_read_b64_tr_b16 v[26:27], %[vaddr0] offset:31744
v_mfma_f32_16x16x32_bf16 a[16:19], v[12:15], v[164:167], a[16:19]
s_add_u32 m0, s82, 106496
v_mfma_f32_16x16x32_bf16 a[20:23], v[12:15], v[168:171], a[20:23]
buffer_load_dwordx4 v248, %[srdG], s93 offen offset:0 lds
v_mfma_f32_16x16x32_bf16 a[4:7], v[8:11], v[168:171], a[4:7]
v_exp_f32 v96, v96
v_mfma_f32_16x16x32_bf16 a[8:11], v[8:11], v[172:175], a[8:11]
ds_read_b64_tr_b16 v[28:29], v41 offset:25600
ds_read_b64_tr_b16 v[30:31], v41 offset:31744
v_mfma_f32_16x16x32_bf16 a[24:27], v[12:15], v[172:175], a[24:27]
v_exp_f32 v97, v97
v_mfma_f32_16x16x32_bf16 a[28:31], v[12:15], v[176:179], a[28:31]
v_exp_f32 v98, v98
v_mfma_f32_16x16x32_bf16 a[12:15], v[8:11], v[176:179], a[12:15]
v_add_f32 v50, v50, v96
s_waitcnt lgkmcnt(6)
v_mfma_f32_16x16x32_bf16 a[44:47], v[16:19], v[176:179], a[44:47]
ds_read_b64_tr_b16 v[32:33], v42 offset:25600
ds_read_b64_tr_b16 v[34:35], v42 offset:31744
v_mfma_f32_16x16x32_bf16 a[60:63], v[20:23], v[176:179], a[60:63]
v_exp_f32 v99, v99
v_mfma_f32_16x16x32_bf16 a[56:59], v[20:23], v[172:175], a[56:59]
buffer_load_dwordx4 v249, %[srdG], s93 offen offset:1024 lds
v_mfma_f32_16x16x32_bf16 a[40:43], v[16:19], v[172:175], a[40:43]
v_add_f32 v50, v50, v97
v_add_f32 v50, v50, v98
v_mfma_f32_16x16x32_bf16 a[36:39], v[16:19], v[168:171], a[36:39]
ds_read_b64_tr_b16 v[36:37], v43 offset:25600
ds_read_b64_tr_b16 v[38:39], v43 offset:31744
v_mfma_f32_16x16x32_bf16 a[52:55], v[20:23], v[168:171], a[52:55]
v_exp_f32 v112, v112
s_waitcnt lgkmcnt(8)
v_mfma_f32_16x16x32_bf16 v[216:219], v[224:227], v[236:239], v[216:219]
ds_read_b64_tr_b16 v[224:225], v244 offset:17408
ds_read_b64_tr_b16 v[226:227], v244 offset:21504
v_mfma_f32_16x16x32_bf16 a[48:51], v[20:23], v[164:167], a[48:51]
v_add_f32 v50, v50, v99
v_cvt_pk_bf16_f32 v160, v96, v97
v_mfma_f32_16x16x32_bf16 a[32:35], v[16:19], v[164:167], a[32:35]
v_cvt_pk_bf16_f32 v161, v98, v99
s_waitcnt lgkmcnt(6)
v_mfma_f32_16x16x32_bf16 a[64:67], v[24:27], v[164:167], a[64:67]
ds_read_b64_tr_b16 v[8:9], %[vaddr0] offset:26624
ds_read_b64_tr_b16 v[10:11], %[vaddr0] offset:32768
v_mfma_f32_16x16x32_bf16 a[80:83], v[28:31], v[164:167], a[80:83]
v_exp_f32 v113, v113
v_mfma_f32_16x16x32_bf16 a[84:87], v[28:31], v[168:171], a[84:87]
buffer_load_dwordx4 v[232:235], %[lane16], s[72:75], s94 offen sc1
v_mfma_f32_16x16x32_bf16 a[68:71], v[24:27], v[168:171], a[68:71]
v_exp_f32 v114, v114
v_mfma_f32_16x16x32_bf16 a[72:75], v[24:27], v[172:175], a[72:75]
ds_read_b64_tr_b16 v[12:13], v41 offset:26624
ds_read_b64_tr_b16 v[14:15], v41 offset:32768
s_add_u32 s93, s93, 40960
s_add_u32 s94, s94, 1024
v_mfma_f32_16x16x32_bf16 a[88:91], v[28:31], v[172:175], a[88:91]
v_add_f32 v50, v50, v112
v_add_f32 v50, v50, v113
v_mfma_f32_16x16x32_bf16 a[92:95], v[28:31], v[176:179], a[92:95]
v_exp_f32 v115, v115
v_mfma_f32_16x16x32_bf16 a[76:79], v[24:27], v[176:179], a[76:79]
v_add_f32 v50, v50, v114
s_waitcnt lgkmcnt(6)
v_mfma_f32_16x16x32_bf16 a[108:111], v[32:35], v[176:179], a[108:111]
ds_read_b64_tr_b16 v[16:17], v42 offset:26624
ds_read_b64_tr_b16 v[18:19], v42 offset:32768
v_mfma_f32_16x16x32_bf16 a[124:127], v[36:39], v[176:179], a[124:127]
v_exp_f32 v92, v92
v_mfma_f32_16x16x32_bf16 a[120:123], v[36:39], v[172:175], a[120:123]
v_add_f32 v50, v50, v115
v_cvt_pk_bf16_f32 v162, v112, v113
v_mfma_f32_16x16x32_bf16 a[104:107], v[32:35], v[172:175], a[104:107]
v_exp_f32 v93, v93
v_mfma_f32_16x16x32_bf16 a[100:103], v[32:35], v[168:171], a[100:103]
ds_read_b64_tr_b16 v[20:21], v43 offset:26624
ds_read_b64_tr_b16 v[22:23], v43 offset:32768
v_mfma_f32_16x16x32_bf16 a[116:119], v[36:39], v[168:171], a[116:119]
v_cvt_pk_bf16_f32 v163, v114, v115
v_add_f32 v49, v49, v92
v_mfma_f32_16x16x32_bf16 a[112:115], v[36:39], v[164:167], a[112:115]
v_exp_f32 v94, v94
v_mfma_f32_16x16x32_bf16 a[96:99], v[32:35], v[164:167], a[96:99]
v_add_f32 v49, v49, v93
s_waitcnt lgkmcnt(4)
v_mfma_f32_16x16x32_bf16 a[128:131], v[8:11], v[164:167], a[128:131]
s_waitcnt vmcnt(18) lgkmcnt(0)
s_barrier
ds_read_b128 v[24:27], %[kaddr0] offset:36864
v_mfma_f32_16x16x32_bf16 a[144:147], v[12:15], v[164:167], a[144:147]
v_exp_f32 v95, v95
v_mfma_f32_16x16x32_bf16 a[148:151], v[12:15], v[168:171], a[148:151]
v_add_f32 v49, v49, v94
v_add_f32 v49, v49, v95
v_mfma_f32_16x16x32_bf16 a[132:135], v[8:11], v[168:171], a[132:135]
v_exp_f32 v108, v108
v_mfma_f32_16x16x32_bf16 a[136:139], v[8:11], v[172:175], a[136:139]
ds_read_b128 v[28:31], v40 offset:36864
v_cvt_pk_bf16_f32 v156, v92, v93
v_mfma_f32_16x16x32_bf16 a[152:155], v[12:15], v[172:175], a[152:155]
v_exp_f32 v109, v109
v_mfma_f32_16x16x32_bf16 v[220:223], v[224:227], v[236:239], v[220:223]
ds_read_b64_tr_b16 v[224:225], v244 offset:24576
ds_read_b64_tr_b16 v[226:227], v244 offset:28672
v_mfma_f32_16x16x32_bf16 a[156:159], v[12:15], v[176:179], a[156:159]
v_cvt_pk_bf16_f32 v157, v94, v95
v_add_f32 v49, v49, v108
v_mfma_f32_16x16x32_bf16 a[140:143], v[8:11], v[176:179], a[140:143]
v_add_f32 v49, v49, v109
v_mfma_f32_16x16x32_bf16 a[172:175], v[16:19], v[176:179], a[172:175]
ds_read_b128 v[32:35], %[kaddr0] offset:37888
v_cvt_pk_bf16_f32 v158, v108, v109
v_mfma_f32_16x16x32_bf16 a[188:191], v[20:23], v[176:179], a[188:191]
v_exp_f32 v110, v110
v_mfma_f32_16x16x32_bf16 a[184:187], v[20:23], v[172:175], a[184:187]
v_exp_f32 v111, v111
v_mfma_f32_16x16x32_bf16 a[168:171], v[16:19], v[172:175], a[168:171]
v_exp_f32 v88, v88
v_mfma_f32_16x16x32_bf16 a[164:167], v[16:19], v[168:171], a[164:167]
ds_read_b128 v[36:39], v40 offset:37888
v_add_f32 v49, v49, v110
)"
R"(v_mfma_f32_16x16x32_bf16 a[180:183], v[20:23], v[168:171], a[180:183]
v_exp_f32 v89, v89
v_mfma_f32_16x16x32_bf16 a[176:179], v[20:23], v[164:167], a[176:179]
v_add_f32 v49, v49, v111
v_cvt_pk_bf16_f32 v159, v110, v111
v_mfma_f32_16x16x32_bf16 a[160:163], v[16:19], v[164:167], a[160:163]
v_add_f32 v48, v48, v88
s_waitcnt lgkmcnt(1)
v_mfma_f32_16x16x32_bf16 v[116:119], v[24:27], a[192:195], v[180:183]
ds_read_b128 v[8:11], %[kaddr0] offset:38912
s_add_u32 m0, s81, 24576
v_mfma_f32_16x16x32_bf16 v[116:119], v[28:31], a[208:211], v[116:119]
v_exp_f32 v90, v90
v_mfma_f32_16x16x32_bf16 v[116:119], v[32:35], a[224:227], v[116:119]
buffer_load_dwordx4 v44, %[srdK], s76 offen offset:0 lds
v_mfma_f32_16x16x32_bf16 v[120:123], v[32:35], a[228:231], v[184:187]
v_exp_f32 v91, v91
v_mfma_f32_16x16x32_bf16 v[120:123], v[28:31], a[212:215], v[120:123]
ds_read_b128 v[12:15], v40 offset:38912
v_add_f32 v48, v48, v89
v_mfma_f32_16x16x32_bf16 v[120:123], v[24:27], a[196:199], v[120:123]
v_add_f32 v48, v48, v90
v_add_f32 v48, v48, v91
v_mfma_f32_16x16x32_bf16 v[124:127], v[24:27], a[200:203], v[188:191]
v_exp_f32 v104, v104
v_mfma_f32_16x16x32_bf16 v[124:127], v[28:31], a[216:219], v[124:127]
v_exp_f32 v105, v105
v_mfma_f32_16x16x32_bf16 v[124:127], v[32:35], a[232:235], v[124:127]
ds_read_b128 v[16:19], %[kaddr0] offset:43008
v_cvt_pk_bf16_f32 v152, v88, v89
v_mfma_f32_16x16x32_bf16 v[204:207], v[224:227], v[240:243], v[204:207]
ds_read_b64_tr_b16 v[224:225], v245 offset:24576
ds_read_b64_tr_b16 v[226:227], v245 offset:28672
v_mfma_f32_16x16x32_bf16 v[128:131], v[32:35], a[236:239], v[192:195]
v_cvt_pk_bf16_f32 v153, v90, v91
v_add_f32 v48, v48, v104
v_mfma_f32_16x16x32_bf16 v[128:131], v[28:31], a[220:223], v[128:131]
buffer_load_dwordx4 v45, %[srdK], s76 offen offset:1024 lds
v_mfma_f32_16x16x32_bf16 v[128:131], v[24:27], a[204:207], v[128:131]
v_add_f32 v48, v48, v105
s_waitcnt lgkmcnt(3)
v_mfma_f32_16x16x32_bf16 v[128:131], v[36:39], a[252:255], v[128:131]
ds_read_b128 v[20:23], v40 offset:43008
v_cvt_pk_bf16_f32 v154, v104, v105
v_mfma_f32_16x16x32_bf16 v[128:131], v[8:11], v[64:67], v[128:131]
v_exp_f32 v106, v106
v_mfma_f32_16x16x32_bf16 v[128:131], v[12:15], v[80:83], v[128:131]
v_exp_f32 v107, v107
v_mfma_f32_16x16x32_bf16 v[124:127], v[12:15], v[76:79], v[124:127]
v_exp_f32 v84, v84
v_mfma_f32_16x16x32_bf16 v[124:127], v[8:11], v[60:63], v[124:127]
ds_read_b128 v[24:27], %[kaddr0] offset:44032
v_add_f32 v48, v48, v106
v_mfma_f32_16x16x32_bf16 v[124:127], v[36:39], a[248:251], v[124:127]
v_exp_f32 v85, v85
v_mfma_f32_16x16x32_bf16 v[120:123], v[36:39], a[244:247], v[120:123]
buffer_load_dwordx4 v46, %[srdK], s76 offen offset:2048 lds
v_mfma_f32_16x16x32_bf16 v[120:123], v[8:11], v[56:59], v[120:123]
v_add_f32 v48, v48, v107
v_cvt_pk_bf16_f32 v155, v106, v107
v_mfma_f32_16x16x32_bf16 v[120:123], v[12:15], v[72:75], v[120:123]
ds_read_b128 v[28:31], v40 offset:44032
s_add_u32 m0, s81, 73728
v_mfma_f32_16x16x32_bf16 v[116:119], v[12:15], v[68:71], v[116:119]
v_exp_f32 v86, v86
v_mfma_f32_16x16x32_bf16 v[116:119], v[8:11], v[52:55], v[116:119]
v_add_f32 v47, v47, v84
v_add_f32 v47, v47, v85
v_mfma_f32_16x16x32_bf16 v[116:119], v[36:39], a[240:243], v[116:119]
v_add_f32 v47, v47, v86
s_waitcnt lgkmcnt(1)
v_mfma_f32_16x16x32_bf16 v[132:135], v[16:19], a[192:195], v[180:183]
ds_read_b128 v[32:35], %[kaddr0] offset:45056
v_cvt_pk_bf16_f32 v148, v84, v85
v_mfma_f32_16x16x32_bf16 v[132:135], v[20:23], a[208:211], v[132:135]
v_exp_f32 v87, v87
v_mfma_f32_16x16x32_bf16 v[132:135], v[24:27], a[224:227], v[132:135]
buffer_load_dwordx4 v44, %[srdV], s76 offen offset:0 lds
v_mfma_f32_16x16x32_bf16 v[208:211], v[224:227], v[240:243], v[208:211]
ds_read_b64_tr_b16 v[224:225], v246 offset:24576
ds_read_b64_tr_b16 v[226:227], v246 offset:28672
v_mfma_f32_16x16x32_bf16 v[136:139], v[24:27], a[228:231], v[184:187]
v_exp_f32 v100, v100
v_mfma_f32_16x16x32_bf16 v[136:139], v[20:23], a[212:215], v[136:139]
ds_read_b128 v[36:39], v40 offset:45056
v_add_f32 v47, v47, v87
v_mfma_f32_16x16x32_bf16 v[136:139], v[16:19], a[196:199], v[136:139]
v_exp_f32 v101, v101
v_mfma_f32_16x16x32_bf16 v[140:143], v[16:19], a[200:203], v[188:191]
v_cvt_pk_bf16_f32 v149, v86, v87
v_add_f32 v47, v47, v100
v_mfma_f32_16x16x32_bf16 v[140:143], v[20:23], a[216:219], v[140:143]
v_exp_f32 v102, v102
v_mfma_f32_16x16x32_bf16 v[140:143], v[24:27], a[232:235], v[140:143]
ds_read_b64_tr_b16 v[8:9], %[vaddr0] offset:36864
ds_read_b64_tr_b16 v[10:11], %[vaddr0] offset:43008
v_mfma_f32_16x16x32_bf16 v[144:147], v[24:27], a[236:239], v[192:195]
v_exp_f32 v103, v103
v_mfma_f32_16x16x32_bf16 v[144:147], v[20:23], a[220:223], v[144:147]
buffer_load_dwordx4 v45, %[srdV], s76 offen offset:1024 lds
v_mfma_f32_16x16x32_bf16 v[144:147], v[16:19], a[204:207], v[144:147]
v_add_f32 v47, v47, v101
s_waitcnt lgkmcnt(2)
v_mfma_f32_16x16x32_bf16 v[144:147], v[28:31], a[252:255], v[144:147]
ds_read_b64_tr_b16 v[12:13], v41 offset:36864
ds_read_b64_tr_b16 v[14:15], v41 offset:43008
v_mfma_f32_16x16x32_bf16 v[144:147], v[32:35], v[64:67], v[144:147]
v_add_f32 v47, v47, v102
v_add_f32 v47, v47, v103
v_mfma_f32_16x16x32_bf16 v[144:147], v[36:39], v[80:83], v[144:147]
v_cvt_pk_bf16_f32 v150, v100, v101
v_cvt_pk_bf16_f32 v151, v102, v103
v_mfma_f32_16x16x32_bf16 v[140:143], v[36:39], v[76:79], v[140:143]
v_mfma_f32_16x16x32_bf16 v[140:143], v[32:35], v[60:63], v[140:143]
ds_read_b64_tr_b16 v[16:17], v42 offset:36864
ds_read_b64_tr_b16 v[18:19], v42 offset:43008
v_mfma_f32_16x16x32_bf16 v[140:143], v[28:31], a[248:251], v[140:143]
v_mfma_f32_16x16x32_bf16 v[136:139], v[28:31], a[244:247], v[136:139]
buffer_load_dwordx4 v46, %[srdV], s76 offen offset:2048 lds
v_mfma_f32_16x16x32_bf16 v[136:139], v[32:35], v[56:59], v[136:139]
v_mfma_f32_16x16x32_bf16 v[136:139], v[36:39], v[72:75], v[136:139]
ds_read_b64_tr_b16 v[20:21], v43 offset:36864
ds_read_b64_tr_b16 v[22:23], v43 offset:43008
v_mfma_f32_16x16x32_bf16 v[212:215], v[224:227], v[240:243], v[212:215]
ds_read_b64_tr_b16 v[224:225], v247 offset:24576
ds_read_b64_tr_b16 v[226:227], v247 offset:28672
v_mfma_f32_16x16x32_bf16 v[132:135], v[36:39], v[68:71], v[132:135]
v_mfma_f32_16x16x32_bf16 v[132:135], v[32:35], v[52:55], v[132:135]
s_add_u32 s76, s76, 12288
v_mfma_f32_16x16x32_bf16 v[132:135], v[28:31], a[240:243], v[132:135]
s_waitcnt lgkmcnt(6)
v_mfma_f32_16x16x32_bf16 a[0:3], v[8:11], v[148:151], a[0:3]
ds_read_b64_tr_b16 v[24:25], %[vaddr0] offset:37888
ds_read_b64_tr_b16 v[26:27], %[vaddr0] offset:44032
v_mfma_f32_16x16x32_bf16 a[16:19], v[12:15], v[148:151], a[16:19]
s_add_u32 m0, s82, 114688
v_mfma_f32_16x16x32_bf16 a[20:23], v[12:15], v[152:155], a[20:23]
buffer_load_dwordx4 v248, %[srdG], s93 offen offset:0 lds
v_mfma_f32_16x16x32_bf16 a[4:7], v[8:11], v[152:155], a[4:7]
v_exp_f32 v128, v128
v_mfma_f32_16x16x32_bf16 a[8:11], v[8:11], v[156:159], a[8:11]
ds_read_b64_tr_b16 v[28:29], v41 offset:37888
ds_read_b64_tr_b16 v[30:31], v41 offset:44032
v_mfma_f32_16x16x32_bf16 a[24:27], v[12:15], v[156:159], a[24:27]
v_exp_f32 v129, v129
v_mfma_f32_16x16x32_bf16 a[28:31], v[12:15], v[160:163], a[28:31]
v_exp_f32 v130, v130
v_mfma_f32_16x16x32_bf16 a[12:15], v[8:11], v[160:163], a[12:15]
v_add_f32 v50, v50, v128
s_waitcnt lgkmcnt(6)
v_mfma_f32_16x16x32_bf16 a[44:47], v[16:19], v[160:163], a[44:47]
ds_read_b64_tr_b16 v[32:33], v42 offset:37888
ds_read_b64_tr_b16 v[34:35], v42 offset:44032
v_mfma_f32_16x16x32_bf16 a[60:63], v[20:23], v[160:163], a[60:63]
v_exp_f32 v131, v131
v_mfma_f32_16x16x32_bf16 a[56:59], v[20:23], v[156:159], a[56:59]
buffer_load_dwordx4 v249, %[srdG], s93 offen offset:1024 lds
v_mfma_f32_16x16x32_bf16 a[40:43], v[16:19], v[156:159], a[40:43]
v_add_f32 v50, v50, v129
v_add_f32 v50, v50, v130
v_mfma_f32_16x16x32_bf16 a[36:39], v[16:19], v[152:155], a[36:39]
ds_read_b64_tr_b16 v[36:37], v43 offset:37888
ds_read_b64_tr_b16 v[38:39], v43 offset:44032
v_mfma_f32_16x16x32_bf16 a[52:55], v[20:23], v[152:155], a[52:55]
v_exp_f32 v144, v144
s_waitcnt lgkmcnt(8)
v_mfma_f32_16x16x32_bf16 v[216:219], v[224:227], v[240:243], v[216:219]
ds_read_b64_tr_b16 v[224:225], v244 offset:25600
ds_read_b64_tr_b16 v[226:227], v244 offset:29696
v_mfma_f32_16x16x32_bf16 a[48:51], v[20:23], v[148:151], a[48:51]
v_add_f32 v50, v50, v131
v_cvt_pk_bf16_f32 v176, v128, v129
v_mfma_f32_16x16x32_bf16 a[32:35], v[16:19], v[148:151], a[32:35]
v_cvt_pk_bf16_f32 v177, v130, v131
s_waitcnt lgkmcnt(6)
v_mfma_f32_16x16x32_bf16 a[64:67], v[24:27], v[148:151], a[64:67]
ds_read_b64_tr_b16 v[8:9], %[vaddr0] offset:38912
ds_read_b64_tr_b16 v[10:11], %[vaddr0] offset:45056
v_mfma_f32_16x16x32_bf16 a[80:83], v[28:31], v[148:151], a[80:83]
v_exp_f32 v145, v145
v_mfma_f32_16x16x32_bf16 a[84:87], v[28:31], v[152:155], a[84:87]
buffer_load_dwordx4 v[236:239], %[lane16], s[72:75], s94 offen sc1
v_mfma_f32_16x16x32_bf16 a[68:71], v[24:27], v[152:155], a[68:71]
v_exp_f32 v146, v146
v_mfma_f32_16x16x32_bf16 a[72:75], v[24:27], v[156:159], a[72:75]
ds_read_b64_tr_b16 v[12:13], v41 offset:38912
ds_read_b64_tr_b16 v[14:15], v41 offset:45056
s_add_u32 s93, s93, 40960
s_add_u32 s94, s94, 1024
v_mfma_f32_16x16x32_bf16 a[88:91], v[28:31], v[156:159], a[88:91]
v_add_f32 v50, v50, v144
v_add_f32 v50, v50, v145
v_mfma_f32_16x16x32_bf16 a[92:95], v[28:31], v[160:163], a[92:95]
v_exp_f32 v147, v147
v_mfma_f32_16x16x32_bf16 a[76:79], v[24:27], v[160:163], a[76:79]
v_add_f32 v50, v50, v146
s_waitcnt lgkmcnt(6)
v_mfma_f32_16x16x32_bf16 a[108:111], v[32:35], v[160:163], a[108:111]
ds_read_b64_tr_b16 v[16:17], v42 offset:38912
ds_read_b64_tr_b16 v[18:19], v42 offset:45056
)"
R"(v_mfma_f32_16x16x32_bf16 a[124:127], v[36:39], v[160:163], a[124:127]
v_exp_f32 v124, v124
v_mfma_f32_16x16x32_bf16 a[120:123], v[36:39], v[156:159], a[120:123]
v_add_f32 v50, v50, v147
v_cvt_pk_bf16_f32 v178, v144, v145
v_mfma_f32_16x16x32_bf16 a[104:107], v[32:35], v[156:159], a[104:107]
v_exp_f32 v125, v125
v_mfma_f32_16x16x32_bf16 a[100:103], v[32:35], v[152:155], a[100:103]
ds_read_b64_tr_b16 v[20:21], v43 offset:38912
ds_read_b64_tr_b16 v[22:23], v43 offset:45056
v_mfma_f32_16x16x32_bf16 a[116:119], v[36:39], v[152:155], a[116:119]
v_cvt_pk_bf16_f32 v179, v146, v147
v_add_f32 v49, v49, v124
v_mfma_f32_16x16x32_bf16 a[112:115], v[36:39], v[148:151], a[112:115]
v_exp_f32 v126, v126
v_mfma_f32_16x16x32_bf16 a[96:99], v[32:35], v[148:151], a[96:99]
v_add_f32 v49, v49, v125
s_waitcnt lgkmcnt(4)
v_mfma_f32_16x16x32_bf16 a[128:131], v[8:11], v[148:151], a[128:131]
s_waitcnt vmcnt(18) lgkmcnt(0)
s_barrier
ds_read_b128 v[24:27], %[kaddr0] offset:0
v_mfma_f32_16x16x32_bf16 a[144:147], v[12:15], v[148:151], a[144:147]
v_exp_f32 v127, v127
v_mfma_f32_16x16x32_bf16 a[148:151], v[12:15], v[152:155], a[148:151]
v_add_f32 v49, v49, v126
v_add_f32 v49, v49, v127
v_mfma_f32_16x16x32_bf16 a[132:135], v[8:11], v[152:155], a[132:135]
v_exp_f32 v140, v140
v_mfma_f32_16x16x32_bf16 a[136:139], v[8:11], v[156:159], a[136:139]
ds_read_b128 v[28:31], v40 offset:0
v_cvt_pk_bf16_f32 v172, v124, v125
v_mfma_f32_16x16x32_bf16 a[152:155], v[12:15], v[156:159], a[152:155]
v_exp_f32 v141, v141
v_mfma_f32_16x16x32_bf16 v[220:223], v[224:227], v[240:243], v[220:223]
ds_read_b64_tr_b16 v[224:225], v244 offset:0
ds_read_b64_tr_b16 v[226:227], v244 offset:4096
v_mfma_f32_16x16x32_bf16 a[156:159], v[12:15], v[160:163], a[156:159]
v_cvt_pk_bf16_f32 v173, v126, v127
v_add_f32 v49, v49, v140
v_mfma_f32_16x16x32_bf16 a[140:143], v[8:11], v[160:163], a[140:143]
v_add_f32 v49, v49, v141
v_mfma_f32_16x16x32_bf16 a[172:175], v[16:19], v[160:163], a[172:175]
ds_read_b128 v[32:35], %[kaddr0] offset:1024
v_cvt_pk_bf16_f32 v174, v140, v141
v_mfma_f32_16x16x32_bf16 a[188:191], v[20:23], v[160:163], a[188:191]
v_exp_f32 v142, v142
v_mfma_f32_16x16x32_bf16 a[184:187], v[20:23], v[156:159], a[184:187]
v_exp_f32 v143, v143
v_mfma_f32_16x16x32_bf16 a[168:171], v[16:19], v[156:159], a[168:171]
v_exp_f32 v120, v120
v_mfma_f32_16x16x32_bf16 a[164:167], v[16:19], v[152:155], a[164:167]
ds_read_b128 v[36:39], v40 offset:1024
v_add_f32 v49, v49, v142
v_mfma_f32_16x16x32_bf16 a[180:183], v[20:23], v[152:155], a[180:183]
v_exp_f32 v121, v121
v_mfma_f32_16x16x32_bf16 a[176:179], v[20:23], v[148:151], a[176:179]
v_add_f32 v49, v49, v143
v_cvt_pk_bf16_f32 v175, v142, v143
v_mfma_f32_16x16x32_bf16 a[160:163], v[16:19], v[148:151], a[160:163]
v_add_f32 v48, v48, v120
s_sub_u32 s80, s80, 1
s_cmp_lg_u32 s80, 0
s_cbranch_scc1 .Lk2_loop
.Lk2_loop_end:
v_lshrrev_b32 v196, 4, %[lane16]
v_min_u32 v196, 17, v196
v_mul_u32_u24 v197, 0x5556, v196
v_lshrrev_b32 v197, 16, v197
v_mul_u32_u24 v198, 3, v197
v_sub_u32 v198, v196, v198
v_mul_u32_u24 v197, 1280, v197
v_lshl_add_u32 v197, v198, 7, v197
s_lshl_b32 s84, s88, 2
s_add_u32 s84, s84, %[wid]
s_mul_i32 s92, s84, 30720
s_add_u32 s92, s92, 2048
s_waitcnt lgkmcnt(1)
v_mfma_f32_16x16x32_bf16 v[84:87], v[24:27], a[192:195], v[180:183]
ds_read_b128 v[8:11], %[kaddr0] offset:2048
s_add_u32 m0, s81, 36864
v_mfma_f32_16x16x32_bf16 v[84:87], v[28:31], a[208:211], v[84:87]
v_exp_f32 v122, v122
v_mfma_f32_16x16x32_bf16 v[84:87], v[32:35], a[224:227], v[84:87]
buffer_load_dwordx4 v44, %[srdK], s76 offen offset:0 lds
v_mfma_f32_16x16x32_bf16 v[88:91], v[32:35], a[228:231], v[184:187]
v_exp_f32 v123, v123
v_mfma_f32_16x16x32_bf16 v[88:91], v[28:31], a[212:215], v[88:91]
ds_read_b128 v[12:15], v40 offset:2048
v_add_f32 v48, v48, v121
v_mfma_f32_16x16x32_bf16 v[88:91], v[24:27], a[196:199], v[88:91]
v_add_f32 v48, v48, v122
v_add_f32 v48, v48, v123
v_mfma_f32_16x16x32_bf16 v[92:95], v[24:27], a[200:203], v[188:191]
v_exp_f32 v136, v136
v_mfma_f32_16x16x32_bf16 v[92:95], v[28:31], a[216:219], v[92:95]
v_exp_f32 v137, v137
v_mfma_f32_16x16x32_bf16 v[92:95], v[32:35], a[232:235], v[92:95]
ds_read_b128 v[16:19], %[kaddr0] offset:6144
v_cvt_pk_bf16_f32 v168, v120, v121
v_mfma_f32_16x16x32_bf16 v[204:207], v[224:227], v[228:231], v[204:207]
ds_read_b64_tr_b16 v[224:225], v245 offset:0
ds_read_b64_tr_b16 v[226:227], v245 offset:4096
v_mfma_f32_16x16x32_bf16 v[96:99], v[32:35], a[236:239], v[192:195]
v_cvt_pk_bf16_f32 v169, v122, v123
v_add_f32 v48, v48, v136
v_mfma_f32_16x16x32_bf16 v[96:99], v[28:31], a[220:223], v[96:99]
buffer_load_dwordx4 v45, %[srdK], s76 offen offset:1024 lds
v_mfma_f32_16x16x32_bf16 v[96:99], v[24:27], a[204:207], v[96:99]
v_add_f32 v48, v48, v137
s_waitcnt lgkmcnt(3)
v_mfma_f32_16x16x32_bf16 v[96:99], v[36:39], a[252:255], v[96:99]
ds_read_b128 v[20:23], v40 offset:6144
v_cvt_pk_bf16_f32 v170, v136, v137
v_mfma_f32_16x16x32_bf16 v[96:99], v[8:11], v[64:67], v[96:99]
v_exp_f32 v138, v138
v_mfma_f32_16x16x32_bf16 v[96:99], v[12:15], v[80:83], v[96:99]
v_exp_f32 v139, v139
v_mfma_f32_16x16x32_bf16 v[92:95], v[12:15], v[76:79], v[92:95]
v_exp_f32 v116, v116
v_mfma_f32_16x16x32_bf16 v[92:95], v[8:11], v[60:63], v[92:95]
ds_read_b128 v[24:27], %[kaddr0] offset:7168
v_add_f32 v48, v48, v138
v_mfma_f32_16x16x32_bf16 v[92:95], v[36:39], a[248:251], v[92:95]
v_exp_f32 v117, v117
v_mfma_f32_16x16x32_bf16 v[88:91], v[36:39], a[244:247], v[88:91]
buffer_load_dwordx4 v46, %[srdK], s76 offen offset:2048 lds
v_mfma_f32_16x16x32_bf16 v[88:91], v[8:11], v[56:59], v[88:91]
v_add_f32 v48, v48, v139
v_cvt_pk_bf16_f32 v171, v138, v139
v_mfma_f32_16x16x32_bf16 v[88:91], v[12:15], v[72:75], v[88:91]
ds_read_b128 v[28:31], v40 offset:7168
s_add_u32 m0, s81, 86016
v_mfma_f32_16x16x32_bf16 v[84:87], v[12:15], v[68:71], v[84:87]
v_exp_f32 v118, v118
v_mfma_f32_16x16x32_bf16 v[84:87], v[8:11], v[52:55], v[84:87]
v_add_f32 v47, v47, v116
v_add_f32 v47, v47, v117
v_mfma_f32_16x16x32_bf16 v[84:87], v[36:39], a[240:243], v[84:87]
v_add_f32 v47, v47, v118
s_waitcnt lgkmcnt(1)
v_mfma_f32_16x16x32_bf16 v[100:103], v[16:19], a[192:195], v[180:183]
ds_read_b128 v[32:35], %[kaddr0] offset:8192
v_cvt_pk_bf16_f32 v164, v116, v117
v_mfma_f32_16x16x32_bf16 v[100:103], v[20:23], a[208:211], v[100:103]
v_exp_f32 v119, v119
v_mfma_f32_16x16x32_bf16 v[100:103], v[24:27], a[224:227], v[100:103]
buffer_load_dwordx4 v44, %[srdV], s76 offen offset:0 lds
v_mfma_f32_16x16x32_bf16 v[208:211], v[224:227], v[228:231], v[208:211]
ds_read_b64_tr_b16 v[224:225], v246 offset:0
ds_read_b64_tr_b16 v[226:227], v246 offset:4096
v_mfma_f32_16x16x32_bf16 v[104:107], v[24:27], a[228:231], v[184:187]
v_exp_f32 v132, v132
v_mfma_f32_16x16x32_bf16 v[104:107], v[20:23], a[212:215], v[104:107]
ds_read_b128 v[36:39], v40 offset:8192
v_add_f32 v47, v47, v119
v_mfma_f32_16x16x32_bf16 v[104:107], v[16:19], a[196:199], v[104:107]
v_exp_f32 v133, v133
v_mfma_f32_16x16x32_bf16 v[108:111], v[16:19], a[200:203], v[188:191]
v_cvt_pk_bf16_f32 v165, v118, v119
v_add_f32 v47, v47, v132
v_mfma_f32_16x16x32_bf16 v[108:111], v[20:23], a[216:219], v[108:111]
v_exp_f32 v134, v134
v_mfma_f32_16x16x32_bf16 v[108:111], v[24:27], a[232:235], v[108:111]
ds_read_b64_tr_b16 v[8:9], %[vaddr0] offset:0
ds_read_b64_tr_b16 v[10:11], %[vaddr0] offset:6144
v_mfma_f32_16x16x32_bf16 v[112:115], v[24:27], a[236:239], v[192:195]
v_exp_f32 v135, v135
v_mfma_f32_16x16x32_bf16 v[112:115], v[20:23], a[220:223], v[112:115]
buffer_load_dwordx4 v45, %[srdV], s76 offen offset:1024 lds
v_mfma_f32_16x16x32_bf16 v[112:115], v[16:19], a[204:207], v[112:115]
v_add_f32 v47, v47, v133
s_waitcnt lgkmcnt(2)
v_mfma_f32_16x16x32_bf16 v[112:115], v[28:31], a[252:255], v[112:115]
ds_read_b64_tr_b16 v[12:13], v41 offset:0
ds_read_b64_tr_b16 v[14:15], v41 offset:6144
v_mfma_f32_16x16x32_bf16 v[112:115], v[32:35], v[64:67], v[112:115]
v_add_f32 v47, v47, v134
v_add_f32 v47, v47, v135
v_mfma_f32_16x16x32_bf16 v[112:115], v[36:39], v[80:83], v[112:115]
v_cvt_pk_bf16_f32 v166, v132, v133
v_cvt_pk_bf16_f32 v167, v134, v135
v_mfma_f32_16x16x32_bf16 v[108:111], v[36:39], v[76:79], v[108:111]
v_mfma_f32_16x16x32_bf16 v[108:111], v[32:35], v[60:63], v[108:111]
ds_read_b64_tr_b16 v[16:17], v42 offset:0
ds_read_b64_tr_b16 v[18:19], v42 offset:6144
v_mfma_f32_16x16x32_bf16 v[108:111], v[28:31], a[248:251], v[108:111]
v_mfma_f32_16x16x32_bf16 v[104:107], v[28:31], a[244:247], v[104:107]
buffer_load_dwordx4 v46, %[srdV], s76 offen offset:2048 lds
v_mfma_f32_16x16x32_bf16 v[104:107], v[32:35], v[56:59], v[104:107]
v_mfma_f32_16x16x32_bf16 v[104:107], v[36:39], v[72:75], v[104:107]
ds_read_b64_tr_b16 v[20:21], v43 offset:0
ds_read_b64_tr_b16 v[22:23], v43 offset:6144
v_mfma_f32_16x16x32_bf16 v[212:215], v[224:227], v[228:231], v[212:215]
ds_read_b64_tr_b16 v[224:225], v247 offset:0
ds_read_b64_tr_b16 v[226:227], v247 offset:4096
v_mfma_f32_16x16x32_bf16 v[100:103], v[36:39], v[68:71], v[100:103]
v_mfma_f32_16x16x32_bf16 v[100:103], v[32:35], v[52:55], v[100:103]
s_add_u32 s76, s76, 12288
v_mfma_f32_16x16x32_bf16 v[100:103], v[28:31], a[240:243], v[100:103]
)"
R"(s_waitcnt lgkmcnt(6)
v_mfma_f32_16x16x32_bf16 a[0:3], v[8:11], v[164:167], a[0:3]
ds_read_b64_tr_b16 v[24:25], %[vaddr0] offset:1024
ds_read_b64_tr_b16 v[26:27], %[vaddr0] offset:7168
v_mfma_f32_16x16x32_bf16 a[16:19], v[12:15], v[164:167], a[16:19]
s_add_u32 m0, s82, 122880
v_mfma_f32_16x16x32_bf16 a[20:23], v[12:15], v[168:171], a[20:23]
buffer_load_dwordx4 v248, %[srdG], s93 offen offset:0 lds
v_mfma_f32_16x16x32_bf16 a[4:7], v[8:11], v[168:171], a[4:7]
v_exp_f32 v96, v96
v_mfma_f32_16x16x32_bf16 a[8:11], v[8:11], v[172:175], a[8:11]
ds_read_b64_tr_b16 v[28:29], v41 offset:1024
ds_read_b64_tr_b16 v[30:31], v41 offset:7168
v_mfma_f32_16x16x32_bf16 a[24:27], v[12:15], v[172:175], a[24:27]
v_exp_f32 v97, v97
v_mfma_f32_16x16x32_bf16 a[28:31], v[12:15], v[176:179], a[28:31]
v_exp_f32 v98, v98
v_mfma_f32_16x16x32_bf16 a[12:15], v[8:11], v[176:179], a[12:15]
v_add_f32 v50, v50, v96
s_waitcnt lgkmcnt(6)
v_mfma_f32_16x16x32_bf16 a[44:47], v[16:19], v[176:179], a[44:47]
ds_read_b64_tr_b16 v[32:33], v42 offset:1024
ds_read_b64_tr_b16 v[34:35], v42 offset:7168
v_mfma_f32_16x16x32_bf16 a[60:63], v[20:23], v[176:179], a[60:63]
v_exp_f32 v99, v99
v_mfma_f32_16x16x32_bf16 a[56:59], v[20:23], v[172:175], a[56:59]
buffer_load_dwordx4 v249, %[srdG], s93 offen offset:1024 lds
v_mfma_f32_16x16x32_bf16 a[40:43], v[16:19], v[172:175], a[40:43]
v_add_f32 v50, v50, v97
v_add_f32 v50, v50, v98
v_mfma_f32_16x16x32_bf16 a[36:39], v[16:19], v[168:171], a[36:39]
ds_read_b64_tr_b16 v[36:37], v43 offset:1024
ds_read_b64_tr_b16 v[38:39], v43 offset:7168
v_mfma_f32_16x16x32_bf16 a[52:55], v[20:23], v[168:171], a[52:55]
v_exp_f32 v112, v112
s_waitcnt lgkmcnt(8)
v_mfma_f32_16x16x32_bf16 v[216:219], v[224:227], v[228:231], v[216:219]
ds_read_b64_tr_b16 v[224:225], v244 offset:1024
ds_read_b64_tr_b16 v[226:227], v244 offset:5120
v_mfma_f32_16x16x32_bf16 a[48:51], v[20:23], v[164:167], a[48:51]
v_add_f32 v50, v50, v99
v_cvt_pk_bf16_f32 v160, v96, v97
v_mfma_f32_16x16x32_bf16 a[32:35], v[16:19], v[164:167], a[32:35]
v_cvt_pk_bf16_f32 v161, v98, v99
s_waitcnt lgkmcnt(6)
v_mfma_f32_16x16x32_bf16 a[64:67], v[24:27], v[164:167], a[64:67]
ds_read_b64_tr_b16 v[8:9], %[vaddr0] offset:2048
ds_read_b64_tr_b16 v[10:11], %[vaddr0] offset:8192
v_mfma_f32_16x16x32_bf16 a[80:83], v[28:31], v[164:167], a[80:83]
v_exp_f32 v113, v113
v_mfma_f32_16x16x32_bf16 a[84:87], v[28:31], v[168:171], a[84:87]
buffer_load_dwordx4 v[240:243], %[lane16], s[72:75], s94 offen sc1
v_mfma_f32_16x16x32_bf16 a[68:71], v[24:27], v[168:171], a[68:71]
v_exp_f32 v114, v114
v_mfma_f32_16x16x32_bf16 a[72:75], v[24:27], v[172:175], a[72:75]
ds_read_b64_tr_b16 v[12:13], v41 offset:2048
ds_read_b64_tr_b16 v[14:15], v41 offset:8192
s_add_u32 s93, s93, 40960
s_add_u32 s94, s94, 1024
v_mfma_f32_16x16x32_bf16 a[88:91], v[28:31], v[172:175], a[88:91]
v_add_f32 v50, v50, v112
v_add_f32 v50, v50, v113
v_mfma_f32_16x16x32_bf16 a[92:95], v[28:31], v[176:179], a[92:95]
v_exp_f32 v115, v115
v_mfma_f32_16x16x32_bf16 a[76:79], v[24:27], v[176:179], a[76:79]
v_add_f32 v50, v50, v114
s_waitcnt lgkmcnt(6)
v_mfma_f32_16x16x32_bf16 a[108:111], v[32:35], v[176:179], a[108:111]
ds_read_b64_tr_b16 v[16:17], v42 offset:2048
ds_read_b64_tr_b16 v[18:19], v42 offset:8192
v_mfma_f32_16x16x32_bf16 a[124:127], v[36:39], v[176:179], a[124:127]
v_exp_f32 v92, v92
v_mfma_f32_16x16x32_bf16 a[120:123], v[36:39], v[172:175], a[120:123]
v_add_f32 v50, v50, v115
v_cvt_pk_bf16_f32 v162, v112, v113
buffer_load_dword v199, v197, %[srdS], s92 offen
v_mfma_f32_16x16x32_bf16 a[104:107], v[32:35], v[172:175], a[104:107]
v_exp_f32 v93, v93
s_add_u32 s92, s92, 7680
v_mfma_f32_16x16x32_bf16 a[100:103], v[32:35], v[168:171], a[100:103]
ds_read_b64_tr_b16 v[20:21], v43 offset:2048
ds_read_b64_tr_b16 v[22:23], v43 offset:8192
v_mfma_f32_16x16x32_bf16 a[116:119], v[36:39], v[168:171], a[116:119]
v_cvt_pk_bf16_f32 v163, v114, v115
v_add_f32 v49, v49, v92
v_mfma_f32_16x16x32_bf16 a[112:115], v[36:39], v[164:167], a[112:115]
v_exp_f32 v94, v94
v_mfma_f32_16x16x32_bf16 a[96:99], v[32:35], v[164:167], a[96:99]
v_add_f32 v49, v49, v93
s_waitcnt lgkmcnt(4)
v_mfma_f32_16x16x32_bf16 a[128:131], v[8:11], v[164:167], a[128:131]
s_waitcnt vmcnt(19) lgkmcnt(0)
s_barrier
ds_read_b128 v[24:27], %[kaddr0] offset:12288
v_mfma_f32_16x16x32_bf16 a[144:147], v[12:15], v[164:167], a[144:147]
v_exp_f32 v95, v95
v_mfma_f32_16x16x32_bf16 a[148:151], v[12:15], v[168:171], a[148:151]
v_add_f32 v49, v49, v94
v_add_f32 v49, v49, v95
v_mfma_f32_16x16x32_bf16 a[132:135], v[8:11], v[168:171], a[132:135]
v_exp_f32 v108, v108
v_mfma_f32_16x16x32_bf16 a[136:139], v[8:11], v[172:175], a[136:139]
ds_read_b128 v[28:31], v40 offset:12288
v_cvt_pk_bf16_f32 v156, v92, v93
v_mfma_f32_16x16x32_bf16 a[152:155], v[12:15], v[172:175], a[152:155]
v_exp_f32 v109, v109
v_mfma_f32_16x16x32_bf16 v[220:223], v[224:227], v[228:231], v[220:223]
ds_read_b64_tr_b16 v[224:225], v244 offset:8192
ds_read_b64_tr_b16 v[226:227], v244 offset:12288
v_mfma_f32_16x16x32_bf16 a[156:159], v[12:15], v[176:179], a[156:159]
v_cvt_pk_bf16_f32 v157, v94, v95
v_add_f32 v49, v49, v108
v_mfma_f32_16x16x32_bf16 a[140:143], v[8:11], v[176:179], a[140:143]
v_add_f32 v49, v49, v109
v_mfma_f32_16x16x32_bf16 a[172:175], v[16:19], v[176:179], a[172:175]
ds_read_b128 v[32:35], %[kaddr0] offset:13312
v_cvt_pk_bf16_f32 v158, v108, v109
v_mfma_f32_16x16x32_bf16 a[188:191], v[20:23], v[176:179], a[188:191]
v_exp_f32 v110, v110
v_mfma_f32_16x16x32_bf16 a[184:187], v[20:23], v[172:175], a[184:187]
v_exp_f32 v111, v111
v_mfma_f32_16x16x32_bf16 a[168:171], v[16:19], v[172:175], a[168:171]
v_exp_f32 v88, v88
v_mfma_f32_16x16x32_bf16 a[164:167], v[16:19], v[168:171], a[164:167]
ds_read_b128 v[36:39], v40 offset:13312
v_add_f32 v49, v49, v110
v_mfma_f32_16x16x32_bf16 a[180:183], v[20:23], v[168:171], a[180:183]
v_exp_f32 v89, v89
v_mfma_f32_16x16x32_bf16 a[176:179], v[20:23], v[164:167], a[176:179]
v_add_f32 v49, v49, v111
v_cvt_pk_bf16_f32 v159, v110, v111
v_mfma_f32_16x16x32_bf16 a[160:163], v[16:19], v[164:167], a[160:163]
v_add_f32 v48, v48, v88
s_waitcnt lgkmcnt(1)
v_mfma_f32_16x16x32_bf16 v[116:119], v[24:27], a[192:195], v[180:183]
ds_read_b128 v[8:11], %[kaddr0] offset:14336
s_add_u32 m0, s81, 0
v_mfma_f32_16x16x32_bf16 v[116:119], v[28:31], a[208:211], v[116:119]
v_exp_f32 v90, v90
v_mfma_f32_16x16x32_bf16 v[116:119], v[32:35], a[224:227], v[116:119]
buffer_load_dwordx4 v44, %[srdK], s76 offen offset:0 lds
v_mfma_f32_16x16x32_bf16 v[120:123], v[32:35], a[228:231], v[184:187]
v_exp_f32 v91, v91
v_mfma_f32_16x16x32_bf16 v[120:123], v[28:31], a[212:215], v[120:123]
ds_read_b128 v[12:15], v40 offset:14336
v_add_f32 v48, v48, v89
v_mfma_f32_16x16x32_bf16 v[120:123], v[24:27], a[196:199], v[120:123]
v_add_f32 v48, v48, v90
v_add_f32 v48, v48, v91
v_mfma_f32_16x16x32_bf16 v[124:127], v[24:27], a[200:203], v[188:191]
v_exp_f32 v104, v104
v_mfma_f32_16x16x32_bf16 v[124:127], v[28:31], a[216:219], v[124:127]
v_exp_f32 v105, v105
v_mfma_f32_16x16x32_bf16 v[124:127], v[32:35], a[232:235], v[124:127]
ds_read_b128 v[16:19], %[kaddr0] offset:18432
v_cvt_pk_bf16_f32 v152, v88, v89
v_mfma_f32_16x16x32_bf16 v[204:207], v[224:227], v[232:235], v[204:207]
ds_read_b64_tr_b16 v[224:225], v245 offset:8192
ds_read_b64_tr_b16 v[226:227], v245 offset:12288
v_mfma_f32_16x16x32_bf16 v[128:131], v[32:35], a[236:239], v[192:195]
v_cvt_pk_bf16_f32 v153, v90, v91
v_add_f32 v48, v48, v104
v_mfma_f32_16x16x32_bf16 v[128:131], v[28:31], a[220:223], v[128:131]
buffer_load_dwordx4 v45, %[srdK], s76 offen offset:1024 lds
v_mfma_f32_16x16x32_bf16 v[128:131], v[24:27], a[204:207], v[128:131]
v_add_f32 v48, v48, v105
s_waitcnt lgkmcnt(3)
v_mfma_f32_16x16x32_bf16 v[128:131], v[36:39], a[252:255], v[128:131]
ds_read_b128 v[20:23], v40 offset:18432
v_cvt_pk_bf16_f32 v154, v104, v105
v_mfma_f32_16x16x32_bf16 v[128:131], v[8:11], v[64:67], v[128:131]
v_exp_f32 v106, v106
v_mfma_f32_16x16x32_bf16 v[128:131], v[12:15], v[80:83], v[128:131]
v_exp_f32 v107, v107
v_mfma_f32_16x16x32_bf16 v[124:127], v[12:15], v[76:79], v[124:127]
v_exp_f32 v84, v84
v_mfma_f32_16x16x32_bf16 v[124:127], v[8:11], v[60:63], v[124:127]
ds_read_b128 v[24:27], %[kaddr0] offset:19456
v_add_f32 v48, v48, v106
v_mfma_f32_16x16x32_bf16 v[124:127], v[36:39], a[248:251], v[124:127]
v_exp_f32 v85, v85
v_mfma_f32_16x16x32_bf16 v[120:123], v[36:39], a[244:247], v[120:123]
buffer_load_dwordx4 v46, %[srdK], s76 offen offset:2048 lds
v_mfma_f32_16x16x32_bf16 v[120:123], v[8:11], v[56:59], v[120:123]
v_add_f32 v48, v48, v107
v_cvt_pk_bf16_f32 v155, v106, v107
v_mfma_f32_16x16x32_bf16 v[120:123], v[12:15], v[72:75], v[120:123]
ds_read_b128 v[28:31], v40 offset:19456
s_add_u32 m0, s81, 49152
v_mfma_f32_16x16x32_bf16 v[116:119], v[12:15], v[68:71], v[116:119]
v_exp_f32 v86, v86
v_mfma_f32_16x16x32_bf16 v[116:119], v[8:11], v[52:55], v[116:119]
v_add_f32 v47, v47, v84
v_add_f32 v47, v47, v85
v_mfma_f32_16x16x32_bf16 v[116:119], v[36:39], a[240:243], v[116:119]
v_add_f32 v47, v47, v86
s_waitcnt lgkmcnt(1)
v_mfma_f32_16x16x32_bf16 v[132:135], v[16:19], a[192:195], v[180:183]
ds_read_b128 v[32:35], %[kaddr0] offset:20480
v_cvt_pk_bf16_f32 v148, v84, v85
)"
R"(v_mfma_f32_16x16x32_bf16 v[132:135], v[20:23], a[208:211], v[132:135]
v_exp_f32 v87, v87
v_mfma_f32_16x16x32_bf16 v[132:135], v[24:27], a[224:227], v[132:135]
buffer_load_dwordx4 v44, %[srdV], s76 offen offset:0 lds
v_mfma_f32_16x16x32_bf16 v[208:211], v[224:227], v[232:235], v[208:211]
ds_read_b64_tr_b16 v[224:225], v246 offset:8192
ds_read_b64_tr_b16 v[226:227], v246 offset:12288
v_mfma_f32_16x16x32_bf16 v[136:139], v[24:27], a[228:231], v[184:187]
v_exp_f32 v100, v100
v_mfma_f32_16x16x32_bf16 v[136:139], v[20:23], a[212:215], v[136:139]
ds_read_b128 v[36:39], v40 offset:20480
v_add_f32 v47, v47, v87
v_mfma_f32_16x16x32_bf16 v[136:139], v[16:19], a[196:199], v[136:139]
v_exp_f32 v101, v101
v_mfma_f32_16x16x32_bf16 v[140:143], v[16:19], a[200:203], v[188:191]
v_cvt_pk_bf16_f32 v149, v86, v87
v_add_f32 v47, v47, v100
v_mfma_f32_16x16x32_bf16 v[140:143], v[20:23], a[216:219], v[140:143]
v_exp_f32 v102, v102
v_mfma_f32_16x16x32_bf16 v[140:143], v[24:27], a[232:235], v[140:143]
ds_read_b64_tr_b16 v[8:9], %[vaddr0] offset:12288
ds_read_b64_tr_b16 v[10:11], %[vaddr0] offset:18432
v_mfma_f32_16x16x32_bf16 v[144:147], v[24:27], a[236:239], v[192:195]
v_exp_f32 v103, v103
v_mfma_f32_16x16x32_bf16 v[144:147], v[20:23], a[220:223], v[144:147]
buffer_load_dwordx4 v45, %[srdV], s76 offen offset:1024 lds
v_mfma_f32_16x16x32_bf16 v[144:147], v[16:19], a[204:207], v[144:147]
v_add_f32 v47, v47, v101
s_waitcnt lgkmcnt(2)
v_mfma_f32_16x16x32_bf16 v[144:147], v[28:31], a[252:255], v[144:147]
ds_read_b64_tr_b16 v[12:13], v41 offset:12288
ds_read_b64_tr_b16 v[14:15], v41 offset:18432
v_mfma_f32_16x16x32_bf16 v[144:147], v[32:35], v[64:67], v[144:147]
v_add_f32 v47, v47, v102
v_add_f32 v47, v47, v103
v_mfma_f32_16x16x32_bf16 v[144:147], v[36:39], v[80:83], v[144:147]
v_cvt_pk_bf16_f32 v150, v100, v101
v_cvt_pk_bf16_f32 v151, v102, v103
v_mfma_f32_16x16x32_bf16 v[140:143], v[36:39], v[76:79], v[140:143]
v_mfma_f32_16x16x32_bf16 v[140:143], v[32:35], v[60:63], v[140:143]
ds_read_b64_tr_b16 v[16:17], v42 offset:12288
ds_read_b64_tr_b16 v[18:19], v42 offset:18432
v_mfma_f32_16x16x32_bf16 v[140:143], v[28:31], a[248:251], v[140:143]
v_mfma_f32_16x16x32_bf16 v[136:139], v[28:31], a[244:247], v[136:139]
buffer_load_dwordx4 v46, %[srdV], s76 offen offset:2048 lds
v_mfma_f32_16x16x32_bf16 v[136:139], v[32:35], v[56:59], v[136:139]
v_mfma_f32_16x16x32_bf16 v[136:139], v[36:39], v[72:75], v[136:139]
ds_read_b64_tr_b16 v[20:21], v43 offset:12288
ds_read_b64_tr_b16 v[22:23], v43 offset:18432
v_mfma_f32_16x16x32_bf16 v[212:215], v[224:227], v[232:235], v[212:215]
ds_read_b64_tr_b16 v[224:225], v247 offset:8192
ds_read_b64_tr_b16 v[226:227], v247 offset:12288
v_mfma_f32_16x16x32_bf16 v[132:135], v[36:39], v[68:71], v[132:135]
v_mfma_f32_16x16x32_bf16 v[132:135], v[32:35], v[52:55], v[132:135]
s_add_u32 s76, s76, 12288
v_mfma_f32_16x16x32_bf16 v[132:135], v[28:31], a[240:243], v[132:135]
s_waitcnt lgkmcnt(6)
v_mfma_f32_16x16x32_bf16 a[0:3], v[8:11], v[148:151], a[0:3]
ds_read_b64_tr_b16 v[24:25], %[vaddr0] offset:13312
ds_read_b64_tr_b16 v[26:27], %[vaddr0] offset:19456
v_mfma_f32_16x16x32_bf16 a[16:19], v[12:15], v[148:151], a[16:19]
v_mfma_f32_16x16x32_bf16 a[20:23], v[12:15], v[152:155], a[20:23]
v_mfma_f32_16x16x32_bf16 a[4:7], v[8:11], v[152:155], a[4:7]
v_exp_f32 v128, v128
v_mfma_f32_16x16x32_bf16 a[8:11], v[8:11], v[156:159], a[8:11]
ds_read_b64_tr_b16 v[28:29], v41 offset:13312
ds_read_b64_tr_b16 v[30:31], v41 offset:19456
v_mfma_f32_16x16x32_bf16 a[24:27], v[12:15], v[156:159], a[24:27]
v_exp_f32 v129, v129
v_mfma_f32_16x16x32_bf16 a[28:31], v[12:15], v[160:163], a[28:31]
v_exp_f32 v130, v130
v_mfma_f32_16x16x32_bf16 a[12:15], v[8:11], v[160:163], a[12:15]
v_add_f32 v50, v50, v128
s_waitcnt lgkmcnt(6)
v_mfma_f32_16x16x32_bf16 a[44:47], v[16:19], v[160:163], a[44:47]
ds_read_b64_tr_b16 v[32:33], v42 offset:13312
ds_read_b64_tr_b16 v[34:35], v42 offset:19456
v_mfma_f32_16x16x32_bf16 a[60:63], v[20:23], v[160:163], a[60:63]
v_exp_f32 v131, v131
v_mfma_f32_16x16x32_bf16 a[56:59], v[20:23], v[156:159], a[56:59]
v_mfma_f32_16x16x32_bf16 a[40:43], v[16:19], v[156:159], a[40:43]
v_add_f32 v50, v50, v129
v_add_f32 v50, v50, v130
v_mfma_f32_16x16x32_bf16 a[36:39], v[16:19], v[152:155], a[36:39]
ds_read_b64_tr_b16 v[36:37], v43 offset:13312
ds_read_b64_tr_b16 v[38:39], v43 offset:19456
v_mfma_f32_16x16x32_bf16 a[52:55], v[20:23], v[152:155], a[52:55]
v_exp_f32 v144, v144
s_waitcnt lgkmcnt(8)
v_mfma_f32_16x16x32_bf16 v[216:219], v[224:227], v[232:235], v[216:219]
ds_read_b64_tr_b16 v[224:225], v244 offset:9216
ds_read_b64_tr_b16 v[226:227], v244 offset:13312
v_mfma_f32_16x16x32_bf16 a[48:51], v[20:23], v[148:151], a[48:51]
v_add_f32 v50, v50, v131
v_cvt_pk_bf16_f32 v176, v128, v129
v_mfma_f32_16x16x32_bf16 a[32:35], v[16:19], v[148:151], a[32:35]
v_cvt_pk_bf16_f32 v177, v130, v131
s_waitcnt lgkmcnt(6)
v_mfma_f32_16x16x32_bf16 a[64:67], v[24:27], v[148:151], a[64:67]
ds_read_b64_tr_b16 v[8:9], %[vaddr0] offset:14336
ds_read_b64_tr_b16 v[10:11], %[vaddr0] offset:20480
v_mfma_f32_16x16x32_bf16 a[80:83], v[28:31], v[148:151], a[80:83]
v_exp_f32 v145, v145
v_mfma_f32_16x16x32_bf16 a[84:87], v[28:31], v[152:155], a[84:87]
v_mfma_f32_16x16x32_bf16 a[68:71], v[24:27], v[152:155], a[68:71]
v_exp_f32 v146, v146
v_mfma_f32_16x16x32_bf16 a[72:75], v[24:27], v[156:159], a[72:75]
ds_read_b64_tr_b16 v[12:13], v41 offset:14336
ds_read_b64_tr_b16 v[14:15], v41 offset:20480
v_mfma_f32_16x16x32_bf16 a[88:91], v[28:31], v[156:159], a[88:91]
v_add_f32 v50, v50, v144
v_add_f32 v50, v50, v145
v_mfma_f32_16x16x32_bf16 a[92:95], v[28:31], v[160:163], a[92:95]
v_exp_f32 v147, v147
v_mfma_f32_16x16x32_bf16 a[76:79], v[24:27], v[160:163], a[76:79]
v_add_f32 v50, v50, v146
s_waitcnt lgkmcnt(6)
v_mfma_f32_16x16x32_bf16 a[108:111], v[32:35], v[160:163], a[108:111]
ds_read_b64_tr_b16 v[16:17], v42 offset:14336
ds_read_b64_tr_b16 v[18:19], v42 offset:20480
v_mfma_f32_16x16x32_bf16 a[124:127], v[36:39], v[160:163], a[124:127]
v_exp_f32 v124, v124
v_mfma_f32_16x16x32_bf16 a[120:123], v[36:39], v[156:159], a[120:123]
v_add_f32 v50, v50, v147
v_cvt_pk_bf16_f32 v178, v144, v145
buffer_load_dword v199, v197, %[srdS], s92 offen
v_mfma_f32_16x16x32_bf16 a[104:107], v[32:35], v[156:159], a[104:107]
v_exp_f32 v125, v125
s_add_u32 s92, s92, 7680
v_mfma_f32_16x16x32_bf16 a[100:103], v[32:35], v[152:155], a[100:103]
ds_read_b64_tr_b16 v[20:21], v43 offset:14336
ds_read_b64_tr_b16 v[22:23], v43 offset:20480
v_mfma_f32_16x16x32_bf16 a[116:119], v[36:39], v[152:155], a[116:119]
v_cvt_pk_bf16_f32 v179, v146, v147
v_add_f32 v49, v49, v124
v_mfma_f32_16x16x32_bf16 a[112:115], v[36:39], v[148:151], a[112:115]
v_exp_f32 v126, v126
v_mfma_f32_16x16x32_bf16 a[96:99], v[32:35], v[148:151], a[96:99]
v_add_f32 v49, v49, v125
s_waitcnt lgkmcnt(4)
v_mfma_f32_16x16x32_bf16 a[128:131], v[8:11], v[148:151], a[128:131]
s_waitcnt vmcnt(17) lgkmcnt(0)
s_barrier
ds_read_b128 v[24:27], %[kaddr0] offset:24576
v_mfma_f32_16x16x32_bf16 a[144:147], v[12:15], v[148:151], a[144:147]
v_exp_f32 v127, v127
v_mfma_f32_16x16x32_bf16 a[148:151], v[12:15], v[152:155], a[148:151]
v_add_f32 v49, v49, v126
v_add_f32 v49, v49, v127
v_mfma_f32_16x16x32_bf16 a[132:135], v[8:11], v[152:155], a[132:135]
v_exp_f32 v140, v140
v_mfma_f32_16x16x32_bf16 a[136:139], v[8:11], v[156:159], a[136:139]
ds_read_b128 v[28:31], v40 offset:24576
v_cvt_pk_bf16_f32 v172, v124, v125
v_mfma_f32_16x16x32_bf16 a[152:155], v[12:15], v[156:159], a[152:155]
v_exp_f32 v141, v141
v_mfma_f32_16x16x32_bf16 v[220:223], v[224:227], v[232:235], v[220:223]
ds_read_b64_tr_b16 v[224:225], v244 offset:16384
ds_read_b64_tr_b16 v[226:227], v244 offset:20480
v_mfma_f32_16x16x32_bf16 a[156:159], v[12:15], v[160:163], a[156:159]
v_cvt_pk_bf16_f32 v173, v126, v127
v_add_f32 v49, v49, v140
v_mfma_f32_16x16x32_bf16 a[140:143], v[8:11], v[160:163], a[140:143]
v_add_f32 v49, v49, v141
v_mfma_f32_16x16x32_bf16 a[172:175], v[16:19], v[160:163], a[172:175]
ds_read_b128 v[32:35], %[kaddr0] offset:25600
v_cvt_pk_bf16_f32 v174, v140, v141
v_mfma_f32_16x16x32_bf16 a[188:191], v[20:23], v[160:163], a[188:191]
v_exp_f32 v142, v142
v_mfma_f32_16x16x32_bf16 a[184:187], v[20:23], v[156:159], a[184:187]
v_exp_f32 v143, v143
v_mfma_f32_16x16x32_bf16 a[168:171], v[16:19], v[156:159], a[168:171]
v_exp_f32 v120, v120
v_mfma_f32_16x16x32_bf16 a[164:167], v[16:19], v[152:155], a[164:167]
ds_read_b128 v[36:39], v40 offset:25600
v_add_f32 v49, v49, v142
v_mfma_f32_16x16x32_bf16 a[180:183], v[20:23], v[152:155], a[180:183]
v_exp_f32 v121, v121
v_mfma_f32_16x16x32_bf16 a[176:179], v[20:23], v[148:151], a[176:179]
v_add_f32 v49, v49, v143
v_cvt_pk_bf16_f32 v175, v142, v143
v_mfma_f32_16x16x32_bf16 a[160:163], v[16:19], v[148:151], a[160:163]
v_add_f32 v48, v48, v120
s_waitcnt lgkmcnt(1)
v_mfma_f32_16x16x32_bf16 v[84:87], v[24:27], a[192:195], v[180:183]
ds_read_b128 v[8:11], %[kaddr0] offset:26624
s_add_u32 m0, s81, 12288
v_mfma_f32_16x16x32_bf16 v[84:87], v[28:31], a[208:211], v[84:87]
v_exp_f32 v122, v122
v_mfma_f32_16x16x32_bf16 v[84:87], v[32:35], a[224:227], v[84:87]
buffer_load_dwordx4 v44, %[srdK], s76 offen offset:0 lds
v_mfma_f32_16x16x32_bf16 v[88:91], v[32:35], a[228:231], v[184:187]
v_exp_f32 v123, v123
v_mfma_f32_16x16x32_bf16 v[88:91], v[28:31], a[212:215], v[88:91]
ds_read_b128 v[12:15], v40 offset:26624
v_add_f32 v48, v48, v121
v_mfma_f32_16x16x32_bf16 v[88:91], v[24:27], a[196:199], v[88:91]
v_add_f32 v48, v48, v122
v_add_f32 v48, v48, v123
v_mfma_f32_16x16x32_bf16 v[92:95], v[24:27], a[200:203], v[188:191]
)"
R"(v_exp_f32 v136, v136
v_mfma_f32_16x16x32_bf16 v[92:95], v[28:31], a[216:219], v[92:95]
v_exp_f32 v137, v137
v_mfma_f32_16x16x32_bf16 v[92:95], v[32:35], a[232:235], v[92:95]
ds_read_b128 v[16:19], %[kaddr0] offset:30720
v_cvt_pk_bf16_f32 v168, v120, v121
v_mfma_f32_16x16x32_bf16 v[204:207], v[224:227], v[236:239], v[204:207]
ds_read_b64_tr_b16 v[224:225], v245 offset:16384
ds_read_b64_tr_b16 v[226:227], v245 offset:20480
v_mfma_f32_16x16x32_bf16 v[96:99], v[32:35], a[236:239], v[192:195]
v_cvt_pk_bf16_f32 v169, v122, v123
v_add_f32 v48, v48, v136
v_mfma_f32_16x16x32_bf16 v[96:99], v[28:31], a[220:223], v[96:99]
buffer_load_dwordx4 v45, %[srdK], s76 offen offset:1024 lds
v_mfma_f32_16x16x32_bf16 v[96:99], v[24:27], a[204:207], v[96:99]
v_add_f32 v48, v48, v137
s_waitcnt lgkmcnt(3)
v_mfma_f32_16x16x32_bf16 v[96:99], v[36:39], a[252:255], v[96:99]
ds_read_b128 v[20:23], v40 offset:30720
v_cvt_pk_bf16_f32 v170, v136, v137
v_mfma_f32_16x16x32_bf16 v[96:99], v[8:11], v[64:67], v[96:99]
v_exp_f32 v138, v138
v_mfma_f32_16x16x32_bf16 v[96:99], v[12:15], v[80:83], v[96:99]
v_exp_f32 v139, v139
v_mfma_f32_16x16x32_bf16 v[92:95], v[12:15], v[76:79], v[92:95]
v_exp_f32 v116, v116
v_mfma_f32_16x16x32_bf16 v[92:95], v[8:11], v[60:63], v[92:95]
ds_read_b128 v[24:27], %[kaddr0] offset:31744
v_add_f32 v48, v48, v138
v_mfma_f32_16x16x32_bf16 v[92:95], v[36:39], a[248:251], v[92:95]
v_exp_f32 v117, v117
v_mfma_f32_16x16x32_bf16 v[88:91], v[36:39], a[244:247], v[88:91]
buffer_load_dwordx4 v46, %[srdK], s76 offen offset:2048 lds
v_mfma_f32_16x16x32_bf16 v[88:91], v[8:11], v[56:59], v[88:91]
v_add_f32 v48, v48, v139
v_cvt_pk_bf16_f32 v171, v138, v139
v_mfma_f32_16x16x32_bf16 v[88:91], v[12:15], v[72:75], v[88:91]
ds_read_b128 v[28:31], v40 offset:31744
s_add_u32 m0, s81, 61440
v_mfma_f32_16x16x32_bf16 v[84:87], v[12:15], v[68:71], v[84:87]
v_exp_f32 v118, v118
v_mfma_f32_16x16x32_bf16 v[84:87], v[8:11], v[52:55], v[84:87]
v_add_f32 v47, v47, v116
v_add_f32 v47, v47, v117
v_mfma_f32_16x16x32_bf16 v[84:87], v[36:39], a[240:243], v[84:87]
v_add_f32 v47, v47, v118
s_waitcnt lgkmcnt(1)
v_mfma_f32_16x16x32_bf16 v[100:103], v[16:19], a[192:195], v[180:183]
ds_read_b128 v[32:35], %[kaddr0] offset:32768
v_cvt_pk_bf16_f32 v164, v116, v117
v_mfma_f32_16x16x32_bf16 v[100:103], v[20:23], a[208:211], v[100:103]
v_exp_f32 v119, v119
v_mfma_f32_16x16x32_bf16 v[100:103], v[24:27], a[224:227], v[100:103]
buffer_load_dwordx4 v44, %[srdV], s76 offen offset:0 lds
v_mfma_f32_16x16x32_bf16 v[208:211], v[224:227], v[236:239], v[208:211]
ds_read_b64_tr_b16 v[224:225], v246 offset:16384
ds_read_b64_tr_b16 v[226:227], v246 offset:20480
v_mfma_f32_16x16x32_bf16 v[104:107], v[24:27], a[228:231], v[184:187]
v_exp_f32 v132, v132
v_mfma_f32_16x16x32_bf16 v[104:107], v[20:23], a[212:215], v[104:107]
ds_read_b128 v[36:39], v40 offset:32768
v_add_f32 v47, v47, v119
v_mfma_f32_16x16x32_bf16 v[104:107], v[16:19], a[196:199], v[104:107]
v_exp_f32 v133, v133
v_mfma_f32_16x16x32_bf16 v[108:111], v[16:19], a[200:203], v[188:191]
v_cvt_pk_bf16_f32 v165, v118, v119
v_add_f32 v47, v47, v132
v_mfma_f32_16x16x32_bf16 v[108:111], v[20:23], a[216:219], v[108:111]
v_exp_f32 v134, v134
v_mfma_f32_16x16x32_bf16 v[108:111], v[24:27], a[232:235], v[108:111]
ds_read_b64_tr_b16 v[8:9], %[vaddr0] offset:24576
ds_read_b64_tr_b16 v[10:11], %[vaddr0] offset:30720
v_mfma_f32_16x16x32_bf16 v[112:115], v[24:27], a[236:239], v[192:195]
v_exp_f32 v135, v135
v_mfma_f32_16x16x32_bf16 v[112:115], v[20:23], a[220:223], v[112:115]
buffer_load_dwordx4 v45, %[srdV], s76 offen offset:1024 lds
v_mfma_f32_16x16x32_bf16 v[112:115], v[16:19], a[204:207], v[112:115]
v_add_f32 v47, v47, v133
s_waitcnt lgkmcnt(2)
v_mfma_f32_16x16x32_bf16 v[112:115], v[28:31], a[252:255], v[112:115]
ds_read_b64_tr_b16 v[12:13], v41 offset:24576
ds_read_b64_tr_b16 v[14:15], v41 offset:30720
v_mfma_f32_16x16x32_bf16 v[112:115], v[32:35], v[64:67], v[112:115]
v_add_f32 v47, v47, v134
v_add_f32 v47, v47, v135
v_mfma_f32_16x16x32_bf16 v[112:115], v[36:39], v[80:83], v[112:115]
v_cvt_pk_bf16_f32 v166, v132, v133
v_cvt_pk_bf16_f32 v167, v134, v135
v_mfma_f32_16x16x32_bf16 v[108:111], v[36:39], v[76:79], v[108:111]
v_mfma_f32_16x16x32_bf16 v[108:111], v[32:35], v[60:63], v[108:111]
ds_read_b64_tr_b16 v[16:17], v42 offset:24576
ds_read_b64_tr_b16 v[18:19], v42 offset:30720
v_mfma_f32_16x16x32_bf16 v[108:111], v[28:31], a[248:251], v[108:111]
v_mfma_f32_16x16x32_bf16 v[104:107], v[28:31], a[244:247], v[104:107]
buffer_load_dwordx4 v46, %[srdV], s76 offen offset:2048 lds
v_mfma_f32_16x16x32_bf16 v[104:107], v[32:35], v[56:59], v[104:107]
v_mfma_f32_16x16x32_bf16 v[104:107], v[36:39], v[72:75], v[104:107]
ds_read_b64_tr_b16 v[20:21], v43 offset:24576
ds_read_b64_tr_b16 v[22:23], v43 offset:30720
v_mfma_f32_16x16x32_bf16 v[212:215], v[224:227], v[236:239], v[212:215]
ds_read_b64_tr_b16 v[224:225], v247 offset:16384
ds_read_b64_tr_b16 v[226:227], v247 offset:20480
v_mfma_f32_16x16x32_bf16 v[100:103], v[36:39], v[68:71], v[100:103]
v_mfma_f32_16x16x32_bf16 v[100:103], v[32:35], v[52:55], v[100:103]
s_add_u32 s76, s76, 12288
v_mfma_f32_16x16x32_bf16 v[100:103], v[28:31], a[240:243], v[100:103]
s_waitcnt lgkmcnt(6)
v_mfma_f32_16x16x32_bf16 a[0:3], v[8:11], v[164:167], a[0:3]
ds_read_b64_tr_b16 v[24:25], %[vaddr0] offset:25600
ds_read_b64_tr_b16 v[26:27], %[vaddr0] offset:31744
v_mfma_f32_16x16x32_bf16 a[16:19], v[12:15], v[164:167], a[16:19]
v_mfma_f32_16x16x32_bf16 a[20:23], v[12:15], v[168:171], a[20:23]
v_mfma_f32_16x16x32_bf16 a[4:7], v[8:11], v[168:171], a[4:7]
v_exp_f32 v96, v96
v_mfma_f32_16x16x32_bf16 a[8:11], v[8:11], v[172:175], a[8:11]
ds_read_b64_tr_b16 v[28:29], v41 offset:25600
ds_read_b64_tr_b16 v[30:31], v41 offset:31744
v_mfma_f32_16x16x32_bf16 a[24:27], v[12:15], v[172:175], a[24:27]
v_exp_f32 v97, v97
v_mfma_f32_16x16x32_bf16 a[28:31], v[12:15], v[176:179], a[28:31]
v_exp_f32 v98, v98
v_mfma_f32_16x16x32_bf16 a[12:15], v[8:11], v[176:179], a[12:15]
v_add_f32 v50, v50, v96
s_waitcnt lgkmcnt(6)
v_mfma_f32_16x16x32_bf16 a[44:47], v[16:19], v[176:179], a[44:47]
ds_read_b64_tr_b16 v[32:33], v42 offset:25600
ds_read_b64_tr_b16 v[34:35], v42 offset:31744
v_mfma_f32_16x16x32_bf16 a[60:63], v[20:23], v[176:179], a[60:63]
v_exp_f32 v99, v99
v_mfma_f32_16x16x32_bf16 a[56:59], v[20:23], v[172:175], a[56:59]
v_mfma_f32_16x16x32_bf16 a[40:43], v[16:19], v[172:175], a[40:43]
v_add_f32 v50, v50, v97
v_add_f32 v50, v50, v98
v_mfma_f32_16x16x32_bf16 a[36:39], v[16:19], v[168:171], a[36:39]
ds_read_b64_tr_b16 v[36:37], v43 offset:25600
ds_read_b64_tr_b16 v[38:39], v43 offset:31744
v_mfma_f32_16x16x32_bf16 a[52:55], v[20:23], v[168:171], a[52:55]
v_exp_f32 v112, v112
s_waitcnt lgkmcnt(8)
v_mfma_f32_16x16x32_bf16 v[216:219], v[224:227], v[236:239], v[216:219]
ds_read_b64_tr_b16 v[224:225], v244 offset:17408
ds_read_b64_tr_b16 v[226:227], v244 offset:21504
v_mfma_f32_16x16x32_bf16 a[48:51], v[20:23], v[164:167], a[48:51]
v_add_f32 v50, v50, v99
v_cvt_pk_bf16_f32 v160, v96, v97
v_mfma_f32_16x16x32_bf16 a[32:35], v[16:19], v[164:167], a[32:35]
v_cvt_pk_bf16_f32 v161, v98, v99
s_waitcnt lgkmcnt(6)
v_mfma_f32_16x16x32_bf16 a[64:67], v[24:27], v[164:167], a[64:67]
ds_read_b64_tr_b16 v[8:9], %[vaddr0] offset:26624
ds_read_b64_tr_b16 v[10:11], %[vaddr0] offset:32768
v_mfma_f32_16x16x32_bf16 a[80:83], v[28:31], v[164:167], a[80:83]
v_exp_f32 v113, v113
v_mfma_f32_16x16x32_bf16 a[84:87], v[28:31], v[168:171], a[84:87]
v_mfma_f32_16x16x32_bf16 a[68:71], v[24:27], v[168:171], a[68:71]
v_exp_f32 v114, v114
v_mfma_f32_16x16x32_bf16 a[72:75], v[24:27], v[172:175], a[72:75]
ds_read_b64_tr_b16 v[12:13], v41 offset:26624
ds_read_b64_tr_b16 v[14:15], v41 offset:32768
v_mfma_f32_16x16x32_bf16 a[88:91], v[28:31], v[172:175], a[88:91]
v_add_f32 v50, v50, v112
v_add_f32 v50, v50, v113
v_mfma_f32_16x16x32_bf16 a[92:95], v[28:31], v[176:179], a[92:95]
v_exp_f32 v115, v115
v_mfma_f32_16x16x32_bf16 a[76:79], v[24:27], v[176:179], a[76:79]
v_add_f32 v50, v50, v114
s_waitcnt lgkmcnt(6)
v_mfma_f32_16x16x32_bf16 a[108:111], v[32:35], v[176:179], a[108:111]
ds_read_b64_tr_b16 v[16:17], v42 offset:26624
ds_read_b64_tr_b16 v[18:19], v42 offset:32768
v_mfma_f32_16x16x32_bf16 a[124:127], v[36:39], v[176:179], a[124:127]
v_exp_f32 v92, v92
v_mfma_f32_16x16x32_bf16 a[120:123], v[36:39], v[172:175], a[120:123]
v_add_f32 v50, v50, v115
v_cvt_pk_bf16_f32 v162, v112, v113
buffer_load_dword v199, v197, %[srdS], s92 offen
v_mfma_f32_16x16x32_bf16 a[104:107], v[32:35], v[172:175], a[104:107]
v_exp_f32 v93, v93
s_add_u32 s92, s92, 7680
v_mfma_f32_16x16x32_bf16 a[100:103], v[32:35], v[168:171], a[100:103]
ds_read_b64_tr_b16 v[20:21], v43 offset:26624
ds_read_b64_tr_b16 v[22:23], v43 offset:32768
v_mfma_f32_16x16x32_bf16 a[116:119], v[36:39], v[168:171], a[116:119]
v_cvt_pk_bf16_f32 v163, v114, v115
v_add_f32 v49, v49, v92
v_mfma_f32_16x16x32_bf16 a[112:115], v[36:39], v[164:167], a[112:115]
v_exp_f32 v94, v94
v_mfma_f32_16x16x32_bf16 a[96:99], v[32:35], v[164:167], a[96:99]
v_add_f32 v49, v49, v93
s_waitcnt lgkmcnt(4)
v_mfma_f32_16x16x32_bf16 a[128:131], v[8:11], v[164:167], a[128:131]
s_waitcnt vmcnt(15) lgkmcnt(0)
s_barrier
ds_read_b128 v[24:27], %[kaddr0] offset:36864
v_mfma_f32_16x16x32_bf16 a[144:147], v[12:15], v[164:167], a[144:147]
v_exp_f32 v95, v95
v_mfma_f32_16x16x32_bf16 a[148:151], v[12:15], v[168:171], a[148:151]
v_add_f32 v49, v49, v94
v_add_f32 v49, v49, v95
v_mfma_f32_16x16x32_bf16 a[132:135], v[8:11], v[168:171], a[132:135]
)"
R"(v_exp_f32 v108, v108
v_mfma_f32_16x16x32_bf16 a[136:139], v[8:11], v[172:175], a[136:139]
ds_read_b128 v[28:31], v40 offset:36864
v_cvt_pk_bf16_f32 v156, v92, v93
v_mfma_f32_16x16x32_bf16 a[152:155], v[12:15], v[172:175], a[152:155]
v_exp_f32 v109, v109
v_mfma_f32_16x16x32_bf16 v[220:223], v[224:227], v[236:239], v[220:223]
ds_read_b64_tr_b16 v[224:225], v244 offset:24576
ds_read_b64_tr_b16 v[226:227], v244 offset:28672
v_mfma_f32_16x16x32_bf16 a[156:159], v[12:15], v[176:179], a[156:159]
v_cvt_pk_bf16_f32 v157, v94, v95
v_add_f32 v49, v49, v108
v_mfma_f32_16x16x32_bf16 a[140:143], v[8:11], v[176:179], a[140:143]
v_add_f32 v49, v49, v109
v_mfma_f32_16x16x32_bf16 a[172:175], v[16:19], v[176:179], a[172:175]
ds_read_b128 v[32:35], %[kaddr0] offset:37888
v_cvt_pk_bf16_f32 v158, v108, v109
v_mfma_f32_16x16x32_bf16 a[188:191], v[20:23], v[176:179], a[188:191]
v_exp_f32 v110, v110
v_mfma_f32_16x16x32_bf16 a[184:187], v[20:23], v[172:175], a[184:187]
v_exp_f32 v111, v111
v_mfma_f32_16x16x32_bf16 a[168:171], v[16:19], v[172:175], a[168:171]
v_exp_f32 v88, v88
v_mfma_f32_16x16x32_bf16 a[164:167], v[16:19], v[168:171], a[164:167]
ds_read_b128 v[36:39], v40 offset:37888
v_add_f32 v49, v49, v110
v_mfma_f32_16x16x32_bf16 a[180:183], v[20:23], v[168:171], a[180:183]
v_exp_f32 v89, v89
v_mfma_f32_16x16x32_bf16 a[176:179], v[20:23], v[164:167], a[176:179]
v_add_f32 v49, v49, v111
v_cvt_pk_bf16_f32 v159, v110, v111
v_mfma_f32_16x16x32_bf16 a[160:163], v[16:19], v[164:167], a[160:163]
v_add_f32 v48, v48, v88
s_waitcnt lgkmcnt(1)
v_mfma_f32_16x16x32_bf16 v[116:119], v[24:27], a[192:195], v[180:183]
ds_read_b128 v[8:11], %[kaddr0] offset:38912
s_add_u32 m0, s81, 73728
v_mfma_f32_16x16x32_bf16 v[116:119], v[28:31], a[208:211], v[116:119]
v_exp_f32 v90, v90
v_mfma_f32_16x16x32_bf16 v[116:119], v[32:35], a[224:227], v[116:119]
buffer_load_dwordx4 v44, %[srdV], s76 offen offset:0 lds
v_mfma_f32_16x16x32_bf16 v[120:123], v[32:35], a[228:231], v[184:187]
v_exp_f32 v91, v91
v_mfma_f32_16x16x32_bf16 v[120:123], v[28:31], a[212:215], v[120:123]
ds_read_b128 v[12:15], v40 offset:38912
v_add_f32 v48, v48, v89
v_mfma_f32_16x16x32_bf16 v[120:123], v[24:27], a[196:199], v[120:123]
v_add_f32 v48, v48, v90
v_add_f32 v48, v48, v91
v_mfma_f32_16x16x32_bf16 v[124:127], v[24:27], a[200:203], v[188:191]
v_exp_f32 v104, v104
v_mfma_f32_16x16x32_bf16 v[124:127], v[28:31], a[216:219], v[124:127]
v_exp_f32 v105, v105
v_mfma_f32_16x16x32_bf16 v[124:127], v[32:35], a[232:235], v[124:127]
ds_read_b128 v[16:19], %[kaddr0] offset:43008
v_cvt_pk_bf16_f32 v152, v88, v89
v_mfma_f32_16x16x32_bf16 v[204:207], v[224:227], v[240:243], v[204:207]
ds_read_b64_tr_b16 v[224:225], v245 offset:24576
ds_read_b64_tr_b16 v[226:227], v245 offset:28672
v_mfma_f32_16x16x32_bf16 v[128:131], v[32:35], a[236:239], v[192:195]
v_cvt_pk_bf16_f32 v153, v90, v91
v_add_f32 v48, v48, v104
v_mfma_f32_16x16x32_bf16 v[128:131], v[28:31], a[220:223], v[128:131]
buffer_load_dwordx4 v45, %[srdV], s76 offen offset:1024 lds
v_mfma_f32_16x16x32_bf16 v[128:131], v[24:27], a[204:207], v[128:131]
v_add_f32 v48, v48, v105
s_waitcnt lgkmcnt(3)
v_mfma_f32_16x16x32_bf16 v[128:131], v[36:39], a[252:255], v[128:131]
ds_read_b128 v[20:23], v40 offset:43008
v_cvt_pk_bf16_f32 v154, v104, v105
v_mfma_f32_16x16x32_bf16 v[128:131], v[8:11], v[64:67], v[128:131]
v_exp_f32 v106, v106
v_mfma_f32_16x16x32_bf16 v[128:131], v[12:15], v[80:83], v[128:131]
v_exp_f32 v107, v107
v_mfma_f32_16x16x32_bf16 v[124:127], v[12:15], v[76:79], v[124:127]
v_exp_f32 v84, v84
v_mfma_f32_16x16x32_bf16 v[124:127], v[8:11], v[60:63], v[124:127]
ds_read_b128 v[24:27], %[kaddr0] offset:44032
v_add_f32 v48, v48, v106
v_mfma_f32_16x16x32_bf16 v[124:127], v[36:39], a[248:251], v[124:127]
v_exp_f32 v85, v85
v_mfma_f32_16x16x32_bf16 v[120:123], v[36:39], a[244:247], v[120:123]
buffer_load_dwordx4 v46, %[srdV], s76 offen offset:2048 lds
v_mfma_f32_16x16x32_bf16 v[120:123], v[8:11], v[56:59], v[120:123]
v_add_f32 v48, v48, v107
v_cvt_pk_bf16_f32 v155, v106, v107
v_mfma_f32_16x16x32_bf16 v[120:123], v[12:15], v[72:75], v[120:123]
ds_read_b128 v[28:31], v40 offset:44032
s_add_u32 s76, s76, 12288
v_mfma_f32_16x16x32_bf16 v[116:119], v[12:15], v[68:71], v[116:119]
v_exp_f32 v86, v86
v_mfma_f32_16x16x32_bf16 v[116:119], v[8:11], v[52:55], v[116:119]
v_add_f32 v47, v47, v84
v_add_f32 v47, v47, v85
v_mfma_f32_16x16x32_bf16 v[116:119], v[36:39], a[240:243], v[116:119]
v_add_f32 v47, v47, v86
s_waitcnt lgkmcnt(1)
v_mfma_f32_16x16x32_bf16 v[132:135], v[16:19], a[192:195], v[180:183]
ds_read_b128 v[32:35], %[kaddr0] offset:45056
v_cvt_pk_bf16_f32 v148, v84, v85
v_mfma_f32_16x16x32_bf16 v[132:135], v[20:23], a[208:211], v[132:135]
v_exp_f32 v87, v87
v_mfma_f32_16x16x32_bf16 v[132:135], v[24:27], a[224:227], v[132:135]
v_mfma_f32_16x16x32_bf16 v[208:211], v[224:227], v[240:243], v[208:211]
ds_read_b64_tr_b16 v[224:225], v246 offset:24576
ds_read_b64_tr_b16 v[226:227], v246 offset:28672
v_mfma_f32_16x16x32_bf16 v[136:139], v[24:27], a[228:231], v[184:187]
v_exp_f32 v100, v100
v_mfma_f32_16x16x32_bf16 v[136:139], v[20:23], a[212:215], v[136:139]
ds_read_b128 v[36:39], v40 offset:45056
v_add_f32 v47, v47, v87
v_mfma_f32_16x16x32_bf16 v[136:139], v[16:19], a[196:199], v[136:139]
v_exp_f32 v101, v101
v_mfma_f32_16x16x32_bf16 v[140:143], v[16:19], a[200:203], v[188:191]
v_cvt_pk_bf16_f32 v149, v86, v87
v_add_f32 v47, v47, v100
v_mfma_f32_16x16x32_bf16 v[140:143], v[20:23], a[216:219], v[140:143]
v_exp_f32 v102, v102
v_mfma_f32_16x16x32_bf16 v[140:143], v[24:27], a[232:235], v[140:143]
ds_read_b64_tr_b16 v[8:9], %[vaddr0] offset:36864
ds_read_b64_tr_b16 v[10:11], %[vaddr0] offset:43008
v_mfma_f32_16x16x32_bf16 v[144:147], v[24:27], a[236:239], v[192:195]
v_exp_f32 v103, v103
v_mfma_f32_16x16x32_bf16 v[144:147], v[20:23], a[220:223], v[144:147]
v_mfma_f32_16x16x32_bf16 v[144:147], v[16:19], a[204:207], v[144:147]
v_add_f32 v47, v47, v101
s_waitcnt lgkmcnt(2)
v_mfma_f32_16x16x32_bf16 v[144:147], v[28:31], a[252:255], v[144:147]
ds_read_b64_tr_b16 v[12:13], v41 offset:36864
ds_read_b64_tr_b16 v[14:15], v41 offset:43008
v_mfma_f32_16x16x32_bf16 v[144:147], v[32:35], v[64:67], v[144:147]
v_add_f32 v47, v47, v102
v_add_f32 v47, v47, v103
v_mfma_f32_16x16x32_bf16 v[144:147], v[36:39], v[80:83], v[144:147]
v_cvt_pk_bf16_f32 v150, v100, v101
v_cvt_pk_bf16_f32 v151, v102, v103
v_mfma_f32_16x16x32_bf16 v[140:143], v[36:39], v[76:79], v[140:143]
v_mfma_f32_16x16x32_bf16 v[140:143], v[32:35], v[60:63], v[140:143]
ds_read_b64_tr_b16 v[16:17], v42 offset:36864
ds_read_b64_tr_b16 v[18:19], v42 offset:43008
v_mfma_f32_16x16x32_bf16 v[140:143], v[28:31], a[248:251], v[140:143]
v_mfma_f32_16x16x32_bf16 v[136:139], v[28:31], a[244:247], v[136:139]
v_mfma_f32_16x16x32_bf16 v[136:139], v[32:35], v[56:59], v[136:139]
v_mfma_f32_16x16x32_bf16 v[136:139], v[36:39], v[72:75], v[136:139]
ds_read_b64_tr_b16 v[20:21], v43 offset:36864
ds_read_b64_tr_b16 v[22:23], v43 offset:43008
v_mfma_f32_16x16x32_bf16 v[212:215], v[224:227], v[240:243], v[212:215]
ds_read_b64_tr_b16 v[224:225], v247 offset:24576
ds_read_b64_tr_b16 v[226:227], v247 offset:28672
v_mfma_f32_16x16x32_bf16 v[132:135], v[36:39], v[68:71], v[132:135]
v_mfma_f32_16x16x32_bf16 v[132:135], v[32:35], v[52:55], v[132:135]
v_mfma_f32_16x16x32_bf16 v[132:135], v[28:31], a[240:243], v[132:135]
s_waitcnt lgkmcnt(6)
v_mfma_f32_16x16x32_bf16 a[0:3], v[8:11], v[148:151], a[0:3]
ds_read_b64_tr_b16 v[24:25], %[vaddr0] offset:37888
ds_read_b64_tr_b16 v[26:27], %[vaddr0] offset:44032
v_mfma_f32_16x16x32_bf16 a[16:19], v[12:15], v[148:151], a[16:19]
v_mfma_f32_16x16x32_bf16 a[20:23], v[12:15], v[152:155], a[20:23]
v_mfma_f32_16x16x32_bf16 a[4:7], v[8:11], v[152:155], a[4:7]
v_exp_f32 v128, v128
v_mfma_f32_16x16x32_bf16 a[8:11], v[8:11], v[156:159], a[8:11]
ds_read_b64_tr_b16 v[28:29], v41 offset:37888
ds_read_b64_tr_b16 v[30:31], v41 offset:44032
v_mfma_f32_16x16x32_bf16 a[24:27], v[12:15], v[156:159], a[24:27]
v_exp_f32 v129, v129
v_mfma_f32_16x16x32_bf16 a[28:31], v[12:15], v[160:163], a[28:31]
v_exp_f32 v130, v130
v_mfma_f32_16x16x32_bf16 a[12:15], v[8:11], v[160:163], a[12:15]
v_add_f32 v50, v50, v128
s_waitcnt lgkmcnt(6)
v_mfma_f32_16x16x32_bf16 a[44:47], v[16:19], v[160:163], a[44:47]
ds_read_b64_tr_b16 v[32:33], v42 offset:37888
ds_read_b64_tr_b16 v[34:35], v42 offset:44032
v_mfma_f32_16x16x32_bf16 a[60:63], v[20:23], v[160:163], a[60:63]
v_exp_f32 v131, v131
v_mfma_f32_16x16x32_bf16 a[56:59], v[20:23], v[156:159], a[56:59]
v_mfma_f32_16x16x32_bf16 a[40:43], v[16:19], v[156:159], a[40:43]
v_add_f32 v50, v50, v129
v_add_f32 v50, v50, v130
v_mfma_f32_16x16x32_bf16 a[36:39], v[16:19], v[152:155], a[36:39]
ds_read_b64_tr_b16 v[36:37], v43 offset:37888
ds_read_b64_tr_b16 v[38:39], v43 offset:44032
v_mfma_f32_16x16x32_bf16 a[52:55], v[20:23], v[152:155], a[52:55]
v_exp_f32 v144, v144
s_waitcnt lgkmcnt(8)
v_mfma_f32_16x16x32_bf16 v[216:219], v[224:227], v[240:243], v[216:219]
ds_read_b64_tr_b16 v[224:225], v244 offset:25600
ds_read_b64_tr_b16 v[226:227], v244 offset:29696
v_mfma_f32_16x16x32_bf16 a[48:51], v[20:23], v[148:151], a[48:51]
v_add_f32 v50, v50, v131
v_cvt_pk_bf16_f32 v176, v128, v129
v_mfma_f32_16x16x32_bf16 a[32:35], v[16:19], v[148:151], a[32:35]
v_cvt_pk_bf16_f32 v177, v130, v131
s_waitcnt lgkmcnt(6)
v_mfma_f32_16x16x32_bf16 a[64:67], v[24:27], v[148:151], a[64:67]
ds_read_b64_tr_b16 v[8:9], %[vaddr0] offset:38912
ds_read_b64_tr_b16 v[10:11], %[vaddr0] offset:45056
v_mfma_f32_16x16x32_bf16 a[80:83], v[28:31], v[148:151], a[80:83]
v_exp_f32 v145, v145
v_mfma_f32_16x16x32_bf16 a[84:87], v[28:31], v[152:155], a[84:87]
)"
R"(v_mfma_f32_16x16x32_bf16 a[68:71], v[24:27], v[152:155], a[68:71]
v_exp_f32 v146, v146
v_mfma_f32_16x16x32_bf16 a[72:75], v[24:27], v[156:159], a[72:75]
ds_read_b64_tr_b16 v[12:13], v41 offset:38912
ds_read_b64_tr_b16 v[14:15], v41 offset:45056
v_mfma_f32_16x16x32_bf16 a[88:91], v[28:31], v[156:159], a[88:91]
v_add_f32 v50, v50, v144
v_add_f32 v50, v50, v145
v_mfma_f32_16x16x32_bf16 a[92:95], v[28:31], v[160:163], a[92:95]
v_exp_f32 v147, v147
v_mfma_f32_16x16x32_bf16 a[76:79], v[24:27], v[160:163], a[76:79]
v_add_f32 v50, v50, v146
s_waitcnt lgkmcnt(6)
v_mfma_f32_16x16x32_bf16 a[108:111], v[32:35], v[160:163], a[108:111]
ds_read_b64_tr_b16 v[16:17], v42 offset:38912
ds_read_b64_tr_b16 v[18:19], v42 offset:45056
v_mfma_f32_16x16x32_bf16 a[124:127], v[36:39], v[160:163], a[124:127]
v_exp_f32 v124, v124
v_mfma_f32_16x16x32_bf16 a[120:123], v[36:39], v[156:159], a[120:123]
v_add_f32 v50, v50, v147
v_cvt_pk_bf16_f32 v178, v144, v145
buffer_load_dword v199, v197, %[srdS], s92 offen
v_mfma_f32_16x16x32_bf16 a[104:107], v[32:35], v[156:159], a[104:107]
v_exp_f32 v125, v125
s_add_u32 s92, s92, 7680
v_mfma_f32_16x16x32_bf16 a[100:103], v[32:35], v[152:155], a[100:103]
ds_read_b64_tr_b16 v[20:21], v43 offset:38912
ds_read_b64_tr_b16 v[22:23], v43 offset:45056
v_mfma_f32_16x16x32_bf16 a[116:119], v[36:39], v[152:155], a[116:119]
v_cvt_pk_bf16_f32 v179, v146, v147
v_add_f32 v49, v49, v124
v_mfma_f32_16x16x32_bf16 a[112:115], v[36:39], v[148:151], a[112:115]
v_exp_f32 v126, v126
v_mfma_f32_16x16x32_bf16 a[96:99], v[32:35], v[148:151], a[96:99]
v_add_f32 v49, v49, v125
s_waitcnt lgkmcnt(4)
v_mfma_f32_16x16x32_bf16 a[128:131], v[8:11], v[148:151], a[128:131]
s_waitcnt vmcnt(12) lgkmcnt(0)
s_barrier
ds_read_b128 v[24:27], %[kaddr0] offset:0
v_mfma_f32_16x16x32_bf16 a[144:147], v[12:15], v[148:151], a[144:147]
v_exp_f32 v127, v127
v_mfma_f32_16x16x32_bf16 a[148:151], v[12:15], v[152:155], a[148:151]
v_add_f32 v49, v49, v126
v_add_f32 v49, v49, v127
v_mfma_f32_16x16x32_bf16 a[132:135], v[8:11], v[152:155], a[132:135]
v_exp_f32 v140, v140
v_mfma_f32_16x16x32_bf16 a[136:139], v[8:11], v[156:159], a[136:139]
ds_read_b128 v[28:31], v40 offset:0
v_cvt_pk_bf16_f32 v172, v124, v125
v_mfma_f32_16x16x32_bf16 a[152:155], v[12:15], v[156:159], a[152:155]
v_exp_f32 v141, v141
v_mfma_f32_16x16x32_bf16 v[220:223], v[224:227], v[240:243], v[220:223]
v_mfma_f32_16x16x32_bf16 a[156:159], v[12:15], v[160:163], a[156:159]
v_cvt_pk_bf16_f32 v173, v126, v127
v_add_f32 v49, v49, v140
v_mfma_f32_16x16x32_bf16 a[140:143], v[8:11], v[160:163], a[140:143]
v_add_f32 v49, v49, v141
v_mfma_f32_16x16x32_bf16 a[172:175], v[16:19], v[160:163], a[172:175]
ds_read_b128 v[32:35], %[kaddr0] offset:1024
v_cvt_pk_bf16_f32 v174, v140, v141
v_mfma_f32_16x16x32_bf16 a[188:191], v[20:23], v[160:163], a[188:191]
v_exp_f32 v142, v142
v_mfma_f32_16x16x32_bf16 a[184:187], v[20:23], v[156:159], a[184:187]
v_exp_f32 v143, v143
v_mfma_f32_16x16x32_bf16 a[168:171], v[16:19], v[156:159], a[168:171]
v_exp_f32 v120, v120
v_mfma_f32_16x16x32_bf16 a[164:167], v[16:19], v[152:155], a[164:167]
ds_read_b128 v[36:39], v40 offset:1024
v_add_f32 v49, v49, v142
v_mfma_f32_16x16x32_bf16 a[180:183], v[20:23], v[152:155], a[180:183]
v_exp_f32 v121, v121
v_mfma_f32_16x16x32_bf16 a[176:179], v[20:23], v[148:151], a[176:179]
v_add_f32 v49, v49, v143
v_cvt_pk_bf16_f32 v175, v142, v143
v_mfma_f32_16x16x32_bf16 a[160:163], v[16:19], v[148:151], a[160:163]
v_add_f32 v48, v48, v120
s_waitcnt lgkmcnt(1)
v_mfma_f32_16x16x32_bf16 v[84:87], v[24:27], a[192:195], v[180:183]
ds_read_b128 v[8:11], %[kaddr0] offset:2048
v_mfma_f32_16x16x32_bf16 v[84:87], v[28:31], a[208:211], v[84:87]
v_exp_f32 v122, v122
v_mfma_f32_16x16x32_bf16 v[84:87], v[32:35], a[224:227], v[84:87]
v_mfma_f32_16x16x32_bf16 v[88:91], v[32:35], a[228:231], v[184:187]
v_exp_f32 v123, v123
v_mfma_f32_16x16x32_bf16 v[88:91], v[28:31], a[212:215], v[88:91]
ds_read_b128 v[12:15], v40 offset:2048
v_add_f32 v48, v48, v121
v_mfma_f32_16x16x32_bf16 v[88:91], v[24:27], a[196:199], v[88:91]
v_add_f32 v48, v48, v122
v_add_f32 v48, v48, v123
v_mfma_f32_16x16x32_bf16 v[92:95], v[24:27], a[200:203], v[188:191]
v_exp_f32 v136, v136
v_mfma_f32_16x16x32_bf16 v[92:95], v[28:31], a[216:219], v[92:95]
v_exp_f32 v137, v137
v_mfma_f32_16x16x32_bf16 v[92:95], v[32:35], a[232:235], v[92:95]
ds_read_b128 v[16:19], %[kaddr0] offset:6144
v_cvt_pk_bf16_f32 v168, v120, v121
v_mfma_f32_16x16x32_bf16 v[96:99], v[32:35], a[236:239], v[192:195]
v_cvt_pk_bf16_f32 v169, v122, v123
v_add_f32 v48, v48, v136
v_mfma_f32_16x16x32_bf16 v[96:99], v[28:31], a[220:223], v[96:99]
v_mfma_f32_16x16x32_bf16 v[96:99], v[24:27], a[204:207], v[96:99]
v_add_f32 v48, v48, v137
s_waitcnt lgkmcnt(1)
v_mfma_f32_16x16x32_bf16 v[96:99], v[36:39], a[252:255], v[96:99]
ds_read_b128 v[20:23], v40 offset:6144
v_cvt_pk_bf16_f32 v170, v136, v137
v_mfma_f32_16x16x32_bf16 v[96:99], v[8:11], v[64:67], v[96:99]
v_exp_f32 v138, v138
v_mfma_f32_16x16x32_bf16 v[96:99], v[12:15], v[80:83], v[96:99]
v_exp_f32 v139, v139
v_mfma_f32_16x16x32_bf16 v[92:95], v[12:15], v[76:79], v[92:95]
v_exp_f32 v116, v116
v_mfma_f32_16x16x32_bf16 v[92:95], v[8:11], v[60:63], v[92:95]
ds_read_b128 v[24:27], %[kaddr0] offset:7168
v_add_f32 v48, v48, v138
v_mfma_f32_16x16x32_bf16 v[92:95], v[36:39], a[248:251], v[92:95]
v_exp_f32 v117, v117
v_mfma_f32_16x16x32_bf16 v[88:91], v[36:39], a[244:247], v[88:91]
v_mfma_f32_16x16x32_bf16 v[88:91], v[8:11], v[56:59], v[88:91]
v_add_f32 v48, v48, v139
v_cvt_pk_bf16_f32 v171, v138, v139
v_mfma_f32_16x16x32_bf16 v[88:91], v[12:15], v[72:75], v[88:91]
ds_read_b128 v[28:31], v40 offset:7168
v_mfma_f32_16x16x32_bf16 v[84:87], v[12:15], v[68:71], v[84:87]
v_exp_f32 v118, v118
v_mfma_f32_16x16x32_bf16 v[84:87], v[8:11], v[52:55], v[84:87]
v_add_f32 v47, v47, v116
v_add_f32 v47, v47, v117
v_mfma_f32_16x16x32_bf16 v[84:87], v[36:39], a[240:243], v[84:87]
v_add_f32 v47, v47, v118
s_waitcnt lgkmcnt(1)
v_mfma_f32_16x16x32_bf16 v[100:103], v[16:19], a[192:195], v[180:183]
ds_read_b128 v[32:35], %[kaddr0] offset:8192
v_cvt_pk_bf16_f32 v164, v116, v117
v_mfma_f32_16x16x32_bf16 v[100:103], v[20:23], a[208:211], v[100:103]
v_exp_f32 v119, v119
v_mfma_f32_16x16x32_bf16 v[100:103], v[24:27], a[224:227], v[100:103]
v_mfma_f32_16x16x32_bf16 v[104:107], v[24:27], a[228:231], v[184:187]
v_exp_f32 v132, v132
v_mfma_f32_16x16x32_bf16 v[104:107], v[20:23], a[212:215], v[104:107]
ds_read_b128 v[36:39], v40 offset:8192
v_add_f32 v47, v47, v119
v_mfma_f32_16x16x32_bf16 v[104:107], v[16:19], a[196:199], v[104:107]
v_exp_f32 v133, v133
v_mfma_f32_16x16x32_bf16 v[108:111], v[16:19], a[200:203], v[188:191]
v_cvt_pk_bf16_f32 v165, v118, v119
v_add_f32 v47, v47, v132
v_mfma_f32_16x16x32_bf16 v[108:111], v[20:23], a[216:219], v[108:111]
v_exp_f32 v134, v134
v_mfma_f32_16x16x32_bf16 v[108:111], v[24:27], a[232:235], v[108:111]
ds_read_b64_tr_b16 v[8:9], %[vaddr0] offset:0
ds_read_b64_tr_b16 v[10:11], %[vaddr0] offset:6144
v_mfma_f32_16x16x32_bf16 v[112:115], v[24:27], a[236:239], v[192:195]
v_exp_f32 v135, v135
v_mfma_f32_16x16x32_bf16 v[112:115], v[20:23], a[220:223], v[112:115]
v_mfma_f32_16x16x32_bf16 v[112:115], v[16:19], a[204:207], v[112:115]
v_add_f32 v47, v47, v133
s_waitcnt lgkmcnt(2)
v_mfma_f32_16x16x32_bf16 v[112:115], v[28:31], a[252:255], v[112:115]
ds_read_b64_tr_b16 v[12:13], v41 offset:0
ds_read_b64_tr_b16 v[14:15], v41 offset:6144
v_mfma_f32_16x16x32_bf16 v[112:115], v[32:35], v[64:67], v[112:115]
v_add_f32 v47, v47, v134
v_add_f32 v47, v47, v135
v_mfma_f32_16x16x32_bf16 v[112:115], v[36:39], v[80:83], v[112:115]
v_cvt_pk_bf16_f32 v166, v132, v133
v_cvt_pk_bf16_f32 v167, v134, v135
v_mfma_f32_16x16x32_bf16 v[108:111], v[36:39], v[76:79], v[108:111]
v_mfma_f32_16x16x32_bf16 v[108:111], v[32:35], v[60:63], v[108:111]
ds_read_b64_tr_b16 v[16:17], v42 offset:0
ds_read_b64_tr_b16 v[18:19], v42 offset:6144
v_mfma_f32_16x16x32_bf16 v[108:111], v[28:31], a[248:251], v[108:111]
v_mfma_f32_16x16x32_bf16 v[104:107], v[28:31], a[244:247], v[104:107]
v_mfma_f32_16x16x32_bf16 v[104:107], v[32:35], v[56:59], v[104:107]
v_mfma_f32_16x16x32_bf16 v[104:107], v[36:39], v[72:75], v[104:107]
ds_read_b64_tr_b16 v[20:21], v43 offset:0
ds_read_b64_tr_b16 v[22:23], v43 offset:6144
v_mfma_f32_16x16x32_bf16 v[100:103], v[36:39], v[68:71], v[100:103]
v_mfma_f32_16x16x32_bf16 v[100:103], v[32:35], v[52:55], v[100:103]
v_mfma_f32_16x16x32_bf16 v[100:103], v[28:31], a[240:243], v[100:103]
s_waitcnt lgkmcnt(4)
v_mfma_f32_16x16x32_bf16 a[0:3], v[8:11], v[164:167], a[0:3]
ds_read_b64_tr_b16 v[24:25], %[vaddr0] offset:1024
ds_read_b64_tr_b16 v[26:27], %[vaddr0] offset:7168
v_mfma_f32_16x16x32_bf16 a[16:19], v[12:15], v[164:167], a[16:19]
v_mfma_f32_16x16x32_bf16 a[20:23], v[12:15], v[168:171], a[20:23]
v_mfma_f32_16x16x32_bf16 a[4:7], v[8:11], v[168:171], a[4:7]
v_exp_f32 v96, v96
v_mfma_f32_16x16x32_bf16 a[8:11], v[8:11], v[172:175], a[8:11]
ds_read_b64_tr_b16 v[28:29], v41 offset:1024
ds_read_b64_tr_b16 v[30:31], v41 offset:7168
v_mfma_f32_16x16x32_bf16 a[24:27], v[12:15], v[172:175], a[24:27]
v_exp_f32 v97, v97
v_mfma_f32_16x16x32_bf16 a[28:31], v[12:15], v[176:179], a[28:31]
v_exp_f32 v98, v98
v_mfma_f32_16x16x32_bf16 a[12:15], v[8:11], v[176:179], a[12:15]
v_add_f32 v50, v50, v96
s_waitcnt lgkmcnt(4)
)"
R"(v_mfma_f32_16x16x32_bf16 a[44:47], v[16:19], v[176:179], a[44:47]
ds_read_b64_tr_b16 v[32:33], v42 offset:1024
ds_read_b64_tr_b16 v[34:35], v42 offset:7168
v_mfma_f32_16x16x32_bf16 a[60:63], v[20:23], v[176:179], a[60:63]
v_exp_f32 v99, v99
v_mfma_f32_16x16x32_bf16 a[56:59], v[20:23], v[172:175], a[56:59]
v_mfma_f32_16x16x32_bf16 a[40:43], v[16:19], v[172:175], a[40:43]
v_add_f32 v50, v50, v97
v_add_f32 v50, v50, v98
v_mfma_f32_16x16x32_bf16 a[36:39], v[16:19], v[168:171], a[36:39]
ds_read_b64_tr_b16 v[36:37], v43 offset:1024
ds_read_b64_tr_b16 v[38:39], v43 offset:7168
v_mfma_f32_16x16x32_bf16 a[52:55], v[20:23], v[168:171], a[52:55]
v_exp_f32 v112, v112
v_mfma_f32_16x16x32_bf16 a[48:51], v[20:23], v[164:167], a[48:51]
v_add_f32 v50, v50, v99
v_cvt_pk_bf16_f32 v160, v96, v97
v_mfma_f32_16x16x32_bf16 a[32:35], v[16:19], v[164:167], a[32:35]
v_cvt_pk_bf16_f32 v161, v98, v99
s_waitcnt lgkmcnt(4)
v_mfma_f32_16x16x32_bf16 a[64:67], v[24:27], v[164:167], a[64:67]
ds_read_b64_tr_b16 v[8:9], %[vaddr0] offset:2048
ds_read_b64_tr_b16 v[10:11], %[vaddr0] offset:8192
v_mfma_f32_16x16x32_bf16 a[80:83], v[28:31], v[164:167], a[80:83]
v_exp_f32 v113, v113
v_mfma_f32_16x16x32_bf16 a[84:87], v[28:31], v[168:171], a[84:87]
v_mfma_f32_16x16x32_bf16 a[68:71], v[24:27], v[168:171], a[68:71]
v_exp_f32 v114, v114
v_mfma_f32_16x16x32_bf16 a[72:75], v[24:27], v[172:175], a[72:75]
ds_read_b64_tr_b16 v[12:13], v41 offset:2048
ds_read_b64_tr_b16 v[14:15], v41 offset:8192
v_mfma_f32_16x16x32_bf16 a[88:91], v[28:31], v[172:175], a[88:91]
v_add_f32 v50, v50, v112
v_add_f32 v50, v50, v113
v_mfma_f32_16x16x32_bf16 a[92:95], v[28:31], v[176:179], a[92:95]
v_exp_f32 v115, v115
v_mfma_f32_16x16x32_bf16 a[76:79], v[24:27], v[176:179], a[76:79]
v_add_f32 v50, v50, v114
s_waitcnt lgkmcnt(4)
v_mfma_f32_16x16x32_bf16 a[108:111], v[32:35], v[176:179], a[108:111]
ds_read_b64_tr_b16 v[16:17], v42 offset:2048
ds_read_b64_tr_b16 v[18:19], v42 offset:8192
v_mfma_f32_16x16x32_bf16 a[124:127], v[36:39], v[176:179], a[124:127]
v_exp_f32 v92, v92
v_mfma_f32_16x16x32_bf16 a[120:123], v[36:39], v[172:175], a[120:123]
v_add_f32 v50, v50, v115
v_cvt_pk_bf16_f32 v162, v112, v113
v_mfma_f32_16x16x32_bf16 a[104:107], v[32:35], v[172:175], a[104:107]
v_exp_f32 v93, v93
v_mfma_f32_16x16x32_bf16 a[100:103], v[32:35], v[168:171], a[100:103]
ds_read_b64_tr_b16 v[20:21], v43 offset:2048
ds_read_b64_tr_b16 v[22:23], v43 offset:8192
v_mfma_f32_16x16x32_bf16 a[116:119], v[36:39], v[168:171], a[116:119]
v_cvt_pk_bf16_f32 v163, v114, v115
v_add_f32 v49, v49, v92
v_mfma_f32_16x16x32_bf16 a[112:115], v[36:39], v[164:167], a[112:115]
v_exp_f32 v94, v94
v_mfma_f32_16x16x32_bf16 a[96:99], v[32:35], v[164:167], a[96:99]
v_add_f32 v49, v49, v93
s_waitcnt lgkmcnt(4)
v_mfma_f32_16x16x32_bf16 a[128:131], v[8:11], v[164:167], a[128:131]
s_waitcnt vmcnt(5) lgkmcnt(0)
s_barrier
ds_read_b128 v[24:27], %[kaddr0] offset:12288
v_mfma_f32_16x16x32_bf16 a[144:147], v[12:15], v[164:167], a[144:147]
v_exp_f32 v95, v95
s_add_u32 s84, s87, 0
s_and_b32 s84, s84, 3
s_mul_i32 s84, s84, 245760
s_mul_i32 s92, %[wid], 10240
s_add_u32 s92, s92, s84
s_add_u32 m0, s81, 98304
v_mfma_f32_16x16x32_bf16 a[148:151], v[12:15], v[168:171], a[148:151]
v_add_f32 v49, v49, v94
v_add_f32 v49, v49, v95
buffer_load_dwordx4 v248, %[srdS], s92 offen offset:0 lds
v_mfma_f32_16x16x32_bf16 a[132:135], v[8:11], v[168:171], a[132:135]
v_exp_f32 v108, v108
v_mfma_f32_16x16x32_bf16 a[136:139], v[8:11], v[172:175], a[136:139]
ds_read_b128 v[28:31], v40 offset:12288
v_cvt_pk_bf16_f32 v156, v92, v93
buffer_load_dwordx4 v249, %[srdS], s92 offen offset:1024 lds
v_mfma_f32_16x16x32_bf16 a[152:155], v[12:15], v[172:175], a[152:155]
v_exp_f32 v109, v109
v_mfma_f32_16x16x32_bf16 a[156:159], v[12:15], v[176:179], a[156:159]
v_cvt_pk_bf16_f32 v157, v94, v95
v_add_f32 v49, v49, v108
buffer_load_dwordx4 v250, %[srdS], s92 offen offset:2048 lds
v_mfma_f32_16x16x32_bf16 a[140:143], v[8:11], v[176:179], a[140:143]
v_add_f32 v49, v49, v109
s_add_u32 s92, s92, 40960
s_add_u32 m0, s81, 110592
v_mfma_f32_16x16x32_bf16 a[172:175], v[16:19], v[176:179], a[172:175]
ds_read_b128 v[32:35], %[kaddr0] offset:13312
v_cvt_pk_bf16_f32 v158, v108, v109
buffer_load_dwordx4 v248, %[srdS], s92 offen offset:0 lds
v_mfma_f32_16x16x32_bf16 a[188:191], v[20:23], v[176:179], a[188:191]
v_exp_f32 v110, v110
v_mfma_f32_16x16x32_bf16 a[184:187], v[20:23], v[172:175], a[184:187]
v_exp_f32 v111, v111
buffer_load_dwordx4 v249, %[srdS], s92 offen offset:1024 lds
v_mfma_f32_16x16x32_bf16 a[168:171], v[16:19], v[172:175], a[168:171]
v_exp_f32 v88, v88
v_mfma_f32_16x16x32_bf16 a[164:167], v[16:19], v[168:171], a[164:167]
ds_read_b128 v[36:39], v40 offset:13312
v_add_f32 v49, v49, v110
buffer_load_dwordx4 v250, %[srdS], s92 offen offset:2048 lds
s_add_u32 s92, s92, 40960
v_mfma_f32_16x16x32_bf16 a[180:183], v[20:23], v[168:171], a[180:183]
v_exp_f32 v89, v89
v_mfma_f32_16x16x32_bf16 a[176:179], v[20:23], v[164:167], a[176:179]
v_add_f32 v49, v49, v111
v_cvt_pk_bf16_f32 v159, v110, v111
v_mfma_f32_16x16x32_bf16 a[160:163], v[16:19], v[164:167], a[160:163]
v_add_f32 v48, v48, v88
s_waitcnt lgkmcnt(1)
v_mfma_f32_16x16x32_bf16 v[116:119], v[24:27], a[192:195], v[180:183]
ds_read_b128 v[8:11], %[kaddr0] offset:14336
v_mfma_f32_16x16x32_bf16 v[116:119], v[28:31], a[208:211], v[116:119]
v_exp_f32 v90, v90
s_add_u32 m0, s81, 122880
v_mfma_f32_16x16x32_bf16 v[116:119], v[32:35], a[224:227], v[116:119]
buffer_load_dwordx4 v248, %[srdS], s92 offen offset:0 lds
v_mfma_f32_16x16x32_bf16 v[120:123], v[32:35], a[228:231], v[184:187]
v_exp_f32 v91, v91
v_mfma_f32_16x16x32_bf16 v[120:123], v[28:31], a[212:215], v[120:123]
ds_read_b128 v[12:15], v40 offset:14336
v_add_f32 v48, v48, v89
v_mfma_f32_16x16x32_bf16 v[120:123], v[24:27], a[196:199], v[120:123]
v_add_f32 v48, v48, v90
v_add_f32 v48, v48, v91
v_mfma_f32_16x16x32_bf16 v[124:127], v[24:27], a[200:203], v[188:191]
v_exp_f32 v104, v104
v_mfma_f32_16x16x32_bf16 v[124:127], v[28:31], a[216:219], v[124:127]
v_exp_f32 v105, v105
v_mfma_f32_16x16x32_bf16 v[124:127], v[32:35], a[232:235], v[124:127]
ds_read_b128 v[16:19], %[kaddr0] offset:18432
v_cvt_pk_bf16_f32 v152, v88, v89
v_mfma_f32_16x16x32_bf16 v[128:131], v[32:35], a[236:239], v[192:195]
v_cvt_pk_bf16_f32 v153, v90, v91
v_add_f32 v48, v48, v104
v_mfma_f32_16x16x32_bf16 v[128:131], v[28:31], a[220:223], v[128:131]
buffer_load_dwordx4 v249, %[srdS], s92 offen offset:1024 lds
v_mfma_f32_16x16x32_bf16 v[128:131], v[24:27], a[204:207], v[128:131]
v_add_f32 v48, v48, v105
s_waitcnt lgkmcnt(1)
v_mfma_f32_16x16x32_bf16 v[128:131], v[36:39], a[252:255], v[128:131]
ds_read_b128 v[20:23], v40 offset:18432
v_cvt_pk_bf16_f32 v154, v104, v105
v_mfma_f32_16x16x32_bf16 v[128:131], v[8:11], v[64:67], v[128:131]
v_exp_f32 v106, v106
v_mfma_f32_16x16x32_bf16 v[128:131], v[12:15], v[80:83], v[128:131]
v_exp_f32 v107, v107
v_mfma_f32_16x16x32_bf16 v[124:127], v[12:15], v[76:79], v[124:127]
v_exp_f32 v84, v84
v_mfma_f32_16x16x32_bf16 v[124:127], v[8:11], v[60:63], v[124:127]
ds_read_b128 v[24:27], %[kaddr0] offset:19456
v_add_f32 v48, v48, v106
v_mfma_f32_16x16x32_bf16 v[124:127], v[36:39], a[248:251], v[124:127]
v_exp_f32 v85, v85
v_mfma_f32_16x16x32_bf16 v[120:123], v[36:39], a[244:247], v[120:123]
buffer_load_dwordx4 v250, %[srdS], s92 offen offset:2048 lds
v_mfma_f32_16x16x32_bf16 v[120:123], v[8:11], v[56:59], v[120:123]
v_add_f32 v48, v48, v107
v_cvt_pk_bf16_f32 v155, v106, v107
v_mfma_f32_16x16x32_bf16 v[120:123], v[12:15], v[72:75], v[120:123]
ds_read_b128 v[28:31], v40 offset:19456
v_mfma_f32_16x16x32_bf16 v[116:119], v[12:15], v[68:71], v[116:119]
v_exp_f32 v86, v86
v_mfma_f32_16x16x32_bf16 v[116:119], v[8:11], v[52:55], v[116:119]
v_add_f32 v47, v47, v84
v_add_f32 v47, v47, v85
v_mfma_f32_16x16x32_bf16 v[116:119], v[36:39], a[240:243], v[116:119]
v_add_f32 v47, v47, v86
s_waitcnt lgkmcnt(1)
v_mfma_f32_16x16x32_bf16 v[132:135], v[16:19], a[192:195], v[180:183]
ds_read_b128 v[32:35], %[kaddr0] offset:20480
v_cvt_pk_bf16_f32 v148, v84, v85
v_mfma_f32_16x16x32_bf16 v[132:135], v[20:23], a[208:211], v[132:135]
v_exp_f32 v87, v87
s_add_u32 s92, s92, 40960
s_add_u32 m0, s81, 135168
v_mfma_f32_16x16x32_bf16 v[132:135], v[24:27], a[224:227], v[132:135]
buffer_load_dwordx4 v248, %[srdS], s92 offen offset:0 lds
v_mfma_f32_16x16x32_bf16 v[136:139], v[24:27], a[228:231], v[184:187]
v_exp_f32 v100, v100
v_mfma_f32_16x16x32_bf16 v[136:139], v[20:23], a[212:215], v[136:139]
ds_read_b128 v[36:39], v40 offset:20480
v_add_f32 v47, v47, v87
v_mfma_f32_16x16x32_bf16 v[136:139], v[16:19], a[196:199], v[136:139]
v_exp_f32 v101, v101
v_mfma_f32_16x16x32_bf16 v[140:143], v[16:19], a[200:203], v[188:191]
v_cvt_pk_bf16_f32 v149, v86, v87
v_add_f32 v47, v47, v100
v_mfma_f32_16x16x32_bf16 v[140:143], v[20:23], a[216:219], v[140:143]
v_exp_f32 v102, v102
v_mfma_f32_16x16x32_bf16 v[140:143], v[24:27], a[232:235], v[140:143]
ds_read_b64_tr_b16 v[8:9], %[vaddr0] offset:12288
ds_read_b64_tr_b16 v[10:11], %[vaddr0] offset:18432
v_mfma_f32_16x16x32_bf16 v[144:147], v[24:27], a[236:239], v[192:195]
)"
R"(v_exp_f32 v103, v103
v_mfma_f32_16x16x32_bf16 v[144:147], v[20:23], a[220:223], v[144:147]
buffer_load_dwordx4 v249, %[srdS], s92 offen offset:1024 lds
v_mfma_f32_16x16x32_bf16 v[144:147], v[16:19], a[204:207], v[144:147]
v_add_f32 v47, v47, v101
s_waitcnt lgkmcnt(2)
v_mfma_f32_16x16x32_bf16 v[144:147], v[28:31], a[252:255], v[144:147]
ds_read_b64_tr_b16 v[12:13], v41 offset:12288
ds_read_b64_tr_b16 v[14:15], v41 offset:18432
v_mfma_f32_16x16x32_bf16 v[144:147], v[32:35], v[64:67], v[144:147]
v_add_f32 v47, v47, v102
v_add_f32 v47, v47, v103
v_mfma_f32_16x16x32_bf16 v[144:147], v[36:39], v[80:83], v[144:147]
v_cvt_pk_bf16_f32 v150, v100, v101
v_cvt_pk_bf16_f32 v151, v102, v103
v_mfma_f32_16x16x32_bf16 v[140:143], v[36:39], v[76:79], v[140:143]
v_mfma_f32_16x16x32_bf16 v[140:143], v[32:35], v[60:63], v[140:143]
ds_read_b64_tr_b16 v[16:17], v42 offset:12288
ds_read_b64_tr_b16 v[18:19], v42 offset:18432
v_mfma_f32_16x16x32_bf16 v[140:143], v[28:31], a[248:251], v[140:143]
v_mfma_f32_16x16x32_bf16 v[136:139], v[28:31], a[244:247], v[136:139]
buffer_load_dwordx4 v250, %[srdS], s92 offen offset:2048 lds
v_mfma_f32_16x16x32_bf16 v[136:139], v[32:35], v[56:59], v[136:139]
v_mfma_f32_16x16x32_bf16 v[136:139], v[36:39], v[72:75], v[136:139]
ds_read_b64_tr_b16 v[20:21], v43 offset:12288
ds_read_b64_tr_b16 v[22:23], v43 offset:18432
v_mfma_f32_16x16x32_bf16 v[132:135], v[36:39], v[68:71], v[132:135]
v_mfma_f32_16x16x32_bf16 v[132:135], v[32:35], v[52:55], v[132:135]
v_mfma_f32_16x16x32_bf16 v[132:135], v[28:31], a[240:243], v[132:135]
s_waitcnt lgkmcnt(4)
v_mfma_f32_16x16x32_bf16 a[0:3], v[8:11], v[148:151], a[0:3]
ds_read_b64_tr_b16 v[24:25], %[vaddr0] offset:13312
ds_read_b64_tr_b16 v[26:27], %[vaddr0] offset:19456
v_mfma_f32_16x16x32_bf16 a[16:19], v[12:15], v[148:151], a[16:19]
v_mfma_f32_16x16x32_bf16 a[20:23], v[12:15], v[152:155], a[20:23]
v_mfma_f32_16x16x32_bf16 a[4:7], v[8:11], v[152:155], a[4:7]
v_exp_f32 v128, v128
v_mfma_f32_16x16x32_bf16 a[8:11], v[8:11], v[156:159], a[8:11]
ds_read_b64_tr_b16 v[28:29], v41 offset:13312
ds_read_b64_tr_b16 v[30:31], v41 offset:19456
v_mfma_f32_16x16x32_bf16 a[24:27], v[12:15], v[156:159], a[24:27]
v_exp_f32 v129, v129
v_mfma_f32_16x16x32_bf16 a[28:31], v[12:15], v[160:163], a[28:31]
v_exp_f32 v130, v130
v_mfma_f32_16x16x32_bf16 a[12:15], v[8:11], v[160:163], a[12:15]
v_add_f32 v50, v50, v128
s_waitcnt lgkmcnt(4)
v_mfma_f32_16x16x32_bf16 a[44:47], v[16:19], v[160:163], a[44:47]
ds_read_b64_tr_b16 v[32:33], v42 offset:13312
ds_read_b64_tr_b16 v[34:35], v42 offset:19456
v_mfma_f32_16x16x32_bf16 a[60:63], v[20:23], v[160:163], a[60:63]
v_exp_f32 v131, v131
s_add_u32 s92, s92, 40960
s_add_u32 m0, s81, 147456
v_mfma_f32_16x16x32_bf16 a[56:59], v[20:23], v[156:159], a[56:59]
buffer_load_dwordx4 v248, %[srdS], s92 offen offset:0 lds
v_mfma_f32_16x16x32_bf16 a[40:43], v[16:19], v[156:159], a[40:43]
v_add_f32 v50, v50, v129
v_add_f32 v50, v50, v130
v_mfma_f32_16x16x32_bf16 a[36:39], v[16:19], v[152:155], a[36:39]
ds_read_b64_tr_b16 v[36:37], v43 offset:13312
ds_read_b64_tr_b16 v[38:39], v43 offset:19456
v_mfma_f32_16x16x32_bf16 a[52:55], v[20:23], v[152:155], a[52:55]
v_exp_f32 v144, v144
v_mfma_f32_16x16x32_bf16 a[48:51], v[20:23], v[148:151], a[48:51]
v_add_f32 v50, v50, v131
v_cvt_pk_bf16_f32 v176, v128, v129
v_mfma_f32_16x16x32_bf16 a[32:35], v[16:19], v[148:151], a[32:35]
v_cvt_pk_bf16_f32 v177, v130, v131
s_waitcnt lgkmcnt(4)
v_mfma_f32_16x16x32_bf16 a[64:67], v[24:27], v[148:151], a[64:67]
ds_read_b64_tr_b16 v[8:9], %[vaddr0] offset:14336
ds_read_b64_tr_b16 v[10:11], %[vaddr0] offset:20480
v_mfma_f32_16x16x32_bf16 a[80:83], v[28:31], v[148:151], a[80:83]
v_exp_f32 v145, v145
v_mfma_f32_16x16x32_bf16 a[84:87], v[28:31], v[152:155], a[84:87]
buffer_load_dwordx4 v249, %[srdS], s92 offen offset:1024 lds
v_mfma_f32_16x16x32_bf16 a[68:71], v[24:27], v[152:155], a[68:71]
v_exp_f32 v146, v146
v_mfma_f32_16x16x32_bf16 a[72:75], v[24:27], v[156:159], a[72:75]
ds_read_b64_tr_b16 v[12:13], v41 offset:14336
ds_read_b64_tr_b16 v[14:15], v41 offset:20480
v_mfma_f32_16x16x32_bf16 a[88:91], v[28:31], v[156:159], a[88:91]
v_add_f32 v50, v50, v144
v_add_f32 v50, v50, v145
v_mfma_f32_16x16x32_bf16 a[92:95], v[28:31], v[160:163], a[92:95]
v_exp_f32 v147, v147
v_mfma_f32_16x16x32_bf16 a[76:79], v[24:27], v[160:163], a[76:79]
v_add_f32 v50, v50, v146
s_waitcnt lgkmcnt(4)
v_mfma_f32_16x16x32_bf16 a[108:111], v[32:35], v[160:163], a[108:111]
ds_read_b64_tr_b16 v[16:17], v42 offset:14336
ds_read_b64_tr_b16 v[18:19], v42 offset:20480
v_mfma_f32_16x16x32_bf16 a[124:127], v[36:39], v[160:163], a[124:127]
v_exp_f32 v124, v124
v_mfma_f32_16x16x32_bf16 a[120:123], v[36:39], v[156:159], a[120:123]
v_add_f32 v50, v50, v147
v_cvt_pk_bf16_f32 v178, v144, v145
buffer_load_dwordx4 v250, %[srdS], s92 offen offset:2048 lds
s_add_u32 s92, s92, 40960
v_mfma_f32_16x16x32_bf16 a[104:107], v[32:35], v[156:159], a[104:107]
v_exp_f32 v125, v125
v_mfma_f32_16x16x32_bf16 a[100:103], v[32:35], v[152:155], a[100:103]
ds_read_b64_tr_b16 v[20:21], v43 offset:14336
ds_read_b64_tr_b16 v[22:23], v43 offset:20480
v_mfma_f32_16x16x32_bf16 a[116:119], v[36:39], v[152:155], a[116:119]
v_cvt_pk_bf16_f32 v179, v146, v147
v_add_f32 v49, v49, v124
v_mfma_f32_16x16x32_bf16 a[112:115], v[36:39], v[148:151], a[112:115]
v_exp_f32 v126, v126
v_mfma_f32_16x16x32_bf16 a[96:99], v[32:35], v[148:151], a[96:99]
v_add_f32 v49, v49, v125
s_waitcnt lgkmcnt(4)
v_mfma_f32_16x16x32_bf16 a[128:131], v[8:11], v[148:151], a[128:131]
s_waitcnt vmcnt(16) lgkmcnt(0)
s_barrier
ds_read_b64_tr_b16 v[24:25], %[vaddr0] offset:24576
ds_read_b64_tr_b16 v[26:27], %[vaddr0] offset:30720
v_mfma_f32_16x16x32_bf16 a[144:147], v[12:15], v[148:151], a[144:147]
v_exp_f32 v127, v127
v_mfma_f32_16x16x32_bf16 a[148:151], v[12:15], v[152:155], a[148:151]
v_add_f32 v49, v49, v126
v_add_f32 v49, v49, v127
v_mfma_f32_16x16x32_bf16 a[132:135], v[8:11], v[152:155], a[132:135]
v_exp_f32 v140, v140
v_mfma_f32_16x16x32_bf16 a[136:139], v[8:11], v[156:159], a[136:139]
ds_read_b64_tr_b16 v[28:29], v41 offset:24576
ds_read_b64_tr_b16 v[30:31], v41 offset:30720
v_cvt_pk_bf16_f32 v172, v124, v125
v_mfma_f32_16x16x32_bf16 a[152:155], v[12:15], v[156:159], a[152:155]
v_exp_f32 v141, v141
v_mfma_f32_16x16x32_bf16 a[156:159], v[12:15], v[160:163], a[156:159]
v_cvt_pk_bf16_f32 v173, v126, v127
v_add_f32 v49, v49, v140
v_mfma_f32_16x16x32_bf16 a[140:143], v[8:11], v[160:163], a[140:143]
v_add_f32 v49, v49, v141
v_mfma_f32_16x16x32_bf16 a[172:175], v[16:19], v[160:163], a[172:175]
ds_read_b64_tr_b16 v[32:33], v42 offset:24576
ds_read_b64_tr_b16 v[34:35], v42 offset:30720
v_cvt_pk_bf16_f32 v174, v140, v141
v_mfma_f32_16x16x32_bf16 a[188:191], v[20:23], v[160:163], a[188:191]
v_exp_f32 v142, v142
v_mfma_f32_16x16x32_bf16 a[184:187], v[20:23], v[156:159], a[184:187]
v_exp_f32 v143, v143
v_mfma_f32_16x16x32_bf16 a[168:171], v[16:19], v[156:159], a[168:171]
v_exp_f32 v120, v120
v_mfma_f32_16x16x32_bf16 a[164:167], v[16:19], v[152:155], a[164:167]
ds_read_b64_tr_b16 v[36:37], v43 offset:24576
ds_read_b64_tr_b16 v[38:39], v43 offset:30720
v_add_f32 v49, v49, v142
v_mfma_f32_16x16x32_bf16 a[180:183], v[20:23], v[152:155], a[180:183]
v_exp_f32 v121, v121
v_mfma_f32_16x16x32_bf16 a[176:179], v[20:23], v[148:151], a[176:179]
v_add_f32 v49, v49, v143
v_cvt_pk_bf16_f32 v175, v142, v143
v_mfma_f32_16x16x32_bf16 a[160:163], v[16:19], v[148:151], a[160:163]
v_add_f32 v48, v48, v120
v_exp_f32 v122, v122
v_exp_f32 v123, v123
v_add_f32 v48, v48, v121
v_add_f32 v48, v48, v122
v_add_f32 v48, v48, v123
v_cvt_pk_bf16_f32 v168, v120, v121
v_cvt_pk_bf16_f32 v169, v122, v123
v_exp_f32 v136, v136
v_exp_f32 v137, v137
v_exp_f32 v138, v138
v_exp_f32 v139, v139
v_add_f32 v48, v48, v136
v_add_f32 v48, v48, v137
v_add_f32 v48, v48, v138
v_add_f32 v48, v48, v139
v_cvt_pk_bf16_f32 v170, v136, v137
v_cvt_pk_bf16_f32 v171, v138, v139
v_exp_f32 v116, v116
v_exp_f32 v117, v117
v_exp_f32 v118, v118
v_exp_f32 v119, v119
v_add_f32 v47, v47, v116
v_add_f32 v47, v47, v117
v_add_f32 v47, v47, v118
v_add_f32 v47, v47, v119
v_cvt_pk_bf16_f32 v164, v116, v117
v_cvt_pk_bf16_f32 v165, v118, v119
v_exp_f32 v132, v132
v_exp_f32 v133, v133
v_exp_f32 v134, v134
v_exp_f32 v135, v135
v_add_f32 v47, v47, v132
v_add_f32 v47, v47, v133
v_add_f32 v47, v47, v134
v_add_f32 v47, v47, v135
v_cvt_pk_bf16_f32 v166, v132, v133
v_cvt_pk_bf16_f32 v167, v134, v135
v_mov_b32 v196, v47
v_mov_b32 v197, v48
v_mov_b32 v198, v49
s_waitcnt vmcnt(15)
v_mov_b32 v199, v50
v_permlane32_swap_b32 v47, v196
)"
R"(v_permlane32_swap_b32 v48, v197
v_permlane32_swap_b32 v49, v198
v_permlane32_swap_b32 v50, v199
v_add_f32 v47, v47, v196
v_add_f32 v48, v48, v197
v_add_f32 v49, v49, v198
v_add_f32 v50, v50, v199
v_mov_b32 v196, v47
v_mov_b32 v197, v48
v_mov_b32 v198, v49
v_mov_b32 v199, v50
v_permlane16_swap_b32 v47, v196
v_permlane16_swap_b32 v48, v197
v_permlane16_swap_b32 v49, v198
v_permlane16_swap_b32 v50, v199
v_add_f32 v47, v47, v196
v_add_f32 v48, v48, v197
v_add_f32 v49, v49, v198
v_add_f32 v50, v50, v199
v_add_f32 v201, v47, v48
v_add_f32 v202, v49, v50
v_add_f32 v201, v201, v202
s_mov_b32 s83, 0x53800000
v_cmp_nlt_f32 vcc, v201, s83
s_nop 0
s_cmp_lg_u64 vcc, 0
s_cselect_b32 s83, 1, 0
v_mov_b32 v200, s83
s_lshl_b32 s84, %[wid], 2
s_add_u32 s84, s84, 161792
v_mov_b32 v201, s84
ds_write_b32 v201, v200 offset:0
v_mov_b32 v201, 161792
v_rcp_f32 v180, v47
v_rcp_f32 v184, v48
v_rcp_f32 v188, v49
v_rcp_f32 v192, v50
v_mov_b32 v181, v180
v_mov_b32 v185, v184
v_mov_b32 v189, v188
v_mov_b32 v193, v192
s_xor_b32 s83, s90, 0
s_mul_i32 s83, s83, 6144
v_add_u32 v228, s83, %[lane16]
s_xor_b32 s83, s90, 1
s_mul_i32 s83, s83, 6144
v_add_u32 v229, s83, %[lane16]
s_xor_b32 s83, s90, 2
s_mul_i32 s83, s83, 6144
v_add_u32 v230, s83, %[lane16]
s_xor_b32 s83, s90, 3
s_mul_i32 s83, s83, 6144
v_add_u32 v231, s83, %[lane16]
s_mul_i32 s84, s91, 6144
s_mul_i32 s83, s86, 3145728
s_add_u32 s84, s84, s83
s_mov_b64 s[50:51], %[ws]
s_add_u32 s50, s50, s84
s_addc_u32 s51, s51, 0
s_add_u32 s50, s50, 16779264
s_addc_u32 s51, s51, 0
s_mul_i32 s83, s87, 786432
s_add_u32 s56, s50, s83
s_addc_u32 s57, s51, 0
s_lshl_b32 s78, s86, 2
s_lshl_b32 s78, 15, s78
s_waitcnt lgkmcnt(0)
s_barrier
ds_read_b128 v[196:199], v201 offset:0
; ---- last PV + epilogue
v_mfma_f32_16x16x32_bf16 a[0:3], v[24:27], v[164:167], a[0:3]
ds_read_b64_tr_b16 v[8:9], %[vaddr0] offset:25600
ds_read_b64_tr_b16 v[10:11], %[vaddr0] offset:31744
v_mfma_f32_16x16x32_bf16 a[16:19], v[28:31], v[164:167], a[16:19]
v_mfma_f32_16x16x32_bf16 a[20:23], v[28:31], v[168:171], a[20:23]
v_mfma_f32_16x16x32_bf16 a[4:7], v[24:27], v[168:171], a[4:7]
v_mfma_f32_16x16x32_bf16 a[8:11], v[24:27], v[172:175], a[8:11]
ds_read_b64_tr_b16 v[12:13], v41 offset:25600
ds_read_b64_tr_b16 v[14:15], v41 offset:31744
v_mfma_f32_16x16x32_bf16 a[24:27], v[28:31], v[172:175], a[24:27]
v_mfma_f32_16x16x32_bf16 a[28:31], v[28:31], v[176:179], a[28:31]
v_mfma_f32_16x16x32_bf16 a[12:15], v[24:27], v[176:179], a[12:15]
s_waitcnt lgkmcnt(4)
v_or3_b32 v196, v196, v197, v198
v_or_b32 v196, v196, v199
s_nop 0
v_readfirstlane_b32 s83, v196
s_nop 0
s_andn2_b32 s83, s83, s85
s_cbranch_scc1 .Lfallback
v_mfma_f32_16x16x32_bf16 a[44:47], v[32:35], v[176:179], a[44:47]
ds_read_b64_tr_b16 v[16:17], v42 offset:25600
ds_read_b64_tr_b16 v[18:19], v42 offset:31744
v_mfma_f32_16x16x32_bf16 a[60:63], v[36:39], v[176:179], a[60:63]
v_mfma_f32_16x16x32_bf16 a[56:59], v[36:39], v[172:175], a[56:59]
v_accvgpr_read_b32 v148, a0
v_accvgpr_read_b32 v149, a1
v_accvgpr_read_b32 v150, a2
v_accvgpr_read_b32 v151, a3
v_accvgpr_read_b32 v152, a16
v_accvgpr_read_b32 v153, a17
v_accvgpr_read_b32 v154, a18
v_accvgpr_read_b32 v155, a19
v_pk_mul_f32 v[148:149], v[148:149], v[180:181]
v_pk_mul_f32 v[150:151], v[150:151], v[180:181]
v_pk_mul_f32 v[152:153], v[152:153], v[180:181]
v_pk_mul_f32 v[154:155], v[154:155], v[180:181]
v_mfma_f32_16x16x32_bf16 a[40:43], v[32:35], v[172:175], a[40:43]
v_cvt_pk_bf16_f32 v52, v148, v149
v_cvt_pk_bf16_f32 v53, v150, v151
v_cvt_pk_bf16_f32 v54, v152, v153
v_cvt_pk_bf16_f32 v55, v154, v155
global_store_dwordx4 v228, v[52:55], s[56:57] offset:-2048 sc1
v_accvgpr_read_b32 v156, a4
v_accvgpr_read_b32 v157, a5
v_accvgpr_read_b32 v158, a6
v_accvgpr_read_b32 v159, a7
v_accvgpr_read_b32 v160, a20
v_accvgpr_read_b32 v161, a21
v_accvgpr_read_b32 v162, a22
v_mfma_f32_16x16x32_bf16 a[36:39], v[32:35], v[168:171], a[36:39]
ds_read_b64_tr_b16 v[20:21], v43 offset:25600
ds_read_b64_tr_b16 v[22:23], v43 offset:31744
v_accvgpr_read_b32 v163, a23
v_pk_mul_f32 v[156:157], v[156:157], v[184:185]
v_pk_mul_f32 v[158:159], v[158:159], v[184:185]
v_pk_mul_f32 v[160:161], v[160:161], v[184:185]
v_pk_mul_f32 v[162:163], v[162:163], v[184:185]
v_cvt_pk_bf16_f32 v56, v156, v157
v_cvt_pk_bf16_f32 v57, v158, v159
v_cvt_pk_bf16_f32 v58, v160, v161
v_cvt_pk_bf16_f32 v59, v162, v163
global_store_dwordx4 v229, v[56:59], s[56:57] offset:-2048 sc1
v_accvgpr_read_b32 v148, a8
v_mfma_f32_16x16x32_bf16 a[52:55], v[36:39], v[168:171], a[52:55]
v_accvgpr_read_b32 v149, a9
v_accvgpr_read_b32 v150, a10
v_accvgpr_read_b32 v151, a11
v_accvgpr_read_b32 v152, a24
v_accvgpr_read_b32 v153, a25
v_accvgpr_read_b32 v154, a26
v_accvgpr_read_b32 v155, a27
v_pk_mul_f32 v[148:149], v[148:149], v[188:189]
v_pk_mul_f32 v[150:151], v[150:151], v[188:189]
v_pk_mul_f32 v[152:153], v[152:153], v[188:189]
v_pk_mul_f32 v[154:155], v[154:155], v[188:189]
v_mfma_f32_16x16x32_bf16 a[48:51], v[36:39], v[164:167], a[48:51]
v_cvt_pk_bf16_f32 v60, v148, v149
v_cvt_pk_bf16_f32 v61, v150, v151
v_cvt_pk_bf16_f32 v62, v152, v153
v_cvt_pk_bf16_f32 v63, v154, v155
global_store_dwordx4 v230, v[60:63], s[56:57] offset:-2048 sc1
v_accvgpr_read_b32 v156, a12
v_accvgpr_read_b32 v157, a13
v_accvgpr_read_b32 v158, a14
v_accvgpr_read_b32 v159, a15
v_accvgpr_read_b32 v160, a28
v_accvgpr_read_b32 v161, a29
v_mfma_f32_16x16x32_bf16 a[32:35], v[32:35], v[164:167], a[32:35]
v_accvgpr_read_b32 v162, a30
v_accvgpr_read_b32 v163, a31
v_pk_mul_f32 v[156:157], v[156:157], v[192:193]
v_pk_mul_f32 v[158:159], v[158:159], v[192:193]
v_pk_mul_f32 v[160:161], v[160:161], v[192:193]
v_pk_mul_f32 v[162:163], v[162:163], v[192:193]
v_cvt_pk_bf16_f32 v64, v156, v157
v_cvt_pk_bf16_f32 v65, v158, v159
v_cvt_pk_bf16_f32 v66, v160, v161
v_cvt_pk_bf16_f32 v67, v162, v163
global_store_dwordx4 v231, v[64:67], s[56:57] offset:-2048 sc1
s_waitcnt lgkmcnt(4)
v_mfma_f32_16x16x32_bf16 a[64:67], v[8:11], v[164:167], a[64:67]
ds_read_b64_tr_b16 v[24:25], %[vaddr0] offset:26624
ds_read_b64_tr_b16 v[26:27], %[vaddr0] offset:32768
v_mfma_f32_16x16x32_bf16 a[80:83], v[12:15], v[164:167], a[80:83]
v_mfma_f32_16x16x32_bf16 a[84:87], v[12:15], v[168:171], a[84:87]
v_accvgpr_read_b32 v148, a32
v_accvgpr_read_b32 v149, a33
v_accvgpr_read_b32 v150, a34
v_accvgpr_read_b32 v151, a35
v_accvgpr_read_b32 v152, a48
v_accvgpr_read_b32 v153, a49
v_accvgpr_read_b32 v154, a50
v_accvgpr_read_b32 v155, a51
v_pk_mul_f32 v[148:149], v[148:149], v[180:181]
v_pk_mul_f32 v[150:151], v[150:151], v[180:181]
v_pk_mul_f32 v[152:153], v[152:153], v[180:181]
v_pk_mul_f32 v[154:155], v[154:155], v[180:181]
v_mfma_f32_16x16x32_bf16 a[68:71], v[8:11], v[168:171], a[68:71]
v_cvt_pk_bf16_f32 v68, v148, v149
v_cvt_pk_bf16_f32 v69, v150, v151
v_cvt_pk_bf16_f32 v70, v152, v153
v_cvt_pk_bf16_f32 v71, v154, v155
global_store_dwordx4 v228, v[68:71], s[56:57] offset:-1024 sc1
v_accvgpr_read_b32 v156, a36
v_accvgpr_read_b32 v157, a37
v_accvgpr_read_b32 v158, a38
v_accvgpr_read_b32 v159, a39
v_accvgpr_read_b32 v160, a52
v_accvgpr_read_b32 v161, a53
)"
R"(v_accvgpr_read_b32 v162, a54
v_mfma_f32_16x16x32_bf16 a[72:75], v[8:11], v[172:175], a[72:75]
ds_read_b64_tr_b16 v[28:29], v41 offset:26624
ds_read_b64_tr_b16 v[30:31], v41 offset:32768
v_accvgpr_read_b32 v163, a55
v_pk_mul_f32 v[156:157], v[156:157], v[184:185]
v_pk_mul_f32 v[158:159], v[158:159], v[184:185]
v_pk_mul_f32 v[160:161], v[160:161], v[184:185]
v_pk_mul_f32 v[162:163], v[162:163], v[184:185]
v_cvt_pk_bf16_f32 v72, v156, v157
v_cvt_pk_bf16_f32 v73, v158, v159
v_cvt_pk_bf16_f32 v74, v160, v161
v_cvt_pk_bf16_f32 v75, v162, v163
global_store_dwordx4 v229, v[72:75], s[56:57] offset:-1024 sc1
v_accvgpr_read_b32 v148, a40
v_mfma_f32_16x16x32_bf16 a[88:91], v[12:15], v[172:175], a[88:91]
v_accvgpr_read_b32 v149, a41
v_accvgpr_read_b32 v150, a42
v_accvgpr_read_b32 v151, a43
v_accvgpr_read_b32 v152, a56
v_accvgpr_read_b32 v153, a57
v_accvgpr_read_b32 v154, a58
v_accvgpr_read_b32 v155, a59
v_pk_mul_f32 v[148:149], v[148:149], v[188:189]
v_pk_mul_f32 v[150:151], v[150:151], v[188:189]
v_pk_mul_f32 v[152:153], v[152:153], v[188:189]
v_pk_mul_f32 v[154:155], v[154:155], v[188:189]
v_mfma_f32_16x16x32_bf16 a[92:95], v[12:15], v[176:179], a[92:95]
v_cvt_pk_bf16_f32 v76, v148, v149
v_cvt_pk_bf16_f32 v77, v150, v151
v_cvt_pk_bf16_f32 v78, v152, v153
v_cvt_pk_bf16_f32 v79, v154, v155
global_store_dwordx4 v230, v[76:79], s[56:57] offset:-1024 sc1
v_accvgpr_read_b32 v156, a44
v_accvgpr_read_b32 v157, a45
v_accvgpr_read_b32 v158, a46
v_accvgpr_read_b32 v159, a47
v_accvgpr_read_b32 v160, a60
v_accvgpr_read_b32 v161, a61
v_mfma_f32_16x16x32_bf16 a[76:79], v[8:11], v[176:179], a[76:79]
v_accvgpr_read_b32 v162, a62
v_accvgpr_read_b32 v163, a63
v_pk_mul_f32 v[156:157], v[156:157], v[192:193]
v_pk_mul_f32 v[158:159], v[158:159], v[192:193]
v_pk_mul_f32 v[160:161], v[160:161], v[192:193]
v_pk_mul_f32 v[162:163], v[162:163], v[192:193]
v_cvt_pk_bf16_f32 v80, v156, v157
v_cvt_pk_bf16_f32 v81, v158, v159
v_cvt_pk_bf16_f32 v82, v160, v161
v_cvt_pk_bf16_f32 v83, v162, v163
global_store_dwordx4 v231, v[80:83], s[56:57] offset:-1024 sc1
s_waitcnt lgkmcnt(4)
v_mfma_f32_16x16x32_bf16 a[108:111], v[16:19], v[176:179], a[108:111]
ds_read_b64_tr_b16 v[32:33], v42 offset:26624
ds_read_b64_tr_b16 v[34:35], v42 offset:32768
v_mfma_f32_16x16x32_bf16 a[124:127], v[20:23], v[176:179], a[124:127]
v_mfma_f32_16x16x32_bf16 a[120:123], v[20:23], v[172:175], a[120:123]
v_accvgpr_read_b32 v148, a64
v_accvgpr_read_b32 v149, a65
v_accvgpr_read_b32 v150, a66
v_accvgpr_read_b32 v151, a67
v_accvgpr_read_b32 v152, a80
v_accvgpr_read_b32 v153, a81
v_accvgpr_read_b32 v154, a82
v_accvgpr_read_b32 v155, a83
v_pk_mul_f32 v[148:149], v[148:149], v[180:181]
v_pk_mul_f32 v[150:151], v[150:151], v[180:181]
v_pk_mul_f32 v[152:153], v[152:153], v[180:181]
v_pk_mul_f32 v[154:155], v[154:155], v[180:181]
v_mfma_f32_16x16x32_bf16 a[104:107], v[16:19], v[172:175], a[104:107]
v_cvt_pk_bf16_f32 v84, v148, v149
v_cvt_pk_bf16_f32 v85, v150, v151
v_cvt_pk_bf16_f32 v86, v152, v153
v_cvt_pk_bf16_f32 v87, v154, v155
global_store_dwordx4 v228, v[84:87], s[56:57] offset:0 sc1
v_accvgpr_read_b32 v156, a68
v_accvgpr_read_b32 v157, a69
v_accvgpr_read_b32 v158, a70
v_accvgpr_read_b32 v159, a71
v_accvgpr_read_b32 v160, a84
v_accvgpr_read_b32 v161, a85
v_accvgpr_read_b32 v162, a86
v_mfma_f32_16x16x32_bf16 a[100:103], v[16:19], v[168:171], a[100:103]
ds_read_b64_tr_b16 v[36:37], v43 offset:26624
ds_read_b64_tr_b16 v[38:39], v43 offset:32768
v_accvgpr_read_b32 v163, a87
v_pk_mul_f32 v[156:157], v[156:157], v[184:185]
v_pk_mul_f32 v[158:159], v[158:159], v[184:185]
v_pk_mul_f32 v[160:161], v[160:161], v[184:185]
v_pk_mul_f32 v[162:163], v[162:163], v[184:185]
v_cvt_pk_bf16_f32 v88, v156, v157
v_cvt_pk_bf16_f32 v89, v158, v159
v_cvt_pk_bf16_f32 v90, v160, v161
v_cvt_pk_bf16_f32 v91, v162, v163
global_store_dwordx4 v229, v[88:91], s[56:57] offset:0 sc1
v_accvgpr_read_b32 v148, a72
v_mfma_f32_16x16x32_bf16 a[116:119], v[20:23], v[168:171], a[116:119]
v_accvgpr_read_b32 v149, a73
v_accvgpr_read_b32 v150, a74
v_accvgpr_read_b32 v151, a75
v_accvgpr_read_b32 v152, a88
v_accvgpr_read_b32 v153, a89
v_accvgpr_read_b32 v154, a90
v_accvgpr_read_b32 v155, a91
v_pk_mul_f32 v[148:149], v[148:149], v[188:189]
v_pk_mul_f32 v[150:151], v[150:151], v[188:189]
v_pk_mul_f32 v[152:153], v[152:153], v[188:189]
v_pk_mul_f32 v[154:155], v[154:155], v[188:189]
v_mfma_f32_16x16x32_bf16 a[112:115], v[20:23], v[164:167], a[112:115]
v_cvt_pk_bf16_f32 v92, v148, v149
v_cvt_pk_bf16_f32 v93, v150, v151
v_cvt_pk_bf16_f32 v94, v152, v153
v_cvt_pk_bf16_f32 v95, v154, v155
global_store_dwordx4 v230, v[92:95], s[56:57] offset:0 sc1
v_accvgpr_read_b32 v156, a76
v_accvgpr_read_b32 v157, a77
v_accvgpr_read_b32 v158, a78
v_accvgpr_read_b32 v159, a79
v_accvgpr_read_b32 v160, a92
v_accvgpr_read_b32 v161, a93
v_mfma_f32_16x16x32_bf16 a[96:99], v[16:19], v[164:167], a[96:99]
v_accvgpr_read_b32 v162, a94
v_accvgpr_read_b32 v163, a95
v_pk_mul_f32 v[156:157], v[156:157], v[192:193]
v_pk_mul_f32 v[158:159], v[158:159], v[192:193]
v_pk_mul_f32 v[160:161], v[160:161], v[192:193]
v_pk_mul_f32 v[162:163], v[162:163], v[192:193]
v_cvt_pk_bf16_f32 v96, v156, v157
v_cvt_pk_bf16_f32 v97, v158, v159
v_cvt_pk_bf16_f32 v98, v160, v161
v_cvt_pk_bf16_f32 v99, v162, v163
global_store_dwordx4 v231, v[96:99], s[56:57] offset:0 sc1
s_waitcnt lgkmcnt(4)
v_mfma_f32_16x16x32_bf16 a[128:131], v[24:27], v[164:167], a[128:131]
v_mfma_f32_16x16x32_bf16 a[144:147], v[28:31], v[164:167], a[144:147]
v_mfma_f32_16x16x32_bf16 a[148:151], v[28:31], v[168:171], a[148:151]
v_accvgpr_read_b32 v148, a96
v_accvgpr_read_b32 v149, a97
v_accvgpr_read_b32 v150, a98
v_accvgpr_read_b32 v151, a99
v_accvgpr_read_b32 v152, a112
v_accvgpr_read_b32 v153, a113
v_accvgpr_read_b32 v154, a114
v_accvgpr_read_b32 v155, a115
v_pk_mul_f32 v[148:149], v[148:149], v[180:181]
v_pk_mul_f32 v[150:151], v[150:151], v[180:181]
v_pk_mul_f32 v[152:153], v[152:153], v[180:181]
v_mfma_f32_16x16x32_bf16 a[132:135], v[24:27], v[168:171], a[132:135]
v_pk_mul_f32 v[154:155], v[154:155], v[180:181]
v_cvt_pk_bf16_f32 v100, v148, v149
v_cvt_pk_bf16_f32 v101, v150, v151
v_cvt_pk_bf16_f32 v102, v152, v153
v_cvt_pk_bf16_f32 v103, v154, v155
v_accvgpr_read_b32 v156, a100
v_accvgpr_read_b32 v157, a101
v_accvgpr_read_b32 v158, a102
v_accvgpr_read_b32 v159, a103
v_accvgpr_read_b32 v160, a116
v_accvgpr_read_b32 v161, a117
v_mfma_f32_16x16x32_bf16 a[136:139], v[24:27], v[172:175], a[136:139]
v_accvgpr_read_b32 v162, a118
v_accvgpr_read_b32 v163, a119
v_pk_mul_f32 v[156:157], v[156:157], v[184:185]
v_pk_mul_f32 v[158:159], v[158:159], v[184:185]
v_pk_mul_f32 v[160:161], v[160:161], v[184:185]
v_pk_mul_f32 v[162:163], v[162:163], v[184:185]
v_cvt_pk_bf16_f32 v104, v156, v157
v_cvt_pk_bf16_f32 v105, v158, v159
v_cvt_pk_bf16_f32 v106, v160, v161
v_cvt_pk_bf16_f32 v107, v162, v163
v_accvgpr_read_b32 v148, a104
v_mfma_f32_16x16x32_bf16 a[152:155], v[28:31], v[172:175], a[152:155]
v_accvgpr_read_b32 v149, a105
v_accvgpr_read_b32 v150, a106
v_accvgpr_read_b32 v151, a107
v_accvgpr_read_b32 v152, a120
v_accvgpr_read_b32 v153, a121
v_accvgpr_read_b32 v154, a122
v_accvgpr_read_b32 v155, a123
v_pk_mul_f32 v[148:149], v[148:149], v[188:189]
v_pk_mul_f32 v[150:151], v[150:151], v[188:189]
v_pk_mul_f32 v[152:153], v[152:153], v[188:189]
v_pk_mul_f32 v[154:155], v[154:155], v[188:189]
v_mfma_f32_16x16x32_bf16 a[156:159], v[28:31], v[176:179], a[156:159]
v_cvt_pk_bf16_f32 v108, v148, v149
v_cvt_pk_bf16_f32 v109, v150, v151
v_cvt_pk_bf16_f32 v110, v152, v153
v_cvt_pk_bf16_f32 v111, v154, v155
v_accvgpr_read_b32 v156, a108
v_accvgpr_read_b32 v157, a109
v_accvgpr_read_b32 v158, a110
v_accvgpr_read_b32 v159, a111
v_accvgpr_read_b32 v160, a124
v_accvgpr_read_b32 v161, a125
v_mfma_f32_16x16x32_bf16 a[140:143], v[24:27], v[176:179], a[140:143]
v_accvgpr_read_b32 v162, a126
v_accvgpr_read_b32 v163, a127
v_pk_mul_f32 v[156:157], v[156:157], v[192:193]
v_pk_mul_f32 v[158:159], v[158:159], v[192:193]
v_pk_mul_f32 v[160:161], v[160:161], v[192:193]
)"
R"(v_pk_mul_f32 v[162:163], v[162:163], v[192:193]
v_cvt_pk_bf16_f32 v112, v156, v157
v_cvt_pk_bf16_f32 v113, v158, v159
v_cvt_pk_bf16_f32 v114, v160, v161
v_cvt_pk_bf16_f32 v115, v162, v163
s_waitcnt lgkmcnt(0)
v_mfma_f32_16x16x32_bf16 a[172:175], v[32:35], v[176:179], a[172:175]
v_mfma_f32_16x16x32_bf16 a[188:191], v[36:39], v[176:179], a[188:191]
v_mfma_f32_16x16x32_bf16 a[184:187], v[36:39], v[172:175], a[184:187]
v_accvgpr_read_b32 v148, a128
v_accvgpr_read_b32 v149, a129
v_accvgpr_read_b32 v150, a130
v_accvgpr_read_b32 v151, a131
v_accvgpr_read_b32 v152, a144
v_accvgpr_read_b32 v153, a145
v_accvgpr_read_b32 v154, a146
v_accvgpr_read_b32 v155, a147
v_pk_mul_f32 v[148:149], v[148:149], v[180:181]
v_pk_mul_f32 v[150:151], v[150:151], v[180:181]
v_pk_mul_f32 v[152:153], v[152:153], v[180:181]
v_mfma_f32_16x16x32_bf16 a[168:171], v[32:35], v[172:175], a[168:171]
v_pk_mul_f32 v[154:155], v[154:155], v[180:181]
v_cvt_pk_bf16_f32 v116, v148, v149
v_cvt_pk_bf16_f32 v117, v150, v151
v_cvt_pk_bf16_f32 v118, v152, v153
v_cvt_pk_bf16_f32 v119, v154, v155
v_accvgpr_read_b32 v156, a132
v_accvgpr_read_b32 v157, a133
v_accvgpr_read_b32 v158, a134
v_accvgpr_read_b32 v159, a135
v_accvgpr_read_b32 v160, a148
v_accvgpr_read_b32 v161, a149
v_mfma_f32_16x16x32_bf16 a[164:167], v[32:35], v[168:171], a[164:167]
v_accvgpr_read_b32 v162, a150
v_accvgpr_read_b32 v163, a151
v_pk_mul_f32 v[156:157], v[156:157], v[184:185]
v_pk_mul_f32 v[158:159], v[158:159], v[184:185]
v_pk_mul_f32 v[160:161], v[160:161], v[184:185]
v_pk_mul_f32 v[162:163], v[162:163], v[184:185]
v_cvt_pk_bf16_f32 v120, v156, v157
v_cvt_pk_bf16_f32 v121, v158, v159
v_cvt_pk_bf16_f32 v122, v160, v161
v_cvt_pk_bf16_f32 v123, v162, v163
v_accvgpr_read_b32 v148, a136
v_mfma_f32_16x16x32_bf16 a[180:183], v[36:39], v[168:171], a[180:183]
v_accvgpr_read_b32 v149, a137
v_accvgpr_read_b32 v150, a138
v_accvgpr_read_b32 v151, a139
v_accvgpr_read_b32 v152, a152
v_accvgpr_read_b32 v153, a153
v_accvgpr_read_b32 v154, a154
v_accvgpr_read_b32 v155, a155
v_pk_mul_f32 v[148:149], v[148:149], v[188:189]
v_pk_mul_f32 v[150:151], v[150:151], v[188:189]
v_pk_mul_f32 v[152:153], v[152:153], v[188:189]
v_pk_mul_f32 v[154:155], v[154:155], v[188:189]
v_mfma_f32_16x16x32_bf16 a[176:179], v[36:39], v[164:167], a[176:179]
v_cvt_pk_bf16_f32 v124, v148, v149
v_cvt_pk_bf16_f32 v125, v150, v151
v_cvt_pk_bf16_f32 v126, v152, v153
v_cvt_pk_bf16_f32 v127, v154, v155
v_accvgpr_read_b32 v156, a140
v_accvgpr_read_b32 v157, a141
v_accvgpr_read_b32 v158, a142
v_accvgpr_read_b32 v159, a143
v_accvgpr_read_b32 v160, a156
v_accvgpr_read_b32 v161, a157
v_mfma_f32_16x16x32_bf16 a[160:163], v[32:35], v[164:167], a[160:163]
v_accvgpr_read_b32 v162, a158
v_accvgpr_read_b32 v163, a159
v_pk_mul_f32 v[156:157], v[156:157], v[192:193]
v_pk_mul_f32 v[158:159], v[158:159], v[192:193]
v_pk_mul_f32 v[160:161], v[160:161], v[192:193]
v_pk_mul_f32 v[162:163], v[162:163], v[192:193]
v_cvt_pk_bf16_f32 v128, v156, v157
v_cvt_pk_bf16_f32 v129, v158, v159
v_cvt_pk_bf16_f32 v130, v160, v161
v_cvt_pk_bf16_f32 v131, v162, v163
v_accvgpr_read_b32 v148, a160
v_accvgpr_read_b32 v149, a161
v_accvgpr_read_b32 v150, a162
v_accvgpr_read_b32 v151, a163
v_accvgpr_read_b32 v152, a176
v_accvgpr_read_b32 v153, a177
v_accvgpr_read_b32 v154, a178
v_accvgpr_read_b32 v155, a179
v_pk_mul_f32 v[148:149], v[148:149], v[180:181]
v_pk_mul_f32 v[150:151], v[150:151], v[180:181]
v_pk_mul_f32 v[152:153], v[152:153], v[180:181]
v_pk_mul_f32 v[154:155], v[154:155], v[180:181]
v_cvt_pk_bf16_f32 v132, v148, v149
v_cvt_pk_bf16_f32 v133, v150, v151
v_cvt_pk_bf16_f32 v134, v152, v153
v_cvt_pk_bf16_f32 v135, v154, v155
v_accvgpr_read_b32 v156, a164
v_accvgpr_read_b32 v157, a165
v_accvgpr_read_b32 v158, a166
v_accvgpr_read_b32 v159, a167
v_accvgpr_read_b32 v160, a180
v_accvgpr_read_b32 v161, a181
v_accvgpr_read_b32 v162, a182
v_accvgpr_read_b32 v163, a183
v_pk_mul_f32 v[156:157], v[156:157], v[184:185]
v_pk_mul_f32 v[158:159], v[158:159], v[184:185]
v_pk_mul_f32 v[160:161], v[160:161], v[184:185]
v_pk_mul_f32 v[162:163], v[162:163], v[184:185]
v_cvt_pk_bf16_f32 v136, v156, v157
v_cvt_pk_bf16_f32 v137, v158, v159
v_cvt_pk_bf16_f32 v138, v160, v161
v_cvt_pk_bf16_f32 v139, v162, v163
v_accvgpr_read_b32 v148, a168
v_accvgpr_read_b32 v149, a169
v_accvgpr_read_b32 v150, a170
v_accvgpr_read_b32 v151, a171
v_accvgpr_read_b32 v152, a184
v_accvgpr_read_b32 v153, a185
v_accvgpr_read_b32 v154, a186
v_accvgpr_read_b32 v155, a187
v_pk_mul_f32 v[148:149], v[148:149], v[188:189]
v_pk_mul_f32 v[150:151], v[150:151], v[188:189]
v_pk_mul_f32 v[152:153], v[152:153], v[188:189]
v_pk_mul_f32 v[154:155], v[154:155], v[188:189]
v_cvt_pk_bf16_f32 v140, v148, v149
v_cvt_pk_bf16_f32 v141, v150, v151
v_cvt_pk_bf16_f32 v142, v152, v153
v_cvt_pk_bf16_f32 v143, v154, v155
v_accvgpr_read_b32 v156, a172
v_accvgpr_read_b32 v157, a173
v_accvgpr_read_b32 v158, a174
v_accvgpr_read_b32 v159, a175
v_accvgpr_read_b32 v160, a188
v_accvgpr_read_b32 v161, a189
v_accvgpr_read_b32 v162, a190
v_accvgpr_read_b32 v163, a191
v_pk_mul_f32 v[156:157], v[156:157], v[192:193]
v_pk_mul_f32 v[158:159], v[158:159], v[192:193]
v_pk_mul_f32 v[160:161], v[160:161], v[192:193]
v_pk_mul_f32 v[162:163], v[162:163], v[192:193]
v_cvt_pk_bf16_f32 v144, v156, v157
v_cvt_pk_bf16_f32 v145, v158, v159
v_cvt_pk_bf16_f32 v146, v160, v161
v_cvt_pk_bf16_f32 v147, v162, v163
; ---- phase 3
v_subrev_u32 v196, 49152, %[vaddr0]
s_lshr_b32 s84, s86, 2
s_xor_b32 s84, s84, 0
s_mul_i32 s84, s84, 5
s_lshl_b32 s83, s89, 1
s_add_u32 s84, s84, s83
s_add_u32 s83, s84, 0
s_and_b32 s95, s83, 3
s_lshl_b32 s95, s95, 5
s_lshr_b32 s83, s83, 2
s_lshl_b32 s83, s83, 10
v_xor_b32 v168, s95, v196
v_add_u32 v168, s83, v168
v_add_u32 v172, 53248, v168
v_add_u32 v176, 106496, v168
s_add_u32 s83, s84, 1
s_and_b32 s95, s83, 3
s_lshl_b32 s95, s95, 5
s_lshr_b32 s83, s83, 2
s_lshl_b32 s83, s83, 10
v_xor_b32 v169, s95, v196
v_add_u32 v169, s83, v169
v_add_u32 v173, 53248, v169
v_add_u32 v177, 106496, v169
s_add_u32 s83, s84, 2
s_and_b32 s95, s83, 3
s_lshl_b32 s95, s95, 5
s_lshr_b32 s83, s83, 2
s_lshl_b32 s83, s83, 10
v_xor_b32 v170, s95, v196
v_add_u32 v170, s83, v170
v_add_u32 v174, 53248, v170
v_add_u32 v178, 106496, v170
s_add_u32 s83, s84, 3
s_and_b32 s95, s83, 3
s_lshl_b32 s95, s95, 5
s_lshr_b32 s83, s83, 2
s_lshl_b32 s83, s83, 10
v_xor_b32 v171, s95, v196
v_add_u32 v171, s83, v171
v_add_u32 v175, 53248, v171
v_add_u32 v179, 106496, v171
s_lshr_b32 s84, s86, 2
s_xor_b32 s84, s84, 1
s_mul_i32 s84, s84, 5
s_lshl_b32 s83, s89, 1
s_add_u32 s84, s84, s83
s_add_u32 s83, s84, 0
s_and_b32 s95, s83, 3
s_lshl_b32 s95, s95, 5
s_lshr_b32 s83, s83, 2
s_lshl_b32 s83, s83, 10
v_xor_b32 v180, s95, v196
v_add_u32 v180, s83, v180
v_add_u32 v184, 53248, v180
v_add_u32 v188, 106496, v180
s_add_u32 s83, s84, 1
)"
R"(s_and_b32 s95, s83, 3
s_lshl_b32 s95, s95, 5
s_lshr_b32 s83, s83, 2
s_lshl_b32 s83, s83, 10
v_xor_b32 v181, s95, v196
v_add_u32 v181, s83, v181
v_add_u32 v185, 53248, v181
v_add_u32 v189, 106496, v181
s_add_u32 s83, s84, 2
s_and_b32 s95, s83, 3
s_lshl_b32 s95, s95, 5
s_lshr_b32 s83, s83, 2
s_lshl_b32 s83, s83, 10
v_xor_b32 v182, s95, v196
v_add_u32 v182, s83, v182
v_add_u32 v186, 53248, v182
v_add_u32 v190, 106496, v182
s_add_u32 s83, s84, 3
s_and_b32 s95, s83, 3
s_lshl_b32 s95, s95, 5
s_lshr_b32 s83, s83, 2
s_lshl_b32 s83, s83, 10
v_xor_b32 v183, s95, v196
v_add_u32 v183, s83, v183
v_add_u32 v187, 53248, v183
v_add_u32 v191, 106496, v183
s_waitcnt vmcnt(0)
s_barrier
s_cmp_lg_u32 %[wid], 0
s_cbranch_scc1 .Losig0
s_mov_b64 exec, 1
global_store_dword v251, v252, s[64:65] offset:256 sc1
s_mov_b64 exec, -1
.Losig0:
ds_read_b64_tr_b16 v[8:9], v172 offset:45056
ds_read_b64_tr_b16 v[10:11], v172 offset:51200
ds_read_b64_tr_b16 v[12:13], v176 offset:4096
ds_read_b64_tr_b16 v[14:15], v176 offset:10240
ds_read_b64_tr_b16 v[16:17], v176 offset:16384
ds_read_b64_tr_b16 v[18:19], v176 offset:22528
ds_read_b64_tr_b16 v[20:21], v173 offset:45056
ds_read_b64_tr_b16 v[22:23], v173 offset:51200
s_add_u32 m0, s81, 0
s_nop 0
buffer_load_dwordx4 v248, %[srdS], s92 offen offset:0 lds
buffer_load_dwordx4 v249, %[srdS], s92 offen offset:1024 lds
buffer_load_dwordx4 v250, %[srdS], s92 offen offset:2048 lds
s_add_u32 s92, s92, 40960
s_waitcnt lgkmcnt(2)
v_mfma_f32_16x16x32_bf16 a[0:3], v[8:11], v[52:55], 0
ds_read_b64_tr_b16 v[24:25], v177 offset:4096
ds_read_b64_tr_b16 v[26:27], v177 offset:10240
v_lshrrev_b32 v192, 1, %[lane16]
v_mfma_f32_16x16x32_bf16 a[0:3], v[12:15], v[68:71], a[0:3]
v_mfma_f32_16x16x32_bf16 a[0:3], v[16:19], v[84:87], a[0:3]
global_store_dwordx4 v228, v[100:103], s[56:57] offset:1024 sc1
v_mfma_f32_16x16x32_bf16 a[4:7], v[16:19], v[88:91], 0
v_lshrrev_b32 v193, 8, %[lane16]
v_mfma_f32_16x16x32_bf16 a[4:7], v[12:15], v[72:75], a[4:7]
ds_read_b64_tr_b16 v[28:29], v177 offset:16384
ds_read_b64_tr_b16 v[30:31], v177 offset:22528
v_mfma_f32_16x16x32_bf16 a[4:7], v[8:11], v[56:59], a[4:7]
v_mfma_f32_16x16x32_bf16 a[8:11], v[8:11], v[60:63], 0
v_mfma_f32_16x16x32_bf16 a[8:11], v[12:15], v[76:79], a[8:11]
global_store_dwordx4 v229, v[104:107], s[56:57] offset:1024 sc1
v_lshlrev_b32 v193, 3, v193
v_mfma_f32_16x16x32_bf16 a[8:11], v[16:19], v[92:95], a[8:11]
ds_read_b64_tr_b16 v[32:33], v174 offset:45056
ds_read_b64_tr_b16 v[34:35], v174 offset:51200
v_mfma_f32_16x16x32_bf16 a[12:15], v[16:19], v[96:99], 0
v_mfma_f32_16x16x32_bf16 a[12:15], v[12:15], v[80:83], a[12:15]
s_lshl_b32 s84, s88, 2
s_add_u32 s84, s84, s87
s_mul_i32 s84, s84, 786432
s_mul_i32 s83, %[wid], 3072
s_add_u32 s84, s84, s83
s_mov_b64 s[58:59], %[ws]
s_add_u32 s58, s58, s84
s_addc_u32 s59, s59, 0
s_add_u32 s58, s58, 50331648
s_addc_u32 s59, s59, 0
s_mul_i32 s83, s86, 98304
s_add_u32 s60, s58, s83
s_addc_u32 s61, s59, 0
v_mfma_f32_16x16x32_bf16 a[12:15], v[8:11], v[64:67], a[12:15]
s_waitcnt lgkmcnt(2)
v_mfma_f32_16x16x32_bf16 a[28:31], v[20:23], v[64:67], 0
ds_read_b64_tr_b16 v[36:37], v178 offset:4096
ds_read_b64_tr_b16 v[38:39], v178 offset:10240
v_mfma_f32_16x16x32_bf16 a[28:31], v[24:27], v[80:83], a[28:31]
global_store_dwordx4 v230, v[108:111], s[56:57] offset:1024 sc1
v_mfma_f32_16x16x32_bf16 a[28:31], v[28:31], v[96:99], a[28:31]
v_lshrrev_b32 v194, 4, %[lane16]
v_mfma_f32_16x16x32_bf16 a[24:27], v[28:31], v[92:95], 0
v_mfma_f32_16x16x32_bf16 a[24:27], v[24:27], v[76:79], a[24:27]
ds_read_b64_tr_b16 v[8:9], v178 offset:16384
ds_read_b64_tr_b16 v[10:11], v178 offset:22528
v_mfma_f32_16x16x32_bf16 a[24:27], v[20:23], v[60:63], a[24:27]
v_mfma_f32_16x16x32_bf16 a[20:23], v[20:23], v[56:59], 0
global_store_dwordx4 v231, v[112:115], s[56:57] offset:1024 sc1
v_xor_b32 v194, s86, v194
v_mfma_f32_16x16x32_bf16 a[20:23], v[24:27], v[72:75], a[20:23]
v_mfma_f32_16x16x32_bf16 a[20:23], v[28:31], v[88:91], a[20:23]
ds_read_b64_tr_b16 v[12:13], v175 offset:45056
ds_read_b64_tr_b16 v[14:15], v175 offset:51200
v_mfma_f32_16x16x32_bf16 a[16:19], v[28:31], v[84:87], 0
v_and_b32 v194, 7, v194
v_mfma_f32_16x16x32_bf16 a[16:19], v[24:27], v[68:71], a[16:19]
v_mfma_f32_16x16x32_bf16 a[16:19], v[20:23], v[52:55], a[16:19]
s_waitcnt lgkmcnt(2)
v_mfma_f32_16x16x32_bf16 a[32:35], v[32:35], v[52:55], 0
ds_read_b64_tr_b16 v[16:17], v179 offset:4096
ds_read_b64_tr_b16 v[18:19], v179 offset:10240
global_store_dwordx4 v228, v[116:119], s[56:57] offset:2048 sc1
v_mfma_f32_16x16x32_bf16 a[32:35], v[36:39], v[68:71], a[32:35]
v_lshlrev_b32 v194, 2, v194
v_mfma_f32_16x16x32_bf16 a[32:35], v[8:11], v[84:87], a[32:35]
v_mfma_f32_16x16x32_bf16 a[36:39], v[8:11], v[88:91], 0
v_mfma_f32_16x16x32_bf16 a[36:39], v[36:39], v[72:75], a[36:39]
ds_read_b64_tr_b16 v[20:21], v179 offset:16384
ds_read_b64_tr_b16 v[22:23], v179 offset:22528
v_mfma_f32_16x16x32_bf16 a[36:39], v[32:35], v[56:59], a[36:39]
v_add_u32 v194, s87, v194
v_mfma_f32_16x16x32_bf16 a[40:43], v[32:35], v[60:63], 0
global_store_dwordx4 v229, v[120:123], s[56:57] offset:2048 sc1
v_mfma_f32_16x16x32_bf16 a[40:43], v[36:39], v[76:79], a[40:43]
v_mfma_f32_16x16x32_bf16 a[40:43], v[8:11], v[92:95], a[40:43]
ds_read_b64_tr_b16 v[24:25], v172 offset:46080
ds_read_b64_tr_b16 v[26:27], v172 offset:52224
v_lshlrev_b32 v194, 2, v194
v_mfma_f32_16x16x32_bf16 a[44:47], v[8:11], v[96:99], 0
v_mfma_f32_16x16x32_bf16 a[44:47], v[36:39], v[80:83], a[44:47]
v_mfma_f32_16x16x32_bf16 a[44:47], v[32:35], v[64:67], a[44:47]
global_store_dwordx4 v230, v[124:127], s[56:57] offset:2048 sc1
s_waitcnt lgkmcnt(2)
v_mfma_f32_16x16x32_bf16 a[60:63], v[12:15], v[64:67], 0
ds_read_b64_tr_b16 v[28:29], v176 offset:5120
ds_read_b64_tr_b16 v[30:31], v176 offset:11264
v_bfe_u32 v196, %[lane16], 6, 2
v_lshrrev_b32 v197, 8, %[lane16]
v_bfe_u32 v198, %[lane16], 4, 1
v_lshlrev_b32 v198, 4, v198
v_mov_b32 v199, 0x3f80
v_lshlrev_b32 v199, v198, v199
v_cmp_eq_u32 vcc, v196, v197
v_bfe_u32 v198, %[lane16], 5, 1
v_mov_b32 v234, 0
v_cndmask_b32 v199, 0, v199, vcc
v_cmp_eq_u32 vcc, 0, v198
v_mov_b32 v235, 0
s_nop 0
v_cndmask_b32 v232, 0, v199, vcc
v_sub_u32 v233, v199, v232
v_mov_b32 v236, v232
v_mov_b32 v237, v233
v_mfma_f32_16x16x32_bf16 a[60:63], v[16:19], v[80:83], a[60:63]
v_mfma_f32_16x16x32_bf16 a[60:63], v[20:23], v[96:99], a[60:63]
v_mfma_f32_16x16x32_bf16 a[56:59], v[20:23], v[92:95], 0
v_mfma_f32_16x16x32_bf16 a[56:59], v[16:19], v[76:79], a[56:59]
ds_read_b64_tr_b16 v[32:33], v176 offset:17408
ds_read_b64_tr_b16 v[34:35], v176 offset:23552
v_mfma_f32_16x16x32_bf16 a[56:59], v[12:15], v[60:63], a[56:59]
global_store_dwordx4 v231, v[128:131], s[56:57] offset:2048 sc1
v_mfma_f32_16x16x32_bf16 a[52:55], v[12:15], v[56:59], 0
v_mfma_f32_16x16x32_bf16 a[52:55], v[16:19], v[72:75], a[52:55]
v_mfma_f32_16x16x32_bf16 a[52:55], v[20:23], v[88:91], a[52:55]
ds_read_b64_tr_b16 v[36:37], v184 offset:45056
ds_read_b64_tr_b16 v[38:39], v184 offset:51200
v_mfma_f32_16x16x32_bf16 a[48:51], v[20:23], v[84:87], 0
v_mfma_f32_16x16x32_bf16 a[48:51], v[16:19], v[68:71], a[48:51]
global_store_dwordx4 v228, v[132:135], s[56:57] offset:3072 sc1
v_mfma_f32_16x16x32_bf16 a[48:51], v[12:15], v[52:55], a[48:51]
s_waitcnt lgkmcnt(2)
v_mfma_f32_16x16x32_bf16 a[64:67], v[24:27], v[52:55], 0
ds_read_b64_tr_b16 v[8:9], v188 offset:4096
ds_read_b64_tr_b16 v[10:11], v188 offset:10240
v_mfma_f32_16x16x32_bf16 a[64:67], v[28:31], v[68:71], a[64:67]
v_mfma_f32_16x16x32_bf16 a[64:67], v[32:35], v[84:87], a[64:67]
v_mfma_f32_16x16x32_bf16 a[68:71], v[32:35], v[88:91], 0
v_mfma_f32_16x16x32_bf16 a[68:71], v[28:31], v[72:75], a[68:71]
ds_read_b64_tr_b16 v[12:13], v188 offset:16384
ds_read_b64_tr_b16 v[14:15], v188 offset:22528
global_store_dwordx4 v229, v[136:139], s[56:57] offset:3072 sc1
v_mfma_f32_16x16x32_bf16 a[68:71], v[24:27], v[56:59], a[68:71]
v_mfma_f32_16x16x32_bf16 a[72:75], v[24:27], v[60:63], 0
v_mfma_f32_16x16x32_bf16 a[72:75], v[28:31], v[76:79], a[72:75]
v_mfma_f32_16x16x32_bf16 a[72:75], v[32:35], v[92:95], a[72:75]
ds_read_b64_tr_b16 v[16:17], v185 offset:45056
ds_read_b64_tr_b16 v[18:19], v185 offset:51200
v_mfma_f32_16x16x32_bf16 a[76:79], v[32:35], v[96:99], 0
v_mfma_f32_16x16x32_bf16 a[76:79], v[28:31], v[80:83], a[76:79]
global_store_dwordx4 v230, v[140:143], s[56:57] offset:3072 sc1
v_mfma_f32_16x16x32_bf16 a[76:79], v[24:27], v[64:67], a[76:79]
s_waitcnt lgkmcnt(2)
v_mfma_f32_16x16x32_bf16 a[92:95], v[36:39], v[64:67], 0
ds_read_b64_tr_b16 v[20:21], v189 offset:4096
ds_read_b64_tr_b16 v[22:23], v189 offset:10240
v_mfma_f32_16x16x32_bf16 a[92:95], v[8:11], v[80:83], a[92:95]
v_mfma_f32_16x16x32_bf16 a[92:95], v[12:15], v[96:99], a[92:95]
v_mfma_f32_16x16x32_bf16 a[88:91], v[12:15], v[92:95], 0
)"
R"(global_store_dwordx4 v231, v[144:147], s[56:57] offset:3072 sc1
v_mfma_f32_16x16x32_bf16 a[88:91], v[8:11], v[76:79], a[88:91]
ds_read_b64_tr_b16 v[24:25], v189 offset:16384
ds_read_b64_tr_b16 v[26:27], v189 offset:22528
v_mfma_f32_16x16x32_bf16 a[88:91], v[36:39], v[60:63], a[88:91]
v_mfma_f32_16x16x32_bf16 a[84:87], v[36:39], v[56:59], 0
v_mfma_f32_16x16x32_bf16 a[84:87], v[8:11], v[72:75], a[84:87]
v_mfma_f32_16x16x32_bf16 a[84:87], v[12:15], v[88:91], a[84:87]
ds_read_b64_tr_b16 v[28:29], v186 offset:45056
ds_read_b64_tr_b16 v[30:31], v186 offset:51200
s_add_u32 s84, s87, 1
s_and_b32 s84, s84, 3
s_mul_i32 s84, s84, 245760
s_mul_i32 s92, %[wid], 10240
s_add_u32 s92, s92, s84
s_add_u32 m0, s81, 12288
v_mfma_f32_16x16x32_bf16 a[80:83], v[12:15], v[84:87], 0
buffer_load_dwordx4 v248, %[srdS], s92 offen offset:0 lds
v_mfma_f32_16x16x32_bf16 a[80:83], v[8:11], v[68:71], a[80:83]
v_mfma_f32_16x16x32_bf16 a[80:83], v[36:39], v[52:55], a[80:83]
s_waitcnt lgkmcnt(2)
v_mfma_f32_16x16x32_bf16 a[96:99], v[16:19], v[52:55], 0
ds_read_b64_tr_b16 v[32:33], v190 offset:4096
ds_read_b64_tr_b16 v[34:35], v190 offset:10240
v_mfma_f32_16x16x32_bf16 a[96:99], v[20:23], v[68:71], a[96:99]
v_mfma_f32_16x16x32_bf16 a[96:99], v[24:27], v[84:87], a[96:99]
v_mfma_f32_16x16x32_bf16 a[100:103], v[24:27], v[88:91], 0
buffer_load_dwordx4 v249, %[srdS], s92 offen offset:1024 lds
v_mfma_f32_16x16x32_bf16 a[100:103], v[20:23], v[72:75], a[100:103]
ds_read_b64_tr_b16 v[36:37], v190 offset:16384
ds_read_b64_tr_b16 v[38:39], v190 offset:22528
v_mfma_f32_16x16x32_bf16 a[100:103], v[16:19], v[56:59], a[100:103]
v_mfma_f32_16x16x32_bf16 a[104:107], v[16:19], v[60:63], 0
v_mfma_f32_16x16x32_bf16 a[104:107], v[20:23], v[76:79], a[104:107]
v_mfma_f32_16x16x32_bf16 a[104:107], v[24:27], v[92:95], a[104:107]
ds_read_b64_tr_b16 v[8:9], v187 offset:45056
ds_read_b64_tr_b16 v[10:11], v187 offset:51200
buffer_load_dwordx4 v250, %[srdS], s92 offen offset:2048 lds
v_mfma_f32_16x16x32_bf16 a[108:111], v[24:27], v[96:99], 0
v_mfma_f32_16x16x32_bf16 a[108:111], v[20:23], v[80:83], a[108:111]
v_mfma_f32_16x16x32_bf16 a[108:111], v[16:19], v[64:67], a[108:111]
s_waitcnt lgkmcnt(2)
v_mfma_f32_16x16x32_bf16 a[124:127], v[28:31], v[64:67], 0
ds_read_b64_tr_b16 v[12:13], v191 offset:4096
ds_read_b64_tr_b16 v[14:15], v191 offset:10240
v_mfma_f32_16x16x32_bf16 a[124:127], v[32:35], v[80:83], a[124:127]
s_add_u32 s92, s92, 40960
s_add_u32 m0, s81, 24576
v_mfma_f32_16x16x32_bf16 a[124:127], v[36:39], v[96:99], a[124:127]
buffer_load_dwordx4 v248, %[srdS], s92 offen offset:0 lds
v_mfma_f32_16x16x32_bf16 a[120:123], v[36:39], v[92:95], 0
v_mfma_f32_16x16x32_bf16 a[120:123], v[32:35], v[76:79], a[120:123]
ds_read_b64_tr_b16 v[16:17], v191 offset:16384
ds_read_b64_tr_b16 v[18:19], v191 offset:22528
v_mfma_f32_16x16x32_bf16 a[120:123], v[28:31], v[60:63], a[120:123]
v_mfma_f32_16x16x32_bf16 a[116:119], v[28:31], v[56:59], 0
v_mfma_f32_16x16x32_bf16 a[116:119], v[32:35], v[72:75], a[116:119]
buffer_load_dwordx4 v249, %[srdS], s92 offen offset:1024 lds
v_mfma_f32_16x16x32_bf16 a[116:119], v[36:39], v[88:91], a[116:119]
ds_read_b64_tr_b16 v[20:21], v184 offset:46080
ds_read_b64_tr_b16 v[22:23], v184 offset:52224
v_mfma_f32_16x16x32_bf16 a[112:115], v[36:39], v[84:87], 0
v_mfma_f32_16x16x32_bf16 a[112:115], v[32:35], v[68:71], a[112:115]
v_mfma_f32_16x16x32_bf16 a[112:115], v[28:31], v[52:55], a[112:115]
s_waitcnt lgkmcnt(2)
v_mfma_f32_16x16x32_bf16 a[128:131], v[8:11], v[52:55], 0
ds_read_b64_tr_b16 v[24:25], v188 offset:5120
ds_read_b64_tr_b16 v[26:27], v188 offset:11264
v_mfma_f32_16x16x32_bf16 a[128:131], v[12:15], v[68:71], a[128:131]
buffer_load_dwordx4 v250, %[srdS], s92 offen offset:2048 lds
v_mfma_f32_16x16x32_bf16 a[128:131], v[16:19], v[84:87], a[128:131]
v_mfma_f32_16x16x32_bf16 a[132:135], v[16:19], v[88:91], 0
v_mfma_f32_16x16x32_bf16 a[132:135], v[12:15], v[72:75], a[132:135]
ds_read_b64_tr_b16 v[28:29], v188 offset:17408
ds_read_b64_tr_b16 v[30:31], v188 offset:23552
global_load_dword v253, v251, s[62:63] offset:256 sc1
v_mfma_f32_16x16x32_bf16 a[132:135], v[8:11], v[56:59], a[132:135]
v_mfma_f32_16x16x32_bf16 a[136:139], v[8:11], v[60:63], 0
s_add_u32 s92, s92, 40960
s_add_u32 m0, s81, 36864
v_mfma_f32_16x16x32_bf16 a[136:139], v[12:15], v[76:79], a[136:139]
buffer_load_dwordx4 v248, %[srdS], s92 offen offset:0 lds
v_mfma_f32_16x16x32_bf16 a[136:139], v[16:19], v[92:95], a[136:139]
s_waitcnt vmcnt(20)
s_barrier
ds_read_b64_tr_b16 v[32:33], v176 offset:28672
ds_read_b64_tr_b16 v[34:35], v176 offset:34816
v_mfma_f32_16x16x32_bf16 a[140:143], v[16:19], v[96:99], 0
v_mfma_f32_16x16x32_bf16 a[140:143], v[12:15], v[80:83], a[140:143]
v_mfma_f32_16x16x32_bf16 a[140:143], v[8:11], v[64:67], a[140:143]
s_waitcnt lgkmcnt(2)
v_mfma_f32_16x16x32_bf16 a[156:159], v[20:23], v[64:67], 0
ds_read_b64_tr_b16 v[36:37], v176 offset:40960
ds_read_b64_tr_b16 v[38:39], v176 offset:47104
buffer_load_dwordx4 v249, %[srdS], s92 offen offset:1024 lds
v_mfma_f32_16x16x32_bf16 a[156:159], v[24:27], v[80:83], a[156:159]
v_mfma_f32_16x16x32_bf16 a[156:159], v[28:31], v[96:99], a[156:159]
v_mfma_f32_16x16x32_bf16 a[152:155], v[28:31], v[92:95], 0
v_mfma_f32_16x16x32_bf16 a[152:155], v[24:27], v[76:79], a[152:155]
ds_read_b64_tr_b16 v[8:9], v168 offset:0
ds_read_b64_tr_b16 v[10:11], v168 offset:6144
v_mfma_f32_16x16x32_bf16 a[152:155], v[20:23], v[60:63], a[152:155]
v_mfma_f32_16x16x32_bf16 a[148:151], v[20:23], v[56:59], 0
buffer_load_dwordx4 v250, %[srdS], s92 offen offset:2048 lds
v_mfma_f32_16x16x32_bf16 a[148:151], v[24:27], v[72:75], a[148:151]
v_mfma_f32_16x16x32_bf16 a[148:151], v[28:31], v[88:91], a[148:151]
ds_read_b64_tr_b16 v[12:13], v177 offset:28672
ds_read_b64_tr_b16 v[14:15], v177 offset:34816
v_mfma_f32_16x16x32_bf16 a[144:147], v[28:31], v[84:87], 0
v_mfma_f32_16x16x32_bf16 a[144:147], v[24:27], v[68:71], a[144:147]
s_add_u32 s92, s92, 40960
v_mfma_f32_16x16x32_bf16 a[144:147], v[20:23], v[52:55], a[144:147]
s_waitcnt lgkmcnt(2)
v_mfma_f32_16x16x32_bf16 a[0:3], v[32:35], v[100:103], a[0:3]
ds_read_b64_tr_b16 v[16:17], v177 offset:40960
ds_read_b64_tr_b16 v[18:19], v177 offset:47104
global_load_dwordx2 v[158:159], v193, %[b2base] offset:0
v_mfma_f32_16x16x32_bf16 a[0:3], v[36:39], v[116:119], a[0:3]
v_mfma_f32_16x16x32_bf16 a[0:3], v[8:11], v[132:135], a[0:3]
v_mfma_f32_16x16x32_bf16 a[4:7], v[8:11], v[136:139], a[4:7]
global_load_dwordx2 v[160:161], v193, %[b2base] offset:32
v_mfma_f32_16x16x32_bf16 a[4:7], v[36:39], v[120:123], a[4:7]
ds_read_b64_tr_b16 v[20:21], v169 offset:0
ds_read_b64_tr_b16 v[22:23], v169 offset:6144
v_mfma_f32_16x16x32_bf16 a[4:7], v[32:35], v[104:107], a[4:7]
v_mfma_f32_16x16x32_bf16 a[8:11], v[32:35], v[108:111], a[8:11]
v_mfma_f32_16x16x32_bf16 a[8:11], v[36:39], v[124:127], a[8:11]
global_load_dwordx2 v[162:163], v193, %[b2base] offset:64
v_mfma_f32_16x16x32_bf16 a[8:11], v[8:11], v[140:143], a[8:11]
ds_read_b64_tr_b16 v[24:25], v178 offset:28672
ds_read_b64_tr_b16 v[26:27], v178 offset:34816
v_mfma_f32_16x16x32_bf16 a[12:15], v[8:11], v[144:147], a[12:15]
v_mfma_f32_16x16x32_bf16 a[12:15], v[36:39], v[128:131], a[12:15]
v_mfma_f32_16x16x32_bf16 a[12:15], v[32:35], v[112:115], a[12:15]
global_load_dwordx2 v[164:165], v193, %[b2base] offset:96
s_waitcnt lgkmcnt(2)
v_mfma_f32_16x16x32_bf16 a[28:31], v[12:15], v[112:115], a[28:31]
ds_read_b64_tr_b16 v[28:29], v178 offset:40960
ds_read_b64_tr_b16 v[30:31], v178 offset:47104
v_mfma_f32_16x16x32_bf16 a[28:31], v[16:19], v[128:131], a[28:31]
v_mfma_f32_16x16x32_bf16 a[28:31], v[20:23], v[144:147], a[28:31]
v_mfma_f32_16x16x32_bf16 a[24:27], v[20:23], v[140:143], a[24:27]
global_load_dwordx2 v[166:167], v193, %[b2base] offset:128
v_mfma_f32_16x16x32_bf16 a[24:27], v[16:19], v[124:127], a[24:27]
ds_read_b64_tr_b16 v[32:33], v170 offset:0
ds_read_b64_tr_b16 v[34:35], v170 offset:6144
v_mfma_f32_16x16x32_bf16 a[24:27], v[12:15], v[108:111], a[24:27]
v_mfma_f32_16x16x32_bf16 a[20:23], v[12:15], v[104:107], a[20:23]
global_load_dwordx4 v[148:151], %[xoffw], %[xres] offset:0
v_mfma_f32_16x16x32_bf16 a[20:23], v[16:19], v[120:123], a[20:23]
v_mfma_f32_16x16x32_bf16 a[20:23], v[20:23], v[136:139], a[20:23]
ds_read_b64_tr_b16 v[36:37], v179 offset:28672
ds_read_b64_tr_b16 v[38:39], v179 offset:34816
v_mfma_f32_16x16x32_bf16 a[16:19], v[20:23], v[132:135], a[16:19]
v_mfma_f32_16x16x32_bf16 a[16:19], v[16:19], v[116:119], a[16:19]
global_load_dwordx4 v[152:155], %[xoffw], %[xres] offset:64
v_mfma_f32_16x16x32_bf16 a[16:19], v[12:15], v[100:103], a[16:19]
s_waitcnt lgkmcnt(2)
v_mfma_f32_16x16x32_bf16 a[32:35], v[24:27], v[100:103], a[32:35]
ds_read_b64_tr_b16 v[8:9], v179 offset:40960
ds_read_b64_tr_b16 v[10:11], v179 offset:47104
v_mfma_f32_16x16x32_bf16 a[32:35], v[28:31], v[116:119], a[32:35]
v_mfma_f32_16x16x32_bf16 a[32:35], v[32:35], v[132:135], a[32:35]
global_load_dwordx2 v[156:157], %[xoffn], %[xres] offset:128
v_mfma_f32_16x16x32_bf16 a[36:39], v[32:35], v[136:139], a[36:39]
v_mfma_f32_16x16x32_bf16 a[36:39], v[28:31], v[120:123], a[36:39]
ds_read_b64_tr_b16 v[12:13], v171 offset:0
ds_read_b64_tr_b16 v[14:15], v171 offset:6144
v_mfma_f32_16x16x32_bf16 a[36:39], v[24:27], v[104:107], a[36:39]
s_waitcnt vmcnt(11)
v_cmp_ne_u32 vcc, s96, v253
s_nop 0
s_and_b32 s83, vcc_lo, s78
s_cbranch_scc0 .Lopoll_go
s_mov_b32 s95, 0x20000
.Lopoll:
global_load_dword v253, v251, s[62:63] offset:256 sc1
s_waitcnt vmcnt(0)
v_cmp_ne_u32 vcc, s96, v253
s_sub_u32 s95, s95, 1
s_cbranch_scc1 .Lopoll_go
s_and_b32 s83, vcc_lo, s78
s_cbranch_scc0 .Lopoll_go
s_sleep 2
s_branch .Lopoll
.Lopoll_go:
v_mfma_f32_16x16x32_bf16 a[40:43], v[24:27], v[108:111], a[40:43]
s_add_u32 s84, s87, 1
s_and_b32 s84, s84, 3
s_mul_i32 s84, s84, 786432
s_add_u32 s54, s50, s84
s_addc_u32 s55, s51, 0
v_mfma_f32_16x16x32_bf16 a[40:43], v[28:31], v[124:127], a[40:43]
global_load_dwordx4 a[160:163], v228, s[54:55] offset:-2048 sc1
v_mfma_f32_16x16x32_bf16 a[40:43], v[32:35], v[140:143], a[40:43]
ds_read_b64_tr_b16 v[16:17], v176 offset:29696
ds_read_b64_tr_b16 v[18:19], v176 offset:35840
v_mfma_f32_16x16x32_bf16 a[44:47], v[32:35], v[144:147], a[44:47]
global_load_dwordx4 a[164:167], v229, s[54:55] offset:-2048 sc1
v_mfma_f32_16x16x32_bf16 a[44:47], v[28:31], v[128:131], a[44:47]
)"
R"(v_mfma_f32_16x16x32_bf16 a[44:47], v[24:27], v[112:115], a[44:47]
s_waitcnt lgkmcnt(2)
v_mfma_f32_16x16x32_bf16 a[60:63], v[36:39], v[112:115], a[60:63]
ds_read_b64_tr_b16 v[20:21], v176 offset:41984
ds_read_b64_tr_b16 v[22:23], v176 offset:48128
global_load_dwordx4 a[168:171], v230, s[54:55] offset:-2048 sc1
v_mfma_f32_16x16x32_bf16 a[60:63], v[8:11], v[128:131], a[60:63]
v_mfma_f32_16x16x32_bf16 a[60:63], v[12:15], v[144:147], a[60:63]
v_mfma_f32_16x16x32_bf16 a[56:59], v[12:15], v[140:143], a[56:59]
global_load_dwordx4 a[172:175], v231, s[54:55] offset:-2048 sc1
v_mfma_f32_16x16x32_bf16 a[56:59], v[8:11], v[124:127], a[56:59]
ds_read_b64_tr_b16 v[24:25], v168 offset:1024
ds_read_b64_tr_b16 v[26:27], v168 offset:7168
v_mfma_f32_16x16x32_bf16 a[56:59], v[36:39], v[108:111], a[56:59]
global_load_dwordx4 a[176:179], v228, s[54:55] offset:-1024 sc1
v_mfma_f32_16x16x32_bf16 a[52:55], v[36:39], v[104:107], a[52:55]
v_mfma_f32_16x16x32_bf16 a[52:55], v[8:11], v[120:123], a[52:55]
v_mfma_f32_16x16x32_bf16 a[52:55], v[12:15], v[136:139], a[52:55]
ds_read_b64_tr_b16 v[28:29], v188 offset:28672
ds_read_b64_tr_b16 v[30:31], v188 offset:34816
global_load_dwordx4 a[180:183], v229, s[54:55] offset:-1024 sc1
v_mfma_f32_16x16x32_bf16 a[48:51], v[12:15], v[132:135], a[48:51]
v_mfma_f32_16x16x32_bf16 a[48:51], v[8:11], v[116:119], a[48:51]
v_mfma_f32_16x16x32_bf16 a[48:51], v[36:39], v[100:103], a[48:51]
global_load_dwordx4 a[184:187], v230, s[54:55] offset:-1024 sc1
s_waitcnt lgkmcnt(2)
v_mfma_f32_16x16x32_bf16 a[64:67], v[16:19], v[100:103], a[64:67]
ds_read_b64_tr_b16 v[32:33], v188 offset:40960
ds_read_b64_tr_b16 v[34:35], v188 offset:47104
v_mfma_f32_16x16x32_bf16 a[64:67], v[20:23], v[116:119], a[64:67]
global_load_dwordx4 a[188:191], v231, s[54:55] offset:-1024 sc1
v_mfma_f32_16x16x32_bf16 a[64:67], v[24:27], v[132:135], a[64:67]
v_mfma_f32_16x16x32_bf16 a[68:71], v[24:27], v[136:139], a[68:71]
v_mfma_f32_16x16x32_bf16 a[68:71], v[20:23], v[120:123], a[68:71]
ds_read_b64_tr_b16 v[36:37], v180 offset:0
ds_read_b64_tr_b16 v[38:39], v180 offset:6144
global_load_dwordx4 a[192:195], v228, s[54:55] offset:0 sc1
v_mfma_f32_16x16x32_bf16 a[68:71], v[16:19], v[104:107], a[68:71]
v_mfma_f32_16x16x32_bf16 a[72:75], v[16:19], v[108:111], a[72:75]
v_mfma_f32_16x16x32_bf16 a[72:75], v[20:23], v[124:127], a[72:75]
global_load_dwordx4 a[196:199], v229, s[54:55] offset:0 sc1
v_mfma_f32_16x16x32_bf16 a[72:75], v[24:27], v[140:143], a[72:75]
ds_read_b64_tr_b16 v[8:9], v189 offset:28672
ds_read_b64_tr_b16 v[10:11], v189 offset:34816
v_mfma_f32_16x16x32_bf16 a[76:79], v[24:27], v[144:147], a[76:79]
global_load_dwordx4 a[200:203], v230, s[54:55] offset:0 sc1
v_mfma_f32_16x16x32_bf16 a[76:79], v[20:23], v[128:131], a[76:79]
v_mfma_f32_16x16x32_bf16 a[76:79], v[16:19], v[112:115], a[76:79]
s_waitcnt lgkmcnt(2)
v_mfma_f32_16x16x32_bf16 a[92:95], v[28:31], v[112:115], a[92:95]
ds_read_b64_tr_b16 v[12:13], v189 offset:40960
ds_read_b64_tr_b16 v[14:15], v189 offset:47104
global_load_dwordx4 a[204:207], v231, s[54:55] offset:0 sc1
v_mfma_f32_16x16x32_bf16 a[92:95], v[32:35], v[128:131], a[92:95]
v_mfma_f32_16x16x32_bf16 a[92:95], v[36:39], v[144:147], a[92:95]
s_add_u32 s84, s87, 2
s_and_b32 s84, s84, 3
s_mul_i32 s84, s84, 786432
s_add_u32 s54, s50, s84
s_addc_u32 s55, s51, 0
v_mfma_f32_16x16x32_bf16 a[88:91], v[36:39], v[140:143], a[88:91]
global_load_dwordx4 a[208:211], v228, s[54:55] offset:-2048 sc1
v_mfma_f32_16x16x32_bf16 a[88:91], v[32:35], v[124:127], a[88:91]
ds_read_b64_tr_b16 v[16:17], v181 offset:0
ds_read_b64_tr_b16 v[18:19], v181 offset:6144
v_mfma_f32_16x16x32_bf16 a[88:91], v[28:31], v[108:111], a[88:91]
v_mfma_f32_16x16x32_bf16 a[84:87], v[28:31], v[104:107], a[84:87]
global_load_dwordx4 a[212:215], v229, s[54:55] offset:-2048 sc1
v_mfma_f32_16x16x32_bf16 a[84:87], v[32:35], v[120:123], a[84:87]
v_mfma_f32_16x16x32_bf16 a[84:87], v[36:39], v[136:139], a[84:87]
ds_read_b64_tr_b16 v[20:21], v190 offset:28672
ds_read_b64_tr_b16 v[22:23], v190 offset:34816
global_load_dwordx4 a[216:219], v230, s[54:55] offset:-2048 sc1
v_mfma_f32_16x16x32_bf16 a[80:83], v[36:39], v[132:135], a[80:83]
v_mfma_f32_16x16x32_bf16 a[80:83], v[32:35], v[116:119], a[80:83]
v_mfma_f32_16x16x32_bf16 a[80:83], v[28:31], v[100:103], a[80:83]
global_load_dwordx4 a[220:223], v231, s[54:55] offset:-2048 sc1
s_waitcnt lgkmcnt(2)
v_mfma_f32_16x16x32_bf16 a[96:99], v[8:11], v[100:103], a[96:99]
ds_read_b64_tr_b16 v[24:25], v190 offset:40960
ds_read_b64_tr_b16 v[26:27], v190 offset:47104
v_mfma_f32_16x16x32_bf16 a[96:99], v[12:15], v[116:119], a[96:99]
v_mfma_f32_16x16x32_bf16 a[96:99], v[16:19], v[132:135], a[96:99]
global_load_dwordx4 a[224:227], v228, s[54:55] offset:-1024 sc1
v_mfma_f32_16x16x32_bf16 a[100:103], v[16:19], v[136:139], a[100:103]
v_mfma_f32_16x16x32_bf16 a[100:103], v[12:15], v[120:123], a[100:103]
ds_read_b64_tr_b16 v[28:29], v182 offset:0
ds_read_b64_tr_b16 v[30:31], v182 offset:6144
global_load_dwordx4 a[228:231], v229, s[54:55] offset:-1024 sc1
v_mfma_f32_16x16x32_bf16 a[100:103], v[8:11], v[104:107], a[100:103]
v_mfma_f32_16x16x32_bf16 a[104:107], v[8:11], v[108:111], a[104:107]
v_mfma_f32_16x16x32_bf16 a[104:107], v[12:15], v[124:127], a[104:107]
global_load_dwordx4 a[232:235], v230, s[54:55] offset:-1024 sc1
v_mfma_f32_16x16x32_bf16 a[104:107], v[16:19], v[140:143], a[104:107]
ds_read_b64_tr_b16 v[32:33], v191 offset:28672
ds_read_b64_tr_b16 v[34:35], v191 offset:34816
v_mfma_f32_16x16x32_bf16 a[108:111], v[16:19], v[144:147], a[108:111]
v_mfma_f32_16x16x32_bf16 a[108:111], v[12:15], v[128:131], a[108:111]
global_load_dwordx4 a[236:239], v231, s[54:55] offset:-1024 sc1
v_mfma_f32_16x16x32_bf16 a[108:111], v[8:11], v[112:115], a[108:111]
s_waitcnt lgkmcnt(2)
v_mfma_f32_16x16x32_bf16 a[124:127], v[20:23], v[112:115], a[124:127]
ds_read_b64_tr_b16 v[36:37], v191 offset:40960
ds_read_b64_tr_b16 v[38:39], v191 offset:47104
global_load_dwordx4 a[240:243], v228, s[54:55] offset:0 sc1
v_mfma_f32_16x16x32_bf16 a[124:127], v[24:27], v[128:131], a[124:127]
v_mfma_f32_16x16x32_bf16 a[124:127], v[28:31], v[144:147], a[124:127]
v_mfma_f32_16x16x32_bf16 a[120:123], v[28:31], v[140:143], a[120:123]
global_load_dwordx4 a[244:247], v229, s[54:55] offset:0 sc1
v_mfma_f32_16x16x32_bf16 a[120:123], v[24:27], v[124:127], a[120:123]
ds_read_b64_tr_b16 v[8:9], v183 offset:0
ds_read_b64_tr_b16 v[10:11], v183 offset:6144
v_mfma_f32_16x16x32_bf16 a[120:123], v[20:23], v[108:111], a[120:123]
v_mfma_f32_16x16x32_bf16 a[116:119], v[20:23], v[104:107], a[116:119]
global_load_dwordx4 a[248:251], v230, s[54:55] offset:0 sc1
v_mfma_f32_16x16x32_bf16 a[116:119], v[24:27], v[120:123], a[116:119]
v_mfma_f32_16x16x32_bf16 a[116:119], v[28:31], v[136:139], a[116:119]
ds_read_b64_tr_b16 v[12:13], v188 offset:29696
ds_read_b64_tr_b16 v[14:15], v188 offset:35840
v_mfma_f32_16x16x32_bf16 a[112:115], v[28:31], v[132:135], a[112:115]
global_load_dwordx4 a[252:255], v231, s[54:55] offset:0 sc1
v_mfma_f32_16x16x32_bf16 a[112:115], v[24:27], v[116:119], a[112:115]
s_add_u32 s84, s87, 2
s_and_b32 s84, s84, 3
s_mul_i32 s84, s84, 245760
s_mul_i32 s92, %[wid], 10240
s_add_u32 s92, s92, s84
s_add_u32 m0, s81, 49152
v_mfma_f32_16x16x32_bf16 a[112:115], v[20:23], v[100:103], a[112:115]
buffer_load_dwordx4 v248, %[srdS], s92 offen offset:0 lds
s_waitcnt lgkmcnt(2)
v_mfma_f32_16x16x32_bf16 a[128:131], v[32:35], v[100:103], a[128:131]
ds_read_b64_tr_b16 v[16:17], v188 offset:41984
ds_read_b64_tr_b16 v[18:19], v188 offset:48128
v_mfma_f32_16x16x32_bf16 a[128:131], v[36:39], v[116:119], a[128:131]
v_mfma_f32_16x16x32_bf16 a[128:131], v[8:11], v[132:135], a[128:131]
buffer_load_dwordx4 v249, %[srdS], s92 offen offset:1024 lds
v_mfma_f32_16x16x32_bf16 a[132:135], v[8:11], v[136:139], a[132:135]
v_mfma_f32_16x16x32_bf16 a[132:135], v[36:39], v[120:123], a[132:135]
ds_read_b64_tr_b16 v[20:21], v180 offset:1024
ds_read_b64_tr_b16 v[22:23], v180 offset:7168
v_mfma_f32_16x16x32_bf16 a[132:135], v[32:35], v[104:107], a[132:135]
buffer_load_dwordx4 v250, %[srdS], s92 offen offset:2048 lds
v_mfma_f32_16x16x32_bf16 a[136:139], v[32:35], v[108:111], a[136:139]
s_add_u32 s92, s92, 40960
s_add_u32 m0, s81, 61440
v_mfma_f32_16x16x32_bf16 a[136:139], v[36:39], v[124:127], a[136:139]
buffer_load_dwordx4 v248, %[srdS], s92 offen offset:0 lds
v_mfma_f32_16x16x32_bf16 a[136:139], v[8:11], v[140:143], a[136:139]
s_waitcnt vmcnt(36)
s_barrier
ds_read_b64_tr_b16 v[24:25], v168 offset:12288
ds_read_b64_tr_b16 v[26:27], v168 offset:18432
v_mfma_f32_16x16x32_bf16 a[140:143], v[8:11], v[144:147], a[140:143]
v_mfma_f32_16x16x32_bf16 a[140:143], v[36:39], v[128:131], a[140:143]
buffer_load_dwordx4 v249, %[srdS], s92 offen offset:1024 lds
v_mfma_f32_16x16x32_bf16 a[140:143], v[32:35], v[112:115], a[140:143]
s_waitcnt lgkmcnt(2)
v_mfma_f32_16x16x32_bf16 a[156:159], v[12:15], v[112:115], a[156:159]
ds_read_b64_tr_b16 v[28:29], v168 offset:24576
ds_read_b64_tr_b16 v[30:31], v168 offset:30720
v_mfma_f32_16x16x32_bf16 a[156:159], v[16:19], v[128:131], a[156:159]
buffer_load_dwordx4 v250, %[srdS], s92 offen offset:2048 lds
v_mfma_f32_16x16x32_bf16 a[156:159], v[20:23], v[144:147], a[156:159]
s_add_u32 s92, s92, 40960
s_add_u32 m0, s81, 73728
v_mfma_f32_16x16x32_bf16 a[152:155], v[20:23], v[140:143], a[152:155]
buffer_load_dwordx4 v248, %[srdS], s92 offen offset:0 lds
v_mfma_f32_16x16x32_bf16 a[152:155], v[16:19], v[124:127], a[152:155]
ds_read_b64_tr_b16 v[32:33], v168 offset:36864
ds_read_b64_tr_b16 v[34:35], v168 offset:43008
v_mfma_f32_16x16x32_bf16 a[152:155], v[12:15], v[108:111], a[152:155]
v_mfma_f32_16x16x32_bf16 a[148:151], v[12:15], v[104:107], a[148:151]
buffer_load_dwordx4 v249, %[srdS], s92 offen offset:1024 lds
v_mfma_f32_16x16x32_bf16 a[148:151], v[16:19], v[120:123], a[148:151]
v_mfma_f32_16x16x32_bf16 a[148:151], v[20:23], v[136:139], a[148:151]
ds_read_b64_tr_b16 v[36:37], v169 offset:12288
ds_read_b64_tr_b16 v[38:39], v169 offset:18432
v_mfma_f32_16x16x32_bf16 a[144:147], v[20:23], v[132:135], a[144:147]
buffer_load_dwordx4 v250, %[srdS], s92 offen offset:2048 lds
v_mfma_f32_16x16x32_bf16 a[144:147], v[16:19], v[116:119], a[144:147]
s_add_u32 s92, s92, 40960
v_mfma_f32_16x16x32_bf16 a[144:147], v[12:15], v[100:103], a[144:147]
s_cmp_lg_u32 %[wid], 0
s_cbranch_scc1 .Losig
s_mov_b64 exec, 1
global_store_dword v251, v252, s[64:65] offset:640 sc1
s_mov_b64 exec, -1
.Losig:
s_waitcnt vmcnt(32) lgkmcnt(2)
v_mfma_f32_16x16x32_bf16 a[0:3], v[24:27], a[160:163], a[0:3]
ds_read_b64_tr_b16 v[8:9], v169 offset:24576
ds_read_b64_tr_b16 v[10:11], v169 offset:30720
v_lshlrev_b32 v196, 16, v158
v_and_b32 v197, 0xffff0000, v158
s_waitcnt vmcnt(28)
v_mfma_f32_16x16x32_bf16 a[0:3], v[28:31], a[176:179], a[0:3]
s_add_u32 s84, s87, 3
s_and_b32 s84, s84, 3
s_mul_i32 s84, s84, 786432
)"
R"(s_add_u32 s54, s50, s84
s_addc_u32 s55, s51, 0
v_lshlrev_b32 v198, 16, v159
s_waitcnt vmcnt(24)
v_mfma_f32_16x16x32_bf16 a[0:3], v[32:35], a[192:195], a[0:3]
global_load_dwordx4 v[52:55], v228, s[54:55] offset:-2048 sc1
v_and_b32 v199, 0xffff0000, v159
s_waitcnt vmcnt(24)
v_mfma_f32_16x16x32_bf16 a[4:7], v[32:35], a[196:199], a[4:7]
v_add_f32 v204, v204, v196
v_mfma_f32_16x16x32_bf16 a[4:7], v[28:31], a[180:183], a[4:7]
ds_read_b64_tr_b16 v[12:13], v169 offset:36864
ds_read_b64_tr_b16 v[14:15], v169 offset:43008
v_add_f32 v205, v205, v197
v_add_f32 v206, v206, v198
v_mfma_f32_16x16x32_bf16 a[4:7], v[24:27], a[164:167], a[4:7]
v_add_f32 v207, v207, v199
v_mfma_f32_16x16x32_bf16 a[8:11], v[24:27], a[168:171], a[8:11]
v_mul_f32 v200, 0xbfb8aa3b, v204
v_mfma_f32_16x16x32_bf16 a[8:11], v[28:31], a[184:187], a[8:11]
global_load_dwordx4 v[56:59], v229, s[54:55] offset:-2048 sc1
v_mul_f32 v201, 0xbfb8aa3b, v205
s_waitcnt vmcnt(24)
v_mfma_f32_16x16x32_bf16 a[8:11], v[32:35], a[200:203], a[8:11]
ds_read_b64_tr_b16 v[16:17], v170 offset:12288
ds_read_b64_tr_b16 v[18:19], v170 offset:18432
v_mul_f32 v202, 0xbfb8aa3b, v206
s_waitcnt vmcnt(23)
v_mfma_f32_16x16x32_bf16 a[12:15], v[32:35], a[204:207], a[12:15]
v_mul_f32 v203, 0xbfb8aa3b, v207
v_exp_f32 v200, v200
v_mfma_f32_16x16x32_bf16 a[12:15], v[28:31], a[188:191], a[12:15]
v_exp_f32 v201, v201
v_mfma_f32_16x16x32_bf16 a[12:15], v[24:27], a[172:175], a[12:15]
v_exp_f32 v202, v202
s_waitcnt lgkmcnt(2)
v_mfma_f32_16x16x32_bf16 a[28:31], v[36:39], a[172:175], a[28:31]
ds_read_b64_tr_b16 v[20:21], v170 offset:24576
ds_read_b64_tr_b16 v[22:23], v170 offset:30720
v_exp_f32 v203, v203
v_mfma_f32_16x16x32_bf16 a[28:31], v[8:11], a[188:191], a[28:31]
global_load_dwordx4 v[60:63], v230, s[54:55] offset:-2048 sc1
v_add_f32 v200, 1.0, v200
v_add_f32 v201, 1.0, v201
v_mfma_f32_16x16x32_bf16 a[28:31], v[12:15], a[204:207], a[28:31]
v_add_f32 v202, 1.0, v202
v_mfma_f32_16x16x32_bf16 a[24:27], v[12:15], a[200:203], a[24:27]
v_add_f32 v203, 1.0, v203
v_mfma_f32_16x16x32_bf16 a[24:27], v[8:11], a[184:187], a[24:27]
ds_read_b64_tr_b16 v[24:25], v170 offset:36864
ds_read_b64_tr_b16 v[26:27], v170 offset:43008
v_rcp_f32 v200, v200
v_mfma_f32_16x16x32_bf16 a[24:27], v[36:39], a[168:171], a[24:27]
v_rcp_f32 v201, v201
v_mfma_f32_16x16x32_bf16 a[20:23], v[36:39], a[164:167], a[20:23]
global_load_dwordx4 v[64:67], v231, s[54:55] offset:-2048 sc1
v_rcp_f32 v202, v202
v_rcp_f32 v203, v203
v_mfma_f32_16x16x32_bf16 a[20:23], v[8:11], a[180:183], a[20:23]
v_mul_f32 v204, v204, v200
v_mfma_f32_16x16x32_bf16 a[20:23], v[12:15], a[196:199], a[20:23]
ds_read_b64_tr_b16 v[28:29], v171 offset:12288
ds_read_b64_tr_b16 v[30:31], v171 offset:18432
v_mul_f32 v205, v205, v201
v_mfma_f32_16x16x32_bf16 a[16:19], v[12:15], a[192:195], a[16:19]
v_mul_f32 v206, v206, v202
v_mfma_f32_16x16x32_bf16 a[16:19], v[8:11], a[176:179], a[16:19]
v_mul_f32 v207, v207, v203
v_lshlrev_b32 v196, 16, v160
v_mfma_f32_16x16x32_bf16 a[16:19], v[36:39], a[160:163], a[16:19]
v_and_b32 v197, 0xffff0000, v160
s_waitcnt lgkmcnt(2)
v_mfma_f32_16x16x32_bf16 a[32:35], v[16:19], a[160:163], a[32:35]
ds_read_b64_tr_b16 v[32:33], v171 offset:24576
ds_read_b64_tr_b16 v[34:35], v171 offset:30720
global_load_dwordx4 v[68:71], v228, s[54:55] offset:-1024 sc1
v_lshlrev_b32 v198, 16, v161
v_mfma_f32_16x16x32_bf16 a[32:35], v[20:23], a[176:179], a[32:35]
v_and_b32 v199, 0xffff0000, v161
v_mfma_f32_16x16x32_bf16 a[32:35], v[24:27], a[192:195], a[32:35]
v_add_f32 v208, v208, v196
v_mfma_f32_16x16x32_bf16 a[36:39], v[24:27], a[196:199], a[36:39]
v_add_f32 v209, v209, v197
v_add_f32 v210, v210, v198
v_mfma_f32_16x16x32_bf16 a[36:39], v[20:23], a[180:183], a[36:39]
ds_read_b64_tr_b16 v[36:37], v171 offset:36864
ds_read_b64_tr_b16 v[38:39], v171 offset:43008
v_add_f32 v211, v211, v199
v_mfma_f32_16x16x32_bf16 a[36:39], v[16:19], a[164:167], a[36:39]
v_mul_f32 v200, 0xbfb8aa3b, v208
v_mfma_f32_16x16x32_bf16 a[40:43], v[16:19], a[168:171], a[40:43]
global_load_dwordx4 v[72:75], v229, s[54:55] offset:-1024 sc1
v_mul_f32 v201, 0xbfb8aa3b, v209
v_mfma_f32_16x16x32_bf16 a[40:43], v[20:23], a[184:187], a[40:43]
v_mul_f32 v202, 0xbfb8aa3b, v210
v_mul_f32 v203, 0xbfb8aa3b, v211
v_mfma_f32_16x16x32_bf16 a[40:43], v[24:27], a[200:203], a[40:43]
ds_read_b64_tr_b16 v[8:9], v168 offset:13312
ds_read_b64_tr_b16 v[10:11], v168 offset:19456
v_exp_f32 v200, v200
v_mfma_f32_16x16x32_bf16 a[44:47], v[24:27], a[204:207], a[44:47]
v_exp_f32 v201, v201
v_mfma_f32_16x16x32_bf16 a[44:47], v[20:23], a[188:191], a[44:47]
v_exp_f32 v202, v202
v_mfma_f32_16x16x32_bf16 a[44:47], v[16:19], a[172:175], a[44:47]
global_load_dwordx4 v[76:79], v230, s[54:55] offset:-1024 sc1
v_exp_f32 v203, v203
s_waitcnt lgkmcnt(2)
v_mfma_f32_16x16x32_bf16 a[60:63], v[28:31], a[172:175], a[60:63]
ds_read_b64_tr_b16 v[12:13], v168 offset:25600
ds_read_b64_tr_b16 v[14:15], v168 offset:31744
v_add_f32 v200, 1.0, v200
v_add_f32 v201, 1.0, v201
v_mfma_f32_16x16x32_bf16 a[60:63], v[32:35], a[188:191], a[60:63]
v_add_f32 v202, 1.0, v202
v_mfma_f32_16x16x32_bf16 a[60:63], v[36:39], a[204:207], a[60:63]
v_add_f32 v203, 1.0, v203
v_mfma_f32_16x16x32_bf16 a[56:59], v[36:39], a[200:203], a[56:59]
v_rcp_f32 v200, v200
v_mfma_f32_16x16x32_bf16 a[56:59], v[32:35], a[184:187], a[56:59]
ds_read_b64_tr_b16 v[16:17], v168 offset:37888
ds_read_b64_tr_b16 v[18:19], v168 offset:44032
v_rcp_f32 v201, v201
v_rcp_f32 v202, v202
v_mfma_f32_16x16x32_bf16 a[56:59], v[28:31], a[168:171], a[56:59]
global_load_dwordx4 v[80:83], v231, s[54:55] offset:-1024 sc1
v_rcp_f32 v203, v203
v_mfma_f32_16x16x32_bf16 a[52:55], v[28:31], a[164:167], a[52:55]
v_mul_f32 v208, v208, v200
v_mfma_f32_16x16x32_bf16 a[52:55], v[32:35], a[180:183], a[52:55]
v_mul_f32 v209, v209, v201
v_mfma_f32_16x16x32_bf16 a[52:55], v[36:39], a[196:199], a[52:55]
ds_read_b64_tr_b16 v[20:21], v180 offset:12288
ds_read_b64_tr_b16 v[22:23], v180 offset:18432
v_mul_f32 v210, v210, v202
v_mfma_f32_16x16x32_bf16 a[48:51], v[36:39], a[192:195], a[48:51]
v_mul_f32 v211, v211, v203
v_lshlrev_b32 v196, 16, v162
v_mfma_f32_16x16x32_bf16 a[48:51], v[32:35], a[176:179], a[48:51]
global_load_dwordx4 v[84:87], v228, s[54:55] offset:0 sc1
v_and_b32 v197, 0xffff0000, v162
v_mfma_f32_16x16x32_bf16 a[48:51], v[28:31], a[160:163], a[48:51]
v_lshlrev_b32 v198, 16, v163
s_waitcnt lgkmcnt(2)
v_mfma_f32_16x16x32_bf16 a[64:67], v[8:11], a[160:163], a[64:67]
ds_read_b64_tr_b16 v[24:25], v180 offset:24576
ds_read_b64_tr_b16 v[26:27], v180 offset:30720
v_and_b32 v199, 0xffff0000, v163
v_mfma_f32_16x16x32_bf16 a[64:67], v[12:15], a[176:179], a[64:67]
v_add_f32 v212, v212, v196
v_add_f32 v213, v213, v197
v_mfma_f32_16x16x32_bf16 a[64:67], v[16:19], a[192:195], a[64:67]
v_add_f32 v214, v214, v198
v_mfma_f32_16x16x32_bf16 a[68:71], v[16:19], a[196:199], a[68:71]
v_add_f32 v215, v215, v199
v_mfma_f32_16x16x32_bf16 a[68:71], v[12:15], a[180:183], a[68:71]
ds_read_b64_tr_b16 v[28:29], v180 offset:36864
ds_read_b64_tr_b16 v[30:31], v180 offset:43008
global_load_dwordx4 v[88:91], v229, s[54:55] offset:0 sc1
v_mul_f32 v200, 0xbfb8aa3b, v212
v_mfma_f32_16x16x32_bf16 a[68:71], v[8:11], a[164:167], a[68:71]
v_mul_f32 v201, 0xbfb8aa3b, v213
v_mfma_f32_16x16x32_bf16 a[72:75], v[8:11], a[168:171], a[72:75]
v_mul_f32 v202, 0xbfb8aa3b, v214
v_mul_f32 v203, 0xbfb8aa3b, v215
v_mfma_f32_16x16x32_bf16 a[72:75], v[12:15], a[184:187], a[72:75]
v_exp_f32 v200, v200
v_mfma_f32_16x16x32_bf16 a[72:75], v[16:19], a[200:203], a[72:75]
ds_read_b64_tr_b16 v[32:33], v181 offset:12288
ds_read_b64_tr_b16 v[34:35], v181 offset:18432
v_exp_f32 v201, v201
v_mfma_f32_16x16x32_bf16 a[76:79], v[16:19], a[204:207], a[76:79]
v_exp_f32 v202, v202
v_mfma_f32_16x16x32_bf16 a[76:79], v[12:15], a[188:191], a[76:79]
global_load_dwordx4 v[92:95], v230, s[54:55] offset:0 sc1
v_exp_f32 v203, v203
v_mfma_f32_16x16x32_bf16 a[76:79], v[8:11], a[172:175], a[76:79]
v_add_f32 v200, 1.0, v200
v_add_f32 v201, 1.0, v201
s_waitcnt lgkmcnt(2)
v_mfma_f32_16x16x32_bf16 a[92:95], v[20:23], a[172:175], a[92:95]
ds_read_b64_tr_b16 v[36:37], v181 offset:24576
ds_read_b64_tr_b16 v[38:39], v181 offset:30720
v_add_f32 v202, 1.0, v202
v_mfma_f32_16x16x32_bf16 a[92:95], v[24:27], a[188:191], a[92:95]
v_add_f32 v203, 1.0, v203
v_mfma_f32_16x16x32_bf16 a[92:95], v[28:31], a[204:207], a[92:95]
v_rcp_f32 v200, v200
v_mfma_f32_16x16x32_bf16 a[88:91], v[28:31], a[200:203], a[88:91]
global_load_dwordx4 v[96:99], v231, s[54:55] offset:0 sc1
v_rcp_f32 v201, v201
v_rcp_f32 v202, v202
v_mfma_f32_16x16x32_bf16 a[88:91], v[24:27], a[184:187], a[88:91]
ds_read_b64_tr_b16 v[8:9], v181 offset:36864
ds_read_b64_tr_b16 v[10:11], v181 offset:43008
v_rcp_f32 v203, v203
v_mfma_f32_16x16x32_bf16 a[88:91], v[20:23], a[168:171], a[88:91]
v_mul_f32 v212, v212, v200
v_mfma_f32_16x16x32_bf16 a[84:87], v[20:23], a[164:167], a[84:87]
v_mul_f32 v213, v213, v201
)"
R"(v_mfma_f32_16x16x32_bf16 a[84:87], v[24:27], a[180:183], a[84:87]
v_mul_f32 v214, v214, v202
v_mfma_f32_16x16x32_bf16 a[84:87], v[28:31], a[196:199], a[84:87]
ds_read_b64_tr_b16 v[12:13], v182 offset:12288
ds_read_b64_tr_b16 v[14:15], v182 offset:18432
s_add_u32 s84, s87, 3
s_and_b32 s84, s84, 3
s_mul_i32 s84, s84, 245760
s_mul_i32 s92, %[wid], 10240
s_add_u32 s92, s92, s84
s_add_u32 m0, s81, 86016
v_mul_f32 v215, v215, v203
v_lshlrev_b32 v196, 16, v164
v_mfma_f32_16x16x32_bf16 a[80:83], v[28:31], a[192:195], a[80:83]
buffer_load_dwordx4 v248, %[srdS], s92 offen offset:0 lds
v_and_b32 v197, 0xffff0000, v164
v_mfma_f32_16x16x32_bf16 a[80:83], v[24:27], a[176:179], a[80:83]
v_lshlrev_b32 v198, 16, v165
v_mfma_f32_16x16x32_bf16 a[80:83], v[20:23], a[160:163], a[80:83]
v_and_b32 v199, 0xffff0000, v165
s_waitcnt lgkmcnt(2)
v_mfma_f32_16x16x32_bf16 a[96:99], v[32:35], a[160:163], a[96:99]
ds_read_b64_tr_b16 v[16:17], v182 offset:24576
ds_read_b64_tr_b16 v[18:19], v182 offset:30720
v_add_f32 v216, v216, v196
v_add_f32 v217, v217, v197
v_mfma_f32_16x16x32_bf16 a[96:99], v[36:39], a[176:179], a[96:99]
v_add_f32 v218, v218, v198
v_mfma_f32_16x16x32_bf16 a[96:99], v[8:11], a[192:195], a[96:99]
v_add_f32 v219, v219, v199
v_mfma_f32_16x16x32_bf16 a[100:103], v[8:11], a[196:199], a[100:103]
buffer_load_dwordx4 v249, %[srdS], s92 offen offset:1024 lds
v_mul_f32 v200, 0xbfb8aa3b, v216
v_mfma_f32_16x16x32_bf16 a[100:103], v[36:39], a[180:183], a[100:103]
ds_read_b64_tr_b16 v[20:21], v182 offset:36864
ds_read_b64_tr_b16 v[22:23], v182 offset:43008
v_mul_f32 v201, 0xbfb8aa3b, v217
v_mfma_f32_16x16x32_bf16 a[100:103], v[32:35], a[164:167], a[100:103]
v_mul_f32 v202, 0xbfb8aa3b, v218
v_mul_f32 v203, 0xbfb8aa3b, v219
v_mfma_f32_16x16x32_bf16 a[104:107], v[32:35], a[168:171], a[104:107]
v_exp_f32 v200, v200
v_mfma_f32_16x16x32_bf16 a[104:107], v[36:39], a[184:187], a[104:107]
v_exp_f32 v201, v201
v_mfma_f32_16x16x32_bf16 a[104:107], v[8:11], a[200:203], a[104:107]
ds_read_b64_tr_b16 v[24:25], v183 offset:12288
ds_read_b64_tr_b16 v[26:27], v183 offset:18432
buffer_load_dwordx4 v250, %[srdS], s92 offen offset:2048 lds
v_exp_f32 v202, v202
global_load_dword v253, v251, s[62:63] offset:640 sc1
v_mfma_f32_16x16x32_bf16 a[108:111], v[8:11], a[204:207], a[108:111]
v_exp_f32 v203, v203
v_add_f32 v200, 1.0, v200
v_mfma_f32_16x16x32_bf16 a[108:111], v[36:39], a[188:191], a[108:111]
v_add_f32 v201, 1.0, v201
v_mfma_f32_16x16x32_bf16 a[108:111], v[32:35], a[172:175], a[108:111]
v_add_f32 v202, 1.0, v202
s_waitcnt lgkmcnt(2)
v_mfma_f32_16x16x32_bf16 a[124:127], v[12:15], a[172:175], a[124:127]
ds_read_b64_tr_b16 v[28:29], v183 offset:24576
ds_read_b64_tr_b16 v[30:31], v183 offset:30720
v_add_f32 v203, 1.0, v203
v_mfma_f32_16x16x32_bf16 a[124:127], v[16:19], a[188:191], a[124:127]
s_add_u32 s92, s92, 40960
s_add_u32 m0, s81, 98304
v_rcp_f32 v200, v200
v_mfma_f32_16x16x32_bf16 a[124:127], v[20:23], a[204:207], a[124:127]
buffer_load_dwordx4 v248, %[srdS], s92 offen offset:0 lds
v_rcp_f32 v201, v201
v_rcp_f32 v202, v202
v_mfma_f32_16x16x32_bf16 a[120:123], v[20:23], a[200:203], a[120:123]
v_rcp_f32 v203, v203
v_mfma_f32_16x16x32_bf16 a[120:123], v[16:19], a[184:187], a[120:123]
ds_read_b64_tr_b16 v[32:33], v183 offset:36864
ds_read_b64_tr_b16 v[34:35], v183 offset:43008
v_mul_f32 v216, v216, v200
v_mfma_f32_16x16x32_bf16 a[120:123], v[12:15], a[168:171], a[120:123]
v_mul_f32 v217, v217, v201
v_mfma_f32_16x16x32_bf16 a[116:119], v[12:15], a[164:167], a[116:119]
v_mul_f32 v218, v218, v202
v_mul_f32 v219, v219, v203
v_mfma_f32_16x16x32_bf16 a[116:119], v[16:19], a[180:183], a[116:119]
buffer_load_dwordx4 v249, %[srdS], s92 offen offset:1024 lds
v_lshlrev_b32 v196, 16, v166
v_mfma_f32_16x16x32_bf16 a[116:119], v[20:23], a[196:199], a[116:119]
ds_read_b64_tr_b16 v[36:37], v180 offset:13312
ds_read_b64_tr_b16 v[38:39], v180 offset:19456
v_and_b32 v197, 0xffff0000, v166
v_mfma_f32_16x16x32_bf16 a[112:115], v[20:23], a[192:195], a[112:115]
v_lshlrev_b32 v198, 16, v167
v_mfma_f32_16x16x32_bf16 a[112:115], v[16:19], a[176:179], a[112:115]
v_and_b32 v199, 0xffff0000, v167
v_mfma_f32_16x16x32_bf16 a[112:115], v[12:15], a[160:163], a[112:115]
v_add_f32 v220, v220, v196
v_add_f32 v221, v221, v197
s_waitcnt lgkmcnt(2)
v_mfma_f32_16x16x32_bf16 a[128:131], v[24:27], a[160:163], a[128:131]
ds_read_b64_tr_b16 v[8:9], v180 offset:25600
ds_read_b64_tr_b16 v[10:11], v180 offset:31744
v_add_f32 v222, v222, v198
v_mfma_f32_16x16x32_bf16 a[128:131], v[28:31], a[176:179], a[128:131]
buffer_load_dwordx4 v250, %[srdS], s92 offen offset:2048 lds
v_add_f32 v223, v223, v199
v_mfma_f32_16x16x32_bf16 a[128:131], v[32:35], a[192:195], a[128:131]
v_mul_f32 v200, 0xbfb8aa3b, v220
v_mfma_f32_16x16x32_bf16 a[132:135], v[32:35], a[196:199], a[132:135]
v_mul_f32 v201, 0xbfb8aa3b, v221
v_mul_f32 v202, 0xbfb8aa3b, v222
v_mfma_f32_16x16x32_bf16 a[132:135], v[28:31], a[180:183], a[132:135]
ds_read_b64_tr_b16 v[12:13], v180 offset:37888
ds_read_b64_tr_b16 v[14:15], v180 offset:44032
v_mul_f32 v203, 0xbfb8aa3b, v223
v_mfma_f32_16x16x32_bf16 a[132:135], v[24:27], a[164:167], a[132:135]
v_exp_f32 v200, v200
v_mfma_f32_16x16x32_bf16 a[136:139], v[24:27], a[168:171], a[136:139]
s_add_u32 s92, s92, 40960
s_add_u32 m0, s81, 110592
v_exp_f32 v201, v201
v_mfma_f32_16x16x32_bf16 a[136:139], v[28:31], a[184:187], a[136:139]
buffer_load_dwordx4 v248, %[srdS], s92 offen offset:0 lds
v_exp_f32 v202, v202
v_mfma_f32_16x16x32_bf16 a[136:139], v[32:35], a[200:203], a[136:139]
s_waitcnt vmcnt(20)
s_barrier
ds_read_b64_tr_b16 v[16:17], v168 offset:49152
ds_read_b64_tr_b16 v[18:19], v168 offset:55296
v_exp_f32 v203, v203
v_add_f32 v200, 1.0, v200
v_mfma_f32_16x16x32_bf16 a[140:143], v[32:35], a[204:207], a[140:143]
v_add_f32 v201, 1.0, v201
v_mfma_f32_16x16x32_bf16 a[140:143], v[28:31], a[188:191], a[140:143]
v_add_f32 v202, 1.0, v202
v_mfma_f32_16x16x32_bf16 a[140:143], v[24:27], a[172:175], a[140:143]
v_add_f32 v203, 1.0, v203
s_waitcnt lgkmcnt(2)
v_mfma_f32_16x16x32_bf16 a[156:159], v[36:39], a[172:175], a[156:159]
ds_read_b64_tr_b16 v[20:21], v172 offset:8192
ds_read_b64_tr_b16 v[22:23], v172 offset:14336
buffer_load_dwordx4 v249, %[srdS], s92 offen offset:1024 lds
v_rcp_f32 v200, v200
v_rcp_f32 v201, v201
v_mfma_f32_16x16x32_bf16 a[156:159], v[8:11], a[188:191], a[156:159]
v_rcp_f32 v202, v202
v_mfma_f32_16x16x32_bf16 a[156:159], v[12:15], a[204:207], a[156:159]
v_rcp_f32 v203, v203
v_mfma_f32_16x16x32_bf16 a[152:155], v[12:15], a[200:203], a[152:155]
v_mul_f32 v220, v220, v200
v_mfma_f32_16x16x32_bf16 a[152:155], v[8:11], a[184:187], a[152:155]
ds_read_b64_tr_b16 v[24:25], v172 offset:20480
ds_read_b64_tr_b16 v[26:27], v172 offset:26624
v_mul_f32 v221, v221, v201
v_mfma_f32_16x16x32_bf16 a[152:155], v[36:39], a[168:171], a[152:155]
v_mul_f32 v222, v222, v202
v_mul_f32 v223, v223, v203
v_mfma_f32_16x16x32_bf16 a[148:151], v[36:39], a[164:167], a[148:151]
buffer_load_dwordx4 v250, %[srdS], s92 offen offset:2048 lds
v_permlane16_swap_b32 v148, v150
v_mfma_f32_16x16x32_bf16 a[148:151], v[8:11], a[180:183], a[148:151]
v_permlane16_swap_b32 v149, v151
v_mfma_f32_16x16x32_bf16 a[148:151], v[12:15], a[196:199], a[148:151]
ds_read_b64_tr_b16 v[28:29], v169 offset:49152
ds_read_b64_tr_b16 v[30:31], v169 offset:55296
v_permlane16_swap_b32 v152, v154
v_mfma_f32_16x16x32_bf16 a[144:147], v[12:15], a[192:195], a[144:147]
v_permlane16_swap_b32 v153, v155
v_mfma_f32_16x16x32_bf16 a[144:147], v[8:11], a[176:179], a[144:147]
s_add_u32 s92, s92, 40960
v_mfma_f32_16x16x32_bf16 a[144:147], v[36:39], a[160:163], a[144:147]
s_waitcnt vmcnt(6)
v_cmp_ne_u32 vcc, s96, v253
s_nop 0
s_and_b32 s83, vcc_lo, s78
s_cbranch_scc0 .Lopoll2_go
s_mov_b32 s95, 0x20000
.Lopoll2:
global_load_dword v253, v251, s[62:63] offset:640 sc1
s_waitcnt vmcnt(0)
v_cmp_ne_u32 vcc, s96, v253
s_sub_u32 s95, s95, 1
s_cbranch_scc1 .Lopoll2_go
s_and_b32 s83, vcc_lo, s78
s_cbranch_scc0 .Lopoll2_go
s_sleep 2
s_branch .Lopoll2
.Lopoll2_go:
s_waitcnt lgkmcnt(2)
v_mfma_f32_16x16x32_bf16 a[0:3], v[16:19], a[208:211], a[0:3]
ds_read_b64_tr_b16 v[32:33], v173 offset:8192
ds_read_b64_tr_b16 v[34:35], v173 offset:14336
v_mfma_f32_16x16x32_bf16 a[0:3], v[20:23], a[224:227], a[0:3]
s_add_u32 s84, s87, 1
s_and_b32 s84, s84, 3
s_mul_i32 s84, s84, 786432
s_add_u32 s54, s50, s84
s_addc_u32 s55, s51, 0
v_mfma_f32_16x16x32_bf16 a[0:3], v[24:27], a[240:243], a[0:3]
global_load_dwordx4 v[100:103], v228, s[54:55] offset:1024 sc1
v_mfma_f32_16x16x32_bf16 a[4:7], v[24:27], a[244:247], a[4:7]
v_mfma_f32_16x16x32_bf16 a[4:7], v[20:23], a[228:231], a[4:7]
ds_read_b64_tr_b16 v[36:37], v173 offset:20480
)"
R"(ds_read_b64_tr_b16 v[38:39], v173 offset:26624
v_mfma_f32_16x16x32_bf16 a[4:7], v[16:19], a[212:215], a[4:7]
v_mfma_f32_16x16x32_bf16 a[8:11], v[16:19], a[216:219], a[8:11]
v_mfma_f32_16x16x32_bf16 a[8:11], v[20:23], a[232:235], a[8:11]
global_load_dwordx4 v[104:107], v229, s[54:55] offset:1024 sc1
v_mfma_f32_16x16x32_bf16 a[8:11], v[24:27], a[248:251], a[8:11]
ds_read_b64_tr_b16 v[8:9], v170 offset:49152
ds_read_b64_tr_b16 v[10:11], v170 offset:55296
v_mfma_f32_16x16x32_bf16 a[12:15], v[24:27], a[252:255], a[12:15]
v_mfma_f32_16x16x32_bf16 a[12:15], v[20:23], a[236:239], a[12:15]
v_mfma_f32_16x16x32_bf16 a[12:15], v[16:19], a[220:223], a[12:15]
s_waitcnt lgkmcnt(2)
v_mfma_f32_16x16x32_bf16 a[28:31], v[28:31], a[220:223], a[28:31]
ds_read_b64_tr_b16 v[12:13], v174 offset:8192
ds_read_b64_tr_b16 v[14:15], v174 offset:14336
v_mfma_f32_16x16x32_bf16 a[28:31], v[32:35], a[236:239], a[28:31]
global_load_dwordx4 v[108:111], v230, s[54:55] offset:1024 sc1
v_mfma_f32_16x16x32_bf16 a[28:31], v[36:39], a[252:255], a[28:31]
v_mfma_f32_16x16x32_bf16 a[24:27], v[36:39], a[248:251], a[24:27]
v_mfma_f32_16x16x32_bf16 a[24:27], v[32:35], a[232:235], a[24:27]
ds_read_b64_tr_b16 v[16:17], v174 offset:20480
ds_read_b64_tr_b16 v[18:19], v174 offset:26624
v_mfma_f32_16x16x32_bf16 a[24:27], v[28:31], a[216:219], a[24:27]
v_mfma_f32_16x16x32_bf16 a[20:23], v[28:31], a[212:215], a[20:23]
global_load_dwordx4 v[112:115], v231, s[54:55] offset:1024 sc1
v_mfma_f32_16x16x32_bf16 a[20:23], v[32:35], a[228:231], a[20:23]
v_mfma_f32_16x16x32_bf16 a[20:23], v[36:39], a[244:247], a[20:23]
ds_read_b64_tr_b16 v[20:21], v171 offset:49152
ds_read_b64_tr_b16 v[22:23], v171 offset:55296
v_mfma_f32_16x16x32_bf16 a[16:19], v[36:39], a[240:243], a[16:19]
v_mfma_f32_16x16x32_bf16 a[16:19], v[32:35], a[224:227], a[16:19]
v_mfma_f32_16x16x32_bf16 a[16:19], v[28:31], a[208:211], a[16:19]
s_waitcnt lgkmcnt(2)
v_mfma_f32_16x16x32_bf16 a[32:35], v[8:11], a[208:211], a[32:35]
ds_read_b64_tr_b16 v[24:25], v175 offset:8192
ds_read_b64_tr_b16 v[26:27], v175 offset:14336
global_load_dwordx4 v[116:119], v228, s[54:55] offset:2048 sc1
v_mfma_f32_16x16x32_bf16 a[32:35], v[12:15], a[224:227], a[32:35]
v_mfma_f32_16x16x32_bf16 a[32:35], v[16:19], a[240:243], a[32:35]
v_mfma_f32_16x16x32_bf16 a[36:39], v[16:19], a[244:247], a[36:39]
v_mfma_f32_16x16x32_bf16 a[36:39], v[12:15], a[228:231], a[36:39]
ds_read_b64_tr_b16 v[28:29], v175 offset:20480
ds_read_b64_tr_b16 v[30:31], v175 offset:26624
v_mfma_f32_16x16x32_bf16 a[36:39], v[8:11], a[212:215], a[36:39]
v_mfma_f32_16x16x32_bf16 a[40:43], v[8:11], a[216:219], a[40:43]
global_load_dwordx4 v[120:123], v229, s[54:55] offset:2048 sc1
v_mfma_f32_16x16x32_bf16 a[40:43], v[12:15], a[232:235], a[40:43]
v_mfma_f32_16x16x32_bf16 a[40:43], v[16:19], a[248:251], a[40:43]
ds_read_b64_tr_b16 v[32:33], v168 offset:50176
ds_read_b64_tr_b16 v[34:35], v168 offset:56320
v_mfma_f32_16x16x32_bf16 a[44:47], v[16:19], a[252:255], a[44:47]
v_mfma_f32_16x16x32_bf16 a[44:47], v[12:15], a[236:239], a[44:47]
v_mfma_f32_16x16x32_bf16 a[44:47], v[8:11], a[220:223], a[44:47]
global_load_dwordx4 v[124:127], v230, s[54:55] offset:2048 sc1
s_waitcnt lgkmcnt(2)
v_mfma_f32_16x16x32_bf16 a[60:63], v[20:23], a[220:223], a[60:63]
ds_read_b64_tr_b16 v[36:37], v172 offset:9216
ds_read_b64_tr_b16 v[38:39], v172 offset:15360
v_mfma_f32_16x16x32_bf16 a[60:63], v[24:27], a[236:239], a[60:63]
v_mfma_f32_16x16x32_bf16 a[60:63], v[28:31], a[252:255], a[60:63]
v_mfma_f32_16x16x32_bf16 a[56:59], v[28:31], a[248:251], a[56:59]
v_mfma_f32_16x16x32_bf16 a[56:59], v[24:27], a[232:235], a[56:59]
ds_read_b64_tr_b16 v[8:9], v172 offset:21504
ds_read_b64_tr_b16 v[10:11], v172 offset:27648
v_mfma_f32_16x16x32_bf16 a[56:59], v[20:23], a[216:219], a[56:59]
global_load_dwordx4 v[128:131], v231, s[54:55] offset:2048 sc1
v_mfma_f32_16x16x32_bf16 a[52:55], v[20:23], a[212:215], a[52:55]
v_mfma_f32_16x16x32_bf16 a[52:55], v[24:27], a[228:231], a[52:55]
v_mfma_f32_16x16x32_bf16 a[52:55], v[28:31], a[244:247], a[52:55]
ds_read_b64_tr_b16 v[12:13], v180 offset:49152
ds_read_b64_tr_b16 v[14:15], v180 offset:55296
v_mfma_f32_16x16x32_bf16 a[48:51], v[28:31], a[240:243], a[48:51]
v_mfma_f32_16x16x32_bf16 a[48:51], v[24:27], a[224:227], a[48:51]
global_load_dwordx4 v[132:135], v228, s[54:55] offset:3072 sc1
v_mfma_f32_16x16x32_bf16 a[48:51], v[20:23], a[208:211], a[48:51]
s_waitcnt lgkmcnt(2)
v_mfma_f32_16x16x32_bf16 a[64:67], v[32:35], a[208:211], a[64:67]
ds_read_b64_tr_b16 v[16:17], v184 offset:8192
ds_read_b64_tr_b16 v[18:19], v184 offset:14336
v_mfma_f32_16x16x32_bf16 a[64:67], v[36:39], a[224:227], a[64:67]
v_mfma_f32_16x16x32_bf16 a[64:67], v[8:11], a[240:243], a[64:67]
v_mfma_f32_16x16x32_bf16 a[68:71], v[8:11], a[244:247], a[68:71]
v_mfma_f32_16x16x32_bf16 a[68:71], v[36:39], a[228:231], a[68:71]
ds_read_b64_tr_b16 v[20:21], v184 offset:20480
ds_read_b64_tr_b16 v[22:23], v184 offset:26624
global_load_dwordx4 v[136:139], v229, s[54:55] offset:3072 sc1
v_mfma_f32_16x16x32_bf16 a[68:71], v[32:35], a[212:215], a[68:71]
v_mfma_f32_16x16x32_bf16 a[72:75], v[32:35], a[216:219], a[72:75]
v_mfma_f32_16x16x32_bf16 a[72:75], v[36:39], a[232:235], a[72:75]
v_mfma_f32_16x16x32_bf16 a[72:75], v[8:11], a[248:251], a[72:75]
ds_read_b64_tr_b16 v[24:25], v181 offset:49152
ds_read_b64_tr_b16 v[26:27], v181 offset:55296
v_mfma_f32_16x16x32_bf16 a[76:79], v[8:11], a[252:255], a[76:79]
v_mfma_f32_16x16x32_bf16 a[76:79], v[36:39], a[236:239], a[76:79]
global_load_dwordx4 v[140:143], v230, s[54:55] offset:3072 sc1
v_mfma_f32_16x16x32_bf16 a[76:79], v[32:35], a[220:223], a[76:79]
s_waitcnt lgkmcnt(2)
v_mfma_f32_16x16x32_bf16 a[92:95], v[12:15], a[220:223], a[92:95]
ds_read_b64_tr_b16 v[28:29], v185 offset:8192
ds_read_b64_tr_b16 v[30:31], v185 offset:14336
v_mfma_f32_16x16x32_bf16 a[92:95], v[16:19], a[236:239], a[92:95]
v_mfma_f32_16x16x32_bf16 a[92:95], v[20:23], a[252:255], a[92:95]
v_mfma_f32_16x16x32_bf16 a[88:91], v[20:23], a[248:251], a[88:91]
global_load_dwordx4 v[144:147], v231, s[54:55] offset:3072 sc1
v_mfma_f32_16x16x32_bf16 a[88:91], v[16:19], a[232:235], a[88:91]
ds_read_b64_tr_b16 v[32:33], v185 offset:20480
ds_read_b64_tr_b16 v[34:35], v185 offset:26624
v_mfma_f32_16x16x32_bf16 a[88:91], v[12:15], a[216:219], a[88:91]
v_mfma_f32_16x16x32_bf16 a[84:87], v[12:15], a[212:215], a[84:87]
v_mfma_f32_16x16x32_bf16 a[84:87], v[16:19], a[228:231], a[84:87]
v_mfma_f32_16x16x32_bf16 a[84:87], v[20:23], a[244:247], a[84:87]
ds_read_b64_tr_b16 v[36:37], v182 offset:49152
ds_read_b64_tr_b16 v[38:39], v182 offset:55296
s_add_u32 s84, s87, 1
s_and_b32 s84, s84, 3
s_mul_i32 s84, s84, 245760
s_mul_i32 s92, %[wid], 10240
s_add_u32 s92, s92, s84
s_add_u32 s92, s92, 122880
s_add_u32 m0, s81, 122880
v_mfma_f32_16x16x32_bf16 a[80:83], v[20:23], a[240:243], a[80:83]
buffer_load_dwordx4 v248, %[srdS], s92 offen offset:0 lds
v_mfma_f32_16x16x32_bf16 a[80:83], v[16:19], a[224:227], a[80:83]
v_mfma_f32_16x16x32_bf16 a[80:83], v[12:15], a[208:211], a[80:83]
s_waitcnt lgkmcnt(2)
v_mfma_f32_16x16x32_bf16 a[96:99], v[24:27], a[208:211], a[96:99]
ds_read_b64_tr_b16 v[8:9], v186 offset:8192
ds_read_b64_tr_b16 v[10:11], v186 offset:14336
v_mfma_f32_16x16x32_bf16 a[96:99], v[28:31], a[224:227], a[96:99]
v_mfma_f32_16x16x32_bf16 a[96:99], v[32:35], a[240:243], a[96:99]
v_mfma_f32_16x16x32_bf16 a[100:103], v[32:35], a[244:247], a[100:103]
buffer_load_dwordx4 v249, %[srdS], s92 offen offset:1024 lds
v_mfma_f32_16x16x32_bf16 a[100:103], v[28:31], a[228:231], a[100:103]
ds_read_b64_tr_b16 v[12:13], v186 offset:20480
ds_read_b64_tr_b16 v[14:15], v186 offset:26624
v_mfma_f32_16x16x32_bf16 a[100:103], v[24:27], a[212:215], a[100:103]
v_mfma_f32_16x16x32_bf16 a[104:107], v[24:27], a[216:219], a[104:107]
v_mfma_f32_16x16x32_bf16 a[104:107], v[28:31], a[232:235], a[104:107]
v_mfma_f32_16x16x32_bf16 a[104:107], v[32:35], a[248:251], a[104:107]
ds_read_b64_tr_b16 v[16:17], v183 offset:49152
ds_read_b64_tr_b16 v[18:19], v183 offset:55296
buffer_load_dwordx4 v250, %[srdS], s92 offen offset:2048 lds
v_mfma_f32_16x16x32_bf16 a[108:111], v[32:35], a[252:255], a[108:111]
v_mfma_f32_16x16x32_bf16 a[108:111], v[28:31], a[236:239], a[108:111]
v_mfma_f32_16x16x32_bf16 a[108:111], v[24:27], a[220:223], a[108:111]
s_waitcnt lgkmcnt(2)
v_mfma_f32_16x16x32_bf16 a[124:127], v[36:39], a[220:223], a[124:127]
ds_read_b64_tr_b16 v[20:21], v187 offset:8192
ds_read_b64_tr_b16 v[22:23], v187 offset:14336
v_mfma_f32_16x16x32_bf16 a[124:127], v[8:11], a[236:239], a[124:127]
s_add_u32 s92, s92, 40960
s_add_u32 m0, s81, 135168
v_mfma_f32_16x16x32_bf16 a[124:127], v[12:15], a[252:255], a[124:127]
buffer_load_dwordx4 v248, %[srdS], s92 offen offset:0 lds
v_mfma_f32_16x16x32_bf16 a[120:123], v[12:15], a[248:251], a[120:123]
v_mfma_f32_16x16x32_bf16 a[120:123], v[8:11], a[232:235], a[120:123]
ds_read_b64_tr_b16 v[24:25], v187 offset:20480
ds_read_b64_tr_b16 v[26:27], v187 offset:26624
v_mfma_f32_16x16x32_bf16 a[120:123], v[36:39], a[216:219], a[120:123]
v_mfma_f32_16x16x32_bf16 a[116:119], v[36:39], a[212:215], a[116:119]
v_mfma_f32_16x16x32_bf16 a[116:119], v[8:11], a[228:231], a[116:119]
buffer_load_dwordx4 v249, %[srdS], s92 offen offset:1024 lds
v_mfma_f32_16x16x32_bf16 a[116:119], v[12:15], a[244:247], a[116:119]
ds_read_b64_tr_b16 v[28:29], v180 offset:50176
ds_read_b64_tr_b16 v[30:31], v180 offset:56320
v_mfma_f32_16x16x32_bf16 a[112:115], v[12:15], a[240:243], a[112:115]
v_mfma_f32_16x16x32_bf16 a[112:115], v[8:11], a[224:227], a[112:115]
v_mfma_f32_16x16x32_bf16 a[112:115], v[36:39], a[208:211], a[112:115]
s_waitcnt lgkmcnt(2)
v_mfma_f32_16x16x32_bf16 a[128:131], v[16:19], a[208:211], a[128:131]
ds_read_b64_tr_b16 v[32:33], v184 offset:9216
ds_read_b64_tr_b16 v[34:35], v184 offset:15360
v_mfma_f32_16x16x32_bf16 a[128:131], v[20:23], a[224:227], a[128:131]
buffer_load_dwordx4 v250, %[srdS], s92 offen offset:2048 lds
v_mfma_f32_16x16x32_bf16 a[128:131], v[24:27], a[240:243], a[128:131]
v_mfma_f32_16x16x32_bf16 a[132:135], v[24:27], a[244:247], a[132:135]
v_mfma_f32_16x16x32_bf16 a[132:135], v[20:23], a[228:231], a[132:135]
ds_read_b64_tr_b16 v[36:37], v184 offset:21504
ds_read_b64_tr_b16 v[38:39], v184 offset:27648
v_mfma_f32_16x16x32_bf16 a[132:135], v[16:19], a[212:215], a[132:135]
v_mfma_f32_16x16x32_bf16 a[136:139], v[16:19], a[216:219], a[136:139]
s_add_u32 s92, s92, 40960
s_add_u32 m0, s81, 147456
v_mfma_f32_16x16x32_bf16 a[136:139], v[20:23], a[232:235], a[136:139]
buffer_load_dwordx4 v248, %[srdS], s92 offen offset:0 lds
v_mfma_f32_16x16x32_bf16 a[136:139], v[24:27], a[248:251], a[136:139]
s_waitcnt vmcnt(19)
s_barrier
ds_read_b64_tr_b16 v[8:9], v172 offset:32768
ds_read_b64_tr_b16 v[10:11], v172 offset:38912
v_mfma_f32_16x16x32_bf16 a[140:143], v[24:27], a[252:255], a[140:143]
v_mfma_f32_16x16x32_bf16 a[140:143], v[20:23], a[236:239], a[140:143]
v_mfma_f32_16x16x32_bf16 a[140:143], v[16:19], a[220:223], a[140:143]
s_waitcnt lgkmcnt(2)
v_mfma_f32_16x16x32_bf16 a[156:159], v[28:31], a[220:223], a[156:159]
ds_read_b64_tr_b16 v[12:13], v172 offset:45056
ds_read_b64_tr_b16 v[14:15], v172 offset:51200
buffer_load_dwordx4 v249, %[srdS], s92 offen offset:1024 lds
v_mfma_f32_16x16x32_bf16 a[156:159], v[32:35], a[236:239], a[156:159]
v_mfma_f32_16x16x32_bf16 a[156:159], v[36:39], a[252:255], a[156:159]
)"
R"(v_mfma_f32_16x16x32_bf16 a[152:155], v[36:39], a[248:251], a[152:155]
v_mfma_f32_16x16x32_bf16 a[152:155], v[32:35], a[232:235], a[152:155]
ds_read_b64_tr_b16 v[16:17], v176 offset:4096
ds_read_b64_tr_b16 v[18:19], v176 offset:10240
v_mfma_f32_16x16x32_bf16 a[152:155], v[28:31], a[216:219], a[152:155]
v_mfma_f32_16x16x32_bf16 a[148:151], v[28:31], a[212:215], a[148:151]
buffer_load_dwordx4 v250, %[srdS], s92 offen offset:2048 lds
v_mfma_f32_16x16x32_bf16 a[148:151], v[32:35], a[228:231], a[148:151]
v_mfma_f32_16x16x32_bf16 a[148:151], v[36:39], a[244:247], a[148:151]
ds_read_b64_tr_b16 v[20:21], v173 offset:32768
ds_read_b64_tr_b16 v[22:23], v173 offset:38912
v_mfma_f32_16x16x32_bf16 a[144:147], v[36:39], a[240:243], a[144:147]
v_mfma_f32_16x16x32_bf16 a[144:147], v[32:35], a[224:227], a[144:147]
s_add_u32 s92, s92, 40960
v_mfma_f32_16x16x32_bf16 a[144:147], v[28:31], a[208:211], a[144:147]
s_waitcnt lgkmcnt(2)
v_mfma_f32_16x16x32_bf16 a[0:3], v[8:11], v[52:55], a[0:3]
ds_read_b64_tr_b16 v[24:25], v173 offset:45056
ds_read_b64_tr_b16 v[26:27], v173 offset:51200
v_mfma_f32_16x16x32_bf16 a[0:3], v[12:15], v[68:71], a[0:3]
s_add_u32 s84, s87, 2
s_and_b32 s84, s84, 3
s_mul_i32 s84, s84, 786432
s_add_u32 s54, s50, s84
s_addc_u32 s55, s51, 0
v_mfma_f32_16x16x32_bf16 a[0:3], v[16:19], v[84:87], a[0:3]
global_load_dwordx4 a[160:163], v228, s[54:55] offset:1024 sc1
v_mfma_f32_16x16x32_bf16 a[4:7], v[16:19], v[88:91], a[4:7]
v_mfma_f32_16x16x32_bf16 a[4:7], v[12:15], v[72:75], a[4:7]
ds_read_b64_tr_b16 v[28:29], v177 offset:4096
ds_read_b64_tr_b16 v[30:31], v177 offset:10240
v_mfma_f32_16x16x32_bf16 a[4:7], v[8:11], v[56:59], a[4:7]
v_mfma_f32_16x16x32_bf16 a[8:11], v[8:11], v[60:63], a[8:11]
v_mfma_f32_16x16x32_bf16 a[8:11], v[12:15], v[76:79], a[8:11]
global_load_dwordx4 a[164:167], v229, s[54:55] offset:1024 sc1
v_mfma_f32_16x16x32_bf16 a[8:11], v[16:19], v[92:95], a[8:11]
ds_read_b64_tr_b16 v[32:33], v174 offset:32768
ds_read_b64_tr_b16 v[34:35], v174 offset:38912
v_mfma_f32_16x16x32_bf16 a[12:15], v[16:19], v[96:99], a[12:15]
v_mfma_f32_16x16x32_bf16 a[12:15], v[12:15], v[80:83], a[12:15]
v_mfma_f32_16x16x32_bf16 a[12:15], v[8:11], v[64:67], a[12:15]
s_waitcnt lgkmcnt(2)
v_mfma_f32_16x16x32_bf16 a[28:31], v[20:23], v[64:67], a[28:31]
ds_read_b64_tr_b16 v[36:37], v174 offset:45056
ds_read_b64_tr_b16 v[38:39], v174 offset:51200
v_mfma_f32_16x16x32_bf16 a[28:31], v[24:27], v[80:83], a[28:31]
global_load_dwordx4 a[168:171], v230, s[54:55] offset:1024 sc1
v_mfma_f32_16x16x32_bf16 a[28:31], v[28:31], v[96:99], a[28:31]
v_mfma_f32_16x16x32_bf16 a[24:27], v[28:31], v[92:95], a[24:27]
v_mfma_f32_16x16x32_bf16 a[24:27], v[24:27], v[76:79], a[24:27]
ds_read_b64_tr_b16 v[8:9], v178 offset:4096
ds_read_b64_tr_b16 v[10:11], v178 offset:10240
v_mfma_f32_16x16x32_bf16 a[24:27], v[20:23], v[60:63], a[24:27]
v_mfma_f32_16x16x32_bf16 a[20:23], v[20:23], v[56:59], a[20:23]
global_load_dwordx4 a[172:175], v231, s[54:55] offset:1024 sc1
v_mfma_f32_16x16x32_bf16 a[20:23], v[24:27], v[72:75], a[20:23]
v_mfma_f32_16x16x32_bf16 a[20:23], v[28:31], v[88:91], a[20:23]
ds_read_b64_tr_b16 v[12:13], v175 offset:32768
ds_read_b64_tr_b16 v[14:15], v175 offset:38912
v_mfma_f32_16x16x32_bf16 a[16:19], v[28:31], v[84:87], a[16:19]
v_mfma_f32_16x16x32_bf16 a[16:19], v[24:27], v[68:71], a[16:19]
v_mfma_f32_16x16x32_bf16 a[16:19], v[20:23], v[52:55], a[16:19]
s_waitcnt lgkmcnt(2)
v_mfma_f32_16x16x32_bf16 a[32:35], v[32:35], v[52:55], a[32:35]
ds_read_b64_tr_b16 v[16:17], v175 offset:45056
ds_read_b64_tr_b16 v[18:19], v175 offset:51200
global_load_dwordx4 a[176:179], v228, s[54:55] offset:2048 sc1
v_mfma_f32_16x16x32_bf16 a[32:35], v[36:39], v[68:71], a[32:35]
v_mfma_f32_16x16x32_bf16 a[32:35], v[8:11], v[84:87], a[32:35]
v_mfma_f32_16x16x32_bf16 a[36:39], v[8:11], v[88:91], a[36:39]
v_mfma_f32_16x16x32_bf16 a[36:39], v[36:39], v[72:75], a[36:39]
ds_read_b64_tr_b16 v[20:21], v179 offset:4096
ds_read_b64_tr_b16 v[22:23], v179 offset:10240
v_mfma_f32_16x16x32_bf16 a[36:39], v[32:35], v[56:59], a[36:39]
v_mfma_f32_16x16x32_bf16 a[40:43], v[32:35], v[60:63], a[40:43]
global_load_dwordx4 a[180:183], v229, s[54:55] offset:2048 sc1
v_mfma_f32_16x16x32_bf16 a[40:43], v[36:39], v[76:79], a[40:43]
v_mfma_f32_16x16x32_bf16 a[40:43], v[8:11], v[92:95], a[40:43]
ds_read_b64_tr_b16 v[24:25], v172 offset:33792
ds_read_b64_tr_b16 v[26:27], v172 offset:39936
v_mfma_f32_16x16x32_bf16 a[44:47], v[8:11], v[96:99], a[44:47]
v_mfma_f32_16x16x32_bf16 a[44:47], v[36:39], v[80:83], a[44:47]
v_mfma_f32_16x16x32_bf16 a[44:47], v[32:35], v[64:67], a[44:47]
global_load_dwordx4 a[184:187], v230, s[54:55] offset:2048 sc1
s_waitcnt lgkmcnt(2)
v_mfma_f32_16x16x32_bf16 a[60:63], v[12:15], v[64:67], a[60:63]
ds_read_b64_tr_b16 v[28:29], v172 offset:46080
ds_read_b64_tr_b16 v[30:31], v172 offset:52224
v_mfma_f32_16x16x32_bf16 a[60:63], v[16:19], v[80:83], a[60:63]
v_mfma_f32_16x16x32_bf16 a[60:63], v[20:23], v[96:99], a[60:63]
v_mfma_f32_16x16x32_bf16 a[56:59], v[20:23], v[92:95], a[56:59]
v_mfma_f32_16x16x32_bf16 a[56:59], v[16:19], v[76:79], a[56:59]
ds_read_b64_tr_b16 v[32:33], v176 offset:5120
ds_read_b64_tr_b16 v[34:35], v176 offset:11264
v_mfma_f32_16x16x32_bf16 a[56:59], v[12:15], v[60:63], a[56:59]
global_load_dwordx4 a[188:191], v231, s[54:55] offset:2048 sc1
v_mfma_f32_16x16x32_bf16 a[52:55], v[12:15], v[56:59], a[52:55]
v_mfma_f32_16x16x32_bf16 a[52:55], v[16:19], v[72:75], a[52:55]
v_mfma_f32_16x16x32_bf16 a[52:55], v[20:23], v[88:91], a[52:55]
ds_read_b64_tr_b16 v[36:37], v184 offset:32768
ds_read_b64_tr_b16 v[38:39], v184 offset:38912
v_mfma_f32_16x16x32_bf16 a[48:51], v[20:23], v[84:87], a[48:51]
v_mfma_f32_16x16x32_bf16 a[48:51], v[16:19], v[68:71], a[48:51]
global_load_dwordx4 a[192:195], v228, s[54:55] offset:3072 sc1
v_mfma_f32_16x16x32_bf16 a[48:51], v[12:15], v[52:55], a[48:51]
s_waitcnt lgkmcnt(2)
v_mfma_f32_16x16x32_bf16 a[64:67], v[24:27], v[52:55], a[64:67]
ds_read_b64_tr_b16 v[8:9], v184 offset:45056
ds_read_b64_tr_b16 v[10:11], v184 offset:51200
v_mfma_f32_16x16x32_bf16 a[64:67], v[28:31], v[68:71], a[64:67]
v_mfma_f32_16x16x32_bf16 a[64:67], v[32:35], v[84:87], a[64:67]
v_mfma_f32_16x16x32_bf16 a[68:71], v[32:35], v[88:91], a[68:71]
v_mfma_f32_16x16x32_bf16 a[68:71], v[28:31], v[72:75], a[68:71]
ds_read_b64_tr_b16 v[12:13], v188 offset:4096
ds_read_b64_tr_b16 v[14:15], v188 offset:10240
global_load_dwordx4 a[196:199], v229, s[54:55] offset:3072 sc1
v_mfma_f32_16x16x32_bf16 a[68:71], v[24:27], v[56:59], a[68:71]
v_mfma_f32_16x16x32_bf16 a[72:75], v[24:27], v[60:63], a[72:75]
v_mfma_f32_16x16x32_bf16 a[72:75], v[28:31], v[76:79], a[72:75]
v_mfma_f32_16x16x32_bf16 a[72:75], v[32:35], v[92:95], a[72:75]
ds_read_b64_tr_b16 v[16:17], v185 offset:32768
ds_read_b64_tr_b16 v[18:19], v185 offset:38912
v_mfma_f32_16x16x32_bf16 a[76:79], v[32:35], v[96:99], a[76:79]
v_mfma_f32_16x16x32_bf16 a[76:79], v[28:31], v[80:83], a[76:79]
global_load_dwordx4 a[200:203], v230, s[54:55] offset:3072 sc1
v_mfma_f32_16x16x32_bf16 a[76:79], v[24:27], v[64:67], a[76:79]
s_waitcnt lgkmcnt(2)
v_mfma_f32_16x16x32_bf16 a[92:95], v[36:39], v[64:67], a[92:95]
ds_read_b64_tr_b16 v[20:21], v185 offset:45056
ds_read_b64_tr_b16 v[22:23], v185 offset:51200
v_mfma_f32_16x16x32_bf16 a[92:95], v[8:11], v[80:83], a[92:95]
v_mfma_f32_16x16x32_bf16 a[92:95], v[12:15], v[96:99], a[92:95]
v_mfma_f32_16x16x32_bf16 a[88:91], v[12:15], v[92:95], a[88:91]
global_load_dwordx4 a[204:207], v231, s[54:55] offset:3072 sc1
v_mfma_f32_16x16x32_bf16 a[88:91], v[8:11], v[76:79], a[88:91]
ds_read_b64_tr_b16 v[24:25], v189 offset:4096
ds_read_b64_tr_b16 v[26:27], v189 offset:10240
v_mfma_f32_16x16x32_bf16 a[88:91], v[36:39], v[60:63], a[88:91]
v_mfma_f32_16x16x32_bf16 a[84:87], v[36:39], v[56:59], a[84:87]
v_mfma_f32_16x16x32_bf16 a[84:87], v[8:11], v[72:75], a[84:87]
v_mfma_f32_16x16x32_bf16 a[84:87], v[12:15], v[88:91], a[84:87]
ds_read_b64_tr_b16 v[28:29], v186 offset:32768
ds_read_b64_tr_b16 v[30:31], v186 offset:38912
s_add_u32 s84, s87, 2
s_and_b32 s84, s84, 3
s_mul_i32 s84, s84, 245760
s_mul_i32 s92, %[wid], 10240
s_add_u32 s92, s92, s84
s_add_u32 s92, s92, 122880
s_add_u32 m0, s81, 0
v_mfma_f32_16x16x32_bf16 a[80:83], v[12:15], v[84:87], a[80:83]
buffer_load_dwordx4 v248, %[srdS], s92 offen offset:0 lds
v_mfma_f32_16x16x32_bf16 a[80:83], v[8:11], v[68:71], a[80:83]
v_mfma_f32_16x16x32_bf16 a[80:83], v[36:39], v[52:55], a[80:83]
s_waitcnt lgkmcnt(2)
v_mfma_f32_16x16x32_bf16 a[96:99], v[16:19], v[52:55], a[96:99]
ds_read_b64_tr_b16 v[32:33], v186 offset:45056
ds_read_b64_tr_b16 v[34:35], v186 offset:51200
v_mfma_f32_16x16x32_bf16 a[96:99], v[20:23], v[68:71], a[96:99]
v_mfma_f32_16x16x32_bf16 a[96:99], v[24:27], v[84:87], a[96:99]
v_mfma_f32_16x16x32_bf16 a[100:103], v[24:27], v[88:91], a[100:103]
buffer_load_dwordx4 v249, %[srdS], s92 offen offset:1024 lds
v_mfma_f32_16x16x32_bf16 a[100:103], v[20:23], v[72:75], a[100:103]
ds_read_b64_tr_b16 v[36:37], v190 offset:4096
ds_read_b64_tr_b16 v[38:39], v190 offset:10240
v_mfma_f32_16x16x32_bf16 a[100:103], v[16:19], v[56:59], a[100:103]
v_mfma_f32_16x16x32_bf16 a[104:107], v[16:19], v[60:63], a[104:107]
v_mfma_f32_16x16x32_bf16 a[104:107], v[20:23], v[76:79], a[104:107]
v_mfma_f32_16x16x32_bf16 a[104:107], v[24:27], v[92:95], a[104:107]
ds_read_b64_tr_b16 v[8:9], v187 offset:32768
ds_read_b64_tr_b16 v[10:11], v187 offset:38912
buffer_load_dwordx4 v250, %[srdS], s92 offen offset:2048 lds
v_mfma_f32_16x16x32_bf16 a[108:111], v[24:27], v[96:99], a[108:111]
v_mfma_f32_16x16x32_bf16 a[108:111], v[20:23], v[80:83], a[108:111]
v_mfma_f32_16x16x32_bf16 a[108:111], v[16:19], v[64:67], a[108:111]
s_waitcnt lgkmcnt(2)
v_mfma_f32_16x16x32_bf16 a[124:127], v[28:31], v[64:67], a[124:127]
ds_read_b64_tr_b16 v[12:13], v187 offset:45056
ds_read_b64_tr_b16 v[14:15], v187 offset:51200
v_mfma_f32_16x16x32_bf16 a[124:127], v[32:35], v[80:83], a[124:127]
s_add_u32 s92, s92, 40960
s_add_u32 m0, s81, 12288
v_mfma_f32_16x16x32_bf16 a[124:127], v[36:39], v[96:99], a[124:127]
buffer_load_dwordx4 v248, %[srdS], s92 offen offset:0 lds
v_mfma_f32_16x16x32_bf16 a[120:123], v[36:39], v[92:95], a[120:123]
v_mfma_f32_16x16x32_bf16 a[120:123], v[32:35], v[76:79], a[120:123]
ds_read_b64_tr_b16 v[16:17], v191 offset:4096
ds_read_b64_tr_b16 v[18:19], v191 offset:10240
v_mfma_f32_16x16x32_bf16 a[120:123], v[28:31], v[60:63], a[120:123]
v_mfma_f32_16x16x32_bf16 a[116:119], v[28:31], v[56:59], a[116:119]
v_mfma_f32_16x16x32_bf16 a[116:119], v[32:35], v[72:75], a[116:119]
buffer_load_dwordx4 v249, %[srdS], s92 offen offset:1024 lds
v_mfma_f32_16x16x32_bf16 a[116:119], v[36:39], v[88:91], a[116:119]
ds_read_b64_tr_b16 v[20:21], v184 offset:33792
ds_read_b64_tr_b16 v[22:23], v184 offset:39936
v_mfma_f32_16x16x32_bf16 a[112:115], v[36:39], v[84:87], a[112:115]
v_mfma_f32_16x16x32_bf16 a[112:115], v[32:35], v[68:71], a[112:115]
v_mfma_f32_16x16x32_bf16 a[112:115], v[28:31], v[52:55], a[112:115]
s_waitcnt lgkmcnt(2)
v_mfma_f32_16x16x32_bf16 a[128:131], v[8:11], v[52:55], a[128:131]
)"
R"(ds_read_b64_tr_b16 v[24:25], v184 offset:46080
ds_read_b64_tr_b16 v[26:27], v184 offset:52224
v_mfma_f32_16x16x32_bf16 a[128:131], v[12:15], v[68:71], a[128:131]
buffer_load_dwordx4 v250, %[srdS], s92 offen offset:2048 lds
v_mfma_f32_16x16x32_bf16 a[128:131], v[16:19], v[84:87], a[128:131]
v_mfma_f32_16x16x32_bf16 a[132:135], v[16:19], v[88:91], a[132:135]
v_mfma_f32_16x16x32_bf16 a[132:135], v[12:15], v[72:75], a[132:135]
ds_read_b64_tr_b16 v[28:29], v188 offset:5120
ds_read_b64_tr_b16 v[30:31], v188 offset:11264
v_mfma_f32_16x16x32_bf16 a[132:135], v[8:11], v[56:59], a[132:135]
v_mfma_f32_16x16x32_bf16 a[136:139], v[8:11], v[60:63], a[136:139]
s_add_u32 s92, s92, 40960
s_add_u32 m0, s81, 24576
v_mfma_f32_16x16x32_bf16 a[136:139], v[12:15], v[76:79], a[136:139]
buffer_load_dwordx4 v248, %[srdS], s92 offen offset:0 lds
v_mfma_f32_16x16x32_bf16 a[136:139], v[16:19], v[92:95], a[136:139]
s_waitcnt vmcnt(19)
s_barrier
ds_read_b64_tr_b16 v[32:33], v176 offset:16384
ds_read_b64_tr_b16 v[34:35], v176 offset:22528
v_mfma_f32_16x16x32_bf16 a[140:143], v[16:19], v[96:99], a[140:143]
v_mfma_f32_16x16x32_bf16 a[140:143], v[12:15], v[80:83], a[140:143]
v_mfma_f32_16x16x32_bf16 a[140:143], v[8:11], v[64:67], a[140:143]
s_waitcnt lgkmcnt(2)
v_mfma_f32_16x16x32_bf16 a[156:159], v[20:23], v[64:67], a[156:159]
ds_read_b64_tr_b16 v[36:37], v176 offset:28672
ds_read_b64_tr_b16 v[38:39], v176 offset:34816
buffer_load_dwordx4 v249, %[srdS], s92 offen offset:1024 lds
v_mfma_f32_16x16x32_bf16 a[156:159], v[24:27], v[80:83], a[156:159]
v_mfma_f32_16x16x32_bf16 a[156:159], v[28:31], v[96:99], a[156:159]
v_mfma_f32_16x16x32_bf16 a[152:155], v[28:31], v[92:95], a[152:155]
v_mfma_f32_16x16x32_bf16 a[152:155], v[24:27], v[76:79], a[152:155]
ds_read_b64_tr_b16 v[8:9], v176 offset:40960
ds_read_b64_tr_b16 v[10:11], v176 offset:47104
v_mfma_f32_16x16x32_bf16 a[152:155], v[20:23], v[60:63], a[152:155]
v_mfma_f32_16x16x32_bf16 a[148:151], v[20:23], v[56:59], a[148:151]
buffer_load_dwordx4 v250, %[srdS], s92 offen offset:2048 lds
v_mfma_f32_16x16x32_bf16 a[148:151], v[24:27], v[72:75], a[148:151]
v_mfma_f32_16x16x32_bf16 a[148:151], v[28:31], v[88:91], a[148:151]
ds_read_b64_tr_b16 v[12:13], v177 offset:16384
ds_read_b64_tr_b16 v[14:15], v177 offset:22528
v_mfma_f32_16x16x32_bf16 a[144:147], v[28:31], v[84:87], a[144:147]
v_mfma_f32_16x16x32_bf16 a[144:147], v[24:27], v[68:71], a[144:147]
s_add_u32 s92, s92, 40960
v_mfma_f32_16x16x32_bf16 a[144:147], v[20:23], v[52:55], a[144:147]
s_waitcnt lgkmcnt(2)
v_mfma_f32_16x16x32_bf16 a[0:3], v[32:35], v[100:103], a[0:3]
ds_read_b64_tr_b16 v[16:17], v177 offset:28672
ds_read_b64_tr_b16 v[18:19], v177 offset:34816
v_mfma_f32_16x16x32_bf16 a[0:3], v[36:39], v[116:119], a[0:3]
s_add_u32 s84, s87, 3
s_and_b32 s84, s84, 3
s_mul_i32 s84, s84, 786432
s_add_u32 s54, s50, s84
s_addc_u32 s55, s51, 0
v_mfma_f32_16x16x32_bf16 a[0:3], v[8:11], v[132:135], a[0:3]
global_load_dwordx4 a[208:211], v228, s[54:55] offset:1024 sc1
v_mfma_f32_16x16x32_bf16 a[4:7], v[8:11], v[136:139], a[4:7]
v_mfma_f32_16x16x32_bf16 a[4:7], v[36:39], v[120:123], a[4:7]
ds_read_b64_tr_b16 v[20:21], v177 offset:40960
ds_read_b64_tr_b16 v[22:23], v177 offset:47104
v_mfma_f32_16x16x32_bf16 a[4:7], v[32:35], v[104:107], a[4:7]
v_mfma_f32_16x16x32_bf16 a[8:11], v[32:35], v[108:111], a[8:11]
v_mfma_f32_16x16x32_bf16 a[8:11], v[36:39], v[124:127], a[8:11]
global_load_dwordx4 a[212:215], v229, s[54:55] offset:1024 sc1
v_mfma_f32_16x16x32_bf16 a[8:11], v[8:11], v[140:143], a[8:11]
ds_read_b64_tr_b16 v[24:25], v178 offset:16384
ds_read_b64_tr_b16 v[26:27], v178 offset:22528
v_mfma_f32_16x16x32_bf16 a[12:15], v[8:11], v[144:147], a[12:15]
v_mfma_f32_16x16x32_bf16 a[12:15], v[36:39], v[128:131], a[12:15]
v_mfma_f32_16x16x32_bf16 a[12:15], v[32:35], v[112:115], a[12:15]
s_waitcnt lgkmcnt(2)
v_mfma_f32_16x16x32_bf16 a[28:31], v[12:15], v[112:115], a[28:31]
ds_read_b64_tr_b16 v[28:29], v178 offset:28672
ds_read_b64_tr_b16 v[30:31], v178 offset:34816
v_mfma_f32_16x16x32_bf16 a[28:31], v[16:19], v[128:131], a[28:31]
global_load_dwordx4 a[216:219], v230, s[54:55] offset:1024 sc1
v_mfma_f32_16x16x32_bf16 a[28:31], v[20:23], v[144:147], a[28:31]
v_mfma_f32_16x16x32_bf16 a[24:27], v[20:23], v[140:143], a[24:27]
v_mfma_f32_16x16x32_bf16 a[24:27], v[16:19], v[124:127], a[24:27]
ds_read_b64_tr_b16 v[32:33], v178 offset:40960
ds_read_b64_tr_b16 v[34:35], v178 offset:47104
v_mfma_f32_16x16x32_bf16 a[24:27], v[12:15], v[108:111], a[24:27]
v_mfma_f32_16x16x32_bf16 a[20:23], v[12:15], v[104:107], a[20:23]
global_load_dwordx4 a[220:223], v231, s[54:55] offset:1024 sc1
v_mfma_f32_16x16x32_bf16 a[20:23], v[16:19], v[120:123], a[20:23]
v_mfma_f32_16x16x32_bf16 a[20:23], v[20:23], v[136:139], a[20:23]
ds_read_b64_tr_b16 v[36:37], v179 offset:16384
ds_read_b64_tr_b16 v[38:39], v179 offset:22528
v_mfma_f32_16x16x32_bf16 a[16:19], v[20:23], v[132:135], a[16:19]
v_mfma_f32_16x16x32_bf16 a[16:19], v[16:19], v[116:119], a[16:19]
v_mfma_f32_16x16x32_bf16 a[16:19], v[12:15], v[100:103], a[16:19]
s_waitcnt lgkmcnt(2)
v_mfma_f32_16x16x32_bf16 a[32:35], v[24:27], v[100:103], a[32:35]
ds_read_b64_tr_b16 v[8:9], v179 offset:28672
ds_read_b64_tr_b16 v[10:11], v179 offset:34816
global_load_dwordx4 a[224:227], v228, s[54:55] offset:2048 sc1
v_mfma_f32_16x16x32_bf16 a[32:35], v[28:31], v[116:119], a[32:35]
v_mfma_f32_16x16x32_bf16 a[32:35], v[32:35], v[132:135], a[32:35]
v_mfma_f32_16x16x32_bf16 a[36:39], v[32:35], v[136:139], a[36:39]
v_mfma_f32_16x16x32_bf16 a[36:39], v[28:31], v[120:123], a[36:39]
ds_read_b64_tr_b16 v[12:13], v179 offset:40960
ds_read_b64_tr_b16 v[14:15], v179 offset:47104
v_mfma_f32_16x16x32_bf16 a[36:39], v[24:27], v[104:107], a[36:39]
v_mfma_f32_16x16x32_bf16 a[40:43], v[24:27], v[108:111], a[40:43]
global_load_dwordx4 a[228:231], v229, s[54:55] offset:2048 sc1
v_mfma_f32_16x16x32_bf16 a[40:43], v[28:31], v[124:127], a[40:43]
v_mfma_f32_16x16x32_bf16 a[40:43], v[32:35], v[140:143], a[40:43]
ds_read_b64_tr_b16 v[16:17], v176 offset:17408
ds_read_b64_tr_b16 v[18:19], v176 offset:23552
v_mfma_f32_16x16x32_bf16 a[44:47], v[32:35], v[144:147], a[44:47]
v_mfma_f32_16x16x32_bf16 a[44:47], v[28:31], v[128:131], a[44:47]
v_mfma_f32_16x16x32_bf16 a[44:47], v[24:27], v[112:115], a[44:47]
global_load_dwordx4 a[232:235], v230, s[54:55] offset:2048 sc1
s_waitcnt lgkmcnt(2)
v_mfma_f32_16x16x32_bf16 a[60:63], v[36:39], v[112:115], a[60:63]
ds_read_b64_tr_b16 v[20:21], v176 offset:29696
ds_read_b64_tr_b16 v[22:23], v176 offset:35840
v_mfma_f32_16x16x32_bf16 a[60:63], v[8:11], v[128:131], a[60:63]
v_mfma_f32_16x16x32_bf16 a[60:63], v[12:15], v[144:147], a[60:63]
v_mfma_f32_16x16x32_bf16 a[56:59], v[12:15], v[140:143], a[56:59]
v_mfma_f32_16x16x32_bf16 a[56:59], v[8:11], v[124:127], a[56:59]
ds_read_b64_tr_b16 v[24:25], v176 offset:41984
ds_read_b64_tr_b16 v[26:27], v176 offset:48128
v_mfma_f32_16x16x32_bf16 a[56:59], v[36:39], v[108:111], a[56:59]
global_load_dwordx4 a[236:239], v231, s[54:55] offset:2048 sc1
v_mfma_f32_16x16x32_bf16 a[52:55], v[36:39], v[104:107], a[52:55]
v_mfma_f32_16x16x32_bf16 a[52:55], v[8:11], v[120:123], a[52:55]
v_mfma_f32_16x16x32_bf16 a[52:55], v[12:15], v[136:139], a[52:55]
ds_read_b64_tr_b16 v[28:29], v188 offset:16384
ds_read_b64_tr_b16 v[30:31], v188 offset:22528
v_mfma_f32_16x16x32_bf16 a[48:51], v[12:15], v[132:135], a[48:51]
v_mfma_f32_16x16x32_bf16 a[48:51], v[8:11], v[116:119], a[48:51]
global_load_dwordx4 a[240:243], v228, s[54:55] offset:3072 sc1
v_mfma_f32_16x16x32_bf16 a[48:51], v[36:39], v[100:103], a[48:51]
s_waitcnt lgkmcnt(2)
v_mfma_f32_16x16x32_bf16 a[64:67], v[16:19], v[100:103], a[64:67]
ds_read_b64_tr_b16 v[32:33], v188 offset:28672
ds_read_b64_tr_b16 v[34:35], v188 offset:34816
v_mfma_f32_16x16x32_bf16 a[64:67], v[20:23], v[116:119], a[64:67]
v_mfma_f32_16x16x32_bf16 a[64:67], v[24:27], v[132:135], a[64:67]
v_mfma_f32_16x16x32_bf16 a[68:71], v[24:27], v[136:139], a[68:71]
v_mfma_f32_16x16x32_bf16 a[68:71], v[20:23], v[120:123], a[68:71]
ds_read_b64_tr_b16 v[36:37], v188 offset:40960
ds_read_b64_tr_b16 v[38:39], v188 offset:47104
global_load_dwordx4 a[244:247], v229, s[54:55] offset:3072 sc1
v_mfma_f32_16x16x32_bf16 a[68:71], v[16:19], v[104:107], a[68:71]
v_mfma_f32_16x16x32_bf16 a[72:75], v[16:19], v[108:111], a[72:75]
v_mfma_f32_16x16x32_bf16 a[72:75], v[20:23], v[124:127], a[72:75]
v_mfma_f32_16x16x32_bf16 a[72:75], v[24:27], v[140:143], a[72:75]
ds_read_b64_tr_b16 v[8:9], v189 offset:16384
ds_read_b64_tr_b16 v[10:11], v189 offset:22528
v_mfma_f32_16x16x32_bf16 a[76:79], v[24:27], v[144:147], a[76:79]
v_mfma_f32_16x16x32_bf16 a[76:79], v[20:23], v[128:131], a[76:79]
global_load_dwordx4 a[248:251], v230, s[54:55] offset:3072 sc1
v_mfma_f32_16x16x32_bf16 a[76:79], v[16:19], v[112:115], a[76:79]
s_waitcnt lgkmcnt(2)
v_mfma_f32_16x16x32_bf16 a[92:95], v[28:31], v[112:115], a[92:95]
ds_read_b64_tr_b16 v[12:13], v189 offset:28672
ds_read_b64_tr_b16 v[14:15], v189 offset:34816
v_mfma_f32_16x16x32_bf16 a[92:95], v[32:35], v[128:131], a[92:95]
v_mfma_f32_16x16x32_bf16 a[92:95], v[36:39], v[144:147], a[92:95]
v_mfma_f32_16x16x32_bf16 a[88:91], v[36:39], v[140:143], a[88:91]
global_load_dwordx4 a[252:255], v231, s[54:55] offset:3072 sc1
v_mfma_f32_16x16x32_bf16 a[88:91], v[32:35], v[124:127], a[88:91]
ds_read_b64_tr_b16 v[16:17], v189 offset:40960
ds_read_b64_tr_b16 v[18:19], v189 offset:47104
v_mfma_f32_16x16x32_bf16 a[88:91], v[28:31], v[108:111], a[88:91]
v_mfma_f32_16x16x32_bf16 a[84:87], v[28:31], v[104:107], a[84:87]
v_mfma_f32_16x16x32_bf16 a[84:87], v[32:35], v[120:123], a[84:87]
v_mfma_f32_16x16x32_bf16 a[84:87], v[36:39], v[136:139], a[84:87]
ds_read_b64_tr_b16 v[20:21], v190 offset:16384
ds_read_b64_tr_b16 v[22:23], v190 offset:22528
s_add_u32 s84, s87, 3
s_and_b32 s84, s84, 3
s_mul_i32 s84, s84, 245760
s_mul_i32 s92, %[wid], 10240
s_add_u32 s92, s92, s84
s_add_u32 s92, s92, 122880
s_add_u32 m0, s81, 36864
v_mfma_f32_16x16x32_bf16 a[80:83], v[36:39], v[132:135], a[80:83]
buffer_load_dwordx4 v248, %[srdS], s92 offen offset:0 lds
v_mfma_f32_16x16x32_bf16 a[80:83], v[32:35], v[116:119], a[80:83]
v_mfma_f32_16x16x32_bf16 a[80:83], v[28:31], v[100:103], a[80:83]
s_waitcnt lgkmcnt(2)
v_mfma_f32_16x16x32_bf16 a[96:99], v[8:11], v[100:103], a[96:99]
ds_read_b64_tr_b16 v[24:25], v190 offset:28672
ds_read_b64_tr_b16 v[26:27], v190 offset:34816
v_mfma_f32_16x16x32_bf16 a[96:99], v[12:15], v[116:119], a[96:99]
v_mfma_f32_16x16x32_bf16 a[96:99], v[16:19], v[132:135], a[96:99]
v_mfma_f32_16x16x32_bf16 a[100:103], v[16:19], v[136:139], a[100:103]
buffer_load_dwordx4 v249, %[srdS], s92 offen offset:1024 lds
v_mfma_f32_16x16x32_bf16 a[100:103], v[12:15], v[120:123], a[100:103]
ds_read_b64_tr_b16 v[28:29], v190 offset:40960
ds_read_b64_tr_b16 v[30:31], v190 offset:47104
v_mfma_f32_16x16x32_bf16 a[100:103], v[8:11], v[104:107], a[100:103]
v_mfma_f32_16x16x32_bf16 a[104:107], v[8:11], v[108:111], a[104:107]
v_mfma_f32_16x16x32_bf16 a[104:107], v[12:15], v[124:127], a[104:107]
v_mfma_f32_16x16x32_bf16 a[104:107], v[16:19], v[140:143], a[104:107]
ds_read_b64_tr_b16 v[32:33], v191 offset:16384
)"
R"(ds_read_b64_tr_b16 v[34:35], v191 offset:22528
buffer_load_dwordx4 v250, %[srdS], s92 offen offset:2048 lds
v_mfma_f32_16x16x32_bf16 a[108:111], v[16:19], v[144:147], a[108:111]
v_mfma_f32_16x16x32_bf16 a[108:111], v[12:15], v[128:131], a[108:111]
v_mfma_f32_16x16x32_bf16 a[108:111], v[8:11], v[112:115], a[108:111]
s_waitcnt lgkmcnt(2)
v_mfma_f32_16x16x32_bf16 a[124:127], v[20:23], v[112:115], a[124:127]
ds_read_b64_tr_b16 v[36:37], v191 offset:28672
ds_read_b64_tr_b16 v[38:39], v191 offset:34816
v_mfma_f32_16x16x32_bf16 a[124:127], v[24:27], v[128:131], a[124:127]
s_add_u32 s92, s92, 40960
s_add_u32 m0, s81, 49152
v_mfma_f32_16x16x32_bf16 a[124:127], v[28:31], v[144:147], a[124:127]
buffer_load_dwordx4 v248, %[srdS], s92 offen offset:0 lds
v_mfma_f32_16x16x32_bf16 a[120:123], v[28:31], v[140:143], a[120:123]
v_mfma_f32_16x16x32_bf16 a[120:123], v[24:27], v[124:127], a[120:123]
ds_read_b64_tr_b16 v[8:9], v191 offset:40960
ds_read_b64_tr_b16 v[10:11], v191 offset:47104
v_mfma_f32_16x16x32_bf16 a[120:123], v[20:23], v[108:111], a[120:123]
v_mfma_f32_16x16x32_bf16 a[116:119], v[20:23], v[104:107], a[116:119]
global_load_dword v253, v194, s[62:63] offset:384 sc1
v_mfma_f32_16x16x32_bf16 a[116:119], v[24:27], v[120:123], a[116:119]
buffer_load_dwordx4 v249, %[srdS], s92 offen offset:1024 lds
v_mfma_f32_16x16x32_bf16 a[116:119], v[28:31], v[136:139], a[116:119]
ds_read_b64_tr_b16 v[12:13], v188 offset:17408
ds_read_b64_tr_b16 v[14:15], v188 offset:23552
v_mfma_f32_16x16x32_bf16 a[112:115], v[28:31], v[132:135], a[112:115]
v_mfma_f32_16x16x32_bf16 a[112:115], v[24:27], v[116:119], a[112:115]
v_mfma_f32_16x16x32_bf16 a[112:115], v[20:23], v[100:103], a[112:115]
s_waitcnt lgkmcnt(2)
v_mfma_f32_16x16x32_bf16 a[128:131], v[32:35], v[100:103], a[128:131]
ds_read_b64_tr_b16 v[16:17], v188 offset:29696
ds_read_b64_tr_b16 v[18:19], v188 offset:35840
v_mfma_f32_16x16x32_bf16 a[128:131], v[36:39], v[116:119], a[128:131]
buffer_load_dwordx4 v250, %[srdS], s92 offen offset:2048 lds
v_mfma_f32_16x16x32_bf16 a[128:131], v[8:11], v[132:135], a[128:131]
v_mfma_f32_16x16x32_bf16 a[132:135], v[8:11], v[136:139], a[132:135]
v_mfma_f32_16x16x32_bf16 a[132:135], v[36:39], v[120:123], a[132:135]
ds_read_b64_tr_b16 v[20:21], v188 offset:41984
ds_read_b64_tr_b16 v[22:23], v188 offset:48128
v_mfma_f32_16x16x32_bf16 a[132:135], v[32:35], v[104:107], a[132:135]
v_mfma_f32_16x16x32_bf16 a[136:139], v[32:35], v[108:111], a[136:139]
s_add_u32 s92, s92, 40960
s_add_u32 m0, s81, 61440
v_mfma_f32_16x16x32_bf16 a[136:139], v[36:39], v[124:127], a[136:139]
buffer_load_dwordx4 v248, %[srdS], s92 offen offset:0 lds
v_mfma_f32_16x16x32_bf16 a[136:139], v[8:11], v[140:143], a[136:139]
s_waitcnt vmcnt(20)
s_barrier
ds_read_b64_tr_b16 v[24:25], v168 offset:0
ds_read_b64_tr_b16 v[26:27], v168 offset:6144
v_mfma_f32_16x16x32_bf16 a[140:143], v[8:11], v[144:147], a[140:143]
v_mfma_f32_16x16x32_bf16 a[140:143], v[36:39], v[128:131], a[140:143]
v_mfma_f32_16x16x32_bf16 a[140:143], v[32:35], v[112:115], a[140:143]
s_waitcnt lgkmcnt(2)
v_mfma_f32_16x16x32_bf16 a[156:159], v[12:15], v[112:115], a[156:159]
ds_read_b64_tr_b16 v[28:29], v168 offset:12288
ds_read_b64_tr_b16 v[30:31], v168 offset:18432
buffer_load_dwordx4 v249, %[srdS], s92 offen offset:1024 lds
v_mfma_f32_16x16x32_bf16 a[156:159], v[16:19], v[128:131], a[156:159]
v_mfma_f32_16x16x32_bf16 a[156:159], v[20:23], v[144:147], a[156:159]
v_mfma_f32_16x16x32_bf16 a[152:155], v[20:23], v[140:143], a[152:155]
v_mfma_f32_16x16x32_bf16 a[152:155], v[16:19], v[124:127], a[152:155]
ds_read_b64_tr_b16 v[32:33], v168 offset:24576
ds_read_b64_tr_b16 v[34:35], v168 offset:30720
v_mfma_f32_16x16x32_bf16 a[152:155], v[12:15], v[108:111], a[152:155]
v_mfma_f32_16x16x32_bf16 a[148:151], v[12:15], v[104:107], a[148:151]
buffer_load_dwordx4 v250, %[srdS], s92 offen offset:2048 lds
v_mfma_f32_16x16x32_bf16 a[148:151], v[16:19], v[120:123], a[148:151]
v_mfma_f32_16x16x32_bf16 a[148:151], v[20:23], v[136:139], a[148:151]
ds_read_b64_tr_b16 v[36:37], v169 offset:0
ds_read_b64_tr_b16 v[38:39], v169 offset:6144
v_mfma_f32_16x16x32_bf16 a[144:147], v[20:23], v[132:135], a[144:147]
v_mfma_f32_16x16x32_bf16 a[144:147], v[16:19], v[116:119], a[144:147]
s_add_u32 s92, s92, 40960
v_mfma_f32_16x16x32_bf16 a[144:147], v[12:15], v[100:103], a[144:147]
v_mov_b32 v62, 0
v_mov_b32 v63, 0
v_mov_b32 v74, 0
v_mov_b32 v75, 0
v_mov_b32 v86, 0
v_mov_b32 v87, 0
v_mov_b32 v98, 0
v_mov_b32 v99, 0
v_mov_b32 v110, 0
v_mov_b32 v111, 0
v_mov_b32 v122, 0
v_mov_b32 v123, 0
v_mov_b32 v134, 0
v_mov_b32 v135, 0
s_mov_b32 s77, 1
s_waitcnt lgkmcnt(2)
v_mfma_f32_16x16x32_bf16 a[0:3], v[24:27], a[160:163], a[0:3]
ds_read_b64_tr_b16 v[8:9], v169 offset:12288
ds_read_b64_tr_b16 v[10:11], v169 offset:18432
v_mfma_f32_16x16x32_bf16 a[0:3], v[28:31], a[176:179], a[0:3]
v_mfma_f32_16x16x32_bf16 a[0:3], v[32:35], a[192:195], a[0:3]
s_waitcnt vmcnt(5)
v_cmp_eq_u32 vcc, s96, v253
s_nop 1
s_andn2_b32 s84, vcc_lo, s77
s_and_b32 s84, s84, 2
s_cbranch_scc0 .Lga0_end
s_or_b32 s77, s77, s84
s_bitcmp1_b32 s84, 1
s_cbranch_scc0 .Lga0_1
s_add_u32 s48, s60, 12288
s_addc_u32 s49, s61, 0
global_load_dwordx4 v[52:55], %[lane16], s[48:49] offset:0 sc1
global_load_dwordx4 v[56:59], %[lane16], s[48:49] offset:1024 sc1
global_load_dwordx2 v[60:61], v192, s[48:49] offset:2048 sc1
.Lga0_1:
.Lga0_end:
v_mfma_f32_16x16x32_bf16 a[4:7], v[32:35], a[196:199], a[4:7]
v_mfma_f32_16x16x32_bf16 a[4:7], v[28:31], a[180:183], a[4:7]
ds_read_b64_tr_b16 v[12:13], v169 offset:24576
ds_read_b64_tr_b16 v[14:15], v169 offset:30720
v_mfma_f32_16x16x32_bf16 a[4:7], v[24:27], a[164:167], a[4:7]
v_mfma_f32_16x16x32_bf16 a[8:11], v[24:27], a[168:171], a[8:11]
v_mfma_f32_16x16x32_bf16 a[8:11], v[28:31], a[184:187], a[8:11]
v_mfma_f32_16x16x32_bf16 a[8:11], v[32:35], a[200:203], a[8:11]
ds_read_b64_tr_b16 v[16:17], v170 offset:0
ds_read_b64_tr_b16 v[18:19], v170 offset:6144
v_mfma_f32_16x16x32_bf16 a[12:15], v[32:35], a[204:207], a[12:15]
v_mfma_f32_16x16x32_bf16 a[12:15], v[28:31], a[188:191], a[12:15]
v_mfma_f32_16x16x32_bf16 a[12:15], v[24:27], a[172:175], a[12:15]
s_waitcnt lgkmcnt(2)
v_mfma_f32_16x16x32_bf16 a[28:31], v[36:39], a[172:175], a[28:31]
ds_read_b64_tr_b16 v[20:21], v170 offset:12288
ds_read_b64_tr_b16 v[22:23], v170 offset:18432
v_mfma_f32_16x16x32_bf16 a[28:31], v[8:11], a[188:191], a[28:31]
v_mfma_f32_16x16x32_bf16 a[28:31], v[12:15], a[204:207], a[28:31]
v_mfma_f32_16x16x32_bf16 a[24:27], v[12:15], a[200:203], a[24:27]
v_mfma_f32_16x16x32_bf16 a[24:27], v[8:11], a[184:187], a[24:27]
ds_read_b64_tr_b16 v[24:25], v170 offset:24576
ds_read_b64_tr_b16 v[26:27], v170 offset:30720
v_cmp_eq_u32 vcc, s96, v253
s_nop 1
s_andn2_b32 s84, vcc_lo, s77
s_and_b32 s84, s84, 4
s_cbranch_scc0 .Lga1_end
s_or_b32 s77, s77, s84
s_bitcmp1_b32 s84, 2
s_cbranch_scc0 .Lga1_2
s_add_u32 s48, s60, 24576
s_addc_u32 s49, s61, 0
global_load_dwordx4 v[64:67], %[lane16], s[48:49] offset:0 sc1
global_load_dwordx4 v[68:71], %[lane16], s[48:49] offset:1024 sc1
global_load_dwordx2 v[72:73], v192, s[48:49] offset:2048 sc1
.Lga1_2:
.Lga1_end:
v_mfma_f32_16x16x32_bf16 a[24:27], v[36:39], a[168:171], a[24:27]
v_mfma_f32_16x16x32_bf16 a[20:23], v[36:39], a[164:167], a[20:23]
v_mfma_f32_16x16x32_bf16 a[20:23], v[8:11], a[180:183], a[20:23]
v_mfma_f32_16x16x32_bf16 a[20:23], v[12:15], a[196:199], a[20:23]
ds_read_b64_tr_b16 v[28:29], v171 offset:0
ds_read_b64_tr_b16 v[30:31], v171 offset:6144
v_mfma_f32_16x16x32_bf16 a[16:19], v[12:15], a[192:195], a[16:19]
v_mfma_f32_16x16x32_bf16 a[16:19], v[8:11], a[176:179], a[16:19]
v_mfma_f32_16x16x32_bf16 a[16:19], v[36:39], a[160:163], a[16:19]
s_waitcnt lgkmcnt(2)
v_mfma_f32_16x16x32_bf16 a[32:35], v[16:19], a[160:163], a[32:35]
ds_read_b64_tr_b16 v[32:33], v171 offset:12288
ds_read_b64_tr_b16 v[34:35], v171 offset:18432
v_mfma_f32_16x16x32_bf16 a[32:35], v[20:23], a[176:179], a[32:35]
v_mfma_f32_16x16x32_bf16 a[32:35], v[24:27], a[192:195], a[32:35]
v_mfma_f32_16x16x32_bf16 a[36:39], v[24:27], a[196:199], a[36:39]
v_mfma_f32_16x16x32_bf16 a[36:39], v[20:23], a[180:183], a[36:39]
ds_read_b64_tr_b16 v[36:37], v171 offset:24576
ds_read_b64_tr_b16 v[38:39], v171 offset:30720
v_mfma_f32_16x16x32_bf16 a[36:39], v[16:19], a[164:167], a[36:39]
v_mfma_f32_16x16x32_bf16 a[40:43], v[16:19], a[168:171], a[40:43]
v_cmp_eq_u32 vcc, s96, v253
s_nop 1
s_andn2_b32 s84, vcc_lo, s77
s_and_b32 s84, s84, 8
s_cbranch_scc0 .Lga2_end
s_or_b32 s77, s77, s84
s_bitcmp1_b32 s84, 3
s_cbranch_scc0 .Lga2_3
s_add_u32 s48, s60, 36864
s_addc_u32 s49, s61, 0
global_load_dwordx4 v[76:79], %[lane16], s[48:49] offset:0 sc1
global_load_dwordx4 v[80:83], %[lane16], s[48:49] offset:1024 sc1
global_load_dwordx2 v[84:85], v192, s[48:49] offset:2048 sc1
.Lga2_3:
.Lga2_end:
v_mfma_f32_16x16x32_bf16 a[40:43], v[20:23], a[184:187], a[40:43]
v_mfma_f32_16x16x32_bf16 a[40:43], v[24:27], a[200:203], a[40:43]
ds_read_b64_tr_b16 v[8:9], v168 offset:1024
ds_read_b64_tr_b16 v[10:11], v168 offset:7168
v_mfma_f32_16x16x32_bf16 a[44:47], v[24:27], a[204:207], a[44:47]
v_mfma_f32_16x16x32_bf16 a[44:47], v[20:23], a[188:191], a[44:47]
v_mfma_f32_16x16x32_bf16 a[44:47], v[16:19], a[172:175], a[44:47]
s_waitcnt lgkmcnt(2)
v_mfma_f32_16x16x32_bf16 a[60:63], v[28:31], a[172:175], a[60:63]
ds_read_b64_tr_b16 v[12:13], v168 offset:13312
ds_read_b64_tr_b16 v[14:15], v168 offset:19456
v_mfma_f32_16x16x32_bf16 a[60:63], v[32:35], a[188:191], a[60:63]
v_mfma_f32_16x16x32_bf16 a[60:63], v[36:39], a[204:207], a[60:63]
)"
R"(v_mfma_f32_16x16x32_bf16 a[56:59], v[36:39], a[200:203], a[56:59]
v_mfma_f32_16x16x32_bf16 a[56:59], v[32:35], a[184:187], a[56:59]
ds_read_b64_tr_b16 v[16:17], v168 offset:25600
ds_read_b64_tr_b16 v[18:19], v168 offset:31744
v_mfma_f32_16x16x32_bf16 a[56:59], v[28:31], a[168:171], a[56:59]
v_mfma_f32_16x16x32_bf16 a[52:55], v[28:31], a[164:167], a[52:55]
v_mfma_f32_16x16x32_bf16 a[52:55], v[32:35], a[180:183], a[52:55]
v_mfma_f32_16x16x32_bf16 a[52:55], v[36:39], a[196:199], a[52:55]
ds_read_b64_tr_b16 v[20:21], v180 offset:0
ds_read_b64_tr_b16 v[22:23], v180 offset:6144
v_cmp_eq_u32 vcc, s96, v253
s_nop 1
s_andn2_b32 s84, vcc_lo, s77
s_and_b32 s84, s84, 16
s_cbranch_scc0 .Lga3_end
s_or_b32 s77, s77, s84
s_bitcmp1_b32 s84, 4
s_cbranch_scc0 .Lga3_4
s_add_u32 s48, s60, 49152
s_addc_u32 s49, s61, 0
global_load_dwordx4 v[88:91], %[lane16], s[48:49] offset:0 sc1
global_load_dwordx4 v[92:95], %[lane16], s[48:49] offset:1024 sc1
global_load_dwordx2 v[96:97], v192, s[48:49] offset:2048 sc1
.Lga3_4:
.Lga3_end:
v_mfma_f32_16x16x32_bf16 a[48:51], v[36:39], a[192:195], a[48:51]
v_mfma_f32_16x16x32_bf16 a[48:51], v[32:35], a[176:179], a[48:51]
v_mfma_f32_16x16x32_bf16 a[48:51], v[28:31], a[160:163], a[48:51]
s_waitcnt lgkmcnt(2)
v_mfma_f32_16x16x32_bf16 a[64:67], v[8:11], a[160:163], a[64:67]
ds_read_b64_tr_b16 v[24:25], v180 offset:12288
ds_read_b64_tr_b16 v[26:27], v180 offset:18432
v_mfma_f32_16x16x32_bf16 a[64:67], v[12:15], a[176:179], a[64:67]
v_mfma_f32_16x16x32_bf16 a[64:67], v[16:19], a[192:195], a[64:67]
v_mfma_f32_16x16x32_bf16 a[68:71], v[16:19], a[196:199], a[68:71]
v_mfma_f32_16x16x32_bf16 a[68:71], v[12:15], a[180:183], a[68:71]
ds_read_b64_tr_b16 v[28:29], v180 offset:24576
ds_read_b64_tr_b16 v[30:31], v180 offset:30720
v_mfma_f32_16x16x32_bf16 a[68:71], v[8:11], a[164:167], a[68:71]
v_mfma_f32_16x16x32_bf16 a[72:75], v[8:11], a[168:171], a[72:75]
v_mfma_f32_16x16x32_bf16 a[72:75], v[12:15], a[184:187], a[72:75]
v_mfma_f32_16x16x32_bf16 a[72:75], v[16:19], a[200:203], a[72:75]
ds_read_b64_tr_b16 v[32:33], v181 offset:0
ds_read_b64_tr_b16 v[34:35], v181 offset:6144
v_mfma_f32_16x16x32_bf16 a[76:79], v[16:19], a[204:207], a[76:79]
v_mfma_f32_16x16x32_bf16 a[76:79], v[12:15], a[188:191], a[76:79]
v_cmp_eq_u32 vcc, s96, v253
s_nop 1
s_andn2_b32 s84, vcc_lo, s77
s_and_b32 s84, s84, 32
s_cbranch_scc0 .Lga4_end
s_or_b32 s77, s77, s84
s_bitcmp1_b32 s84, 5
s_cbranch_scc0 .Lga4_5
s_add_u32 s48, s60, 61440
s_addc_u32 s49, s61, 0
global_load_dwordx4 v[100:103], %[lane16], s[48:49] offset:0 sc1
global_load_dwordx4 v[104:107], %[lane16], s[48:49] offset:1024 sc1
global_load_dwordx2 v[108:109], v192, s[48:49] offset:2048 sc1
.Lga4_5:
.Lga4_end:
v_mfma_f32_16x16x32_bf16 a[76:79], v[8:11], a[172:175], a[76:79]
s_waitcnt lgkmcnt(2)
v_mfma_f32_16x16x32_bf16 a[92:95], v[20:23], a[172:175], a[92:95]
ds_read_b64_tr_b16 v[36:37], v181 offset:12288
ds_read_b64_tr_b16 v[38:39], v181 offset:18432
v_mfma_f32_16x16x32_bf16 a[92:95], v[24:27], a[188:191], a[92:95]
v_mfma_f32_16x16x32_bf16 a[92:95], v[28:31], a[204:207], a[92:95]
v_mfma_f32_16x16x32_bf16 a[88:91], v[28:31], a[200:203], a[88:91]
v_mfma_f32_16x16x32_bf16 a[88:91], v[24:27], a[184:187], a[88:91]
ds_read_b64_tr_b16 v[8:9], v181 offset:24576
ds_read_b64_tr_b16 v[10:11], v181 offset:30720
v_mfma_f32_16x16x32_bf16 a[88:91], v[20:23], a[168:171], a[88:91]
v_mfma_f32_16x16x32_bf16 a[84:87], v[20:23], a[164:167], a[84:87]
v_mfma_f32_16x16x32_bf16 a[84:87], v[24:27], a[180:183], a[84:87]
v_mfma_f32_16x16x32_bf16 a[84:87], v[28:31], a[196:199], a[84:87]
ds_read_b64_tr_b16 v[12:13], v182 offset:0
ds_read_b64_tr_b16 v[14:15], v182 offset:6144
v_mfma_f32_16x16x32_bf16 a[80:83], v[28:31], a[192:195], a[80:83]
v_mfma_f32_16x16x32_bf16 a[80:83], v[24:27], a[176:179], a[80:83]
v_mfma_f32_16x16x32_bf16 a[80:83], v[20:23], a[160:163], a[80:83]
s_waitcnt lgkmcnt(2)
v_mfma_f32_16x16x32_bf16 a[96:99], v[32:35], a[160:163], a[96:99]
ds_read_b64_tr_b16 v[16:17], v182 offset:12288
ds_read_b64_tr_b16 v[18:19], v182 offset:18432
v_cmp_eq_u32 vcc, s96, v253
s_nop 1
s_andn2_b32 s84, vcc_lo, s77
s_and_b32 s84, s84, 64
s_cbranch_scc0 .Lga5_end
s_or_b32 s77, s77, s84
s_bitcmp1_b32 s84, 6
s_cbranch_scc0 .Lga5_6
s_add_u32 s48, s60, 73728
s_addc_u32 s49, s61, 0
global_load_dwordx4 v[112:115], %[lane16], s[48:49] offset:0 sc1
global_load_dwordx4 v[116:119], %[lane16], s[48:49] offset:1024 sc1
global_load_dwordx2 v[120:121], v192, s[48:49] offset:2048 sc1
.Lga5_6:
.Lga5_end:
v_mfma_f32_16x16x32_bf16 a[96:99], v[36:39], a[176:179], a[96:99]
v_mfma_f32_16x16x32_bf16 a[96:99], v[8:11], a[192:195], a[96:99]
v_mfma_f32_16x16x32_bf16 a[100:103], v[8:11], a[196:199], a[100:103]
v_mfma_f32_16x16x32_bf16 a[100:103], v[36:39], a[180:183], a[100:103]
ds_read_b64_tr_b16 v[20:21], v182 offset:24576
ds_read_b64_tr_b16 v[22:23], v182 offset:30720
v_mfma_f32_16x16x32_bf16 a[100:103], v[32:35], a[164:167], a[100:103]
v_mfma_f32_16x16x32_bf16 a[104:107], v[32:35], a[168:171], a[104:107]
v_mfma_f32_16x16x32_bf16 a[104:107], v[36:39], a[184:187], a[104:107]
v_mfma_f32_16x16x32_bf16 a[104:107], v[8:11], a[200:203], a[104:107]
ds_read_b64_tr_b16 v[24:25], v183 offset:0
ds_read_b64_tr_b16 v[26:27], v183 offset:6144
v_mfma_f32_16x16x32_bf16 a[108:111], v[8:11], a[204:207], a[108:111]
v_mfma_f32_16x16x32_bf16 a[108:111], v[36:39], a[188:191], a[108:111]
v_mfma_f32_16x16x32_bf16 a[108:111], v[32:35], a[172:175], a[108:111]
s_waitcnt lgkmcnt(2)
v_mfma_f32_16x16x32_bf16 a[124:127], v[12:15], a[172:175], a[124:127]
ds_read_b64_tr_b16 v[28:29], v183 offset:12288
ds_read_b64_tr_b16 v[30:31], v183 offset:18432
v_mfma_f32_16x16x32_bf16 a[124:127], v[16:19], a[188:191], a[124:127]
v_mfma_f32_16x16x32_bf16 a[124:127], v[20:23], a[204:207], a[124:127]
v_cmp_eq_u32 vcc, s96, v253
s_nop 1
s_andn2_b32 s84, vcc_lo, s77
s_and_b32 s84, s84, 128
s_cbranch_scc0 .Lga6_end
s_or_b32 s77, s77, s84
s_bitcmp1_b32 s84, 7
s_cbranch_scc0 .Lga6_7
s_add_u32 s48, s60, 86016
s_addc_u32 s49, s61, 0
global_load_dwordx4 v[124:127], %[lane16], s[48:49] offset:0 sc1
global_load_dwordx4 v[128:131], %[lane16], s[48:49] offset:1024 sc1
global_load_dwordx2 v[132:133], v192, s[48:49] offset:2048 sc1
.Lga6_7:
.Lga6_end:
v_mfma_f32_16x16x32_bf16 a[120:123], v[20:23], a[200:203], a[120:123]
v_mfma_f32_16x16x32_bf16 a[120:123], v[16:19], a[184:187], a[120:123]
ds_read_b64_tr_b16 v[32:33], v183 offset:24576
ds_read_b64_tr_b16 v[34:35], v183 offset:30720
v_mfma_f32_16x16x32_bf16 a[120:123], v[12:15], a[168:171], a[120:123]
v_mfma_f32_16x16x32_bf16 a[116:119], v[12:15], a[164:167], a[116:119]
v_mfma_f32_16x16x32_bf16 a[116:119], v[16:19], a[180:183], a[116:119]
v_mfma_f32_16x16x32_bf16 a[116:119], v[20:23], a[196:199], a[116:119]
ds_read_b64_tr_b16 v[36:37], v180 offset:1024
ds_read_b64_tr_b16 v[38:39], v180 offset:7168
v_mfma_f32_16x16x32_bf16 a[112:115], v[20:23], a[192:195], a[112:115]
v_mfma_f32_16x16x32_bf16 a[112:115], v[16:19], a[176:179], a[112:115]
v_mfma_f32_16x16x32_bf16 a[112:115], v[12:15], a[160:163], a[112:115]
s_waitcnt lgkmcnt(2)
v_mfma_f32_16x16x32_bf16 a[128:131], v[24:27], a[160:163], a[128:131]
ds_read_b64_tr_b16 v[8:9], v180 offset:13312
ds_read_b64_tr_b16 v[10:11], v180 offset:19456
global_load_dword v253, v194, s[62:63] offset:384 sc1
v_mfma_f32_16x16x32_bf16 a[128:131], v[28:31], a[176:179], a[128:131]
v_mfma_f32_16x16x32_bf16 a[128:131], v[32:35], a[192:195], a[128:131]
v_mfma_f32_16x16x32_bf16 a[132:135], v[32:35], a[196:199], a[132:135]
v_mfma_f32_16x16x32_bf16 a[132:135], v[28:31], a[180:183], a[132:135]
ds_read_b64_tr_b16 v[12:13], v180 offset:25600
ds_read_b64_tr_b16 v[14:15], v180 offset:31744
v_mfma_f32_16x16x32_bf16 a[132:135], v[24:27], a[164:167], a[132:135]
v_mfma_f32_16x16x32_bf16 a[136:139], v[24:27], a[168:171], a[136:139]
v_mfma_f32_16x16x32_bf16 a[136:139], v[28:31], a[184:187], a[136:139]
v_mfma_f32_16x16x32_bf16 a[136:139], v[32:35], a[200:203], a[136:139]
s_waitcnt vmcnt(1)
s_barrier
ds_read_b64_tr_b16 v[16:17], v180 offset:36864
ds_read_b64_tr_b16 v[18:19], v180 offset:43008
v_mfma_f32_16x16x32_bf16 a[140:143], v[32:35], a[204:207], a[140:143]
v_mfma_f32_16x16x32_bf16 a[140:143], v[28:31], a[188:191], a[140:143]
v_mfma_f32_16x16x32_bf16 a[140:143], v[24:27], a[172:175], a[140:143]
s_waitcnt lgkmcnt(2)
v_mfma_f32_16x16x32_bf16 a[156:159], v[36:39], a[172:175], a[156:159]
ds_read_b64_tr_b16 v[20:21], v180 offset:49152
ds_read_b64_tr_b16 v[22:23], v180 offset:55296
v_mfma_f32_16x16x32_bf16 a[156:159], v[8:11], a[188:191], a[156:159]
v_mfma_f32_16x16x32_bf16 a[156:159], v[12:15], a[204:207], a[156:159]
v_mfma_f32_16x16x32_bf16 a[152:155], v[12:15], a[200:203], a[152:155]
v_mfma_f32_16x16x32_bf16 a[152:155], v[8:11], a[184:187], a[152:155]
ds_read_b64_tr_b16 v[24:25], v184 offset:8192
ds_read_b64_tr_b16 v[26:27], v184 offset:14336
v_mfma_f32_16x16x32_bf16 a[152:155], v[36:39], a[168:171], a[152:155]
v_mfma_f32_16x16x32_bf16 a[148:151], v[36:39], a[164:167], a[148:151]
v_mfma_f32_16x16x32_bf16 a[148:151], v[8:11], a[180:183], a[148:151]
v_mfma_f32_16x16x32_bf16 a[148:151], v[12:15], a[196:199], a[148:151]
ds_read_b64_tr_b16 v[28:29], v181 offset:36864
ds_read_b64_tr_b16 v[30:31], v181 offset:43008
v_mfma_f32_16x16x32_bf16 a[144:147], v[12:15], a[192:195], a[144:147]
v_mfma_f32_16x16x32_bf16 a[144:147], v[8:11], a[176:179], a[144:147]
v_mfma_f32_16x16x32_bf16 a[144:147], v[36:39], a[160:163], a[144:147]
s_waitcnt lgkmcnt(2)
v_mfma_f32_16x16x32_bf16 a[80:83], v[16:19], a[208:211], a[80:83]
ds_read_b64_tr_b16 v[32:33], v181 offset:49152
ds_read_b64_tr_b16 v[34:35], v181 offset:55296
v_mfma_f32_16x16x32_bf16 a[80:83], v[20:23], a[224:227], a[80:83]
v_mfma_f32_16x16x32_bf16 a[80:83], v[24:27], a[240:243], a[80:83]
s_waitcnt vmcnt(0)
v_cmp_eq_u32 vcc, s96, v253
s_nop 1
s_andn2_b32 s84, vcc_lo, s77
)"
R"(s_and_b32 s84, s84, 2
s_cbranch_scc0 .Lga7_end
s_or_b32 s77, s77, s84
s_bitcmp1_b32 s84, 1
s_cbranch_scc0 .Lga7_1
s_add_u32 s48, s60, 12288
s_addc_u32 s49, s61, 0
global_load_dwordx4 v[52:55], %[lane16], s[48:49] offset:0 sc1
global_load_dwordx4 v[56:59], %[lane16], s[48:49] offset:1024 sc1
global_load_dwordx2 v[60:61], v192, s[48:49] offset:2048 sc1
.Lga7_1:
.Lga7_end:
v_mfma_f32_16x16x32_bf16 a[84:87], v[24:27], a[244:247], a[84:87]
v_mfma_f32_16x16x32_bf16 a[84:87], v[20:23], a[228:231], a[84:87]
ds_read_b64_tr_b16 v[36:37], v185 offset:8192
ds_read_b64_tr_b16 v[38:39], v185 offset:14336
v_mfma_f32_16x16x32_bf16 a[84:87], v[16:19], a[212:215], a[84:87]
v_cmp_eq_u32 vcc, s96, v253
s_nop 1
s_andn2_b32 s84, vcc_lo, s77
s_and_b32 s84, s84, 4
s_cbranch_scc0 .Lga8_end
s_or_b32 s77, s77, s84
s_bitcmp1_b32 s84, 2
s_cbranch_scc0 .Lga8_2
s_add_u32 s48, s60, 24576
s_addc_u32 s49, s61, 0
global_load_dwordx4 v[64:67], %[lane16], s[48:49] offset:0 sc1
global_load_dwordx4 v[68:71], %[lane16], s[48:49] offset:1024 sc1
global_load_dwordx2 v[72:73], v192, s[48:49] offset:2048 sc1
.Lga8_2:
.Lga8_end:
v_mfma_f32_16x16x32_bf16 a[88:91], v[16:19], a[216:219], a[88:91]
v_mfma_f32_16x16x32_bf16 a[88:91], v[20:23], a[232:235], a[88:91]
v_mfma_f32_16x16x32_bf16 a[88:91], v[24:27], a[248:251], a[88:91]
ds_read_b64_tr_b16 v[8:9], v182 offset:36864
ds_read_b64_tr_b16 v[10:11], v182 offset:43008
v_cmp_eq_u32 vcc, s96, v253
s_nop 1
s_andn2_b32 s84, vcc_lo, s77
s_and_b32 s84, s84, 8
s_cbranch_scc0 .Lga9_end
s_or_b32 s77, s77, s84
s_bitcmp1_b32 s84, 3
s_cbranch_scc0 .Lga9_3
s_add_u32 s48, s60, 36864
s_addc_u32 s49, s61, 0
global_load_dwordx4 v[76:79], %[lane16], s[48:49] offset:0 sc1
global_load_dwordx4 v[80:83], %[lane16], s[48:49] offset:1024 sc1
global_load_dwordx2 v[84:85], v192, s[48:49] offset:2048 sc1
.Lga9_3:
.Lga9_end:
v_mfma_f32_16x16x32_bf16 a[92:95], v[24:27], a[252:255], a[92:95]
v_mfma_f32_16x16x32_bf16 a[92:95], v[20:23], a[236:239], a[92:95]
v_mfma_f32_16x16x32_bf16 a[92:95], v[16:19], a[220:223], a[92:95]
v_cmp_eq_u32 vcc, s96, v253
s_nop 1
s_andn2_b32 s84, vcc_lo, s77
s_and_b32 s84, s84, 16
s_cbranch_scc0 .Lga10_end
s_or_b32 s77, s77, s84
s_bitcmp1_b32 s84, 4
s_cbranch_scc0 .Lga10_4
s_add_u32 s48, s60, 49152
s_addc_u32 s49, s61, 0
global_load_dwordx4 v[88:91], %[lane16], s[48:49] offset:0 sc1
global_load_dwordx4 v[92:95], %[lane16], s[48:49] offset:1024 sc1
global_load_dwordx2 v[96:97], v192, s[48:49] offset:2048 sc1
.Lga10_4:
.Lga10_end:
s_waitcnt lgkmcnt(2)
v_mfma_f32_16x16x32_bf16 a[108:111], v[28:31], a[220:223], a[108:111]
ds_read_b64_tr_b16 v[12:13], v182 offset:49152
ds_read_b64_tr_b16 v[14:15], v182 offset:55296
v_mfma_f32_16x16x32_bf16 a[108:111], v[32:35], a[236:239], a[108:111]
v_mfma_f32_16x16x32_bf16 a[108:111], v[36:39], a[252:255], a[108:111]
v_cmp_eq_u32 vcc, s96, v253
s_nop 1
s_andn2_b32 s84, vcc_lo, s77
s_and_b32 s84, s84, 32
s_cbranch_scc0 .Lga11_end
s_or_b32 s77, s77, s84
s_bitcmp1_b32 s84, 5
s_cbranch_scc0 .Lga11_5
s_add_u32 s48, s60, 61440
s_addc_u32 s49, s61, 0
global_load_dwordx4 v[100:103], %[lane16], s[48:49] offset:0 sc1
global_load_dwordx4 v[104:107], %[lane16], s[48:49] offset:1024 sc1
global_load_dwordx2 v[108:109], v192, s[48:49] offset:2048 sc1
.Lga11_5:
.Lga11_end:
v_mfma_f32_16x16x32_bf16 a[104:107], v[36:39], a[248:251], a[104:107]
v_mfma_f32_16x16x32_bf16 a[104:107], v[32:35], a[232:235], a[104:107]
ds_read_b64_tr_b16 v[16:17], v186 offset:8192
ds_read_b64_tr_b16 v[18:19], v186 offset:14336
v_mfma_f32_16x16x32_bf16 a[104:107], v[28:31], a[216:219], a[104:107]
v_cmp_eq_u32 vcc, s96, v253
s_nop 1
s_andn2_b32 s84, vcc_lo, s77
s_and_b32 s84, s84, 64
s_cbranch_scc0 .Lga12_end
s_or_b32 s77, s77, s84
s_bitcmp1_b32 s84, 6
s_cbranch_scc0 .Lga12_6
s_add_u32 s48, s60, 73728
s_addc_u32 s49, s61, 0
global_load_dwordx4 v[112:115], %[lane16], s[48:49] offset:0 sc1
global_load_dwordx4 v[116:119], %[lane16], s[48:49] offset:1024 sc1
global_load_dwordx2 v[120:121], v192, s[48:49] offset:2048 sc1
.Lga12_6:
.Lga12_end:
v_mfma_f32_16x16x32_bf16 a[100:103], v[28:31], a[212:215], a[100:103]
v_mfma_f32_16x16x32_bf16 a[100:103], v[32:35], a[228:231], a[100:103]
v_mfma_f32_16x16x32_bf16 a[100:103], v[36:39], a[244:247], a[100:103]
ds_read_b64_tr_b16 v[20:21], v183 offset:36864
ds_read_b64_tr_b16 v[22:23], v183 offset:43008
v_cmp_eq_u32 vcc, s96, v253
s_nop 1
s_andn2_b32 s84, vcc_lo, s77
s_and_b32 s84, s84, 128
s_cbranch_scc0 .Lga13_end
s_or_b32 s77, s77, s84
s_bitcmp1_b32 s84, 7
s_cbranch_scc0 .Lga13_7
s_add_u32 s48, s60, 86016
s_addc_u32 s49, s61, 0
global_load_dwordx4 v[124:127], %[lane16], s[48:49] offset:0 sc1
global_load_dwordx4 v[128:131], %[lane16], s[48:49] offset:1024 sc1
global_load_dwordx2 v[132:133], v192, s[48:49] offset:2048 sc1
.Lga13_7:
.Lga13_end:
v_mfma_f32_16x16x32_bf16 a[96:99], v[36:39], a[240:243], a[96:99]
v_mfma_f32_16x16x32_bf16 a[96:99], v[32:35], a[224:227], a[96:99]
v_mfma_f32_16x16x32_bf16 a[96:99], v[28:31], a[208:211], a[96:99]
s_waitcnt lgkmcnt(2)
v_mfma_f32_16x16x32_bf16 a[112:115], v[8:11], a[208:211], a[112:115]
ds_read_b64_tr_b16 v[24:25], v183 offset:49152
ds_read_b64_tr_b16 v[26:27], v183 offset:55296
v_mfma_f32_16x16x32_bf16 a[112:115], v[12:15], a[224:227], a[112:115]
v_mfma_f32_16x16x32_bf16 a[112:115], v[16:19], a[240:243], a[112:115]
v_mfma_f32_16x16x32_bf16 a[116:119], v[16:19], a[244:247], a[116:119]
v_accvgpr_read_b32 v196, a80
v_accvgpr_read_b32 v197, a81
v_accvgpr_read_b32 v198, a82
v_accvgpr_read_b32 v199, a83
v_mfma_f32_16x16x32_bf16 a[116:119], v[12:15], a[228:231], a[116:119]
ds_read_b64_tr_b16 v[28:29], v187 offset:8192
ds_read_b64_tr_b16 v[30:31], v187 offset:14336
v_cvt_pk_bf16_f32 v136, v196, v197
v_cvt_pk_bf16_f32 v137, v198, v199
v_accvgpr_read_b32 v200, a96
v_accvgpr_read_b32 v201, a97
v_mfma_f32_16x16x32_bf16 a[116:119], v[8:11], a[212:215], a[116:119]
v_accvgpr_read_b32 v202, a98
v_accvgpr_read_b32 v203, a99
v_cvt_pk_bf16_f32 v138, v200, v201
v_cvt_pk_bf16_f32 v139, v202, v203
v_mfma_f32_16x16x32_bf16 a[120:123], v[8:11], a[216:219], a[120:123]
s_xor_b32 s83, s86, 4
s_lshl_b32 s83, s83, 3
s_add_u32 s83, s83, 4
s_mul_i32 s83, s83, 12288
s_add_u32 s83, s83, 0
s_add_u32 s48, s58, s83
s_addc_u32 s49, s59, 0
global_store_dwordx4 %[lane16], v[136:139], s[48:49] offset:0 sc1
v_accvgpr_read_b32 v196, a84
v_accvgpr_read_b32 v197, a85
v_accvgpr_read_b32 v198, a86
v_mfma_f32_16x16x32_bf16 a[120:123], v[12:15], a[232:235], a[120:123]
v_accvgpr_read_b32 v199, a87
v_cvt_pk_bf16_f32 v140, v196, v197
v_cvt_pk_bf16_f32 v141, v198, v199
v_accvgpr_read_b32 v200, a100
v_mfma_f32_16x16x32_bf16 a[120:123], v[16:19], a[248:251], a[120:123]
ds_read_b64_tr_b16 v[32:33], v180 offset:37888
ds_read_b64_tr_b16 v[34:35], v180 offset:44032
v_accvgpr_read_b32 v201, a101
v_accvgpr_read_b32 v202, a102
v_accvgpr_read_b32 v203, a103
v_cvt_pk_bf16_f32 v142, v200, v201
v_mfma_f32_16x16x32_bf16 a[124:127], v[16:19], a[252:255], a[124:127]
v_cvt_pk_bf16_f32 v143, v202, v203
s_xor_b32 s83, s86, 5
s_lshl_b32 s83, s83, 3
s_add_u32 s83, s83, 5
s_mul_i32 s83, s83, 12288
s_add_u32 s83, s83, 0
s_add_u32 s48, s58, s83
s_addc_u32 s49, s59, 0
global_store_dwordx4 %[lane16], v[140:143], s[48:49] offset:0 sc1
v_accvgpr_read_b32 v196, a88
v_accvgpr_read_b32 v197, a89
v_mfma_f32_16x16x32_bf16 a[124:127], v[12:15], a[236:239], a[124:127]
v_accvgpr_read_b32 v198, a90
v_accvgpr_read_b32 v199, a91
v_cvt_pk_bf16_f32 v144, v196, v197
v_cvt_pk_bf16_f32 v145, v198, v199
v_mfma_f32_16x16x32_bf16 a[124:127], v[8:11], a[220:223], a[124:127]
v_accvgpr_read_b32 v200, a104
)"
R"(v_accvgpr_read_b32 v201, a105
v_accvgpr_read_b32 v202, a106
v_accvgpr_read_b32 v203, a107
s_waitcnt lgkmcnt(2)
v_mfma_f32_16x16x32_bf16 a[140:143], v[20:23], a[220:223], a[140:143]
ds_read_b64_tr_b16 v[36:37], v180 offset:50176
ds_read_b64_tr_b16 v[38:39], v180 offset:56320
v_cvt_pk_bf16_f32 v146, v200, v201
v_cvt_pk_bf16_f32 v147, v202, v203
s_xor_b32 s83, s86, 6
s_lshl_b32 s83, s83, 3
s_add_u32 s83, s83, 6
s_mul_i32 s83, s83, 12288
s_add_u32 s83, s83, 0
s_add_u32 s48, s58, s83
s_addc_u32 s49, s59, 0
global_store_dwordx4 %[lane16], v[144:147], s[48:49] offset:0 sc1
v_accvgpr_read_b32 v196, a92
v_mfma_f32_16x16x32_bf16 a[140:143], v[24:27], a[236:239], a[140:143]
v_accvgpr_read_b32 v197, a93
v_accvgpr_read_b32 v198, a94
v_accvgpr_read_b32 v199, a95
v_cvt_pk_bf16_f32 v136, v196, v197
v_mfma_f32_16x16x32_bf16 a[140:143], v[28:31], a[252:255], a[140:143]
v_cvt_pk_bf16_f32 v137, v198, v199
v_accvgpr_read_b32 v200, a108
v_accvgpr_read_b32 v201, a109
v_accvgpr_read_b32 v202, a110
v_mfma_f32_16x16x32_bf16 a[136:139], v[28:31], a[248:251], a[136:139]
v_accvgpr_read_b32 v203, a111
v_cvt_pk_bf16_f32 v138, v200, v201
v_cvt_pk_bf16_f32 v139, v202, v203
s_xor_b32 s83, s86, 7
s_lshl_b32 s83, s83, 3
s_add_u32 s83, s83, 7
s_mul_i32 s83, s83, 12288
s_add_u32 s83, s83, 0
s_add_u32 s48, s58, s83
s_addc_u32 s49, s59, 0
global_store_dwordx4 %[lane16], v[136:139], s[48:49] offset:0 sc1
v_mfma_f32_16x16x32_bf16 a[136:139], v[24:27], a[232:235], a[136:139]
ds_read_b64_tr_b16 v[8:9], v184 offset:9216
ds_read_b64_tr_b16 v[10:11], v184 offset:15360
v_mfma_f32_16x16x32_bf16 a[136:139], v[20:23], a[216:219], a[136:139]
v_mfma_f32_16x16x32_bf16 a[132:135], v[20:23], a[212:215], a[132:135]
v_mfma_f32_16x16x32_bf16 a[132:135], v[24:27], a[228:231], a[132:135]
v_mfma_f32_16x16x32_bf16 a[132:135], v[28:31], a[244:247], a[132:135]
ds_read_b64_tr_b16 v[12:13], v168 offset:36864
ds_read_b64_tr_b16 v[14:15], v168 offset:43008
v_mfma_f32_16x16x32_bf16 a[128:131], v[28:31], a[240:243], a[128:131]
v_mfma_f32_16x16x32_bf16 a[128:131], v[24:27], a[224:227], a[128:131]
v_mfma_f32_16x16x32_bf16 a[128:131], v[20:23], a[208:211], a[128:131]
s_waitcnt lgkmcnt(2)
v_mfma_f32_16x16x32_bf16 a[144:147], v[32:35], a[208:211], a[144:147]
ds_read_b64_tr_b16 v[16:17], v168 offset:49152
ds_read_b64_tr_b16 v[18:19], v168 offset:55296
v_mfma_f32_16x16x32_bf16 a[144:147], v[36:39], a[224:227], a[144:147]
v_mfma_f32_16x16x32_bf16 a[144:147], v[8:11], a[240:243], a[144:147]
v_mfma_f32_16x16x32_bf16 a[148:151], v[8:11], a[244:247], a[148:151]
v_accvgpr_read_b32 v196, a112
v_accvgpr_read_b32 v197, a113
v_accvgpr_read_b32 v198, a114
v_accvgpr_read_b32 v199, a115
v_mfma_f32_16x16x32_bf16 a[148:151], v[36:39], a[228:231], a[148:151]
ds_read_b64_tr_b16 v[20:21], v172 offset:8192
ds_read_b64_tr_b16 v[22:23], v172 offset:14336
v_cvt_pk_bf16_f32 v140, v196, v197
v_cvt_pk_bf16_f32 v141, v198, v199
v_accvgpr_read_b32 v200, a128
v_accvgpr_read_b32 v201, a129
v_mfma_f32_16x16x32_bf16 a[148:151], v[32:35], a[212:215], a[148:151]
v_accvgpr_read_b32 v202, a130
v_accvgpr_read_b32 v203, a131
v_cvt_pk_bf16_f32 v142, v200, v201
v_cvt_pk_bf16_f32 v143, v202, v203
v_mfma_f32_16x16x32_bf16 a[152:155], v[32:35], a[216:219], a[152:155]
s_xor_b32 s83, s86, 4
s_lshl_b32 s83, s83, 3
s_add_u32 s83, s83, 4
s_mul_i32 s83, s83, 12288
s_add_u32 s83, s83, 0
s_add_u32 s48, s58, s83
s_addc_u32 s49, s59, 0
global_store_dwordx4 %[lane16], v[140:143], s[48:49] offset:1024 sc1
v_accvgpr_read_b32 v196, a116
v_accvgpr_read_b32 v197, a117
v_accvgpr_read_b32 v198, a118
v_mfma_f32_16x16x32_bf16 a[152:155], v[36:39], a[232:235], a[152:155]
v_accvgpr_read_b32 v199, a119
v_cvt_pk_bf16_f32 v144, v196, v197
v_cvt_pk_bf16_f32 v145, v198, v199
v_accvgpr_read_b32 v200, a132
v_mfma_f32_16x16x32_bf16 a[152:155], v[8:11], a[248:251], a[152:155]
ds_read_b64_tr_b16 v[24:25], v169 offset:36864
ds_read_b64_tr_b16 v[26:27], v169 offset:43008
v_accvgpr_read_b32 v201, a133
v_accvgpr_read_b32 v202, a134
v_accvgpr_read_b32 v203, a135
v_cvt_pk_bf16_f32 v146, v200, v201
v_mfma_f32_16x16x32_bf16 a[156:159], v[8:11], a[252:255], a[156:159]
v_cvt_pk_bf16_f32 v147, v202, v203
s_xor_b32 s83, s86, 5
s_lshl_b32 s83, s83, 3
s_add_u32 s83, s83, 5
s_mul_i32 s83, s83, 12288
s_add_u32 s83, s83, 0
s_add_u32 s48, s58, s83
s_addc_u32 s49, s59, 0
global_store_dwordx4 %[lane16], v[144:147], s[48:49] offset:1024 sc1
v_accvgpr_read_b32 v196, a120
v_accvgpr_read_b32 v197, a121
v_mfma_f32_16x16x32_bf16 a[156:159], v[36:39], a[236:239], a[156:159]
v_accvgpr_read_b32 v198, a122
v_accvgpr_read_b32 v199, a123
v_cvt_pk_bf16_f32 v136, v196, v197
v_cvt_pk_bf16_f32 v137, v198, v199
v_mfma_f32_16x16x32_bf16 a[156:159], v[32:35], a[220:223], a[156:159]
v_accvgpr_read_b32 v200, a136
v_accvgpr_read_b32 v201, a137
v_accvgpr_read_b32 v202, a138
v_accvgpr_read_b32 v203, a139
s_waitcnt lgkmcnt(2)
v_mfma_f32_16x16x32_bf16 a[12:15], v[12:15], a[220:223], a[12:15]
ds_read_b64_tr_b16 v[28:29], v169 offset:49152
ds_read_b64_tr_b16 v[30:31], v169 offset:55296
v_cvt_pk_bf16_f32 v138, v200, v201
v_cvt_pk_bf16_f32 v139, v202, v203
s_xor_b32 s83, s86, 6
s_lshl_b32 s83, s83, 3
s_add_u32 s83, s83, 6
s_mul_i32 s83, s83, 12288
s_add_u32 s83, s83, 0
s_add_u32 s48, s58, s83
s_addc_u32 s49, s59, 0
global_store_dwordx4 %[lane16], v[136:139], s[48:49] offset:1024 sc1
v_accvgpr_read_b32 v196, a124
v_mfma_f32_16x16x32_bf16 a[12:15], v[16:19], a[236:239], a[12:15]
v_accvgpr_read_b32 v197, a125
v_accvgpr_read_b32 v198, a126
v_accvgpr_read_b32 v199, a127
v_cvt_pk_bf16_f32 v140, v196, v197
v_mfma_f32_16x16x32_bf16 a[12:15], v[20:23], a[252:255], a[12:15]
v_cvt_pk_bf16_f32 v141, v198, v199
v_accvgpr_read_b32 v200, a140
v_accvgpr_read_b32 v201, a141
v_accvgpr_read_b32 v202, a142
v_mfma_f32_16x16x32_bf16 a[8:11], v[20:23], a[248:251], a[8:11]
v_accvgpr_read_b32 v203, a143
v_cvt_pk_bf16_f32 v142, v200, v201
v_cvt_pk_bf16_f32 v143, v202, v203
s_xor_b32 s83, s86, 7
s_lshl_b32 s83, s83, 3
s_add_u32 s83, s83, 7
s_mul_i32 s83, s83, 12288
s_add_u32 s83, s83, 0
s_add_u32 s48, s58, s83
s_addc_u32 s49, s59, 0
global_store_dwordx4 %[lane16], v[140:143], s[48:49] offset:1024 sc1
v_mfma_f32_16x16x32_bf16 a[8:11], v[16:19], a[232:235], a[8:11]
ds_read_b64_tr_b16 v[32:33], v173 offset:8192
ds_read_b64_tr_b16 v[34:35], v173 offset:14336
v_accvgpr_read_b32 v196, a144
v_accvgpr_read_b32 v197, a145
v_accvgpr_read_b32 v198, a146
v_accvgpr_read_b32 v199, a147
v_mfma_f32_16x16x32_bf16 a[8:11], v[12:15], a[216:219], a[8:11]
v_cvt_pk_bf16_f32 v144, v196, v197
v_cvt_pk_bf16_f32 v145, v198, v199
s_xor_b32 s83, s86, 4
s_lshl_b32 s83, s83, 3
s_add_u32 s83, s83, 4
s_mul_i32 s83, s83, 12288
s_add_u32 s83, s83, 0
s_add_u32 s48, s58, s83
s_addc_u32 s49, s59, 0
global_store_dwordx2 v192, v[144:145], s[48:49] offset:2048 sc1
v_accvgpr_read_b32 v196, a148
v_mfma_f32_16x16x32_bf16 a[4:7], v[12:15], a[212:215], a[4:7]
v_accvgpr_read_b32 v197, a149
v_accvgpr_read_b32 v198, a150
v_accvgpr_read_b32 v199, a151
v_cvt_pk_bf16_f32 v136, v196, v197
v_mfma_f32_16x16x32_bf16 a[4:7], v[16:19], a[228:231], a[4:7]
v_cvt_pk_bf16_f32 v137, v198, v199
s_xor_b32 s83, s86, 5
s_lshl_b32 s83, s83, 3
s_add_u32 s83, s83, 5
s_mul_i32 s83, s83, 12288
s_add_u32 s83, s83, 0
s_add_u32 s48, s58, s83
s_addc_u32 s49, s59, 0
global_store_dwordx2 v192, v[136:137], s[48:49] offset:2048 sc1
v_accvgpr_read_b32 v196, a152
v_accvgpr_read_b32 v197, a153
v_mfma_f32_16x16x32_bf16 a[4:7], v[20:23], a[244:247], a[4:7]
ds_read_b64_tr_b16 v[36:37], v170 offset:36864
ds_read_b64_tr_b16 v[38:39], v170 offset:43008
v_accvgpr_read_b32 v198, a154
v_accvgpr_read_b32 v199, a155
v_cvt_pk_bf16_f32 v140, v196, v197
)"
R"(v_cvt_pk_bf16_f32 v141, v198, v199
v_mfma_f32_16x16x32_bf16 a[0:3], v[20:23], a[240:243], a[0:3]
s_xor_b32 s83, s86, 6
s_lshl_b32 s83, s83, 3
s_add_u32 s83, s83, 6
s_mul_i32 s83, s83, 12288
s_add_u32 s83, s83, 0
s_add_u32 s48, s58, s83
s_addc_u32 s49, s59, 0
global_store_dwordx2 v192, v[140:141], s[48:49] offset:2048 sc1
v_accvgpr_read_b32 v196, a156
v_accvgpr_read_b32 v197, a157
v_accvgpr_read_b32 v198, a158
v_mfma_f32_16x16x32_bf16 a[0:3], v[16:19], a[224:227], a[0:3]
v_accvgpr_read_b32 v199, a159
v_cvt_pk_bf16_f32 v144, v196, v197
v_cvt_pk_bf16_f32 v145, v198, v199
s_xor_b32 s83, s86, 7
s_lshl_b32 s83, s83, 3
s_add_u32 s83, s83, 7
s_mul_i32 s83, s83, 12288
s_add_u32 s83, s83, 0
s_add_u32 s48, s58, s83
s_addc_u32 s49, s59, 0
global_store_dwordx2 v192, v[144:145], s[48:49] offset:2048 sc1
v_mfma_f32_16x16x32_bf16 a[0:3], v[12:15], a[208:211], a[0:3]
s_waitcnt lgkmcnt(2)
v_mfma_f32_16x16x32_bf16 a[16:19], v[24:27], a[208:211], a[16:19]
ds_read_b64_tr_b16 v[8:9], v170 offset:49152
ds_read_b64_tr_b16 v[10:11], v170 offset:55296
v_mfma_f32_16x16x32_bf16 a[16:19], v[28:31], a[224:227], a[16:19]
v_mfma_f32_16x16x32_bf16 a[16:19], v[32:35], a[240:243], a[16:19]
v_mfma_f32_16x16x32_bf16 a[20:23], v[32:35], a[244:247], a[20:23]
v_mfma_f32_16x16x32_bf16 a[20:23], v[28:31], a[228:231], a[20:23]
ds_read_b64_tr_b16 v[12:13], v174 offset:8192
ds_read_b64_tr_b16 v[14:15], v174 offset:14336
v_mfma_f32_16x16x32_bf16 a[20:23], v[24:27], a[212:215], a[20:23]
v_mfma_f32_16x16x32_bf16 a[24:27], v[24:27], a[216:219], a[24:27]
v_mfma_f32_16x16x32_bf16 a[24:27], v[28:31], a[232:235], a[24:27]
v_mfma_f32_16x16x32_bf16 a[24:27], v[32:35], a[248:251], a[24:27]
ds_read_b64_tr_b16 v[16:17], v171 offset:36864
ds_read_b64_tr_b16 v[18:19], v171 offset:43008
v_mfma_f32_16x16x32_bf16 a[28:31], v[32:35], a[252:255], a[28:31]
v_mfma_f32_16x16x32_bf16 a[28:31], v[28:31], a[236:239], a[28:31]
v_mfma_f32_16x16x32_bf16 a[28:31], v[24:27], a[220:223], a[28:31]
s_waitcnt lgkmcnt(2)
v_mfma_f32_16x16x32_bf16 a[44:47], v[36:39], a[220:223], a[44:47]
ds_read_b64_tr_b16 v[20:21], v171 offset:49152
ds_read_b64_tr_b16 v[22:23], v171 offset:55296
v_mfma_f32_16x16x32_bf16 a[44:47], v[8:11], a[236:239], a[44:47]
v_mfma_f32_16x16x32_bf16 a[44:47], v[12:15], a[252:255], a[44:47]
v_mfma_f32_16x16x32_bf16 a[40:43], v[12:15], a[248:251], a[40:43]
v_accvgpr_read_b32 v196, a4
v_accvgpr_read_b32 v197, a5
v_accvgpr_read_b32 v198, a6
v_accvgpr_read_b32 v199, a7
v_mfma_f32_16x16x32_bf16 a[40:43], v[8:11], a[232:235], a[40:43]
ds_read_b64_tr_b16 v[24:25], v175 offset:8192
ds_read_b64_tr_b16 v[26:27], v175 offset:14336
v_cvt_pk_bf16_f32 v136, v196, v197
v_cvt_pk_bf16_f32 v137, v198, v199
v_accvgpr_read_b32 v200, a20
v_accvgpr_read_b32 v201, a21
v_mfma_f32_16x16x32_bf16 a[40:43], v[36:39], a[216:219], a[40:43]
v_accvgpr_read_b32 v202, a22
v_accvgpr_read_b32 v203, a23
v_cvt_pk_bf16_f32 v138, v200, v201
v_cvt_pk_bf16_f32 v139, v202, v203
v_mfma_f32_16x16x32_bf16 a[36:39], v[36:39], a[212:215], a[36:39]
s_xor_b32 s83, s86, 1
s_lshl_b32 s83, s83, 3
s_add_u32 s83, s83, 1
s_mul_i32 s83, s83, 12288
s_add_u32 s83, s83, 0
s_add_u32 s48, s58, s83
s_addc_u32 s49, s59, 0
global_store_dwordx4 %[lane16], v[136:139], s[48:49] offset:0 sc1
v_accvgpr_read_b32 v196, a8
v_accvgpr_read_b32 v197, a9
v_accvgpr_read_b32 v198, a10
v_mfma_f32_16x16x32_bf16 a[36:39], v[8:11], a[228:231], a[36:39]
v_accvgpr_read_b32 v199, a11
v_cvt_pk_bf16_f32 v140, v196, v197
v_cvt_pk_bf16_f32 v141, v198, v199
v_accvgpr_read_b32 v200, a24
v_mfma_f32_16x16x32_bf16 a[36:39], v[12:15], a[244:247], a[36:39]
ds_read_b64_tr_b16 v[28:29], v168 offset:37888
ds_read_b64_tr_b16 v[30:31], v168 offset:44032
v_accvgpr_read_b32 v201, a25
v_accvgpr_read_b32 v202, a26
v_accvgpr_read_b32 v203, a27
v_cvt_pk_bf16_f32 v142, v200, v201
v_mfma_f32_16x16x32_bf16 a[32:35], v[12:15], a[240:243], a[32:35]
v_cvt_pk_bf16_f32 v143, v202, v203
s_xor_b32 s83, s86, 2
s_lshl_b32 s83, s83, 3
s_add_u32 s83, s83, 2
s_mul_i32 s83, s83, 12288
s_add_u32 s83, s83, 0
s_add_u32 s48, s58, s83
s_addc_u32 s49, s59, 0
global_store_dwordx4 %[lane16], v[140:143], s[48:49] offset:0 sc1
v_accvgpr_read_b32 v196, a12
v_accvgpr_read_b32 v197, a13
v_mfma_f32_16x16x32_bf16 a[32:35], v[8:11], a[224:227], a[32:35]
v_accvgpr_read_b32 v198, a14
v_accvgpr_read_b32 v199, a15
v_cvt_pk_bf16_f32 v144, v196, v197
v_cvt_pk_bf16_f32 v145, v198, v199
v_mfma_f32_16x16x32_bf16 a[32:35], v[36:39], a[208:211], a[32:35]
v_accvgpr_read_b32 v200, a28
v_accvgpr_read_b32 v201, a29
v_accvgpr_read_b32 v202, a30
v_accvgpr_read_b32 v203, a31
s_waitcnt lgkmcnt(2)
v_mfma_f32_16x16x32_bf16 a[48:51], v[16:19], a[208:211], a[48:51]
ds_read_b64_tr_b16 v[32:33], v168 offset:50176
ds_read_b64_tr_b16 v[34:35], v168 offset:56320
v_cvt_pk_bf16_f32 v146, v200, v201
v_cvt_pk_bf16_f32 v147, v202, v203
s_xor_b32 s83, s86, 3
s_lshl_b32 s83, s83, 3
s_add_u32 s83, s83, 3
s_mul_i32 s83, s83, 12288
s_add_u32 s83, s83, 0
s_add_u32 s48, s58, s83
s_addc_u32 s49, s59, 0
global_store_dwordx4 %[lane16], v[144:147], s[48:49] offset:0 sc1
v_mfma_f32_16x16x32_bf16 a[48:51], v[20:23], a[224:227], a[48:51]
v_mfma_f32_16x16x32_bf16 a[48:51], v[24:27], a[240:243], a[48:51]
v_mfma_f32_16x16x32_bf16 a[52:55], v[24:27], a[244:247], a[52:55]
v_mfma_f32_16x16x32_bf16 a[52:55], v[20:23], a[228:231], a[52:55]
ds_read_b64_tr_b16 v[36:37], v172 offset:9216
ds_read_b64_tr_b16 v[38:39], v172 offset:15360
v_mfma_f32_16x16x32_bf16 a[52:55], v[16:19], a[212:215], a[52:55]
v_mfma_f32_16x16x32_bf16 a[56:59], v[16:19], a[216:219], a[56:59]
v_mfma_f32_16x16x32_bf16 a[56:59], v[20:23], a[232:235], a[56:59]
v_mfma_f32_16x16x32_bf16 a[56:59], v[24:27], a[248:251], a[56:59]
v_mfma_f32_16x16x32_bf16 a[60:63], v[24:27], a[252:255], a[60:63]
v_mfma_f32_16x16x32_bf16 a[60:63], v[20:23], a[236:239], a[60:63]
v_mfma_f32_16x16x32_bf16 a[60:63], v[16:19], a[220:223], a[60:63]
s_waitcnt lgkmcnt(0)
v_mfma_f32_16x16x32_bf16 a[76:79], v[28:31], a[220:223], a[76:79]
v_mfma_f32_16x16x32_bf16 a[76:79], v[32:35], a[236:239], a[76:79]
v_mfma_f32_16x16x32_bf16 a[76:79], v[36:39], a[252:255], a[76:79]
v_mfma_f32_16x16x32_bf16 a[72:75], v[36:39], a[248:251], a[72:75]
v_accvgpr_read_b32 v196, a36
v_accvgpr_read_b32 v197, a37
v_accvgpr_read_b32 v198, a38
v_accvgpr_read_b32 v199, a39
v_mfma_f32_16x16x32_bf16 a[72:75], v[32:35], a[232:235], a[72:75]
v_cvt_pk_bf16_f32 v136, v196, v197
v_cvt_pk_bf16_f32 v137, v198, v199
v_accvgpr_read_b32 v200, a52
v_accvgpr_read_b32 v201, a53
v_mfma_f32_16x16x32_bf16 a[72:75], v[28:31], a[216:219], a[72:75]
v_accvgpr_read_b32 v202, a54
v_accvgpr_read_b32 v203, a55
v_cvt_pk_bf16_f32 v138, v200, v201
v_cvt_pk_bf16_f32 v139, v202, v203
v_mfma_f32_16x16x32_bf16 a[68:71], v[28:31], a[212:215], a[68:71]
s_xor_b32 s83, s86, 1
s_lshl_b32 s83, s83, 3
s_add_u32 s83, s83, 1
s_mul_i32 s83, s83, 12288
s_add_u32 s83, s83, 0
s_add_u32 s48, s58, s83
s_addc_u32 s49, s59, 0
global_store_dwordx4 %[lane16], v[136:139], s[48:49] offset:1024 sc1
v_accvgpr_read_b32 v196, a40
v_accvgpr_read_b32 v197, a41
v_accvgpr_read_b32 v198, a42
v_mfma_f32_16x16x32_bf16 a[68:71], v[32:35], a[228:231], a[68:71]
v_accvgpr_read_b32 v199, a43
v_cvt_pk_bf16_f32 v140, v196, v197
v_cvt_pk_bf16_f32 v141, v198, v199
v_accvgpr_read_b32 v200, a56
v_mfma_f32_16x16x32_bf16 a[68:71], v[36:39], a[244:247], a[68:71]
v_accvgpr_read_b32 v201, a57
v_accvgpr_read_b32 v202, a58
v_accvgpr_read_b32 v203, a59
v_cvt_pk_bf16_f32 v142, v200, v201
v_mfma_f32_16x16x32_bf16 a[64:67], v[36:39], a[240:243], a[64:67]
v_cvt_pk_bf16_f32 v143, v202, v203
s_xor_b32 s83, s86, 2
s_lshl_b32 s83, s83, 3
s_add_u32 s83, s83, 2
s_mul_i32 s83, s83, 12288
s_add_u32 s83, s83, 0
s_add_u32 s48, s58, s83
s_addc_u32 s49, s59, 0
global_store_dwordx4 %[lane16], v[140:143], s[48:49] offset:1024 sc1
v_accvgpr_read_b32 v196, a44
v_accvgpr_read_b32 v197, a45
v_mfma_f32_16x16x32_bf16 a[64:67], v[32:35], a[224:227], a[64:67]
v_accvgpr_read_b32 v198, a46
v_accvgpr_read_b32 v199, a47
v_cvt_pk_bf16_f32 v144, v196, v197
v_cvt_pk_bf16_f32 v145, v198, v199
v_mfma_f32_16x16x32_bf16 a[64:67], v[28:31], a[208:211], a[64:67]
)"
R"(v_accvgpr_read_b32 v200, a60
v_accvgpr_read_b32 v201, a61
v_accvgpr_read_b32 v202, a62
v_accvgpr_read_b32 v203, a63
v_cvt_pk_bf16_f32 v146, v200, v201
v_cvt_pk_bf16_f32 v147, v202, v203
s_xor_b32 s83, s86, 3
s_lshl_b32 s83, s83, 3
s_add_u32 s83, s83, 3
s_mul_i32 s83, s83, 12288
s_add_u32 s83, s83, 0
s_add_u32 s48, s58, s83
s_addc_u32 s49, s59, 0
global_store_dwordx4 %[lane16], v[144:147], s[48:49] offset:1024 sc1
v_accvgpr_read_b32 v196, a68
v_accvgpr_read_b32 v197, a69
v_accvgpr_read_b32 v198, a70
v_accvgpr_read_b32 v199, a71
v_cvt_pk_bf16_f32 v136, v196, v197
v_cvt_pk_bf16_f32 v137, v198, v199
s_xor_b32 s83, s86, 1
s_lshl_b32 s83, s83, 3
s_add_u32 s83, s83, 1
s_mul_i32 s83, s83, 12288
s_add_u32 s83, s83, 0
s_add_u32 s48, s58, s83
s_addc_u32 s49, s59, 0
global_store_dwordx2 v192, v[136:137], s[48:49] offset:2048 sc1
v_accvgpr_read_b32 v196, a72
v_accvgpr_read_b32 v197, a73
v_accvgpr_read_b32 v198, a74
v_accvgpr_read_b32 v199, a75
v_cvt_pk_bf16_f32 v140, v196, v197
v_cvt_pk_bf16_f32 v141, v198, v199
s_xor_b32 s83, s86, 2
s_lshl_b32 s83, s83, 3
s_add_u32 s83, s83, 2
s_mul_i32 s83, s83, 12288
s_add_u32 s83, s83, 0
s_add_u32 s48, s58, s83
s_addc_u32 s49, s59, 0
global_store_dwordx2 v192, v[140:141], s[48:49] offset:2048 sc1
v_accvgpr_read_b32 v196, a76
v_accvgpr_read_b32 v197, a77
v_accvgpr_read_b32 v198, a78
v_accvgpr_read_b32 v199, a79
v_cvt_pk_bf16_f32 v144, v196, v197
v_cvt_pk_bf16_f32 v145, v198, v199
s_xor_b32 s83, s86, 3
s_lshl_b32 s83, s83, 3
s_add_u32 s83, s83, 3
s_mul_i32 s83, s83, 12288
s_add_u32 s83, s83, 0
s_add_u32 s48, s58, s83
s_addc_u32 s49, s59, 0
global_store_dwordx2 v192, v[144:145], s[48:49] offset:2048 sc1
global_load_dword v253, v194, s[62:63] offset:384 sc1
v_accvgpr_read_b32 v8, a0
v_accvgpr_read_b32 v9, a1
v_accvgpr_read_b32 v10, a2
v_accvgpr_read_b32 v11, a3
v_accvgpr_read_b32 v12, a16
v_accvgpr_read_b32 v13, a17
v_accvgpr_read_b32 v14, a18
v_accvgpr_read_b32 v15, a19
v_accvgpr_read_b32 v16, a32
v_accvgpr_read_b32 v17, a33
v_accvgpr_read_b32 v18, a34
v_accvgpr_read_b32 v19, a35
v_accvgpr_read_b32 v20, a48
v_accvgpr_read_b32 v21, a49
v_accvgpr_read_b32 v22, a50
v_accvgpr_read_b32 v23, a51
v_accvgpr_read_b32 v24, a64
v_accvgpr_read_b32 v25, a65
v_accvgpr_read_b32 v26, a66
v_accvgpr_read_b32 v27, a67
s_waitcnt vmcnt(1)
s_barrier
s_cmp_lg_u32 %[wid], 0
s_cbranch_scc1 .Lpsig
s_mov_b64 exec, 1
global_store_dword v251, v252, s[64:65] offset:384 sc1
s_mov_b64 exec, -1
.Lpsig:
s_waitcnt vmcnt(0)
v_cmp_eq_u32 vcc, s96, v253
s_cmp_eq_u32 s77, 0xff
s_cbranch_scc1 .Lcp1_go
s_mov_b32 s95, 0x20000
.Lcp1:
v_cmp_eq_u32 vcc, s96, v253
s_nop 1
s_andn2_b32 s84, vcc_lo, s77
s_and_b32 s84, s84, 254
s_cbranch_scc0 .Lga14_end
s_or_b32 s77, s77, s84
s_bitcmp1_b32 s84, 1
s_cbranch_scc0 .Lga14_1
s_add_u32 s48, s60, 12288
s_addc_u32 s49, s61, 0
global_load_dwordx4 v[52:55], %[lane16], s[48:49] offset:0 sc1
global_load_dwordx4 v[56:59], %[lane16], s[48:49] offset:1024 sc1
global_load_dwordx2 v[60:61], v192, s[48:49] offset:2048 sc1
.Lga14_1:
s_bitcmp1_b32 s84, 2
s_cbranch_scc0 .Lga14_2
s_add_u32 s48, s60, 24576
s_addc_u32 s49, s61, 0
global_load_dwordx4 v[64:67], %[lane16], s[48:49] offset:0 sc1
global_load_dwordx4 v[68:71], %[lane16], s[48:49] offset:1024 sc1
global_load_dwordx2 v[72:73], v192, s[48:49] offset:2048 sc1
.Lga14_2:
s_bitcmp1_b32 s84, 3
s_cbranch_scc0 .Lga14_3
s_add_u32 s48, s60, 36864
s_addc_u32 s49, s61, 0
global_load_dwordx4 v[76:79], %[lane16], s[48:49] offset:0 sc1
global_load_dwordx4 v[80:83], %[lane16], s[48:49] offset:1024 sc1
global_load_dwordx2 v[84:85], v192, s[48:49] offset:2048 sc1
.Lga14_3:
s_bitcmp1_b32 s84, 4
s_cbranch_scc0 .Lga14_4
s_add_u32 s48, s60, 49152
s_addc_u32 s49, s61, 0
global_load_dwordx4 v[88:91], %[lane16], s[48:49] offset:0 sc1
global_load_dwordx4 v[92:95], %[lane16], s[48:49] offset:1024 sc1
global_load_dwordx2 v[96:97], v192, s[48:49] offset:2048 sc1
.Lga14_4:
s_bitcmp1_b32 s84, 5
s_cbranch_scc0 .Lga14_5
s_add_u32 s48, s60, 61440
s_addc_u32 s49, s61, 0
global_load_dwordx4 v[100:103], %[lane16], s[48:49] offset:0 sc1
global_load_dwordx4 v[104:107], %[lane16], s[48:49] offset:1024 sc1
global_load_dwordx2 v[108:109], v192, s[48:49] offset:2048 sc1
.Lga14_5:
s_bitcmp1_b32 s84, 6
s_cbranch_scc0 .Lga14_6
s_add_u32 s48, s60, 73728
s_addc_u32 s49, s61, 0
global_load_dwordx4 v[112:115], %[lane16], s[48:49] offset:0 sc1
global_load_dwordx4 v[116:119], %[lane16], s[48:49] offset:1024 sc1
global_load_dwordx2 v[120:121], v192, s[48:49] offset:2048 sc1
.Lga14_6:
s_bitcmp1_b32 s84, 7
s_cbranch_scc0 .Lga14_7
s_add_u32 s48, s60, 86016
s_addc_u32 s49, s61, 0
global_load_dwordx4 v[124:127], %[lane16], s[48:49] offset:0 sc1
global_load_dwordx4 v[128:131], %[lane16], s[48:49] offset:1024 sc1
global_load_dwordx2 v[132:133], v192, s[48:49] offset:2048 sc1
.Lga14_7:
.Lga14_end:
s_cmp_eq_u32 s77, 0xff
s_cbranch_scc1 .Lcp1_w
s_sub_u32 s95, s95, 1
s_cbranch_scc1 .Lcp1_w
s_sleep 2
global_load_dword v253, v194, s[62:63] offset:384 sc1
s_waitcnt vmcnt(0)
s_branch .Lcp1
.Lcp1_w:
s_waitcnt vmcnt(0)
.Lcp1_go:
v_mfma_f32_16x16x32_bf16 v[8:11], v[232:235], v[52:55], v[8:11]
v_mfma_f32_16x16x32_bf16 v[12:15], v[234:237], v[52:55], v[12:15]
v_mfma_f32_16x16x32_bf16 v[16:19], v[232:235], v[56:59], v[16:19]
v_mfma_f32_16x16x32_bf16 v[20:23], v[234:237], v[56:59], v[20:23]
v_mfma_f32_16x16x32_bf16 v[24:27], v[232:235], v[60:63], v[24:27]
v_mfma_f32_16x16x32_bf16 v[8:11], v[232:235], v[64:67], v[8:11]
v_mfma_f32_16x16x32_bf16 v[12:15], v[234:237], v[64:67], v[12:15]
v_mfma_f32_16x16x32_bf16 v[16:19], v[232:235], v[68:71], v[16:19]
v_mfma_f32_16x16x32_bf16 v[20:23], v[234:237], v[68:71], v[20:23]
v_mfma_f32_16x16x32_bf16 v[24:27], v[232:235], v[72:75], v[24:27]
v_mfma_f32_16x16x32_bf16 v[8:11], v[232:235], v[76:79], v[8:11]
v_mfma_f32_16x16x32_bf16 v[12:15], v[234:237], v[76:79], v[12:15]
v_mfma_f32_16x16x32_bf16 v[16:19], v[232:235], v[80:83], v[16:19]
v_mfma_f32_16x16x32_bf16 v[20:23], v[234:237], v[80:83], v[20:23]
v_mfma_f32_16x16x32_bf16 v[24:27], v[232:235], v[84:87], v[24:27]
v_mfma_f32_16x16x32_bf16 v[8:11], v[232:235], v[88:91], v[8:11]
v_mfma_f32_16x16x32_bf16 v[12:15], v[234:237], v[88:91], v[12:15]
v_mfma_f32_16x16x32_bf16 v[16:19], v[232:235], v[92:95], v[16:19]
v_mfma_f32_16x16x32_bf16 v[20:23], v[234:237], v[92:95], v[20:23]
v_mfma_f32_16x16x32_bf16 v[24:27], v[232:235], v[96:99], v[24:27]
v_mfma_f32_16x16x32_bf16 v[8:11], v[232:235], v[100:103], v[8:11]
v_mfma_f32_16x16x32_bf16 v[12:15], v[234:237], v[100:103], v[12:15]
v_mfma_f32_16x16x32_bf16 v[16:19], v[232:235], v[104:107], v[16:19]
v_mfma_f32_16x16x32_bf16 v[20:23], v[234:237], v[104:107], v[20:23]
v_mfma_f32_16x16x32_bf16 v[24:27], v[232:235], v[108:111], v[24:27]
v_mfma_f32_16x16x32_bf16 v[8:11], v[232:235], v[112:115], v[8:11]
v_mfma_f32_16x16x32_bf16 v[12:15], v[234:237], v[112:115], v[12:15]
v_mfma_f32_16x16x32_bf16 v[16:19], v[232:235], v[116:119], v[16:19]
v_mfma_f32_16x16x32_bf16 v[20:23], v[234:237], v[116:119], v[20:23]
v_mfma_f32_16x16x32_bf16 v[24:27], v[232:235], v[120:123], v[24:27]
v_mfma_f32_16x16x32_bf16 v[8:11], v[232:235], v[124:127], v[8:11]
v_mfma_f32_16x16x32_bf16 v[12:15], v[234:237], v[124:127], v[12:15]
v_mfma_f32_16x16x32_bf16 v[16:19], v[232:235], v[128:131], v[16:19]
v_mfma_f32_16x16x32_bf16 v[20:23], v[234:237], v[128:131], v[20:23]
v_mfma_f32_16x16x32_bf16 v[24:27], v[232:235], v[132:135], v[24:27]
)"
R"(v_lshlrev_b32 v196, 16, v148
v_and_b32 v197, 0xffff0000, v148
v_lshlrev_b32 v198, 16, v149
v_and_b32 v199, 0xffff0000, v149
v_fma_f32 v8, v8, v204, v196
v_fma_f32 v9, v9, v205, v197
v_fma_f32 v10, v10, v206, v198
v_fma_f32 v11, v11, v207, v199
v_cvt_pk_bf16_f32 v148, v8, v9
v_cvt_pk_bf16_f32 v149, v10, v11
v_lshlrev_b32 v196, 16, v150
v_and_b32 v197, 0xffff0000, v150
v_lshlrev_b32 v198, 16, v151
v_and_b32 v199, 0xffff0000, v151
v_fma_f32 v12, v12, v208, v196
v_fma_f32 v13, v13, v209, v197
v_fma_f32 v14, v14, v210, v198
v_fma_f32 v15, v15, v211, v199
v_cvt_pk_bf16_f32 v150, v12, v13
v_cvt_pk_bf16_f32 v151, v14, v15
s_nop 0
v_permlane16_swap_b32 v148, v150
v_permlane16_swap_b32 v149, v151
global_store_dwordx4 %[xoffw], v[148:151], %[outb] offset:0
v_lshlrev_b32 v196, 16, v152
v_and_b32 v197, 0xffff0000, v152
v_lshlrev_b32 v198, 16, v153
v_and_b32 v199, 0xffff0000, v153
v_fma_f32 v16, v16, v212, v196
v_fma_f32 v17, v17, v213, v197
v_fma_f32 v18, v18, v214, v198
v_fma_f32 v19, v19, v215, v199
v_cvt_pk_bf16_f32 v152, v16, v17
v_cvt_pk_bf16_f32 v153, v18, v19
v_lshlrev_b32 v196, 16, v154
v_and_b32 v197, 0xffff0000, v154
v_lshlrev_b32 v198, 16, v155
v_and_b32 v199, 0xffff0000, v155
v_fma_f32 v20, v20, v216, v196
v_fma_f32 v21, v21, v217, v197
v_fma_f32 v22, v22, v218, v198
v_fma_f32 v23, v23, v219, v199
v_cvt_pk_bf16_f32 v154, v20, v21
v_cvt_pk_bf16_f32 v155, v22, v23
s_nop 0
v_permlane16_swap_b32 v152, v154
v_permlane16_swap_b32 v153, v155
global_store_dwordx4 %[xoffw], v[152:155], %[outb] offset:64
v_lshlrev_b32 v196, 16, v156
v_and_b32 v197, 0xffff0000, v156
v_lshlrev_b32 v198, 16, v157
v_and_b32 v199, 0xffff0000, v157
v_fma_f32 v24, v24, v220, v196
v_fma_f32 v25, v25, v221, v197
v_fma_f32 v26, v26, v222, v198
v_fma_f32 v27, v27, v223, v199
v_cvt_pk_bf16_f32 v156, v24, v25
v_cvt_pk_bf16_f32 v157, v26, v27
global_store_dwordx2 %[xoffn], v[156:157], %[outb] offset:128
s_or_b32 s83, s86, s87
s_cbranch_scc1 .Lgen
s_cmp_lg_u32 %[wid], 0
s_cbranch_scc1 .Lgen
s_mov_b64 exec, 1
global_store_dword v251, v252, s[64:65] offset:512 sc1
s_mov_b64 exec, -1
.Lgen:
s_branch .Lall_end
.Lfallback:
s_waitcnt vmcnt(0) lgkmcnt(0)
s_barrier
v_mov_b32 v196, 0xff800000
v_mov_b32 v197, 0xff800000
v_mov_b32 v198, 0xff800000
v_mov_b32 v199, 0xff800000
s_mov_b32 s76, s81
s_mov_b32 s80, 64
.Lfb_loop:
s_add_u32 m0, s81, 0
s_nop 0
buffer_load_dwordx4 v44, %[srdK], s76 offen offset:0 lds
buffer_load_dwordx4 v45, %[srdK], s76 offen offset:1024 lds
buffer_load_dwordx4 v46, %[srdK], s76 offen offset:2048 lds
s_add_u32 s76, s76, 12288
s_waitcnt vmcnt(0)
s_barrier
ds_read_b128 v[8:11], %[kaddr0] offset:0
s_waitcnt lgkmcnt(0)
v_mfma_f32_16x16x32_bf16 v[84:87], v[8:11], a[192:195], 0
v_mfma_f32_16x16x32_bf16 v[88:91], v[8:11], a[196:199], 0
v_mfma_f32_16x16x32_bf16 v[92:95], v[8:11], a[200:203], 0
v_mfma_f32_16x16x32_bf16 v[96:99], v[8:11], a[204:207], 0
ds_read_b128 v[12:15], %[kaddr0] offset:6144
s_waitcnt lgkmcnt(0)
v_mfma_f32_16x16x32_bf16 v[100:103], v[12:15], a[192:195], 0
v_mfma_f32_16x16x32_bf16 v[104:107], v[12:15], a[196:199], 0
v_mfma_f32_16x16x32_bf16 v[108:111], v[12:15], a[200:203], 0
v_mfma_f32_16x16x32_bf16 v[112:115], v[12:15], a[204:207], 0
ds_read_b128 v[16:19], v40 offset:0
s_waitcnt lgkmcnt(0)
v_mfma_f32_16x16x32_bf16 v[84:87], v[16:19], a[208:211], v[84:87]
v_mfma_f32_16x16x32_bf16 v[88:91], v[16:19], a[212:215], v[88:91]
v_mfma_f32_16x16x32_bf16 v[92:95], v[16:19], a[216:219], v[92:95]
v_mfma_f32_16x16x32_bf16 v[96:99], v[16:19], a[220:223], v[96:99]
ds_read_b128 v[20:23], v40 offset:6144
s_waitcnt lgkmcnt(0)
v_mfma_f32_16x16x32_bf16 v[100:103], v[20:23], a[208:211], v[100:103]
v_mfma_f32_16x16x32_bf16 v[104:107], v[20:23], a[212:215], v[104:107]
v_mfma_f32_16x16x32_bf16 v[108:111], v[20:23], a[216:219], v[108:111]
v_mfma_f32_16x16x32_bf16 v[112:115], v[20:23], a[220:223], v[112:115]
ds_read_b128 v[24:27], %[kaddr0] offset:1024
s_waitcnt lgkmcnt(0)
v_mfma_f32_16x16x32_bf16 v[84:87], v[24:27], a[224:227], v[84:87]
v_mfma_f32_16x16x32_bf16 v[88:91], v[24:27], a[228:231], v[88:91]
v_mfma_f32_16x16x32_bf16 v[92:95], v[24:27], a[232:235], v[92:95]
v_mfma_f32_16x16x32_bf16 v[96:99], v[24:27], a[236:239], v[96:99]
ds_read_b128 v[28:31], %[kaddr0] offset:7168
s_waitcnt lgkmcnt(0)
v_mfma_f32_16x16x32_bf16 v[100:103], v[28:31], a[224:227], v[100:103]
v_mfma_f32_16x16x32_bf16 v[104:107], v[28:31], a[228:231], v[104:107]
v_mfma_f32_16x16x32_bf16 v[108:111], v[28:31], a[232:235], v[108:111]
v_mfma_f32_16x16x32_bf16 v[112:115], v[28:31], a[236:239], v[112:115]
ds_read_b128 v[32:35], v40 offset:1024
s_waitcnt lgkmcnt(0)
v_mfma_f32_16x16x32_bf16 v[84:87], v[32:35], a[240:243], v[84:87]
v_mfma_f32_16x16x32_bf16 v[88:91], v[32:35], a[244:247], v[88:91]
v_mfma_f32_16x16x32_bf16 v[92:95], v[32:35], a[248:251], v[92:95]
v_mfma_f32_16x16x32_bf16 v[96:99], v[32:35], a[252:255], v[96:99]
ds_read_b128 v[36:39], v40 offset:7168
s_waitcnt lgkmcnt(0)
v_mfma_f32_16x16x32_bf16 v[100:103], v[36:39], a[240:243], v[100:103]
v_mfma_f32_16x16x32_bf16 v[104:107], v[36:39], a[244:247], v[104:107]
v_mfma_f32_16x16x32_bf16 v[108:111], v[36:39], a[248:251], v[108:111]
v_mfma_f32_16x16x32_bf16 v[112:115], v[36:39], a[252:255], v[112:115]
ds_read_b128 v[8:11], %[kaddr0] offset:2048
s_waitcnt lgkmcnt(0)
v_mfma_f32_16x16x32_bf16 v[84:87], v[8:11], v[52:55], v[84:87]
v_mfma_f32_16x16x32_bf16 v[88:91], v[8:11], v[56:59], v[88:91]
v_mfma_f32_16x16x32_bf16 v[92:95], v[8:11], v[60:63], v[92:95]
v_mfma_f32_16x16x32_bf16 v[96:99], v[8:11], v[64:67], v[96:99]
ds_read_b128 v[12:15], %[kaddr0] offset:8192
s_waitcnt lgkmcnt(0)
v_mfma_f32_16x16x32_bf16 v[100:103], v[12:15], v[52:55], v[100:103]
v_mfma_f32_16x16x32_bf16 v[104:107], v[12:15], v[56:59], v[104:107]
v_mfma_f32_16x16x32_bf16 v[108:111], v[12:15], v[60:63], v[108:111]
v_mfma_f32_16x16x32_bf16 v[112:115], v[12:15], v[64:67], v[112:115]
ds_read_b128 v[16:19], v40 offset:2048
s_waitcnt lgkmcnt(0)
v_mfma_f32_16x16x32_bf16 v[84:87], v[16:19], v[68:71], v[84:87]
v_mfma_f32_16x16x32_bf16 v[88:91], v[16:19], v[72:75], v[88:91]
v_mfma_f32_16x16x32_bf16 v[92:95], v[16:19], v[76:79], v[92:95]
v_mfma_f32_16x16x32_bf16 v[96:99], v[16:19], v[80:83], v[96:99]
ds_read_b128 v[20:23], v40 offset:8192
s_waitcnt lgkmcnt(0)
v_mfma_f32_16x16x32_bf16 v[100:103], v[20:23], v[68:71], v[100:103]
v_mfma_f32_16x16x32_bf16 v[104:107], v[20:23], v[72:75], v[104:107]
v_mfma_f32_16x16x32_bf16 v[108:111], v[20:23], v[76:79], v[108:111]
v_mfma_f32_16x16x32_bf16 v[112:115], v[20:23], v[80:83], v[112:115]
v_max3_f32 v196, v196, v84, v85
v_max3_f32 v196, v196, v86, v87
s_nop 2
v_max3_f32 v196, v196, v100, v101
v_max3_f32 v196, v196, v102, v103
v_max3_f32 v197, v197, v88, v89
v_max3_f32 v197, v197, v90, v91
v_max3_f32 v197, v197, v104, v105
v_max3_f32 v197, v197, v106, v107
v_max3_f32 v198, v198, v92, v93
v_max3_f32 v198, v198, v94, v95
v_max3_f32 v198, v198, v108, v109
v_max3_f32 v198, v198, v110, v111
v_max3_f32 v199, v199, v96, v97
v_max3_f32 v199, v199, v98, v99
v_max3_f32 v199, v199, v112, v113
v_max3_f32 v199, v199, v114, v115
s_barrier
s_sub_u32 s80, s80, 1
s_cmp_lg_u32 s80, 0
s_cbranch_scc1 .Lfb_loop
v_mov_b32 v200, v196
s_nop 1
v_permlane32_swap_b32 v196, v200
v_max_f32 v196, v196, v200
v_mov_b32 v200, v196
s_nop 1
v_permlane16_swap_b32 v196, v200
v_max_f32 v196, v196, v200
v_sub_f32 v180, 0, v196
v_sub_f32 v181, 0, v196
v_sub_f32 v182, 0, v196
v_sub_f32 v183, 0, v196
v_mov_b32 v200, v197
s_nop 1
v_permlane32_swap_b32 v197, v200
v_max_f32 v197, v197, v200
v_mov_b32 v200, v197
s_nop 1
v_permlane16_swap_b32 v197, v200
v_max_f32 v197, v197, v200
v_sub_f32 v184, 0, v197
)"
R"(v_sub_f32 v185, 0, v197
v_sub_f32 v186, 0, v197
v_sub_f32 v187, 0, v197
v_mov_b32 v200, v198
s_nop 1
v_permlane32_swap_b32 v198, v200
v_max_f32 v198, v198, v200
v_mov_b32 v200, v198
s_nop 1
v_permlane16_swap_b32 v198, v200
v_max_f32 v198, v198, v200
v_sub_f32 v188, 0, v198
v_sub_f32 v189, 0, v198
v_sub_f32 v190, 0, v198
v_sub_f32 v191, 0, v198
v_mov_b32 v200, v199
s_nop 1
v_permlane32_swap_b32 v199, v200
v_max_f32 v199, v199, v200
v_mov_b32 v200, v199
s_nop 1
v_permlane16_swap_b32 v199, v200
v_max_f32 v199, v199, v200
v_sub_f32 v192, 0, v199
v_sub_f32 v193, 0, v199
v_sub_f32 v194, 0, v199
v_sub_f32 v195, 0, v199
s_mov_b32 s85, 1
s_mov_b32 s76, s81
s_add_u32 m0, s81, 24576
s_nop 0
buffer_load_dwordx4 v44, %[srdK], s76 offen offset:0 lds
buffer_load_dwordx4 v45, %[srdK], s76 offen offset:1024 lds
buffer_load_dwordx4 v46, %[srdK], s76 offen offset:2048 lds
s_add_u32 s76, s76, 12288
s_add_u32 m0, s81, 36864
s_nop 0
buffer_load_dwordx4 v44, %[srdK], s76 offen offset:0 lds
buffer_load_dwordx4 v45, %[srdK], s76 offen offset:1024 lds
buffer_load_dwordx4 v46, %[srdK], s76 offen offset:2048 lds
s_add_u32 m0, s81, 86016
s_nop 0
buffer_load_dwordx4 v44, %[srdV], s76 offen offset:0 lds
buffer_load_dwordx4 v45, %[srdV], s76 offen offset:1024 lds
buffer_load_dwordx4 v46, %[srdV], s76 offen offset:2048 lds
s_add_u32 s76, s76, 12288
s_waitcnt vmcnt(6)
s_barrier
ds_read_b128 v[8:11], %[kaddr0] offset:24576
ds_read_b128 v[12:15], v40 offset:24576
ds_read_b128 v[16:19], %[kaddr0] offset:25600
ds_read_b128 v[20:23], v40 offset:25600
s_branch .Lqk0
.Lall_end:
)"
    :
    : [srdK] "s"(srdK), [srdV] "s"(srdV), [srdW] "s"(srdW), [srdF] "s"(srdF), [srdS] "s"(srdS), [srdG] "s"(srdG),
      [ws] "s"(ws), [b1base] "s"(b1base), [xres] "s"(xres), [outb] "s"(outb), [b2base] "s"(b2base), [wid] "s"(w), [bid] "s"(rfl(b)),
      [kaddr0] "v"(kaddr0), [vaddr0] "v"(vaddr0), [drow] "v"(drow), [dch16] "v"(dch16), [lane16] "v"(lane16), [xoffw] "v"(xoffw), [xoffn] "v"(xoffn)
    : "v8", "v9", "v10", "v11", "v12", "v13", "v14", "v15", "v16", "v17", "v18", "v19", "v20", "v21", "v22", "v23", "v24", "v25", "v26", "v27", "v28", "v29", "v30", "v31", "v32", "v33", "v34", "v35", "v36", "v37", "v38", "v39", "v40", "v41", "v42", "v43", "v44", "v45", "v46", "v47", "v48", "v49", "v50", "v51", "v52", "v53", "v54", "v55", "v56", "v57", "v58", "v59", "v60", "v61", "v62", "v63", "v64", "v65", "v66", "v67", "v68", "v69", "v70", "v71", "v72", "v73", "v74", "v75", "v76", "v77", "v78", "v79", "v80", "v81", "v82", "v83", "v84", "v85", "v86", "v87", "v88", "v89", "v90", "v91", "v92", "v93", "v94", "v95", "v96", "v97", "v98", "v99", "v100", "v101", "v102", "v103", "v104", "v105", "v106", "v107", "v108", "v109", "v110", "v111", "v112", "v113", "v114", "v115", "v116", "v117", "v118", "v119", "v120", "v121", "v122", "v123", "v124", "v125", "v126", "v127", "v128", "v129", "v130", "v131", "v132", "v133", "v134", "v135", "v136", "v137", "v138", "v139", "v140", "v141", "v142", "v143", "v144", "v145", "v146", "v147", "v148", "v149", "v150", "v151", "v152", "v153", "v154", "v155", "v156", "v157", "v158", "v159", "v160", "v161", "v162", "v163", "v164", "v165", "v166", "v167", "v168", "v169", "v170", "v171", "v172", "v173", "v174", "v175", "v176", "v177", "v178", "v179", "v180", "v181", "v182", "v183", "v184", "v185", "v186", "v187", "v188", "v189", "v190", "v191", "v192", "v193", "v194", "v195", "v196", "v197", "v198", "v199", "v200", "v201", "v202", "v203", "v204", "v205", "v206", "v207", "v208", "v209", "v210", "v211", "v212", "v213", "v214", "v215", "v216", "v217", "v218", "v219", "v220", "v221", "v222", "v223", "v224", "v225", "v226", "v227", "v228", "v229", "v230", "v231", "v232", "v233", "v234", "v235", "v236", "v237", "v238", "v239", "v240", "v241", "v242", "v243", "v244", "v245", "v246", "v247", "v248", "v249", "v250", "v251", "v252", "v253", "v254", "v255", "a0", "a1", "a2", "a3", "a4", "a5", "a6", "a7", "a8", "a9", "a10", "a11", "a12", "a13", "a14", "a15", "a16", "a17", "a18", "a19", "a20", "a21", "a22", "a23", "a24", "a25", "a26", "a27", "a28", "a29", "a30", "a31", "a32", "a33", "a34", "a35", "a36", "a37", "a38", "a39", "a40", "a41", "a42", "a43", "a44", "a45", "a46", "a47", "a48", "a49", "a50", "a51", "a52", "a53", "a54", "a55", "a56", "a57", "a58", "a59", "a60", "a61", "a62", "a63", "a64", "a65", "a66", "a67", "a68", "a69", "a70", "a71", "a72", "a73", "a74", "a75", "a76", "a77", "a78", "a79", "a80", "a81", "a82", "a83", "a84", "a85", "a86", "a87", "a88", "a89", "a90", "a91", "a92", "a93", "a94", "a95", "a96", "a97", "a98", "a99", "a100", "a101", "a102", "a103", "a104", "a105", "a106", "a107", "a108", "a109", "a110", "a111", "a112", "a113", "a114", "a115", "a116", "a117", "a118", "a119", "a120", "a121", "a122", "a123", "a124", "a125", "a126", "a127", "a128", "a129", "a130", "a131", "a132", "a133", "a134", "a135", "a136", "a137", "a138", "a139", "a140", "a141", "a142", "a143", "a144", "a145", "a146", "a147", "a148", "a149", "a150", "a151", "a152", "a153", "a154", "a155", "a156", "a157", "a158", "a159", "a160", "a161", "a162", "a163", "a164", "a165", "a166", "a167", "a168", "a169", "a170", "a171", "a172", "a173", "a174", "a175", "a176", "a177", "a178", "a179", "a180", "a181", "a182", "a183", "a184", "a185", "a186", "a187", "a188", "a189", "a190", "a191", "a192", "a193", "a194", "a195", "a196", "a197", "a198", "a199", "a200", "a201", "a202", "a203", "a204", "a205", "a206", "a207", "a208", "a209", "a210", "a211", "a212", "a213", "a214", "a215", "a216", "a217", "a218", "a219", "a220", "a221", "a222", "a223", "a224", "a225", "a226", "a227", "a228", "a229", "a230", "a231", "a232", "a233", "a234", "a235", "a236", "a237", "a238", "a239", "a240", "a241", "a242", "a243", "a244", "a245", "a246", "a247", "a248", "a249", "a250", "a251", "a252", "a253", "a254", "a255", "s48", "s49", "s50", "s51", "s52", "s53", "s54", "s55", "s56", "s57", "s58", "s59", "s60", "s61", "s62", "s63", "s64", "s65", "s66", "s67", "s68", "s69", "s70", "s71", "s72", "s73", "s74", "s75", "s76", "s77", "s78", "s79", "s80", "s81", "s82", "s83", "s84", "s85", "s86", "s87", "s88", "s89", "s90", "s91", "s92", "s93", "s94", "s95", "s96", "s97", "s98", "s99", "vcc", "scc", "memory");
}

extern "C" void kernel_launch(void* const* d_in, const int* in_sizes, int n_in, void* d_out, int out_size, void* d_ws, size_t ws_size, hipStream_t stream) {
  const u16* x = (const u16*)d_in[0]; const u16* kc = (const u16*)d_in[1]; const u16* vc = (const u16*)d_in[2];
  const u16* wq = (const u16*)d_in[3]; const u16* wo = (const u16*)d_in[4];
  const u16* w1 = (const u16*)d_in[6]; const u16* b1 = (const u16*)d_in[7]; const u16* w2 = (const u16*)d_in[8]; const u16* b2 = (const u16*)d_in[9];
  char* ws = (char*)d_ws;
  k2_all<<<256, 256, 0, stream>>>(wq, kc, vc, w1, b1, wo, w2, b2, x, (u16*)d_out, ws);
}
```
